```python
import torch

def _softmax(x, axis=-1):
    return torch.softmax(x, dim=axis)
def _gelu(x):
    return torch.nn.functional.gelu(x, approximate="tanh")
def _silu(x):
    return torch.nn.functional.silu(x)
def _sigmoid(x):
    return torch.sigmoid(x)
def _layernorm(x, axis=-1, eps=1e-5):
    m = x.mean(dim=axis, keepdim=True)
    v = x.var(dim=axis, keepdim=True, unbiased=False)
    return (x - m) / torch.sqrt(v + eps)
def _scatter_add(tgt, idx, src, axis=0):
    return tgt.clone().index_add_(axis, idx, src)
def _segment_sum(src, seg, n):
    out = torch.zeros((n,) + src.shape[1:], dtype=src.dtype)
    return out.index_add_(0, seg, src)
def _pad(x, pw):
    flat = tuple(v for pair in reversed(pw) for v in pair)
    return torch.nn.functional.pad(x, flat)

kernel_name = 'sequential_preLN__ssm_conv_s4d__mixer_token_mlp__prompt'

def setup_inputs():

    _w = (lambda a: (lambda t: t.to(torch.bfloat16) if t.is_floating_point() else t)(torch.as_tensor(a)))
    def _A_setup():
        Bsz, L, D, N = 1, 2048, 576, 128
        return {
            "u": _w((lambda *s: torch.randn(*s, dtype=torch.bfloat16) * 0.05)(Bsz, L, D)),
            "log_A_real": _w((lambda *s: torch.randn(*s, dtype=torch.bfloat16) * 6.4)(D, N) * 0.5),
            "A_imag": _w((lambda *s: torch.randn(*s, dtype=torch.bfloat16) * 0.05)(D, N) * 3.14),
            "C_real": _w((lambda *s: torch.randn(*s, dtype=torch.bfloat16) * 0.05)(D, N) / N ** 0.5),
            "C_imag": _w((lambda *s: torch.randn(*s, dtype=torch.bfloat16) * 0.05)(D, N) / N ** 0.5),
            "log_dt": _w((lambda *s: torch.randn(*s, dtype=torch.bfloat16) * 0.05)(D) * 0.5 - 4.0),
            "D_skip": _w((lambda *s: torch.randn(*s, dtype=torch.bfloat16) * 0.05)(D)),
        }
    _A_in = _A_setup()

    def _B_setup():
        B, S, D, F = 1, 2048, 576, 4608
        return {
            "x": _w((lambda *s: torch.randn(*s, dtype=torch.bfloat16) * 0.05)(B, S, D)),
            "w1": _w((lambda *s: torch.randn(*s, dtype=torch.bfloat16) * 0.4)(S, F) * (S ** -0.5)),
            "b1": _w((lambda *s: torch.randn(*s, dtype=torch.bfloat16) * 0.1)(F) * 0.02),
            "w2": _w((lambda *s: torch.randn(*s, dtype=torch.bfloat16) * 0.1)(F, S) * (F ** -0.5)),
            "b2": _w((lambda *s: torch.randn(*s, dtype=torch.bfloat16) * 0.1)(S) * 0.02),
        }
    _B_in = _B_setup()

    return {
        "A_u": _A_in["u"],
        "A_log_A_real": _A_in["log_A_real"],
        "A_A_imag": _A_in["A_imag"],
        "A_C_real": _A_in["C_real"],
        "A_C_imag": _A_in["C_imag"],
        "A_log_dt": _A_in["log_dt"],
        "A_D_skip": _A_in["D_skip"],
        "B_x": _B_in["x"],
        "B_w1": _B_in["w1"],
        "B_b1": _B_in["b1"],
        "B_w2": _B_in["w2"],
        "B_b2": _B_in["b2"],
    }

def reference(A_u, A_log_A_real, A_A_imag, A_C_real, A_C_imag, A_log_dt, A_D_skip, B_x, B_w1, B_b1, B_w2, B_b2):
    def _A_ref(u, log_A_real, A_imag, C_real, C_imag, log_dt, D_skip):
        L = u.shape[1]
        dt = torch.exp(log_dt)
        A = -torch.exp(log_A_real) + 1j * A_imag
        C = (C_real + 1j * C_imag) * dt[:, None]
        pos = (lambda x, dt: x.to(dt))(torch.arange(L), log_dt.dtype)
        P = torch.exp((A * dt[:, None])[:, :, None] * pos[None, None, :])
        K = 2.0 * torch.einsum("dn,dnl->dl", C, P).real
        L2 = 2 * L
        u_t = (lambda x, *d: x.permute(*d))(u, 0, 2, 1)
        Kf = (lambda x, *a, **k: torch.fft.rfft(x.float(), *a, **k))(K, L2)
        Uf = (lambda x, *a, **k: torch.fft.rfft(x.float(), *a, **k))(u_t, L2)
        y = (lambda x, *a, **k: torch.fft.irfft(x, *a, **k).to(torch.bfloat16))(Uf * Kf[None, :, :], L2)[:, :, :L]
        y = (lambda x, *d: x.permute(*d))(y, 0, 2, 1)
        return y + u * D_skip[None, None, :]

    def _B_ref(x, w1, b1, w2, b2):
        B, S, D = x.shape
        h = _layernorm(x, axis=-1)
        u = torch.einsum("bsd,sf->bdf", h, w1) + b1
        u = _gelu(u)
        y = torch.einsum("bdf,fs->bds", u, w2) + b2
        return (lambda x, *d: x.permute(*d))(y, 0, 2, 1)

    x = A_u
    h = x + _A_ref(_layernorm(x, axis=-1), A_log_A_real, A_A_imag, A_C_real, A_C_imag, A_log_dt, A_D_skip)
    h2 = h + _B_ref(_layernorm(h, axis=-1), B_w1, B_b1, B_w2, B_b2)
    return h2
```

```cpp
#include <hip/hip_runtime.h>
#include <hip/hip_bf16.h>
#include <stdint.h>

typedef unsigned short u16;
#define L_ 2048
#define D_ 576
#define N_ 128
#define F_ 4608

__device__ __forceinline__ float bf2f(u16 v) { return __uint_as_float(((unsigned)v) << 16); }
__device__ __forceinline__ u16 f2bf(float f) { __hip_bfloat16 b = __float2bfloat16(f); return *reinterpret_cast<u16*>(&b); }
__device__ __forceinline__ float rbf(float f) { return bf2f(f2bf(f)); }

__device__ __forceinline__ float wave_sum(float v) {
  for (int o = 32; o > 0; o >>= 1) v += __shfl_xor(v, o);
  return v;
}
__device__ __forceinline__ void ln9(const float* x, float* o) {
  float s = 0; for (int i = 0; i < 9; ++i) s += x[i];
  s = wave_sum(s);
  float mean = s * (1.0f / D_);
  float q = 0; for (int i = 0; i < 9; ++i) { float t = x[i] - mean; q += t * t; }
  q = wave_sum(q);
  float var = q * (1.0f / D_);
  float mb = rbf(mean), vb = rbf(var);
  float sd = rbf(sqrtf(rbf(vb + 1e-5f)));
  for (int i = 0; i < 9; ++i) o[i] = rbf(rbf(x[i] - mb) / sd);
}

__global__ void k_ln1(const u16* x, u16* uT) {
  int l = blockIdx.x; int lane = threadIdx.x;
  float v[9], o[9];
  for (int i = 0; i < 9; ++i) v[i] = bf2f(x[l * D_ + lane + 64 * i]);
  ln9(v, o);
  for (int i = 0; i < 9; ++i) uT[(lane + 64 * i) * L_ + l] = f2bf(o[i]);
}

__global__ void k_kgen(const u16* logA, const u16* Aim, const u16* Cre, const u16* Cim, const u16* logdt, float* K) {
  int d = blockIdx.x;
  __shared__ float zr[N_], zi[N_], cr[N_], ci[N_];
  float dt = rbf(expf(bf2f(logdt[d])));
  if (threadIdx.x < N_) {
    int n = threadIdx.x;
    float ar = -rbf(expf(bf2f(logA[d * N_ + n])));
    zr[n] = ar * dt; zi[n] = bf2f(Aim[d * N_ + n]) * dt;
    cr[n] = bf2f(Cre[d * N_ + n]) * dt; ci[n] = bf2f(Cim[d * N_ + n]) * dt;
  }
  __syncthreads();
  for (int l = threadIdx.x; l < L_; l += blockDim.x) {
    float p = rbf((float)l);
    float acc = 0;
    for (int n = 0; n < N_; ++n) {
      float e = expf(zr[n] * p); float s, c; sincosf(zi[n] * p, &s, &c);
      acc += cr[n] * e * c - ci[n] * e * s;
    }
    K[d * L_ + l] = 2.0f * acc;
  }
}

__global__ void k_conv(const float* K, const u16* uT, u16* yT) {
  int d = blockIdx.x;
  __shared__ float ks[L_], us[L_];
  for (int l = threadIdx.x; l < L_; l += blockDim.x) { ks[l] = K[d * L_ + l]; us[l] = bf2f(uT[d * L_ + l]); }
  __syncthreads();
  for (int l = threadIdx.x; l < L_; l += blockDim.x) {
    float acc = 0;
    for (int j = 0; j <= l; ++j) acc += ks[j] * us[l - j];
    yT[d * L_ + l] = f2bf(acc);
  }
}

__global__ void k_ln23(const u16* x, const u16* uT, const u16* yT, const u16* Dsk, u16* h, u16* hnT) {
  int l = blockIdx.x; int lane = threadIdx.x;
  float v[9], o[9], o2[9];
  for (int i = 0; i < 9; ++i) {
    int d = lane + 64 * i;
    float u = bf2f(uT[d * L_ + l]), y = bf2f(yT[d * L_ + l]);
    float a = rbf(y + rbf(u * bf2f(Dsk[d])));
    v[i] = rbf(bf2f(x[l * D_ + d]) + a);
    h[l * D_ + d] = f2bf(v[i]);
  }
  ln9(v, o); ln9(o, o2);
  for (int i = 0; i < 9; ++i) hnT[(lane + 64 * i) * L_ + l] = f2bf(o2[i]);
}

__device__ __forceinline__ float gelu_t(float x) {
  float t = 0.7978845608028654f * (x + 0.044715f * x * x * x);
  return 0.5f * x * (1.0f + tanhf(t));
}
__global__ void k_gemm1(const u16* hnT, const u16* w1, const u16* b1, u16* g) {
  int f = blockIdx.x * 256 + threadIdx.x; int d0 = blockIdx.y * 8;
  __shared__ float a[8][L_];
  for (int i = threadIdx.x; i < 8 * L_; i += 256) a[i / L_][i % L_] = bf2f(hnT[d0 * L_ + i]);
  __syncthreads();
  float acc[8] = {};
  for (int s = 0; s < L_; ++s) { float w = bf2f(w1[(size_t)s * F_ + f]); for (int i = 0; i < 8; ++i) acc[i] += a[i][s] * w; }
  for (int i = 0; i < 8; ++i) { float u = rbf(rbf(acc[i]) + bf2f(b1[f])); g[(d0 + i) * F_ + f] = f2bf(gelu_t(u)); }
}
__global__ void k_gemm2(const u16* g, const u16* w2, const u16* b2, const u16* h, u16* out) {
  int s = blockIdx.x * 256 + threadIdx.x; int d0 = blockIdx.y * 4;
  __shared__ float a[4][F_];
  for (int i = threadIdx.x; i < 4 * F_; i += 256) a[i / F_][i % F_] = bf2f(g[d0 * F_ + i]);
  __syncthreads();
  float acc[4] = {};
  for (int f = 0; f < F_; ++f) { float w = bf2f(w2[(size_t)f * L_ + s]); for (int i = 0; i < 4; ++i) acc[i] += a[i][f] * w; }
  for (int i = 0; i < 4; ++i) { float y = rbf(rbf(acc[i]) + bf2f(b2[s])); out[s * D_ + d0 + i] = f2bf(bf2f(h[s * D_ + d0 + i]) + y); }
}

extern "C" void kernel_launch(void* const* d_in, const int* in_sizes, int n_in,
                              void* d_out, int out_size, void* d_ws, size_t ws_size,
                              hipStream_t stream) {
  const u16* x = (const u16*)d_in[0];
  const u16* logA = (const u16*)d_in[1];
  const u16* Aim = (const u16*)d_in[2];
  const u16* Cre = (const u16*)d_in[3];
  const u16* Cim = (const u16*)d_in[4];
  const u16* logdt = (const u16*)d_in[5];
  const u16* Dsk = (const u16*)d_in[6];
  const u16* w1 = (const u16*)d_in[8];
  const u16* b1 = (const u16*)d_in[9];
  const u16* w2 = (const u16*)d_in[10];
  const u16* b2 = (const u16*)d_in[11];
  char* ws = (char*)d_ws;
  const size_t SZ = (size_t)L_ * D_ * 2;
  u16* uT = (u16*)(ws); u16* yT = (u16*)(ws + SZ); u16* h = (u16*)(ws + 2 * SZ); u16* hnT = (u16*)(ws + 3 * SZ);
  float* K = (float*)(ws + 4 * SZ); u16* g = (u16*)(ws + 6 * SZ);
  k_ln1<<<L_, 64, 0, stream>>>(x, uT);
  k_kgen<<<D_, 256, 0, stream>>>(logA, Aim, Cre, Cim, logdt, K);
  k_conv<<<D_, 256, 0, stream>>>(K, uT, yT);
  k_ln23<<<L_, 64, 0, stream>>>(x, uT, yT, Dsk, h, hnT);
  k_gemm1<<<dim3(F_ / 256, D_ / 8), 256, 0, stream>>>(hnT, w1, b1, g);
  k_gemm2<<<dim3(L_ / 256, D_ / 4), 256, 0, stream>>>(g, w2, b2, h, (u16*)d_out);
}
```

```cpp
#include <hip/hip_runtime.h>
#include <stdint.h>

typedef unsigned short u16;
typedef __attribute__((ext_vector_type(4))) short s16x4;
typedef __attribute__((ext_vector_type(8))) short s16x8;
typedef __attribute__((ext_vector_type(8))) __bf16 bf16x8;
typedef __attribute__((ext_vector_type(2))) __bf16 bf16x2;
typedef __attribute__((ext_vector_type(4))) float f32x4;
typedef __attribute__((ext_vector_type(2))) float f32x2;
typedef __attribute__((ext_vector_type(4))) unsigned u32x4;
typedef __attribute__((ext_vector_type(2))) unsigned u32x2;
typedef __attribute__((ext_vector_type(4))) int i32x4;
#define LAS __attribute__((address_space(3)))
#define DI __device__ __forceinline__

constexpr int L_ = 2048, D_ = 576, N_ = 128, F_ = 4608;

DI float bflo(unsigned w) { return __uint_as_float(w << 16); }
DI float bfhi(unsigned w) { return __uint_as_float(w & 0xffff0000u); }
DI float bf2f(u16 v) { return __uint_as_float(((unsigned)v) << 16); }
DI unsigned pk2(float lo, float hi) { f32x2 v = {lo, hi}; return __builtin_bit_cast(unsigned, __builtin_convertvector(v, bf16x2)); }
DI float rbf(float f) { return bflo(pk2(f, f)); }
DI unsigned perm_lo(unsigned a, unsigned b) { return __builtin_amdgcn_perm(b, a, 0x05040100u); }
DI unsigned perm_hi(unsigned a, unsigned b) { return __builtin_amdgcn_perm(b, a, 0x07060302u); }
DI f32x4 mfma16(u32x4 a, u32x4 b, f32x4 c) { return __builtin_amdgcn_mfma_f32_16x16x32_bf16(__builtin_bit_cast(bf16x8, a), __builtin_bit_cast(bf16x8, b), c, 0, 0, 0); }

DI float half_sum(float v) {
  v += __shfl_xor(v, 1); v += __shfl_xor(v, 2); v += __shfl_xor(v, 4); v += __shfl_xor(v, 8); v += __shfl_xor(v, 16);
  return v;
}
DI void ln18(float* v) {
  float s = 0.f;
#pragma unroll
  for (int i = 0; i < 18; ++i) s += v[i];
  const float mean = half_sum(s) * (1.0f / D_);
  float q = 0.f;
#pragma unroll
  for (int i = 0; i < 18; ++i) { const float t = v[i] - mean; q += t * t; }
  const float var = half_sum(q) * (1.0f / D_);
  const float mb = rbf(mean);
  const float sd = rbf(__builtin_sqrtf(rbf(rbf(var) + 1e-5f)));
  const float inv = 1.0f / sd;
#pragma unroll
  for (int i = 0; i < 18; i += 2) {
    const unsigned t = pk2(v[i] - mb, v[i + 1] - mb);
    const unsigned o = pk2(bflo(t) * inv, bfhi(t) * inv);
    v[i] = bflo(o); v[i + 1] = bfhi(o);
  }
}

struct cpx { float r, i; };
DI cpx cmul(cpx a, cpx b) { cpx o; o.r = a.r * b.r - a.i * b.i; o.i = a.r * b.i + a.i * b.r; return o; }
DI cpx cexp_p(float zl, float zv, float p) {
  const float m = __builtin_amdgcn_exp2f(zl * p);
  const float rev = __builtin_amdgcn_fractf(zv * p);
  cpx o; o.r = m * __builtin_amdgcn_cosf(rev); o.i = m * __builtin_amdgcn_sinf(rev); return o;
}

constexpr int KG_PITCH = 528;
constexpr int KG_A = 0, KG_B = 41 * KG_PITCH, KG_T = KG_B + 16 * KG_PITCH;
constexpr int P1_LDS = KG_T + 2064 * 2;
constexpr int T_PITCH = 288 + 8;

__global__ __launch_bounds__(256) void k_p1(const u16* __restrict__ x, const u16* __restrict__ logA, const u16* __restrict__ Aim,
                                            const u16* __restrict__ Cre, const u16* __restrict__ Cim, const u16* __restrict__ logdt,
                                            u16* __restrict__ uT, u16* __restrict__ Rg) {
  __shared__ __attribute__((aligned(16))) char smem[P1_LDS];
  const int tid = threadIdx.x, lane = tid & 63, wid = __builtin_amdgcn_readfirstlane(tid >> 6);
  const int bid = blockIdx.x;
  if (bid < D_) {
    const int d = (bid & 7) * 72 + (bid >> 3);
    const int n = (wid & 1) * 64 + lane;
    const float la = bf2f(logA[d * N_ + n]), ai = bf2f(Aim[d * N_ + n]), c0 = bf2f(Cre[d * N_ + n]), c1 = bf2f(Cim[d * N_ + n]);
    const float dt = rbf(expf(bf2f(logdt[d])));
    const float zr = -rbf(expf(la)) * dt, zi = ai * dt;
    const float zl = zr * 1.4426950408889634f, zv = zi * 0.15915494309189535f;
    cpx G; G.r = 2.0f * (c0 * dt); G.i = 2.0f * (c1 * dt);
    unsigned* At = (unsigned*)(smem + KG_A) + n;
    unsigned* Bt = (unsigned*)(smem + KG_B) + n;
#define PUT_A(row, v) At[(row) * (KG_PITCH / 4)] = pk2((v).r, -(v).i)
#define PUT_B(row, v) Bt[(row) * (KG_PITCH / 4)] = pk2((v).r, (v).i)
    const cpx E128 = cexp_p(zl, zv, 128.f);
    if (wid < 2) {
      cpx E[8];
      E[1] = cexp_p(zl, zv, 1.f); E[2] = cexp_p(zl, zv, 2.f); E[4] = cexp_p(zl, zv, 4.f);
      E[3] = cmul(E[1], E[2]); E[5] = cmul(E[1], E[4]); E[6] = cmul(E[2], E[4]); E[7] = cmul(E[3], E[4]);
#pragma unroll
      for (int t = 0; t < 2; ++t) {
        PUT_A(8 * t, G);
#pragma unroll
        for (int r = 1; r < 8; ++r) { const cpx v = cmul(G, E[r]); PUT_A(8 * t + r, v); }
        G = cmul(G, E128);
      }
#pragma unroll
      for (int t = 0; t < 2; ++t) {
        PUT_A(16 + 4 * t, G);
#pragma unroll
        for (int r = 1; r < 4; ++r) { const cpx v = cmul(G, E[2 * r]); PUT_A(16 + 4 * t + r, v); }
        if (t == 0) G = cmul(G, E128);
      }
    } else {
      const cpx E8 = cexp_p(zl, zv, 8.f), E64 = cexp_p(zl, zv, 64.f), E4 = cexp_p(zl, zv, 4.f);
      const cpx E512 = cexp_p(zl, zv, 512.f), E1024 = cexp_p(zl, zv, 1024.f);
      cpx b0; b0.r = 1.f; b0.i = 0.f;
      cpx b1 = E64;
      PUT_B(0, b0); PUT_B(8, b1);
      b0 = E8; b1 = cmul(b1, E8);
#pragma unroll
      for (int j = 1; j < 8; ++j) { PUT_B(j, b0); PUT_B(8 + j, b1); if (j < 7) { b0 = cmul(b0, E8); b1 = cmul(b1, E8); } }
      cpx g0 = cmul(G, E512), g1 = cmul(G, E1024);
#pragma unroll
      for (int t = 0; t < 4; ++t) { PUT_A(24 + 2 * t, g0); const cpx v = cmul(g0, E4); PUT_A(25 + 2 * t, v); if (t < 3) g0 = cmul(g0, E128); }
#pragma unroll
      for (int t = 0; t < 9; ++t) { PUT_A(32 + t, g1); if (t < 8) g1 = cmul(g1, E128); }
    }
    __syncthreads();
    u16* Kt = (u16*)(smem + KG_T);
    if (wid < 3) {
      const int r = lane & 15, q = lane >> 4;
      int row = wid * 16 + r; row = row > 40 ? 40 : row;
      const char* ap = smem + KG_A + row * KG_PITCH + q * 16;
      const char* bp = smem + KG_B + r * KG_PITCH + q * 16;
      f32x4 acc = {0.f, 0.f, 0.f, 0.f};
#pragma unroll
      for (int s = 0; s < 8; ++s) acc = mfma16(*(const u32x4*)(ap + s * 64), *(const u32x4*)(bp + s * 64), acc);
#pragma unroll
      for (int e = 0; e < 4; ++e) {
        const int rho = wid * 16 + 4 * q + e;
        int p0;
        if (rho < 16) p0 = (rho & 7) + 128 * (rho >> 3);
        else if (rho < 24) p0 = 256 + 128 * ((rho - 16) >> 2) + 2 * (rho & 3);
        else if (rho < 32) p0 = 512 + 128 * ((rho - 24) >> 1) + 4 * (rho & 1);
        else p0 = 1024 + 128 * (rho - 32);
        const int p = p0 + 8 * r;
        if (p <= 2048) Kt[p] = (u16)pk2(acc[e], acc[e]);
      }
    }
    __syncthreads();
    unsigned w[4];
#pragma unroll
    for (int k = 0; k < 4; ++k) {
      const int j0 = 2047 - (8 * tid + 2 * k);
      const unsigned pr = pk2((float)j0, (float)(j0 - 1));
      w[k] = (unsigned)Kt[(int)bflo(pr)] | ((unsigned)Kt[(int)bfhi(pr)] << 16);
    }
    *(u32x4*)(Rg + d * L_ + 8 * tid) = u32x4{w[0], w[1], w[2], w[3]};
  } else {
    const int b = bid - D_;
    const int tb = (b & 7) * 32 + (b >> 3);
    const int tok = wid * 2 + (lane >> 5), ll = lane & 31;
    const unsigned* xr = (const unsigned*)(x + (tb * 8 + tok) * D_) + ll;
    float v[18];
#pragma unroll
    for (int i = 0; i < 9; ++i) { const unsigned w = xr[32 * i]; v[2 * i] = bflo(w); v[2 * i + 1] = bfhi(w); }
    ln18(v);
    unsigned* T = (unsigned*)smem;
#pragma unroll
    for (int i = 0; i < 9; ++i) T[tok * T_PITCH + 32 * i + ll] = pk2(v[2 * i], v[2 * i + 1]);
    __syncthreads();
    for (int cp = tid; cp < 288; cp += 256) {
      unsigned w[8];
#pragma unroll
      for (int t = 0; t < 8; ++t) w[t] = T[t * T_PITCH + cp];
      *(u32x4*)(uT + (2 * cp) * L_ + tb * 8) = u32x4{perm_lo(w[0], w[1]), perm_lo(w[2], w[3]), perm_lo(w[4], w[5]), perm_lo(w[6], w[7])};
      *(u32x4*)(uT + (2 * cp + 1) * L_ + tb * 8) = u32x4{perm_hi(w[0], w[1]), perm_hi(w[2], w[3]), perm_hi(w[4], w[5]), perm_hi(w[6], w[7])};
    }
  }
}

constexpr int CV_KP = 259 * 16 + 16;
constexpr int CV_U = 4 * CV_KP;
constexpr int CV_UP = (29 + 256) * 16 + 64 + 16;
constexpr int P2_LDS = 32768;
static_assert(CV_KP % 256 == 64, "");
static_assert(CV_U + 2 * CV_UP <= P2_LDS, "");

__global__ __launch_bounds__(256) void k_p2(const u16* __restrict__ uT, const u16* __restrict__ Rg, const u16* __restrict__ Dsk,
                                            u16* __restrict__ aB) {
  __shared__ __attribute__((aligned(16))) char smem[P2_LDS];
  const int tid = threadIdx.x, lane = tid & 63, wid = __builtin_amdgcn_readfirstlane(tid >> 6);
  const int d = (blockIdx.x & 7) * 72 + (blockIdx.x >> 3);
  const u32x4 kw = *(const u32x4*)(Rg + d * L_ + 8 * tid);
  const u32x4 uw = *(const u32x4*)(uT + d * L_ + 8 * tid);
  u32x2 kn = {0u, 0u}, up = {0u, 0u};
  if (tid < 255) kn = *(const u32x2*)(Rg + d * L_ + 8 * tid + 8);
  if (tid > 0) up = *(const u32x2*)(uT + d * L_ + 8 * tid - 4);
  const float dsk = bf2f(Dsk[d]);
  if (tid < 12) *(u32x4*)(smem + (tid & 3) * CV_KP + (256 + (tid >> 2)) * 16) = u32x4{0u, 0u, 0u, 0u};
  if (tid >= 64 && tid < 64 + 58) { const int i = tid - 64; *(u32x4*)(smem + CV_U + (i & 1) * CV_UP + (i >> 1) * 16) = u32x4{0u, 0u, 0u, 0u}; }
  {
    const unsigned a01 = __builtin_amdgcn_alignbit(kw.y, kw.x, 16), a12 = __builtin_amdgcn_alignbit(kw.z, kw.y, 16),
                   a23 = __builtin_amdgcn_alignbit(kw.w, kw.z, 16), a34 = __builtin_amdgcn_alignbit(kn.x, kw.w, 16),
                   a45 = __builtin_amdgcn_alignbit(kn.y, kn.x, 16);
    char* kp = smem + tid * 16;
    *(u32x4*)(kp + 3 * CV_KP) = kw;
    *(u32x4*)(kp + 2 * CV_KP) = u32x4{a01, a12, a23, a34};
    *(u32x4*)(kp + 1 * CV_KP) = u32x4{kw.y, kw.z, kw.w, kn.x};
    *(u32x4*)(kp + 0 * CV_KP) = u32x4{a12, a23, a34, a45};
    char* upp = smem + CV_U + (29 + tid) * 16;
    *(u32x4*)(upp) = u32x4{up.x, up.y, uw.x, uw.y};
    *(u32x4*)(upp + CV_UP) = uw;
  }
  __syncthreads();
  const int r = lane & 15, q = lane >> 4;
  f32x4 acc[8];
#pragma unroll
  for (int c = 0; c < 8; ++c) acc[c] = f32x4{0.f, 0.f, 0.f, 0.f};
#pragma unroll
  for (int ss = 0; ss < 2; ++ss) {
    const int sg = 2 * wid + ss;
    const char* ap = smem + (r & 3) * CV_KP + 2 * (2064 - 256 * 7 - 8 * (r >> 2) - 8 * q - 32 * sg);
    const char* bp = smem + CV_U + (r & 1) * CV_UP + 2 * (232 + 32 * (r >> 1) - 32 * sg + 24 - 8 * q);
    u32x4 A[8], B[8];
#pragma unroll
    for (int c = 0; c < 8; ++c) { A[c] = *(const u32x4*)(ap + 512 * (7 - c)); B[c] = *(const u32x4*)(bp + 512 * c); }
#pragma unroll
    for (int c1 = 0; c1 < 8; ++c1)
#pragma unroll
      for (int c2 = 0; c2 < 8 - c1; ++c2) acc[c1 + c2] = mfma16(A[c1], B[c2], acc[c1 + c2]);
  }
  __syncthreads();
  {
    const int slot = (r & 1) + 2 * (q ^ ((r >> 1) & 3)) + 8 * (r >> 1);
    char* pp = smem + wid * 8192 + slot * 16;
#pragma unroll
    for (int c = 0; c < 8; ++c) *(f32x4*)(pp + c * 1024) = acc[c];
  }
  __syncthreads();
  {
    const int t5 = tid & 31;
    const int slot = 2 * ((t5 & 3) ^ ((t5 >> 2) & 3)) + 8 * (t5 >> 2);
    const char* pp = smem + (tid >> 5) * 1024 + slot * 16;
    f32x4 y0 = *(const f32x4*)(pp), y1 = *(const f32x4*)(pp + 16);
#pragma unroll
    for (int w = 1; w < 4; ++w) { y0 += *(const f32x4*)(pp + w * 8192); y1 += *(const f32x4*)(pp + w * 8192 + 16); }
    const float y[8] = {y0.x, y0.y, y0.z, y0.w, y1.x, y1.y, y1.z, y1.w};
    unsigned o[4];
#pragma unroll
    for (int k = 0; k < 4; ++k) {
      const unsigned yb = pk2(y[2 * k], y[2 * k + 1]);
      const unsigned sk = pk2(bflo(uw[k]) * dsk, bfhi(uw[k]) * dsk);
      o[k] = pk2(bflo(yb) + bflo(sk), bfhi(yb) + bfhi(sk));
    }
    *(u32x4*)(aB + (tid * D_ + d) * 8) = u32x4{o[0], o[1], o[2], o[3]};
  }
}

__global__ __launch_bounds__(256) void k_p3(const u16* __restrict__ x, const u16* __restrict__ aB, u16* __restrict__ h, u16* __restrict__ hf) {
  __shared__ __attribute__((aligned(16))) unsigned T[8 * T_PITCH];
  const int tid = threadIdx.x, lane = tid & 63, wid = __builtin_amdgcn_readfirstlane(tid >> 6);
  const int tb = blockIdx.x;
  const int tok = wid * 2 + (lane >> 5), ll = lane & 31;
  const u32x4* ap = (const u32x4*)(aB + tb * D_ * 8);
  const u32x4 e0 = ap[2 * tid], o0 = ap[2 * tid + 1];
  u32x4 e1 = {0u, 0u, 0u, 0u}, o1 = {0u, 0u, 0u, 0u};
  if (tid < 32) { e1 = ap[512 + 2 * tid]; o1 = ap[512 + 2 * tid + 1]; }
  const unsigned* xr = (const unsigned*)(x + (tb * 8 + tok) * D_) + ll;
  unsigned xw[9];
#pragma unroll
  for (int i = 0; i < 9; ++i) xw[i] = xr[32 * i];
#pragma unroll
  for (int k = 0; k < 4; ++k) { T[(2 * k) * T_PITCH + tid] = perm_lo(e0[k], o0[k]); T[(2 * k + 1) * T_PITCH + tid] = perm_hi(e0[k], o0[k]); }
  if (tid < 32) {
#pragma unroll
    for (int k = 0; k < 4; ++k) { T[(2 * k) * T_PITCH + 256 + tid] = perm_lo(e1[k], o1[k]); T[(2 * k + 1) * T_PITCH + 256 + tid] = perm_hi(e1[k], o1[k]); }
  }
  __syncthreads();
  float v[18];
  unsigned* hr = (unsigned*)(h + (tb * 8 + tok) * D_) + ll;
#pragma unroll
  for (int i = 0; i < 9; ++i) {
    const unsigned a = T[tok * T_PITCH + 32 * i + ll];
    const unsigned hw = pk2(bflo(xw[i]) + bflo(a), bfhi(xw[i]) + bfhi(a));
    hr[32 * i] = hw;
    v[2 * i] = bflo(hw); v[2 * i + 1] = bfhi(hw);
  }
  ln18(v);
  ln18(v);
#pragma unroll
  for (int i = 0; i < 9; ++i) T[tok * T_PITCH + 32 * i + ll] = pk2(v[2 * i], v[2 * i + 1]);
  __syncthreads();
  for (int cp = tid; cp < 288; cp += 256) {
    unsigned w[8];
#pragma unroll
    for (int t = 0; t < 8; ++t) w[t] = T[t * T_PITCH + cp];
    const int ch = 2 * cp, mt = ch / 96, rt = (ch % 96) >> 4, r = ch & 15;
    u32x4* dst = (u32x4*)hf + (((mt * 64 + (tb >> 2)) * 6 + rt) * 64 + (tb & 3) * 16 + r);
    dst[0] = u32x4{perm_lo(w[0], w[1]), perm_lo(w[2], w[3]), perm_lo(w[4], w[5]), perm_lo(w[6], w[7])};
    dst[1] = u32x4{perm_hi(w[0], w[1]), perm_hi(w[2], w[3]), perm_hi(w[4], w[5]), perm_hi(w[6], w[7])};
  }
}

DI i32x4 mk_srd(const void* p, unsigned bytes) {
  const unsigned long long a = (unsigned long long)p;
  i32x4 r; r.x = (int)(unsigned)a; r.y = (int)((unsigned)(a >> 32) & 0xffffu); r.z = (int)bytes; r.w = 0x00020000; return r;
}
#define BLOAD(dst, voff, srd, soff, imm) asm volatile("buffer_load_dwordx4 %0, %1, %2, %3 offen offset:%4" : "=v"(dst) : "v"(voff), "s"(srd), "s"(soff), "i"(imm) : "memory")
#define BDMA(ldsbase, ldsimm, voff, srd, soff) asm volatile("s_add_u32 m0, %0, %1\n\ts_nop 0\n\tbuffer_load_dwordx4 %2, %3, %4 offen lds" :: "s"(ldsbase), "i"(ldsimm), "v"(voff), "s"(srd), "s"(soff) : "memory", "scc")
#define SB0 __builtin_amdgcn_sched_barrier(0)
#define MFMA_A(acc, a, b) asm volatile("v_mfma_f32_16x16x32_bf16 %0, %1, %2, %0" : "+a"(acc) : "v"(a), "v"(b))
DI u32x2 trd(unsigned addr) { return __builtin_bit_cast(u32x2, __builtin_amdgcn_ds_read_tr16_b64_v4i16((LAS s16x4*)addr)); }
DI u32x4 cat(u32x2 a, u32x2 b) { return u32x4{a.x, a.y, b.x, b.y}; }

DI float gelu_f(float x) {
  const float e = __builtin_amdgcn_exp2f(x * (-2.302208198f - 0.1029432397f * x * x));
  return x * __builtin_amdgcn_rcpf(1.0f + e);
}

constexpr int G1_WS = 36864;
__global__ __launch_bounds__(256, 1) void k_g1(const u16* __restrict__ hf, const u16* __restrict__ w1, const u16* __restrict__ b1, u16* __restrict__ gf) {
  __shared__ __attribute__((aligned(16))) char smem[4 * G1_WS];
  const int tid = threadIdx.x, lane = tid & 63, wid = __builtin_amdgcn_readfirstlane(tid >> 6);
  const int lin = (blockIdx.x & 7) * 27 + (blockIdx.x >> 3);
  const int mt = lin % 6, nt = lin / 6;
  const i32x4 sa = mk_srd(hf, D_ * L_ * 2), sb = mk_srd(w1, L_ * F_ * 2);
  const unsigned ring = (unsigned)(size_t)smem + wid * G1_WS;
  const unsigned va0 = lane * 16, va1 = lane * 16 + 4096;
  unsigned vb[8];
#pragma unroll
  for (int j = 0; j < 8; ++j) vb[j] = (4 * j + (lane >> 4)) * (F_ * 2) + (((lane & 15) ^ ((lane >> 4) << 2)) << 4);
  const int g = lane >> 4, q = (lane & 15) >> 2, p = lane & 3;
  unsigned rb[4];
#pragma unroll
  for (int m = 0; m < 4; ++m) rb[m] = ring + 256 * (8 * g + q) + 64 * (((m + wid) & 3) ^ q) + 16 * p;
  const unsigned a_base = (mt * 64 + wid) * 6144, b_base = nt * 256 + wid * 32 * (F_ * 2);
  const unsigned OOB = 0x7ff00000u;
  u32x4 A[4][6];
  f32x4 acc[4][2][6];
#pragma unroll
  for (int m = 0; m < 4; ++m)
#pragma unroll
    for (int c = 0; c < 2; ++c)
#pragma unroll
      for (int t = 0; t < 6; ++t) acc[m][c][t] = f32x4{0.f, 0.f, 0.f, 0.f};

#define G1_LDA(S, so, t) do { if ((t) < 4) BLOAD(A[S][t], va0, sa, so, (t) * 1024); else BLOAD(A[S][t], va1, sa, so, ((t) - 4) * 1024); } while (0)
#define G1_LDB(S, so, j) BDMA(ring, (S) * 8192 + (j) * 1024, vb[j], sb, so)
#pragma unroll
  for (int s = 0; s < 3; ++s) {
    const unsigned ao = a_base + s * 4 * 6144, bo = b_base + s * 128 * (F_ * 2);
#pragma unroll
    for (int j = 0; j < 8; ++j) G1_LDB(s, bo, j);
#pragma unroll
    for (int t = 0; t < 6; ++t) G1_LDA(s, ao, t);
  }
  u32x4 Bf[2][2];
#define G1_RDB(buf, S, m) do { \
    Bf[buf][0] = cat(trd(rb[m] + (S) * 8192), trd(rb[m] + (S) * 8192 + 1024)); \
    Bf[buf][1] = cat(trd(rb[m] + (S) * 8192 + 8), trd(rb[m] + (S) * 8192 + 8 + 1024)); } while (0)
#define G1_WAIT(S) asm volatile("s_waitcnt vmcnt(28)" : "+v"(A[S][0]), "+v"(A[S][1]), "+v"(A[S][2]), "+v"(A[S][3]), "+v"(A[S][4]), "+v"(A[S][5]) :: "memory")
#define G1_MM(buf, S, m, c, t0) do { \
    MFMA_A(acc[m][c][t0], Bf[buf][c], A[S][t0]); \
    MFMA_A(acc[m][c][t0 + 1], Bf[buf][c], A[S][t0 + 1]); \
    MFMA_A(acc[m][c][t0 + 2], Bf[buf][c], A[S][t0 + 2]); } while (0)
#pragma unroll
  for (int m = 0; m < 4; ++m)
#pragma unroll
    for (int c = 0; c < 2; ++c)
#pragma unroll
      for (int t = 0; t < 6; ++t) asm volatile("" : "+a"(acc[m][c][t]));
  G1_WAIT(0);
  G1_RDB(0, 0, 0);
  SB0;
#pragma nounroll
  for (int it = 0; it < 4; ++it) {
#pragma unroll
    for (int S = 0; S < 4; ++S) {
      const int i3 = it * 4 + S + 3;
      const unsigned ao = i3 < 16 ? a_base + i3 * 4 * 6144 : OOB, bo = i3 < 16 ? b_base + i3 * 128 * (F_ * 2) : OOB;
      const int P = (S + 3) & 3, Nx = (S + 1) & 3;
      G1_RDB(1, S, 1); SB0;
      G1_MM(0, S, 0, 0, 0); G1_LDB(P, bo, 0); G1_MM(0, S, 0, 0, 3); G1_LDB(P, bo, 1); G1_MM(0, S, 0, 1, 0); G1_LDB(P, bo, 2); G1_MM(0, S, 0, 1, 3); G1_LDB(P, bo, 3);
      G1_RDB(0, S, 2); G1_LDA(P, ao, 0); SB0;
      G1_MM(1, S, 1, 0, 0); G1_LDB(P, bo, 4); G1_MM(1, S, 1, 0, 3); G1_LDB(P, bo, 5); G1_MM(1, S, 1, 1, 0); G1_LDB(P, bo, 6); G1_MM(1, S, 1, 1, 3); G1_LDB(P, bo, 7);
      G1_RDB(1, S, 3); G1_LDA(P, ao, 1); SB0;
      G1_MM(0, S, 2, 0, 0); G1_LDA(P, ao, 2); G1_MM(0, S, 2, 0, 3); G1_LDA(P, ao, 3); G1_MM(0, S, 2, 1, 0); G1_LDA(P, ao, 4); G1_MM(0, S, 2, 1, 3); G1_LDA(P, ao, 5);
      G1_WAIT(Nx);
      G1_RDB(0, Nx, 0); SB0;
      G1_MM(1, S, 3, 0, 0); G1_MM(1, S, 3, 0, 3); G1_MM(1, S, 3, 1, 0); G1_MM(1, S, 3, 1, 3);
    }
  }
  asm volatile("s_nop 7\n\ts_waitcnt vmcnt(0)" : "+v"(A[1][0]), "+v"(A[1][1]), "+v"(A[1][2]), "+v"(A[1][3]), "+v"(A[1][4]), "+v"(A[1][5]),
                                      "+v"(A[2][0]), "+v"(A[2][1]), "+v"(A[2][2]), "+v"(A[2][3]), "+v"(A[2][4]), "+v"(A[2][5]) :: "memory");
  asm volatile("" :: "v"(Bf[0][0]), "v"(Bf[0][1]));
  SB0;
  {
    char* o = smem + wid * G1_WS + lane * 16;
#pragma unroll
    for (int m = 1; m < 4; ++m)
#pragma unroll
      for (int c = 0; c < 2; ++c)
#pragma unroll
        for (int t = 0; t < 6; ++t) *(f32x4*)(o + ((m - 1) * 12 + c * 6 + t) * 1024) = acc[m][c][t];
  }
  const int f0 = nt * 128 + wid * 32 + 8 * g;
  const u32x4 bw = *(const u32x4*)(b1 + f0);
  __syncthreads();
#pragma unroll
  for (int m = 1; m < 4; ++m) {
    const char* s = smem + ((wid - m) & 3) * G1_WS + (m - 1) * 12 * 1024 + lane * 16;
#pragma unroll
    for (int c = 0; c < 2; ++c)
#pragma unroll
      for (int t = 0; t < 6; ++t) acc[0][c][t] += *(const f32x4*)(s + (c * 6 + t) * 1024);
  }
#pragma unroll
  for (int t = 0; t < 6; ++t) {
    unsigned o[4];
#pragma unroll
    for (int k = 0; k < 4; ++k) {
      const f32x4 a = acc[0][k >> 1][t];
      const unsigned ub = pk2(a[(k & 1) * 2], a[(k & 1) * 2 + 1]);
      const unsigned u = pk2(bflo(ub) + bflo(bw[k]), bfhi(ub) + bfhi(bw[k]));
      o[k] = pk2(gelu_f(bflo(u)), gelu_f(bfhi(u)));
    }
    *((u32x4*)gf + ((mt * 6 + t) * 144 + nt * 4 + wid) * 64 + lane) = u32x4{o[0], o[1], o[2], o[3]};
  }
}

constexpr int G2_WS = 16384;
__global__ __launch_bounds__(256, 1) void k_g2(const u16* __restrict__ gf, const u16* __restrict__ w2, const u16* __restrict__ b2,
                                               const u16* __restrict__ h, u16* __restrict__ out) {
  __shared__ __attribute__((aligned(16))) char smem[4 * G2_WS];
  const int tid = threadIdx.x, lane = tid & 63, wid = __builtin_amdgcn_readfirstlane(tid >> 6);
  const int lin = (blockIdx.x & 7) * 32 + (blockIdx.x >> 3);
  const int db = lin & 7, nt = lin >> 3;
  const int odd = db & 1;
  const i32x4 sa = mk_srd(gf, D_ * F_ * 2), sb = mk_srd(w2, L_ * F_ * 2);
  const unsigned ring = (unsigned)(size_t)smem + wid * G2_WS;
  const int rt0 = 9 * (db >> 1) + 5 * odd, rth = 9 * (db >> 1) + 4;
  unsigned va[5];
#pragma unroll
  for (int t = 0; t < 4; ++t) va[t] = (rt0 + t) * (144 * 1024) + lane * 16;
  va[4] = rth * (144 * 1024) + ((lane & 48) + (lane & 7) + 8 * odd) * 16;
  unsigned vb[4];
#pragma unroll
  for (int j = 0; j < 4; ++j) vb[j] = (8 * j + (lane >> 3)) * (L_ * 2) + (((lane & 7) ^ (2 * (((lane >> 4) & 1) | ((j & 1) << 1)))) << 4);
  const int g = lane >> 4, q = (lane & 15) >> 2, p = lane & 3;
  unsigned rb[4];
#pragma unroll
  for (int c = 0; c < 4; ++c) rb[c] = ring + 128 * (8 * g + q) + 16 * ((2 * ((c + wid) & 3) + (p >> 1)) ^ (2 * ((q >> 1) | ((g & 1) << 1)))) + 8 * (p & 1);
  const unsigned a_base = wid * 1024, b_base = nt * 128 + wid * 32 * (L_ * 2);
  const unsigned OOB = 0x7ff00000u;
  u32x4 A[4][5];
  f32x4 acc[4][5];
#pragma unroll
  for (int c = 0; c < 4; ++c)
#pragma unroll
    for (int t = 0; t < 5; ++t) acc[c][t] = f32x4{0.f, 0.f, 0.f, 0.f};
#define G2_LDA(S, so, t) BLOAD(A[S][t], va[t], sa, so, 0)
#define G2_LDB(S, so, j) BDMA(ring, (S) * 4096 + (j) * 1024, vb[j], sb, so)
#pragma unroll
  for (int s = 0; s < 3; ++s) {
    const unsigned ao = a_base + s * 4096, bo = b_base + s * 128 * (L_ * 2);
#pragma unroll
    for (int j = 0; j < 4; ++j) G2_LDB(s, bo, j);
#pragma unroll
    for (int t = 0; t < 5; ++t) G2_LDA(s, ao, t);
  }
  u32x4 Bf[2][2];
#define G2_RDB(buf, S, c) do { \
    Bf[buf][0] = cat(trd(rb[c] + (S) * 4096), trd(rb[c] + (S) * 4096 + 512)); \
    Bf[buf][1] = cat(trd(rb[(c) + 1] + (S) * 4096), trd(rb[(c) + 1] + (S) * 4096 + 512)); } while (0)
#define G2_WAIT(S) asm volatile("s_waitcnt vmcnt(18)" : "+v"(A[S][0]), "+v"(A[S][1]), "+v"(A[S][2]), "+v"(A[S][3]), "+v"(A[S][4]) :: "memory")
#define G2_M1(buf, S, c, t) MFMA_A(acc[c][t], A[S][t], Bf[buf][(c) & 1])
#pragma unroll
  for (int c = 0; c < 4; ++c)
#pragma unroll
    for (int t = 0; t < 5; ++t) asm volatile("" : "+a"(acc[c][t]));
  G2_WAIT(0);
  G2_RDB(0, 0, 0);
  SB0;
#pragma nounroll
  for (int it = 0; it < 9; ++it) {
#pragma unroll
    for (int S = 0; S < 4; ++S) {
      const int i3 = it * 4 + S + 3;
      const unsigned ao = i3 < 36 ? a_base + i3 * 4096 : OOB, bo = i3 < 36 ? b_base + i3 * 128 * (L_ * 2) : OOB;
      const int P = (S + 3) & 3, Nx = (S + 1) & 3;
      G2_RDB(1, S, 2); SB0;
      G2_M1(0, S, 0, 0); G2_LDB(P, bo, 0); G2_M1(0, S, 0, 1); G2_LDB(P, bo, 1); G2_M1(0, S, 0, 2); G2_LDB(P, bo, 2); G2_M1(0, S, 0, 3); G2_LDB(P, bo, 3);
      G2_M1(0, S, 0, 4); G2_LDA(P, ao, 0); G2_M1(0, S, 1, 0); G2_LDA(P, ao, 1); G2_M1(0, S, 1, 1); G2_LDA(P, ao, 2); G2_M1(0, S, 1, 2); G2_LDA(P, ao, 3);
      G2_M1(0, S, 1, 3); G2_LDA(P, ao, 4); G2_M1(0, S, 1, 4);
      G2_WAIT(Nx);
      G2_RDB(0, Nx, 0); SB0;
#pragma unroll
      for (int t = 0; t < 5; ++t) { G2_M1(1, S, 2, t); }
#pragma unroll
      for (int t = 0; t < 5; ++t) { G2_M1(1, S, 3, t); }
    }
  }
  asm volatile("s_nop 7\n\ts_waitcnt vmcnt(0)" : "+v"(A[1][0]), "+v"(A[1][1]), "+v"(A[1][2]), "+v"(A[1][3]), "+v"(A[1][4]),
                                      "+v"(A[2][0]), "+v"(A[2][1]), "+v"(A[2][2]), "+v"(A[2][3]), "+v"(A[2][4]) :: "memory");
  asm volatile("" :: "v"(Bf[0][0]), "v"(Bf[0][1]));
  SB0;
  {
    char* o = smem + wid * G2_WS + lane * 16;
#pragma unroll
    for (int c = 1; c < 4; ++c)
#pragma unroll
      for (int t = 0; t < 5; ++t) *(f32x4*)(o + ((c - 1) * 5 + t) * 1024) = acc[c][t];
  }
  const int s = nt * 64 + wid * 16 + (lane & 15);
  const float bias = bf2f(b2[s]);
  const int dfull = rt0 * 16 + 4 * g, dhalf = rth * 16 + 8 * odd + 4 * (g & 1);
  u32x2 hw[5];
#pragma unroll
  for (int t = 0; t < 4; ++t) hw[t] = *(const u32x2*)(h + s * D_ + dfull + 16 * t);
  hw[4] = *(const u32x2*)(h + s * D_ + dhalf);
  __syncthreads();
#pragma unroll
  for (int c = 1; c < 4; ++c) {
    const char* sp = smem + ((wid - c) & 3) * G2_WS + (c - 1) * 5 * 1024 + lane * 16;
#pragma unroll
    for (int t = 0; t < 5; ++t) acc[0][t] += *(const f32x4*)(sp + t * 1024);
  }
#pragma unroll
  for (int t = 0; t < 5; ++t) {
    u32x2 o;
#pragma unroll
    for (int k = 0; k < 2; ++k) {
      const unsigned yb = pk2(acc[0][t][2 * k], acc[0][t][2 * k + 1]);
      const unsigned y = pk2(bflo(yb) + bias, bfhi(yb) + bias);
      o[k] = pk2(bflo(hw[t][k]) + bflo(y), bfhi(hw[t][k]) + bfhi(y));
    }
    if (t < 4) *(u32x2*)(out + s * D_ + dfull + 16 * t) = o;
    else if (g < 2) *(u32x2*)(out + s * D_ + dhalf) = o;
  }
}

extern "C" void kernel_launch(void* const* d_in, const int* in_sizes, int n_in,
                              void* d_out, int out_size, void* d_ws, size_t ws_size,
                              hipStream_t stream) {
  const u16* x = (const u16*)d_in[0];
  const u16* logA = (const u16*)d_in[1];
  const u16* Aim = (const u16*)d_in[2];
  const u16* Cre = (const u16*)d_in[3];
  const u16* Cim = (const u16*)d_in[4];
  const u16* logdt = (const u16*)d_in[5];
  const u16* Dsk = (const u16*)d_in[6];
  const u16* w1 = (const u16*)d_in[8];
  const u16* b1 = (const u16*)d_in[9];
  const u16* w2 = (const u16*)d_in[10];
  const u16* b2 = (const u16*)d_in[11];
  char* ws = (char*)d_ws;
  const size_t SZ = (size_t)L_ * D_ * 2;
  u16* uT = (u16*)(ws); u16* Rg = (u16*)(ws + SZ); u16* aB = (u16*)(ws + 2 * SZ); u16* h = (u16*)(ws + 3 * SZ);
  u16* hf = (u16*)(ws + 4 * SZ); u16* gf = (u16*)(ws + 5 * SZ);
  k_p1<<<D_ + 256, 256, 0, stream>>>(x, logA, Aim, Cre, Cim, logdt, uT, Rg);
  k_p2<<<D_, 256, 0, stream>>>(uT, Rg, Dsk, aB);
  k_p3<<<256, 256, 0, stream>>>(x, aB, h, hf);
  k_g1<<<216, 256, 0, stream>>>(hf, w1, b1, gf);
  k_g2<<<256, 256, 0, stream>>>(gf, w2, b2, h, (u16*)d_out);
}
```

```cpp
#include <hip/hip_runtime.h>
#include <stdint.h>
#include <utility>

typedef unsigned short u16;
typedef unsigned long long u64;
typedef __attribute__((ext_vector_type(4))) short s16x4;
typedef __attribute__((ext_vector_type(2))) unsigned short u16x2;
typedef __attribute__((ext_vector_type(8))) __bf16 bf16x8;
typedef __attribute__((ext_vector_type(2))) __bf16 bf16x2;
typedef __attribute__((ext_vector_type(4))) float f32x4;
typedef __attribute__((ext_vector_type(2))) float f32x2;
typedef __attribute__((ext_vector_type(4))) unsigned u32x4;
typedef __attribute__((ext_vector_type(2))) unsigned u32x2;
typedef __attribute__((ext_vector_type(4))) int i32x4;
typedef __amdgpu_buffer_rsrc_t rsrc_t;
#define LAS __attribute__((address_space(3)))
#define DI __device__ __forceinline__

#define TSDECL
#define TS(i)
#define TSOUT()

#define PF_A_BYTES (2048 * 9216)
#define POLL_SLEEP 4

constexpr int L_ = 2048, D_ = 576, N_ = 128, F_ = 4608;
constexpr unsigned SZ = L_ * D_ * 2;
constexpr unsigned WS_UB = 0, WS_AT = SZ, WS_H = 2 * SZ, WS_HF = 3 * SZ, WS_GF = 4 * SZ, WS_SY = 7 * SZ, WS_BYTES = 7 * SZ + 131072;
constexpr unsigned SY_EPOCH = WS_SY, SY_FLAG = WS_SY + 4096, SY_CNT = WS_SY + 32768;
constexpr unsigned OOB = 0x7ff00000u;
constexpr int SC1 = 16;

template <class Fn, int... I> DI void sfor_(Fn&& f, std::integer_sequence<int, I...>) { (f(std::integral_constant<int, I>{}), ...); }
template <int N, class Fn> DI void sfor(Fn&& f) { sfor_(f, std::make_integer_sequence<int, N>{}); }

DI float bflo(unsigned w) { return __uint_as_float(w << 16); }
DI float bfhi(unsigned w) { return __uint_as_float(w & 0xffff0000u); }
DI f32x2 unpk(unsigned w) { return f32x2{bflo(w), bfhi(w)}; }
DI float bf2f(u16 v) { return __uint_as_float(((unsigned)v) << 16); }
DI unsigned pkv(f32x2 v) { return __builtin_bit_cast(unsigned, __builtin_convertvector(v, bf16x2)); }
DI unsigned pk2(float lo, float hi) { return pkv(f32x2{lo, hi}); }
DI float rbf(float f) { return bflo(pk2(f, f)); }
DI unsigned perm_lo(unsigned a, unsigned b) { return __builtin_amdgcn_perm(b, a, 0x05040100u); }
DI unsigned perm_hi(unsigned a, unsigned b) { return __builtin_amdgcn_perm(b, a, 0x07060302u); }
DI f32x4 mfma16(u32x4 a, u32x4 b, f32x4 c) { return __builtin_amdgcn_mfma_f32_16x16x32_bf16(__builtin_bit_cast(bf16x8, a), __builtin_bit_cast(bf16x8, b), c, 0, 0, 0); }
DI rsrc_t mk_rsrc(const void* p, unsigned bytes) { return __builtin_amdgcn_make_buffer_rsrc((void*)p, 0, bytes, 0x00020000); }
DI i32x4 mk_srd(const void* p, unsigned bytes) {
  const u64 a = (u64)p;
  i32x4 r; r.x = (int)(unsigned)a; r.y = (int)((unsigned)(a >> 32) & 0xffffu); r.z = (int)bytes; r.w = 0x00020000; return r;
}
#define LDS_BARRIER() do { asm volatile("s_waitcnt lgkmcnt(0)" ::: "memory"); __builtin_amdgcn_s_barrier(); asm volatile("" ::: "memory"); } while (0)
template <int IMM> DI void bload_(u32x4& dst, unsigned voff, i32x4 srd, unsigned soff) {
  asm volatile("buffer_load_dwordx4 %0, %1, %2, %3 offen offset:%4 sc1" : "=v"(dst) : "v"(voff), "s"(srd), "s"(soff), "i"(IMM) : "memory");
}
template <int IMM> DI void bdma_(unsigned ldsbase, unsigned voff, i32x4 srd, unsigned soff) {
  asm volatile("s_add_u32 m0, %0, %1\n\ts_nop 0\n\tbuffer_load_dwordx4 %2, %3, %4 offen lds" :: "s"(ldsbase), "i"(IMM), "v"(voff), "s"(srd), "s"(soff) : "memory", "scc");
}
#define BLOAD(dst, voff, srd, soff, imm) bload_<(imm)>(dst, voff, srd, soff)
#define BDMA(ldsbase, ldsimm, voff, srd, soff) bdma_<(ldsimm)>(ldsbase, voff, srd, soff)
#define SB0 __builtin_amdgcn_sched_barrier(0)
DI void MFMA_VV(f32x4& acc, const u32x4& a, const u32x4& b) { asm volatile("v_mfma_f32_16x16x32_bf16 %0, %1, %2, %0" : "+a"(acc) : "v"(a), "v"(b)); }
DI void MFMA_AV(f32x4& acc, const u32x4& a, const u32x4& b) { asm volatile("v_mfma_f32_16x16x32_bf16 %0, %1, %2, %0" : "+a"(acc) : "a"(a), "v"(b)); }
DI void pin_a(u32x4& v) { asm volatile("" : "+a"(v)); }
template <int N> DI void wait6(u32x4* a) { asm volatile("s_waitcnt vmcnt(%6)" : "+v"(a[0]), "+v"(a[1]), "+v"(a[2]), "+v"(a[3]), "+v"(a[4]), "+v"(a[5]) : "i"(N) : "memory"); }
#define VMCNT(n) asm volatile("s_waitcnt vmcnt(%0)" :: "i"(n) : "memory")
DI u32x2 trd(unsigned addr) { return __builtin_bit_cast(u32x2, __builtin_amdgcn_ds_read_tr16_b64_v4i16((LAS s16x4*)(size_t)addr)); }
DI u32x4 cat(u32x2 a, u32x2 b) { return u32x4{a.x, a.y, b.x, b.y}; }

template <int CTRL> DI float dpp_add(float v) { return v + __builtin_bit_cast(float, __builtin_amdgcn_update_dpp(0, __builtin_bit_cast(int, v), CTRL, 0xf, 0xf, true)); }
DI float half_sum(float v) {
  v = dpp_add<0xB1>(v); v = dpp_add<0x4E>(v); v = dpp_add<0x141>(v); v = dpp_add<0x140>(v);
  float w = v;
  asm volatile("s_nop 1\n\tv_permlane16_swap_b32 %0, %1" : "+v"(v), "+v"(w));
  return v + w;
}
DI void ln9(const f32x2* v, unsigned* o) {
  const f32x2 s = ((v[0] + v[1]) + (v[2] + v[3])) + ((v[4] + v[5]) + (v[6] + v[7])) + v[8];
  const float mean = half_sum(s.x + s.y) * (1.0f / D_);
  f32x2 q0 = {0.f, 0.f}, q1 = {0.f, 0.f};
#pragma unroll
  for (int i = 0; i < 9; ++i) { const f32x2 t = v[i] - mean; if (i & 1) q1 = __builtin_elementwise_fma(t, t, q1); else q0 = __builtin_elementwise_fma(t, t, q0); }
  q0 += q1;
  const float var = half_sum(q0.x + q0.y) * (1.0f / D_);
  const float mb = rbf(mean);
  const float sd = rbf(__builtin_amdgcn_sqrtf(rbf(rbf(var) + 1e-5f)));
  const float inv = __builtin_amdgcn_rcpf(sd);
#pragma unroll
  for (int i = 0; i < 9; ++i) o[i] = pkv(unpk(pkv(v[i] - mb)) * inv);
}
DI f32x2 cmul(f32x2 a, f32x2 b) { const f32x2 bs = {-b.y, b.x}; return __builtin_elementwise_fma(a.yy, bs, a.xx * b); }
DI f32x2 cexp_p(float zl, float zv, float p) {
  const float m = __builtin_amdgcn_exp2f(zl * p);
  const float rev = __builtin_amdgcn_fractf(zv * p);
  return f32x2{__builtin_amdgcn_cosf(rev), __builtin_amdgcn_sinf(rev)} * m;
}
DI void warm(const void* base, unsigned bytes, unsigned b0, unsigned b1, unsigned idx, unsigned nthr) {
  const rsrc_t rs = mk_rsrc(base, bytes);
#pragma nounroll
  for (unsigned o = b0 + idx * 128; o < b1; o += nthr * 128 * 4) {
    unsigned v[4];
#pragma unroll
    for (int i = 0; i < 4; ++i) { const unsigned e = o + i * nthr * 128; v[i] = __builtin_amdgcn_raw_buffer_load_b32(rs, e < b1 ? e : OOB, 0, 0); }
#pragma unroll
    for (int i = 0; i < 4; ++i) asm volatile("" :: "v"(v[i]));
  }
}

DI void arrive(rsrc_t rw, int ho, int shard) {
  VMCNT(0);
  __builtin_amdgcn_s_barrier();
  if (threadIdx.x < 8) __builtin_amdgcn_raw_ptr_buffer_atomic_add_i32(1, rw, SY_CNT + ((ho * 8 + threadIdx.x) * 16 + shard) * 256, 0, SC1);
}
DI void await(rsrc_t rw, int ho, int rep, int s0, int ns, unsigned want, bool poller, int lane) {
  if (poller) {
    const unsigned off = lane < ns ? SY_CNT + ((ho * 8 + rep) * 16 + s0 + lane) * 256 : OOB;
    const unsigned w = lane < ns ? want : 0u;
#pragma nounroll
    for (int spin = 0; spin < (1 << 16); ++spin) {
      const unsigned f = __builtin_amdgcn_raw_buffer_load_b32(rw, off, 0, SC1);
      if (__all(f == w)) break;
      __builtin_amdgcn_s_sleep(POLL_SLEEP);
      asm volatile("" ::: "memory");
    }
  }
  asm volatile("" ::: "memory");
  __builtin_amdgcn_s_barrier();
  asm volatile("" ::: "memory");
}

constexpr int KG_PITCH = 528;
constexpr int KG_B = 41 * KG_PITCH, KG_CH = 57 * KG_PITCH;
constexpr int KG_T = 3 * KG_CH, KG_TP = 2064 * 2;
constexpr int CV_KP = 259 * 16 + 16;
constexpr int CV_U = 4 * CV_KP;
constexpr int CV_UP = (29 + 256) * 16 + 64 + 16;
constexpr int CV_TB = CV_U + 2 * CV_UP;
constexpr int CV_P = 3 * CV_TB;
constexpr int G_WS = 36864;
constexpr int T_PITCH = 288 + 8;
constexpr int N_T = 4 * G_WS;
constexpr int K_LDS = N_T + 8 * T_PITCH * 4;
static_assert(CV_KP % 256 == 64 && CV_UP % 64 == 32 && CV_TB % 16 == 0 && KG_CH % 16 == 0 && CV_P + 65536 <= N_T && KG_T >= CV_P && KG_T + 3 * KG_TP <= N_T && K_LDS <= 163840, "");

constexpr bool g1_isR(int t) { return t < 12 && t % 3 == 2; }
constexpr int g1_lidx(int t) { return t < 12 ? t - t / 3 : t - 4; }
constexpr int g1_lstep(int l) { return l < 8 ? l + l / 2 : l + 4; }
constexpr int g1_nb(int t) { return (t < 1 || t > 12) ? 0 : ((t - 1) % 3 == 2 ? 4 : 6); }
constexpr int g1_b0(int t) { return 16 * ((t - 1) / 3) + 6 * ((t - 1) % 3); }
constexpr int g1_ops(int t) { return t < 0 ? 6 : g1_nb(t) + (t <= 12 ? 6 : 0); }

__global__ __launch_bounds__(256, 1) void k_all(const u16* __restrict__ x, const u16* __restrict__ logA, const u16* __restrict__ Aim,
                                                const u16* __restrict__ Cre, const u16* __restrict__ Cim, const u16* __restrict__ logdt,
                                                const u16* __restrict__ Dsk, const u16* __restrict__ w1, const u16* __restrict__ b1,
                                                const u16* __restrict__ w2, const u16* __restrict__ b2, char* __restrict__ ws, u16* __restrict__ out) {
  __shared__ __attribute__((aligned(16))) char smem[K_LDS];
  const int tid = threadIdx.x, lane = tid & 63, wid = __builtin_amdgcn_readfirstlane(tid >> 6);
  const int xc = blockIdx.x & 7, idx = blockIdx.x >> 3;
  TSDECL;
  TS(0);
  const rsrc_t rw = mk_rsrc(ws, WS_BYTES);
  const i32x4 sw = mk_srd(ws, WS_BYTES);
  const unsigned ring = (unsigned)(size_t)smem + wid * G_WS;
  const int g = lane >> 4, r16 = lane & 15, q4 = r16 >> 2, p4 = lane & 3;
  const int tb = xc * 32 + idx;
  const int tok = wid * 2 + (lane >> 5), ll = lane & 31;
  unsigned* T = (unsigned*)(smem + N_T);
  const bool cv_on = idx < 24;
  const int d0 = xc * 72 + idx * 3;

  unsigned xw[9];
  {
    const unsigned* xr = (const unsigned*)(x + (tb * 8 + tok) * D_) + ll;
#pragma unroll
    for (int i = 0; i < 9; ++i) xw[i] = xr[32 * i];
  }
  const u32x2 ep = __builtin_amdgcn_raw_buffer_load_b64(rw, SY_EPOCH, 0, SC1);
  u16 pla[3], pai[3], pcr[3], pci[3], pld[3], pds[3];
  if (cv_on) {
    const int n = (wid & 1) * 64 + lane;
#pragma unroll
    for (int c = 0; c < 3; ++c) {
      const int o = (d0 + c) * N_ + n;
      pla[c] = logA[o]; pai[c] = Aim[o]; pcr[c] = Cre[o]; pci[c] = Cim[o]; pld[c] = logdt[d0 + c]; pds[c] = Dsk[d0 + c];
    }
  }
  if (blockIdx.x == 255) {
    __builtin_amdgcn_raw_buffer_store_b32(0u, rw, SY_CNT + tid * 256, 0, SC1);
    if (tid < 128) __builtin_amdgcn_raw_buffer_store_b32(0u, rw, SY_CNT + (256 + tid) * 256, 0, SC1);
  }
  {
    f32x2 v[9];
    unsigned o[9];
#pragma unroll
    for (int i = 0; i < 9; ++i) v[i] = unpk(xw[i]);
    ln9(v, o);
#pragma unroll
    for (int i = 0; i < 9; ++i) T[tok * T_PITCH + 32 * i + ll] = o[i];
    LDS_BARRIER();
#pragma unroll
    for (int rep = 0; rep < 2; ++rep) {
      const int cp = tid + 256 * rep;
      if (rep == 0 || tid < 32) {
        unsigned w[8];
#pragma unroll
        for (int t = 0; t < 8; ++t) w[t] = T[t * T_PITCH + cp];
        const unsigned off = WS_UB + tb * (D_ * 16) + cp * 32;
        __builtin_amdgcn_raw_buffer_store_b128(u32x4{perm_lo(w[0], w[1]), perm_lo(w[2], w[3]), perm_lo(w[4], w[5]), perm_lo(w[6], w[7])}, rw, off, 0, SC1);
        __builtin_amdgcn_raw_buffer_store_b128(u32x4{perm_hi(w[0], w[1]), perm_hi(w[2], w[3]), perm_hi(w[4], w[5]), perm_hi(w[6], w[7])}, rw, off + 16, 0, SC1);
      }
    }
  }
  const u64 tag64 = (((u64)ep.y << 32) | ep.x) * 0x9E3779B97F4A7C15ull + 0xD1B54A32D192ED03ull;
  const u32x2 tag = {(unsigned)tag64, (unsigned)(tag64 >> 32)};
  TS(1);
  VMCNT(0);
  __builtin_amdgcn_s_barrier();
  if (tid < 8) __builtin_amdgcn_raw_buffer_store_b64(tag, rw, SY_FLAG + tid * 2048 + tb * 8, 0, SC1);
  auto await_u = [&](bool poller) {
    if (poller) {
#pragma nounroll
      for (int spin = 0; spin < (1 << 16); ++spin) {
        const u32x4 f0 = __builtin_amdgcn_raw_buffer_load_b128(rw, SY_FLAG + xc * 2048 + lane * 16, 0, SC1);
        const u32x4 f1 = __builtin_amdgcn_raw_buffer_load_b128(rw, SY_FLAG + xc * 2048 + 1024 + lane * 16, 0, SC1);
        const bool ok = f0.x == tag.x && f0.y == tag.y && f0.z == tag.x && f0.w == tag.y && f1.x == tag.x && f1.y == tag.y && f1.z == tag.x && f1.w == tag.y;
        if (__all(ok)) break;
        __builtin_amdgcn_s_sleep(POLL_SLEEP);
        asm volatile("" ::: "memory");
      }
    }
  };
  TS(2);

  if (cv_on) {
    {
      unsigned* At = (unsigned*)smem + (wid & 1) * 64 + lane;
      u16 la = pla[0], ai = pai[0], c0 = pcr[0], c1 = pci[0], ld = pld[0];
      u16 la1 = pla[1], ai1 = pai[1], c01 = pcr[1], c11 = pci[1], ld1 = pld[1];
#pragma nounroll
      for (int c = 0; c < 3; ++c) {
        const float dt = rbf(__expf(bf2f(ld)));
        const float zr = -rbf(__expf(bf2f(la))) * dt, zi = bf2f(ai) * dt;
        const float zl = zr * 1.4426950408889634f, zv = zi * 0.15915494309189535f;
        f32x2 G = {2.0f * (bf2f(c0) * dt), -2.0f * (bf2f(c1) * dt)};
#define PUT_A(row, v) At[(row) * (KG_PITCH / 4)] = pkv(v)
#define PUT_B(row, v) At[(41 + (row)) * (KG_PITCH / 4)] = pkv(v)
        const f32x2 E128 = cexp_p(zl, -zv, 128.f), E4 = cexp_p(zl, -zv, 4.f);
        if (wid < 2) {
          f32x2 E[8];
          E[1] = cexp_p(zl, -zv, 1.f); E[2] = cexp_p(zl, -zv, 2.f); E[4] = E4;
          E[3] = cmul(E[1], E[2]); E[5] = cmul(E[1], E[4]); E[6] = cmul(E[2], E[4]); E[7] = cmul(E[3], E[4]);
#pragma unroll
          for (int t = 0; t < 2; ++t) {
            PUT_A(8 * t, G);
#pragma unroll
            for (int k = 1; k < 8; ++k) PUT_A(8 * t + k, cmul(E[k], G));
            G = cmul(E128, G);
          }
#pragma unroll
          for (int t = 0; t < 2; ++t) {
            PUT_A(16 + 4 * t, G);
#pragma unroll
            for (int k = 1; k < 4; ++k) PUT_A(16 + 4 * t + k, cmul(E[2 * k], G));
            G = cmul(E128, G);
          }
          PUT_A(24, G); PUT_A(25, cmul(E4, G)); G = cmul(E128, G);
          PUT_A(26, G); PUT_A(27, cmul(E4, G));
        } else {
          const f32x2 E8 = cexp_p(zl, zv, 8.f);
          f32x2 b0 = E8, b1v = cexp_p(zl, zv, 64.f);
          f32x2 g0 = cmul(cexp_p(zl, -zv, 768.f), G), g1 = cmul(cexp_p(zl, -zv, 1536.f), G);
          At[41 * (KG_PITCH / 4)] = 0x00003f80u; PUT_B(8, b1v);
          b1v = cmul(b1v, E8);
#pragma unroll
          for (int j = 1; j < 8; ++j) { PUT_B(j, b0); PUT_B(8 + j, b1v); if (j < 7) { b0 = cmul(b0, E8); b1v = cmul(b1v, E8); } }
#pragma unroll
          for (int t = 0; t < 2; ++t) { PUT_A(28 + 2 * t, g0); PUT_A(29 + 2 * t, cmul(g0, E4)); g0 = cmul(g0, E128); }
#pragma unroll
          for (int t = 0; t < 4; ++t) { PUT_A(32 + t, g0); PUT_A(36 + t, g1); if (t < 3) g0 = cmul(g0, E128); g1 = cmul(g1, E128); }
          PUT_A(40, g1);
        }
        At += KG_CH / 4;
        la = la1; ai = ai1; c0 = c01; c1 = c11; ld = ld1;
        la1 = pla[2]; ai1 = pai[2]; c01 = pcr[2]; c11 = pci[2]; ld1 = pld[2];
      }
    }
    LDS_BARRIER();
    TS(3);
    if (wid < 3) {
      int pb, o1, o2;
      if (wid == 0) { pb = 4 * (g & 1) + 128 * (g >> 1); o1 = 1; o2 = 2; }
      else if (wid == 1) { pb = g < 2 ? 256 + 128 * g : 256 * g; o1 = g < 2 ? 2 : 4; o2 = g < 2 ? 4 : 128; }
      else { pb = 1024 + 512 * g; o1 = 128; o2 = 256; }
      pb += 8 * r16;
      const char* ap = smem + min(wid * 16 + r16, 40) * KG_PITCH + g * 16;
      const char* bp = smem + KG_B + r16 * KG_PITCH + g * 16;
#pragma unroll
      for (int c = 0; c < 3; ++c) {
        f32x4 a0 = {0.f, 0.f, 0.f, 0.f};
#pragma unroll
        for (int s = 0; s < 8; ++s) a0 = mfma16(*(const u32x4*)(ap + c * KG_CH + s * 64), *(const u32x4*)(bp + c * KG_CH + s * 64), a0);
        const unsigned k01 = pk2(a0.x, a0.y), k23 = pk2(a0.z, a0.w);
        u16* Kt = (u16*)(smem + KG_T + c * KG_TP);
        Kt[min(pb, 2056)] = (u16)k01; Kt[min(pb + o1, 2056)] = (u16)(k01 >> 16);
        Kt[min(pb + o2, 2056)] = (u16)k23; Kt[min(pb + o1 + o2, 2056)] = (u16)(k23 >> 16);
      }
    } else {
      if (lane < 3) *(u16*)(smem + KG_T + lane * KG_TP + 2060 * 2) = 0;
      await_u(true);
    }
    LDS_BARRIER();
    TS(4);
    if (blockIdx.x == 0 && tid == 0) __builtin_amdgcn_raw_buffer_store_b64(tag, rw, SY_EPOCH, 0, SC1);
    u32x4 uw[3];
#pragma unroll
    for (int c = 0; c < 3; ++c) uw[c] = __builtin_amdgcn_raw_buffer_load_b128(rw, WS_UB + tid * (D_ * 16) + (d0 + c) * 16, 0, SC1);
    {
      int kidx[12];
#pragma unroll
      for (int k = 0; k < 6; ++k) {
        const int j0 = 2047 - (8 * tid + 2 * k);
        const unsigned pr = pk2((float)j0, (float)(j0 - 1));
        kidx[2 * k] = j0 < 0 ? 2060 : (int)bflo(pr); kidx[2 * k + 1] = j0 < 0 ? 2060 : (int)bfhi(pr);
      }
      if (tid < 213) {
        const int c = tid / 71, i = tid - 71 * c;
        const int off = i < 12 ? (i & 3) * CV_KP + (256 + (i >> 2)) * 16 : CV_U + (i & 1) * CV_UP + ((i - 12) >> 1) * 16;
        if (i < 70) *(u32x4*)(smem + c * CV_TB + off) = u32x4{0u, 0u, 0u, 0u};
        else *(u32x2*)(smem + c * CV_TB + off) = u32x2{0u, 0u};
      }
#pragma unroll
      for (int c = 0; c < 3; ++c) {
        const u16* Kt = (const u16*)(smem + KG_T + c * KG_TP);
        unsigned kw[6];
#pragma unroll
        for (int k = 0; k < 6; ++k) kw[k] = __builtin_bit_cast(unsigned, u16x2{Kt[kidx[2 * k]], Kt[kidx[2 * k + 1]]});
        unsigned a[5];
#pragma unroll
        for (int k = 0; k < 5; ++k) a[k] = __builtin_amdgcn_alignbit(kw[k + 1], kw[k], 16);
        char* kp = smem + c * CV_TB + tid * 16;
        *(u32x4*)(kp + 3 * CV_KP) = u32x4{kw[0], kw[1], kw[2], kw[3]};
        *(u32x4*)(kp + 2 * CV_KP) = u32x4{a[0], a[1], a[2], a[3]};
        *(u32x4*)(kp + 1 * CV_KP) = u32x4{kw[1], kw[2], kw[3], kw[4]};
        *(u32x4*)(kp + 0 * CV_KP) = u32x4{a[1], a[2], a[3], a[4]};
      }
#pragma unroll
      for (int c = 0; c < 3; ++c) {
        char* up = smem + c * CV_TB + CV_U + (29 + tid) * 16;
        *(u32x2*)(up + 8) = uw[c].xy; *(u32x2*)(up + 16) = uw[c].zw;
        *(u32x4*)(up + CV_UP) = uw[c];
      }
    }
    LDS_BARRIER();
    TS(5);
    {
      const char* ap0 = smem + (r16 & 3) * CV_KP + 2 * (2064 - 256 * 7 - 8 * (r16 >> 2) - 8 * g - 64 * wid - 32);
      const char* bp0 = smem + CV_U + (r16 & 1) * CV_UP + 2 * (232 + 32 * (r16 >> 1) - 64 * wid - 32 + 24 - 8 * g);
      char* pw = smem + CV_P + wid * 8192 + ((r16 & 1) + 2 * (g ^ ((r16 >> 1) & 3)) + 8 * (r16 >> 1)) * 16;
      const char* pr = smem + CV_P + (tid >> 5) * 1024 + (2 * ((tid & 3) ^ ((tid >> 2) & 3)) + 8 * ((tid & 31) >> 2)) * 16;
      u32x4 FA[2][8], FB[2][8];
      f32x4 acc[2][8];
      auto rd = [&](int u) {
        const char* ap = ap0 + (u >> 1) * CV_TB + 64 * (1 - (u & 1));
        const char* bp = bp0 + (u >> 1) * CV_TB + 64 * (1 - (u & 1));
#pragma unroll
        for (int k = 0; k < 8; ++k) { FA[u & 1][k] = *(const u32x4*)(ap + 512 * (7 - k)); FB[u & 1][k] = *(const u32x4*)(bp + 512 * k); }
      };
      auto mm = [&](int u) {
        f32x4* a = acc[(u >> 1) & 1];
        if (!(u & 1)) {
#pragma unroll
          for (int k = 0; k < 8; ++k) a[k] = f32x4{0.f, 0.f, 0.f, 0.f};
        }
#pragma unroll
        for (int m = 0; m < 8; ++m)
#pragma unroll
          for (int o = 0; o <= m; ++o)
            if (m + o < 8) {
              a[m + o] = mfma16(FA[u & 1][m], FB[u & 1][o], a[m + o]);
              if (o < m) a[m + o] = mfma16(FA[u & 1][o], FB[u & 1][m], a[m + o]);
            }
      };
      auto wp = [&](int c) {
#pragma unroll
        for (int k = 0; k < 8; ++k) *(f32x4*)(pw + (c & 1) * 32768 + k * 1024) = acc[c & 1][k];
      };
      auto fin = [&](int c) {
        const char* pp = pr + (c & 1) * 32768;
        f32x4 y0 = *(const f32x4*)(pp), y1 = *(const f32x4*)(pp + 16);
#pragma unroll
        for (int w = 1; w < 4; ++w) { y0 += *(const f32x4*)(pp + w * 8192); y1 += *(const f32x4*)(pp + w * 8192 + 16); }
        const f32x2 y[4] = {y0.xy, y0.zw, y1.xy, y1.zw};
        const float dsk = bf2f(pds[c]);
        u32x4 o;
#pragma unroll
        for (int k = 0; k < 4; ++k) o[k] = pkv(unpk(pkv(y[k])) + unpk(pkv(unpk(uw[c][k]) * dsk)));
        __builtin_amdgcn_raw_buffer_store_b128(o, rw, WS_AT + (d0 + c) * (L_ * 2) + 16 * tid, 0, SC1);
      };
#define CV_MIX(nds, nvalu) do { _Pragma("unroll") for (int k_ = 0; k_ < 18; ++k_) { __builtin_amdgcn_sched_group_barrier(0x8, 2, 0); \
        __builtin_amdgcn_sched_group_barrier(0x80, nds, 0); if (nvalu) __builtin_amdgcn_sched_group_barrier(0x2, nvalu, 0); } SB0; } while (0)
      rd(0); SB0;
      mm(0); rd(1); CV_MIX(1, 0);
      mm(1); rd(2); CV_MIX(1, 0);
      mm(2); wp(0); rd(3); CV_MIX(2, 0);
      LDS_BARRIER();
      mm(3); fin(0); rd(4); CV_MIX(2, 4);
      mm(4); wp(1); rd(5); CV_MIX(2, 0);
      LDS_BARRIER();
      mm(5); fin(1); CV_MIX(2, 4);
      wp(2);
      LDS_BARRIER();
      fin(2);
    }
    TS(6);
    arrive(rw, 0, (xc * 24 + idx) & 15);
  } else {
    warm(w1, L_ * F_ * 2, 0, PF_A_BYTES, ((idx - 24) * 8 + xc) * 256 + tid, 64 * 256);
    await_u(wid == 0);
    __builtin_amdgcn_s_barrier();
  }
  TS(7);

  const int lin1 = xc * 27 + idx, mt = lin1 % 6, nt1 = lin1 / 6;
  const bool g1_on = idx < 27;
  const i32x4 sb1 = mk_srd(w1, L_ * F_ * 2);
  const unsigned vb1 = g * (F_ * 2) + ((r16 ^ (g << 2)) << 4);
  const unsigned b1_base = nt1 * 256 + wid * 32 * (F_ * 2);
  unsigned rb1[4];
#pragma unroll
  for (int m = 0; m < 4; ++m) rb1[m] = ring + 256 * (8 * g + q4) + 64 * (((m + wid) & 3) ^ q4) + 16 * p4;
#define G1_LDB(slot, t, j) do { const unsigned so_ = b1_base + ((t) * 128 + (j) * 4) * (F_ * 2); BDMA(ring, (slot) * 8192 + (j) * 1024, vb1, sb1, so_); } while (0)
#define G1_FRAG(slot, m, c) cat(trd(rb1[m] + (slot) * 8192 + 8 * (c)), trd(rb1[m] + (slot) * 8192 + 8 * (c) + 1024))
  if (g1_on) sfor<32>([&](auto I) { G1_LDB(I / 8, 3 * (I / 8) + 2, I % 8); });

  u32x4 BR[4][8];
  await(rw, 0, xc, 0, 16, 12, wid == 0, lane);
  TS(8);
  {
    const unsigned ao = WS_AT + 2 * tid * (L_ * 2) + tb * 16;
    const u32x4 e0 = __builtin_amdgcn_raw_buffer_load_b128(rw, ao, 0, SC1);
    const u32x4 o0 = __builtin_amdgcn_raw_buffer_load_b128(rw, ao + L_ * 2, 0, SC1);
    u32x4 e1, o1;
    if (wid == 0) {
      e1 = __builtin_amdgcn_raw_buffer_load_b128(rw, lane < 32 ? ao + 512 * (L_ * 2) : OOB, 0, SC1);
      o1 = __builtin_amdgcn_raw_buffer_load_b128(rw, lane < 32 ? ao + 513 * (L_ * 2) : OOB, 0, SC1);
    }
    if (wid == 0) VMCNT(4); else VMCNT(2);
    sfor<32>([&](auto I) { BR[I / 8][I % 8] = G1_FRAG(I / 8, (I % 8) >> 1, I & 1); if constexpr (I >= 16) pin_a(BR[I / 8][I % 8]); });
    SB0;
#pragma unroll
    for (int k = 0; k < 4; ++k) { T[(2 * k) * T_PITCH + tid] = perm_lo(e0[k], o0[k]); T[(2 * k + 1) * T_PITCH + tid] = perm_hi(e0[k], o0[k]); }
    if (wid == 0 && lane < 32) {
#pragma unroll
      for (int k = 0; k < 4; ++k) { T[(2 * k) * T_PITCH + 256 + tid] = perm_lo(e1[k], o1[k]); T[(2 * k + 1) * T_PITCH + 256 + tid] = perm_hi(e1[k], o1[k]); }
    }
    LDS_BARRIER();
    TS(9);
    f32x2 v[9];
    unsigned o[9];
#pragma unroll
    for (int i = 0; i < 9; ++i) {
      const unsigned hw = pkv(unpk(xw[i]) + unpk(T[tok * T_PITCH + 32 * i + ll]));
      __builtin_amdgcn_raw_buffer_store_b32(hw, rw, WS_H + ((tb * 8 + tok) * (D_ / 2) + ll + 32 * i) * 4, 0, SC1);
      v[i] = unpk(hw);
    }
    ln9(v, o);
#pragma unroll
    for (int i = 0; i < 9; ++i) v[i] = unpk(o[i]);
    ln9(v, o);
#pragma unroll
    for (int i = 0; i < 9; ++i) T[tok * T_PITCH + 32 * i + ll] = o[i];
    LDS_BARRIER();
#pragma unroll
    for (int rep = 0; rep < 2; ++rep) {
      const int cp = tid + 256 * rep;
      if (rep == 0 || tid < 32) {
        unsigned w[8];
#pragma unroll
        for (int t = 0; t < 8; ++t) w[t] = T[t * T_PITCH + cp];
        const unsigned off = WS_HF + ((tb >> 2) * 36 + (cp >> 3)) * 1024 + (tb & 3) * 256 + (cp & 7) * 32;
        __builtin_amdgcn_raw_buffer_store_b128(u32x4{perm_lo(w[0], w[1]), perm_lo(w[2], w[3]), perm_lo(w[4], w[5]), perm_lo(w[6], w[7])}, rw, off, 0, SC1);
        __builtin_amdgcn_raw_buffer_store_b128(u32x4{perm_hi(w[0], w[1]), perm_hi(w[2], w[3]), perm_hi(w[4], w[5]), perm_hi(w[6], w[7])}, rw, off + 16, 0, SC1);
      }
    }
    TS(10);
    arrive(rw, 1, tb & 15);
    TS(11);
  }

  const int db = idx & 7, nt2 = xc * 4 + (idx >> 3), odd = db & 1;
  const i32x4 sb2 = mk_srd(w2, L_ * F_ * 2);
  const unsigned vb2a = (lane >> 3) * (L_ * 2) + (((lane & 7) ^ (2 * (g & 1))) << 4), vb2b = vb2a ^ 64;
  const unsigned b2_base = nt2 * 128 + wid * 32 * (L_ * 2);
#define G2_LDB(slot, i, j) do { const unsigned so_ = b2_base + ((i) * 128 + (j) * 8) * (L_ * 2); \
    if ((j) & 1) BDMA(ring, (slot) * 4096 + (j) * 1024, vb2b, sb2, so_); else BDMA(ring, (slot) * 4096 + (j) * 1024, vb2a, sb2, so_); } while (0)

  if (g1_on) {
    sfor<32>([&](auto I) { G1_LDB(I / 8, g1_lstep(I / 8), I % 8); });
    const unsigned va0 = lane * 16, va1 = lane * 16 + 4096;
    const unsigned a_base = WS_HF + wid * 36864 + mt * 6144;
    u32x4 A[4][6];
    f32x4 acc[4][2][6];
#pragma unroll
    for (int m = 0; m < 4; ++m)
#pragma unroll
      for (int c = 0; c < 2; ++c)
#pragma unroll
        for (int t = 0; t < 6; ++t) { acc[m][c][t] = f32x4{0.f, 0.f, 0.f, 0.f}; asm volatile("" : "+a"(acc[m][c][t])); }
    await(rw, 1, xc, 0, 16, 16, wid == 0, lane);
    TS(12);
#define G1_LDA(t, f) do { const unsigned so_ = a_base + (t) * (4 * 36864); if ((f) < 4) BLOAD(A[(t) & 3][f], va0, sw, so_, ((f) & 3) * 1024); else BLOAD(A[(t) & 3][f], va1, sw, so_, ((f) & 3) * 1024); } while (0)
#define G1_WAIT(t, n) wait6<(n)>(A[(t) & 3])
    sfor<18>([&](auto I) { G1_LDA(I / 6, I % 6); });
    u32x4 Bf[2][2];
    G1_WAIT(0, 12);
    TS(13);
    Bf[0][0] = G1_FRAG(0, 0, 0); Bf[0][1] = G1_FRAG(0, 0, 1); SB0;
    sfor<16>([&](auto TT) {
      constexpr int t = TT;
      constexpr bool R = g1_isR(t);
      constexpr int slot = g1_lidx(t) & 3;
      sfor<16>([&](auto QQ) {
        constexpr int qq = QQ, m = qq >> 2, c = (qq >> 1) & 1, t0 = 3 * (qq & 1);
        if constexpr ((qq & 3) == 0) {
          if constexpr (m < 3) {
            if constexpr (!R) { Bf[(m + 1) & 1][0] = G1_FRAG(slot, m + 1, 0); Bf[(m + 1) & 1][1] = G1_FRAG(slot, m + 1, 1); SB0; }
          } else if constexpr (t < 15) {
            G1_WAIT(t + 1, g1_ops(t - 1) + g1_ops(t));
            if constexpr (!g1_isR(t + 1)) { Bf[0][0] = G1_FRAG(g1_lidx(t + 1) & 3, 0, 0); Bf[0][1] = G1_FRAG(g1_lidx(t + 1) & 3, 0, 1); SB0; }
          }
        }
        sfor<3>([&](auto KK) {
          constexpr int f = t0 + KK;
          if constexpr (!R) MFMA_VV(acc[m][c][f], Bf[m & 1][c], A[t & 3][f]);
          else if constexpr (t / 3 < 2) MFMA_VV(acc[m][c][f], BR[t / 3][2 * m + c], A[t & 3][f]);
          else MFMA_AV(acc[m][c][f], BR[t / 3][2 * m + c], A[t & 3][f]);
        });
        if constexpr (qq < g1_nb(t)) { constexpr int pc = g1_b0(t) + qq; G1_LDB((pc >> 3) & 3, g1_lstep(4 + (pc >> 3)), pc & 7); }
        else if constexpr (qq >= 6 && qq < 12 && t <= 12) G1_LDA(t + 3, qq - 6);
      });
    });
    asm volatile("s_nop 7" ::: "memory");
#pragma unroll
    for (int m = 0; m < 4; ++m)
#pragma unroll
      for (int c = 0; c < 2; ++c)
#pragma unroll
        for (int t = 0; t < 6; ++t) asm volatile("" : "+a"(acc[m][c][t]));
    SB0;
    TS(14);
    const u32x4 bw = *(const u32x4*)(b1 + nt1 * 128 + wid * 32 + 8 * g);
    {
      char* o = smem + wid * G_WS + lane * 16;
#pragma unroll
      for (int m = 1; m < 4; ++m)
#pragma unroll
        for (int c = 0; c < 2; ++c)
#pragma unroll
          for (int t = 0; t < 6; ++t) *(f32x4*)(o + ((m - 1) * 12 + c * 6 + t) * 1024) = acc[m][c][t];
    }
    LDS_BARRIER();
#pragma unroll
    for (int m = 1; m < 4; ++m) {
      const char* s = smem + ((wid - m) & 3) * G_WS + (m - 1) * 12 * 1024 + lane * 16;
#pragma unroll
      for (int c = 0; c < 2; ++c)
#pragma unroll
        for (int t = 0; t < 6; ++t) acc[0][c][t] += *(const f32x4*)(s + (c * 6 + t) * 1024);
    }
    asm volatile("" :: "v"(bw));
    LDS_BARRIER();
    sfor<6>([&](auto TT) {
      constexpr int t = TT;
      u32x4 o;
      sfor<4>([&](auto KK) {
        constexpr int k = KK;
        const f32x4 a = acc[0][k >> 1][t];
        const f32x2 u = ((k & 1) ? a.zw : a.xy) + unpk(bw[k]);
        const f32x2 z = u * __builtin_elementwise_fma(u * u, f32x2{-0.1029432397f, -0.1029432397f}, f32x2{-2.302208198f, -2.302208198f});
        const f32x2 den = f32x2{__builtin_amdgcn_exp2f(z.x), __builtin_amdgcn_exp2f(z.y)} + 1.0f;
        o[k] = pkv(u * f32x2{__builtin_amdgcn_rcpf(den.x), __builtin_amdgcn_rcpf(den.y)});
        constexpr int n = 4 * t + k;
        if constexpr (n < 16) { G2_LDB(n >> 1, n >> 1, 2 * (n & 1)); G2_LDB(n >> 1, n >> 1, 2 * (n & 1) + 1); SB0; }
      });
      __builtin_amdgcn_raw_buffer_store_b128(o, rw, WS_GF + ((nt1 * 4 + wid) * 36 + mt * 6 + t) * 1024 + lane * 16, 0, SC1);
    });
    TS(15);
    arrive(rw, 2, mt * 2 + (nt1 & 1));
  } else {
    warm(w2, L_ * F_ * 2, 0, L_ * F_ * 2, ((idx - 27) * 8 + xc) * 256 + tid, 40 * 256);
    sfor<32>([&](auto I) { G2_LDB(I / 4, I / 4, I % 4); });
    VMCNT(0);
  }

  {
    unsigned rb[4];
#pragma unroll
    for (int c = 0; c < 4; ++c) rb[c] = ring + 128 * (8 * g + q4) + 16 * ((2 * ((c + wid) & 3) + (p4 >> 1)) ^ (2 * ((q4 >> 1) | ((g & 1) << 1)))) + 8 * (p4 & 1);
#define G2_FRAG(slot, c) cat(trd(rb[c] + (slot) * 4096), trd(rb[c] + (slot) * 4096 + 512))
    u32x4 W[8][4];
    sfor<32>([&](auto I) { W[I / 4][I % 4] = G2_FRAG(I / 4, I % 4); });
    asm volatile("s_waitcnt lgkmcnt(0)" ::: "memory");
    SB0;
    sfor<32>([&](auto I) { G2_LDB(I / 4, 8 + I / 4, I % 4); });
    const int rt0 = 9 * (db >> 1) + 5 * odd, rth = 9 * (db >> 1) + 4;
    const unsigned va = rt0 * 1024 + lane * 16;
    const unsigned vh = rth * 1024 + ((lane & 48) + (lane & 7) + 8 * odd) * 16 + ((lane >> 3) & 1) * (4 * 36864);
    const unsigned a_base = WS_GF + wid * 36864;
    u32x4 A[4][4], Ah[2];
    f32x4 acc[4][6];
#pragma unroll
    for (int c = 0; c < 4; ++c)
#pragma unroll
      for (int t = 0; t < 6; ++t) { acc[c][t] = f32x4{0.f, 0.f, 0.f, 0.f}; asm volatile("" : "+a"(acc[c][t])); }
    {
      const int m0 = (72 * db) / 96, m1 = (72 * db + 71) / 96;
      await(rw, 2, xc, 2 * m0, 2 * (m1 - m0 + 1), 18, wid == 0, lane);
    }
    TS(16);
#define G2_LDA(S, i, t) do { const unsigned so_ = a_base + (i) * (4 * 36864); BLOAD(A[S][t], va, sw, so_, (t) * 1024); } while (0)
#define G2_LDH(S, i) do { const unsigned so_ = a_base + (i) * (4 * 36864); BLOAD(Ah[(S) >> 1], vh, sw, so_, 0); } while (0)
    G2_LDA(0, 0, 0); G2_LDA(0, 0, 1); G2_LDA(0, 0, 2); G2_LDA(0, 0, 3); G2_LDH(0, 0);
    G2_LDA(1, 1, 0); G2_LDA(1, 1, 1); G2_LDA(1, 1, 2); G2_LDA(1, 1, 3);
    G2_LDA(2, 2, 0); G2_LDA(2, 2, 1); G2_LDA(2, 2, 2); G2_LDA(2, 2, 3); G2_LDH(2, 2);
    u32x4 Bf[2][2];
#define G2_RDB(buf, SL, c) do { Bf[buf][0] = G2_FRAG(SL, c); Bf[buf][1] = G2_FRAG(SL, (c) + 1); SB0; } while (0)
#define G2_WAIT(S, n) asm volatile("s_waitcnt vmcnt(%5)" : "+v"(A[S][0]), "+v"(A[S][1]), "+v"(A[S][2]), "+v"(A[S][3]), "+v"(Ah[(S) >> 1]) : "i"(n) : "memory")
#define G2_M1(RG, SL, c, t) do { constexpr int S__ = (SL) & 3; \
    if ((t) < 4) MFMA_VV(acc[c][(t) & 3], A[S__][(t) & 3], (RG) ? W[SL][c] : Bf[(c) >> 1][(c) & 1]); \
    else MFMA_VV(acc[c][4 + (S__ & 1)], Ah[S__ >> 1], (RG) ? W[SL][c] : Bf[(c) >> 1][(c) & 1]); } while (0)
#define G2_B(on, SL, i, j) do { if (on) G2_LDB(((SL) + 7) & 7, (i) + 7, j); } while (0)
#define G2_A(on, S, i, t) do { if (on) G2_LDA(((S) + 3) & 3, (i) + 3, t); } while (0)
#define G2_STEP(RG, NL, SL, i, DB, DA, WN) do { constexpr int S_ = (SL) & 3; \
    if (!(RG)) G2_RDB(1, SL, 2); \
    G2_M1(RG, SL, 0, 0); G2_B(DB, SL, i, 0); G2_M1(RG, SL, 0, 1); G2_B(DB, SL, i, 1); G2_M1(RG, SL, 0, 2); G2_B(DB, SL, i, 2); G2_M1(RG, SL, 0, 3); G2_B(DB, SL, i, 3); \
    G2_M1(RG, SL, 0, 4); G2_A(DA, S_, i, 0); G2_M1(RG, SL, 1, 0); G2_A(DA, S_, i, 1); G2_M1(RG, SL, 1, 1); G2_A(DA, S_, i, 2); G2_M1(RG, SL, 1, 2); G2_A(DA, S_, i, 3); \
    G2_M1(RG, SL, 1, 3); G2_M1(RG, SL, 1, 4); \
    if ((WN) >= 0) { G2_WAIT((S_ + 1) & 3, (WN) >= 0 ? (WN) : 0); if (NL) G2_RDB(0, ((SL) + 1) & 7, 0); } \
    G2_M1(RG, SL, 2, 0); G2_M1(RG, SL, 2, 1); G2_M1(RG, SL, 2, 2); G2_M1(RG, SL, 2, 3); G2_M1(RG, SL, 2, 4); \
    G2_M1(RG, SL, 3, 0); G2_M1(RG, SL, 3, 1); G2_M1(RG, SL, 3, 2); G2_M1(RG, SL, 3, 3); G2_M1(RG, SL, 3, 4); \
    if ((DA) && (S_ & 1)) G2_LDH((S_ + 3) & 3, (i) + 3); } while (0)
    G2_WAIT(0, 9);
    TS(17);
    G2_STEP(1, 0, 0, 0, false, true, 9); G2_STEP(1, 0, 1, 1, false, true, 8); G2_STEP(1, 0, 2, 2, false, true, 9); G2_STEP(1, 0, 3, 3, false, true, 8);
    G2_STEP(1, 0, 4, 4, false, true, 9); G2_STEP(1, 0, 5, 5, false, true, 8); G2_STEP(1, 0, 6, 6, false, true, 9); G2_STEP(1, 1, 7, 7, false, true, 8);
    G2_STEP(0, 1, 0, 8, false, true, 9);
    G2_STEP(0, 1, 1, 9, true, true, 12); G2_STEP(0, 1, 2, 10, true, true, 17); G2_STEP(0, 1, 3, 11, true, true, 16); G2_STEP(0, 1, 4, 12, true, true, 17);
#pragma nounroll
    for (int it = 0; it < 2; ++it) {
      const int i = 8 * it + 13;
      G2_STEP(0, 1, 5, i, true, true, 16); G2_STEP(0, 1, 6, i + 1, true, true, 17); G2_STEP(0, 1, 7, i + 2, true, true, 16); G2_STEP(0, 1, 0, i + 3, true, true, 17);
      G2_STEP(0, 1, 1, i + 4, true, true, 16); G2_STEP(0, 1, 2, i + 5, true, true, 17); G2_STEP(0, 1, 3, i + 6, true, true, 16); G2_STEP(0, 1, 4, i + 7, true, true, 17);
    }
    G2_STEP(0, 1, 5, 29, false, true, 12); G2_STEP(0, 1, 6, 30, false, true, 9); G2_STEP(0, 1, 7, 31, false, true, 8); G2_STEP(0, 1, 0, 32, false, true, 9);
    G2_STEP(0, 1, 1, 33, false, false, 4); G2_STEP(0, 1, 2, 34, false, false, 0); G2_STEP(0, 1, 3, 35, false, false, -1);
    asm volatile("s_nop 7" ::: "memory");
#pragma unroll
    for (int c = 0; c < 4; ++c)
#pragma unroll
      for (int t = 0; t < 6; ++t) asm volatile("" : "+a"(acc[c][t]));
    SB0;
    TS(18);
    const int s = nt2 * 64 + wid * 16 + r16;
    const float bias = bf2f(b2[s]);
    const int dfull = rt0 * 16 + 4 * g, dhalf = rth * 16 + 8 * odd + 4 * (g & 1);
    u32x2 hw[5];
#pragma unroll
    for (int t = 0; t < 4; ++t) hw[t] = __builtin_amdgcn_raw_buffer_load_b64(rw, WS_H + (s * D_ + dfull + 16 * t) * 2, 0, SC1);
    hw[4] = __builtin_amdgcn_raw_buffer_load_b64(rw, WS_H + (s * D_ + dhalf) * 2, 0, SC1);
    {
      char* o = smem + wid * G_WS + lane * 16;
#pragma unroll
      for (int c = 0; c < 4; ++c) {
        if (c > 0) {
#pragma unroll
          for (int t = 0; t < 4; ++t) *(f32x4*)(o + (c * 5 + t) * 1024) = acc[c][t];
        }
        *(f32x4*)(o + (c * 5 + 4) * 1024) = g < 2 ? acc[c][4] : acc[c][5];
      }
    }
    LDS_BARRIER();
    f32x4 yh = {0.f, 0.f, 0.f, 0.f};
#pragma unroll
    for (int c = 0; c < 4; ++c) {
      const char* sp = smem + ((wid - c) & 3) * G_WS + c * 5 * 1024;
      if (c > 0) {
#pragma unroll
        for (int t = 0; t < 4; ++t) acc[0][t] += *(const f32x4*)(sp + t * 1024 + lane * 16);
      }
      yh += *(const f32x4*)(sp + 4096 + (lane & 31) * 16) + *(const f32x4*)(sp + 4096 + 512 + (lane & 31) * 16);
    }
#pragma unroll
    for (int t = 0; t < 5; ++t) {
      const f32x4 y = t < 4 ? acc[0][t & 3] : yh;
      u32x2 o;
      o.x = pkv(unpk(hw[t].x) + unpk(pkv(unpk(pkv(y.xy)) + bias)));
      o.y = pkv(unpk(hw[t].y) + unpk(pkv(unpk(pkv(y.zw)) + bias)));
      if (t < 4) *(u32x2*)(out + s * D_ + dfull + 16 * t) = o;
      else if (g < 2) *(u32x2*)(out + s * D_ + dhalf) = o;
    }
    TS(19);
  }
  TSOUT();
}

extern "C" void kernel_launch(void* const* d_in, const int* in_sizes, int n_in,
                              void* d_out, int out_size, void* d_ws, size_t ws_size,
                              hipStream_t stream) {
  k_all<<<256, 256, 0, stream>>>((const u16*)d_in[0], (const u16*)d_in[1], (const u16*)d_in[2], (const u16*)d_in[3], (const u16*)d_in[4],
                                 (const u16*)d_in[5], (const u16*)d_in[6], (const u16*)d_in[8], (const u16*)d_in[9], (const u16*)d_in[10],
                                 (const u16*)d_in[11], (char*)d_ws, (u16*)d_out);
}
```

```cpp
#include <hip/hip_runtime.h>
#include <stdint.h>
#include <utility>

typedef unsigned short u16;
typedef unsigned long long u64;
typedef __attribute__((ext_vector_type(4))) short s16x4;
typedef __attribute__((ext_vector_type(2))) unsigned short u16x2;
typedef __attribute__((ext_vector_type(8))) __bf16 bf16x8;
typedef __attribute__((ext_vector_type(2))) __bf16 bf16x2;
typedef __attribute__((ext_vector_type(4))) float f32x4;
typedef __attribute__((ext_vector_type(2))) float f32x2;
typedef __attribute__((ext_vector_type(4))) unsigned u32x4;
typedef __attribute__((ext_vector_type(2))) unsigned u32x2;
typedef __attribute__((ext_vector_type(4))) int i32x4;
typedef __amdgpu_buffer_rsrc_t rsrc_t;
#define LAS __attribute__((address_space(3)))
#define DI __device__ __forceinline__

#define TSDECL
#define TS(i)
#define TC(i)
#define TSOUT()

#define PF0_N 0
#define G2_LEAD 4
#define PF1_END 16896
#define PF1_NT 0
#define PF2_ROWS 3200
#define PF3_N 1
#define ABL 0
#define POLL_SLEEP 4

constexpr int L_ = 2048, D_ = 576, N_ = 128, F_ = 4608;
constexpr unsigned SZ = L_ * D_ * 2;
constexpr unsigned WS_UB = 0, WS_AT = SZ, WS_H = 2 * SZ, WS_HF = 3 * SZ, WS_GF = 4 * SZ, WS_SY = 7 * SZ, WS_BYTES = 7 * SZ + 131072;
constexpr unsigned SY_EPOCH = WS_SY, SY_FLAG = WS_SY + 4096, SY_CNT = WS_SY + 32768;
constexpr unsigned OOB = 0x7ff00000u;
constexpr int SC1 = 16;

template <class Fn, int... I> DI void sfor_(Fn&& f, std::integer_sequence<int, I...>) { (f(std::integral_constant<int, I>{}), ...); }
template <int N, class Fn> DI void sfor(Fn&& f) { sfor_(f, std::make_integer_sequence<int, N>{}); }

DI float bflo(unsigned w) { return __uint_as_float(w << 16); }
DI float bfhi(unsigned w) { return __uint_as_float(w & 0xffff0000u); }
DI f32x2 unpk(unsigned w) { return f32x2{bflo(w), bfhi(w)}; }
DI float bf2f(u16 v) { return __uint_as_float(((unsigned)v) << 16); }
DI unsigned pkv(f32x2 v) { return __builtin_bit_cast(unsigned, __builtin_convertvector(v, bf16x2)); }
DI unsigned pk2(float lo, float hi) { return pkv(f32x2{lo, hi}); }
DI float rbf(float f) { return bflo(pk2(f, f)); }
DI unsigned perm_lo(unsigned a, unsigned b) { return __builtin_amdgcn_perm(b, a, 0x05040100u); }
DI unsigned perm_hi(unsigned a, unsigned b) { return __builtin_amdgcn_perm(b, a, 0x07060302u); }
DI f32x4 mfma16(u32x4 a, u32x4 b, f32x4 c) { return __builtin_amdgcn_mfma_f32_16x16x32_bf16(__builtin_bit_cast(bf16x8, a), __builtin_bit_cast(bf16x8, b), c, 0, 0, 0); }
DI rsrc_t mk_rsrc(const void* p, unsigned bytes) { return __builtin_amdgcn_make_buffer_rsrc((void*)p, 0, bytes, 0x00020000); }
DI i32x4 mk_srd(const void* p, unsigned bytes) {
  const u64 a = (u64)p;
  i32x4 r; r.x = (int)(unsigned)a; r.y = (int)((unsigned)(a >> 32) & 0xffffu); r.z = (int)bytes; r.w = 0x00020000; return r;
}
#define LDS_BARRIER() do { asm volatile("s_waitcnt lgkmcnt(0)" ::: "memory"); __builtin_amdgcn_s_barrier(); asm volatile("" ::: "memory"); } while (0)
template <int IMM> DI void bload_(u32x4& dst, unsigned voff, i32x4 srd, unsigned soff) {
  asm volatile("buffer_load_dwordx4 %0, %1, %2, %3 offen offset:%4 sc1" : "=v"(dst) : "v"(voff), "s"(srd), "s"(soff), "i"(IMM) : "memory");
}
template <int M0OFF, int IMM> DI void bdma(unsigned ldsbase, unsigned voff, i32x4 srd, unsigned soff) {
  if constexpr (IMM == 0) asm volatile("s_add_u32 m0, %0, %1\n\ts_nop 0\n\tbuffer_load_dwordx4 %2, %3, %4 offen lds" :: "s"(ldsbase), "i"(M0OFF), "v"(voff), "s"(srd), "s"(soff) : "memory", "scc");
  else asm volatile("buffer_load_dwordx4 %0, %1, %2 offen offset:%3 lds" :: "v"(voff), "s"(srd), "s"(soff), "i"(IMM) : "memory");
}
template <int OFF> DI void put_row(unsigned v) { asm volatile("ds_write_addtid_b32 %0 offset:%1" :: "v"(v), "i"(OFF) : "memory"); }
DI void keep(unsigned v) { asm volatile("" :: "v"(v)); }
DI void bload2(u32x2& dst, unsigned voff, i32x4 srd) { asm volatile("buffer_load_dwordx2 %0, %1, %2, 0 offen sc1" : "=v"(dst) : "v"(voff), "s"(srd) : "memory"); }
DI void bload2r(u32x2& dst, unsigned voff, i32x4 srd) { asm volatile("buffer_load_dwordx2 %0, %1, %2, 0 offen sc1" : "+v"(dst) : "v"(voff), "s"(srd) : "memory"); }
DI void pin_af(f32x4& v) { asm volatile("" : "+a"(v)); }
#define BLOAD(dst, voff, srd, soff, imm) bload_<(imm)>(dst, voff, srd, soff)
#define SB0 __builtin_amdgcn_sched_barrier(0)
#define MFMA_STR "v_mfma_f32_16x16x32_bf16 %0, %1, %2, %0"
DI void MFMA_VV(f32x4& acc, const u32x4& a, const u32x4& b) { asm volatile(MFMA_STR : "+a"(acc) : "v"(a), "v"(b)); }
DI void MFMA_AV(f32x4& acc, const u32x4& a, const u32x4& b) { asm volatile(MFMA_STR : "+a"(acc) : "a"(a), "v"(b)); }
DI void MFMA_VA(f32x4& acc, const u32x4& a, const u32x4& b) { asm volatile(MFMA_STR : "+a"(acc) : "v"(a), "a"(b)); }
template <int IMM> DI void gld32(unsigned& d, unsigned voff, const void* base) { asm volatile("global_load_dword %0, %1, %2 offset:%3" : "=v"(d) : "v"(voff), "s"(base), "i"(IMM) : "memory"); }
template <int IMM> DI void gld16(unsigned& d, unsigned voff, const void* base) { asm volatile("global_load_ushort %0, %1, %2 offset:%3" : "=v"(d) : "v"(voff), "s"(base), "i"(IMM) : "memory"); }
DI void gld128(u32x4& d, unsigned voff, const void* base) { asm volatile("global_load_dwordx4 %0, %1, %2" : "=v"(d) : "v"(voff), "s"(base) : "memory"); }
DI void set_m0(unsigned v) { asm volatile("s_mov_b32 m0, %0\n\ts_nop 0" :: "s"(v) : "memory"); }
template <int N> DI void wait5(u32x4* a, u32x4& h) { asm volatile("s_waitcnt vmcnt(%5)" : "+v"(a[0]), "+v"(a[1]), "+v"(a[2]), "+v"(a[3]), "+v"(h) : "i"(N) : "memory"); }
template <int N> DI void wait9(unsigned* a) { asm volatile("s_waitcnt vmcnt(%9)" : "+v"(a[0]), "+v"(a[1]), "+v"(a[2]), "+v"(a[3]), "+v"(a[4]), "+v"(a[5]), "+v"(a[6]), "+v"(a[7]), "+v"(a[8]) : "i"(N) : "memory"); }
DI void pin9(unsigned* a) { asm volatile("" : "+v"(a[0]), "+v"(a[1]), "+v"(a[2]), "+v"(a[3]), "+v"(a[4]), "+v"(a[5]), "+v"(a[6]), "+v"(a[7]), "+v"(a[8])); }
DI void pin_a(u32x4& v) { asm volatile("" : "+a"(v)); }
template <int N> DI void wait6(u32x4* a) { asm volatile("s_waitcnt vmcnt(%6)" : "+v"(a[0]), "+v"(a[1]), "+v"(a[2]), "+v"(a[3]), "+v"(a[4]), "+v"(a[5]) : "i"(N) : "memory"); }
#define VMCNT(n) asm volatile("s_waitcnt vmcnt(%0)" :: "i"(n) : "memory")
DI u32x2 trd(unsigned addr) { return __builtin_bit_cast(u32x2, __builtin_amdgcn_ds_read_tr16_b64_v4i16((LAS s16x4*)(size_t)addr)); }
DI u32x4 cat(u32x2 a, u32x2 b) { return u32x4{a.x, a.y, b.x, b.y}; }

template <int CTRL> DI float dpp_add(float v) { return v + __builtin_bit_cast(float, __builtin_amdgcn_update_dpp(0, __builtin_bit_cast(int, v), CTRL, 0xf, 0xf, true)); }
DI float half_sum(float v) {
  v = dpp_add<0xB1>(v); v = dpp_add<0x4E>(v); v = dpp_add<0x141>(v); v = dpp_add<0x140>(v);
  float w = v;
  asm volatile("s_nop 1\n\tv_permlane16_swap_b32 %0, %1" : "+v"(v), "+v"(w));
  return v + w;
}
DI void ln9(const f32x2* v, unsigned* o) {
  const f32x2 s = ((v[0] + v[1]) + (v[2] + v[3])) + ((v[4] + v[5]) + (v[6] + v[7])) + v[8];
  const float mean = half_sum(s.x + s.y) * (1.0f / D_);
  f32x2 q0 = {0.f, 0.f}, q1 = {0.f, 0.f};
#pragma unroll
  for (int i = 0; i < 9; ++i) { const f32x2 t = v[i] - mean; if (i & 1) q1 = __builtin_elementwise_fma(t, t, q1); else q0 = __builtin_elementwise_fma(t, t, q0); }
  q0 += q1;
  const float var = half_sum(q0.x + q0.y) * (1.0f / D_);
  const float mb = rbf(mean);
  const float sd = rbf(__builtin_amdgcn_sqrtf(rbf(rbf(var) + 1e-5f)));
  const float inv = __builtin_amdgcn_rcpf(sd);
#pragma unroll
  for (int i = 0; i < 9; ++i) o[i] = pkv(unpk(pkv(v[i] - mb)) * inv);
}
DI f32x2 cmul(f32x2 a, f32x2 b) { const f32x2 bs = {-b.y, b.x}; return __builtin_elementwise_fma(a.yy, bs, a.xx * b); }
DI f32x2 cexp_p(float zl, float zv, float p) {
  const float m = __builtin_amdgcn_exp2f(zl * p);
  const float rev = __builtin_amdgcn_fractf(zv * p);
  return f32x2{__builtin_amdgcn_cosf(rev), __builtin_amdgcn_sinf(rev)} * m;
}
template <int LINES> DI unsigned slab_off(unsigned e, unsigned e1, unsigned pitch, unsigned c0) {
  const unsigned r = e / LINES;
  return e < e1 ? r * pitch + c0 + (e - r * LINES) * 128 : OOB;
}
template <int NT = 0> DI void touch(unsigned& sink, unsigned off, i32x4 srd) {
  if constexpr (NT) asm volatile("buffer_load_dword %0, %1, %2, 0 offen nt" : "=v"(sink) : "v"(off), "s"(srd) : "memory");
  else asm volatile("buffer_load_dword %0, %1, %2, 0 offen" : "=v"(sink) : "v"(off), "s"(srd) : "memory");
}
template <int LINES, int NT = 0> DI void warm(i32x4 srd, unsigned pitch, unsigned c0, unsigned e0, unsigned e1, unsigned idx, unsigned nthr) {
  unsigned v = 0;
#pragma nounroll
  for (unsigned e = e0 + idx; e < e1 + idx; e += nthr) {
    const unsigned off = slab_off<LINES>(e, e1, pitch, c0);
    if constexpr (NT) asm volatile("s_waitcnt vmcnt(3)\n\tbuffer_load_dword %0, %1, %2, 0 offen nt" : "+v"(v) : "v"(off), "s"(srd) : "memory");
    else asm volatile("s_waitcnt vmcnt(3)\n\tbuffer_load_dword %0, %1, %2, 0 offen" : "+v"(v) : "v"(off), "s"(srd) : "memory");
  }
  asm volatile("s_waitcnt vmcnt(0)" : "+v"(v) :: "memory");
}

template <int YOUNGER = 0> DI void arrive(rsrc_t rw, int ho, int shard) {
  VMCNT(YOUNGER);
  __builtin_amdgcn_s_barrier();
  if (threadIdx.x < 8) __builtin_amdgcn_raw_ptr_buffer_atomic_add_i32(1, rw, SY_CNT + ((ho * 8 + threadIdx.x) * 16 + shard) * 256, 0, SC1);
}
DI unsigned cnt_off(int ho, int rep, int s0, int ns, int lane) { return lane < ns ? SY_CNT + ((ho * 8 + rep) * 16 + s0 + lane) * 256 : OOB; }
DI void poll_issue(unsigned& f, unsigned off, i32x4 srd) { asm volatile("buffer_load_dword %0, %1, %2, 0 offen sc1" : "+v"(f) : "v"(off), "s"(srd) : "memory"); }
DI void await(rsrc_t rw, int ho, int rep, int s0, int ns, unsigned want, bool poller, int lane) {
  if (poller) {
    const unsigned off = cnt_off(ho, rep, s0, ns, lane);
    const unsigned w = lane < ns ? want : 0u;
#pragma nounroll
    for (int spin = 0; spin < (1 << 16); ++spin) {
      const unsigned f = __builtin_amdgcn_raw_buffer_load_b32(rw, off, 0, SC1);
      if (__all(f == w)) break;
      __builtin_amdgcn_s_sleep(POLL_SLEEP);
      asm volatile("" ::: "memory");
    }
  }
  asm volatile("" ::: "memory");
  __builtin_amdgcn_s_barrier();
  asm volatile("" ::: "memory");
}

constexpr int KG_PITCH = 528;
constexpr int KG_B = 41 * KG_PITCH, KG_CH = 57 * KG_PITCH;
constexpr int KG_T = 3 * KG_CH, KG_TP = 2072 * 2;
constexpr int CV_KP = 259 * 16 + 16;
constexpr int CV_U = 4 * CV_KP;
constexpr int CV_UP = (29 + 256) * 16 + 64 + 16;
constexpr int CV_TB = CV_U + 2 * CV_UP;
constexpr int CV_P = 3 * CV_TB;
constexpr int G_WS = 36864;
constexpr int T_PITCH = 288 + 8;
constexpr int B0 = 9728;
constexpr int K_LDS = B0 + 4 * G_WS;
static_assert(CV_KP % 256 == 64 && CV_UP % 64 == 32 && CV_TB % 16 == 0 && KG_CH % 16 == 0 && CV_P + 65536 <= 4 * G_WS && KG_T >= CV_P && KG_T + 3 * KG_TP <= 4 * G_WS &&
              8 * T_PITCH * 4 <= B0 && K_LDS <= 163840, "");

constexpr int kg_e(int r) { return r < 16 ? r & 7 : r < 24 ? 2 * (r & 3) : 4 * (r & 1); }
constexpr int kg_g(int r) { return r < 16 ? r >> 3 : r < 24 ? 2 + ((r - 16) >> 2) : 4 + ((r - 24) >> 1); }

constexpr bool g2_isR(int t) { return t < 27 && t % 3 != 2; }
constexpr int g2_ridx(int t) { return t - t / 3; }
constexpr int g2_slot(int t) { return t < 27 ? t / 3 : t - 27; }
constexpr int g2_qstep(int q) { return q < 18 ? q + q / 2 : 3 * (q - 18) + 2; }
#define G2_DMAX 5
constexpr int G2_WP = 14;
constexpr int g2_aend(int t) { return t > 35 ? 162 : 9 * (t / 2) + ((t & 1) ? 9 : 5); }
struct G2Sched { short op[36][20]; short wait[36]; };
constexpr G2Sched g2_make() {
  G2Sched s{};
  int apos[162] = {}, bpos[36] = {}, seq = g2_aend(2), na = g2_aend(2);
  for (int i = 0; i < na; ++i) apos[i] = i;
  for (int t = 0; t < 36; ++t) {
    int list[20] = {}, n = 0;
    if (t >= 3 && t < 30) { const int p = 4 * ((t - 3) / 3) + (t % 3 == 0 ? 0 : t % 3 + 1); list[n++] = 2000 + p; if (t % 3 == 0) list[n++] = 2000 + p + 1; }
    const int d = 3 + (t + 1) / 2 < G2_DMAX ? 3 + (t + 1) / 2 : G2_DMAX;
    while (na < g2_aend(t + d) && n < 9) list[n++] = 1000 + na++;
    if (t == 33) for (int i = 0; i < 5; ++i) list[n++] = 3000 + i;
    for (int m = 0, k = 0; m < 20; ++m) {
      if (m == G2_WP && t < 35) {
        int need = apos[g2_aend(t + 1) - 1];
        if (t + 1 >= 27 && bpos[4 * (t + 1 - 27) + 3] > need) need = bpos[4 * (t + 1 - 27) + 3];
        s.wait[t] = (short)(seq - 1 - need);
      }
      if (k < n && m == (k * 20) / n) {
        const int o = list[k++];
        s.op[t][m] = (short)o;
        if (o >= 2000 && o < 3000) bpos[o - 2000] = seq; else if (o < 2000) apos[o - 1000] = seq;
        ++seq;
      }
    }
  }
  return s;
}
constexpr G2Sched G2S = g2_make();

constexpr bool g1_isR(int t) { return t == 2 || t == 5; }
constexpr int g1_lidx(int t) { return t < 2 ? t : t < 5 ? t - 1 : t - 2; }
constexpr int g1_kb(int t) { return t == 2 ? 0 : t == 5 ? 1 : t >= 14 ? t - 12 : 4 + g1_lidx(t); }
constexpr int g1_nb(int t) { return (t < 1 || t > 10) ? 0 : t >= 7 ? 8 : ((t - 1) % 3 == 2 ? 4 : 6); }
constexpr int g1_b0(int t) { return t >= 7 ? 32 + 8 * (t - 7) : 16 * ((t - 1) / 3) + 6 * ((t - 1) % 3); }
constexpr int g1_ops(int t) { return t < 0 ? 6 : g1_nb(t) + 6; }

__global__ __launch_bounds__(256, 1) void k_all(const u16* __restrict__ x, const u16* __restrict__ logA, const u16* __restrict__ Aim,
                                                const u16* __restrict__ Cre, const u16* __restrict__ Cim, const u16* __restrict__ logdt,
                                                const u16* __restrict__ Dsk, const u16* __restrict__ w1, const u16* __restrict__ b1,
                                                const u16* __restrict__ w2, const u16* __restrict__ b2, char* __restrict__ ws, u16* __restrict__ out) {
  __shared__ __attribute__((aligned(16))) char smem_[K_LDS];
  char* const smem = smem_ + B0;
  const int tid = threadIdx.x, lane = tid & 63, wid = __builtin_amdgcn_readfirstlane(tid >> 6);
  const int xc = blockIdx.x & 7, idx = blockIdx.x >> 3;
  TSDECL;
  TS(0);
  const rsrc_t rw = mk_rsrc(ws, WS_BYTES);
  const i32x4 sw = mk_srd(ws, WS_BYTES), sb1 = mk_srd(w1, L_ * F_ * 2), sb2 = mk_srd(w2, L_ * F_ * 2);
  const unsigned ldsT = (unsigned)(size_t)smem_, lds0 = ldsT + B0, ring = lds0 + wid * G_WS;
  const int g = lane >> 4, r16 = lane & 15, q4 = r16 >> 2, p4 = lane & 3;
  const int tb = xc * 32 + idx;
  const int tok = wid * 2 + (lane >> 5), ll = lane & 31;
  unsigned* T = (unsigned*)smem_;
  const bool cv_on = idx < 24;
  const int d0 = xc * 72 + idx * 3;
  const unsigned w1c0 = ((27 * xc) / 6) * 256;

  const int lin1 = xc * 27 + idx, mt = lin1 % 6, nt1 = min(lin1 / 6, 35);
  const int db = idx & 7, nt2 = xc * 4 + (idx >> 3), odd = db & 1;
  unsigned xw[9], pp[18], bias2, pf0[PF0_N + 2];
  u32x2 ep;
  u32x4 bw;
  sfor<9>([&](auto I) { gld32<128 * I>(xw[I], ((tb * 8 + tok) * (D_ / 2) + ll) * 4, x); });
  bload2(ep, SY_EPOCH, sw);
  gld128(bw, (nt1 * 128 + wid * 32 + 8 * g) * 2, b1);
  gld16<0>(bias2, (nt2 * 64 + wid * 16 + r16) * 2, b2);
  if (cv_on) {
    const unsigned o = (d0 * N_ + (wid & 1) * 64 + lane) * 2;
    sfor<3>([&](auto C) {
      gld16<256 * C>(pp[6 * C], o, logA); gld16<256 * C>(pp[6 * C + 1], o, Aim); gld16<256 * C>(pp[6 * C + 2], o, Cre); gld16<256 * C>(pp[6 * C + 3], o, Cim);
      gld16<2 * C>(pp[6 * C + 4], d0 * 2, logdt); gld16<2 * C>(pp[6 * C + 5], d0 * 2, Dsk);
    });
  } else {
    u64 pc; unsigned len;
    asm volatile("s_getpc_b64 %0\n.Lk_all_pc:\n\ts_mov_b32 %1, .Lk_all_end-.Lk_all_pc" : "=s"(pc), "=s"(len));
    const i32x4 sc = mk_srd((const void*)pc, idx == 31 ? len : 0u);
    touch(pf0[PF0_N], tid * 128, sc); touch(pf0[PF0_N + 1], (256 + tid) * 128, sc);
#pragma unroll
    for (int i = 0; i < PF0_N; ++i) touch(pf0[i], slab_off<10>((i * 8 + idx - 24) * 256 + tid, L_ * 10, F_ * 2, w1c0), sb1);
  }
  unsigned kad[11], ugo[3], ulo[3], ufo[3], kto[4], zoff;
  {
#pragma unroll
    for (int k = 0; k < 6; ++k) {
      const int j0 = 2047 - (8 * tid + 2 * k);
      const unsigned pr = pk2((float)j0, (float)(j0 - 1));
      kad[2 * k] = lds0 + KG_T + 8 + 2 * (int)bflo(pr);
      if (k < 5) kad[2 * k + 1] = lds0 + KG_T + 8 + 2 * (int)bfhi(pr);
    }
#pragma unroll
    for (int k = 0; k < 3; ++k) {
      const unsigned i = tid + 256 * k, t3 = i / 3, c = i - 3 * t3;
      ugo[k] = WS_UB + t3 * (D_ * 16) + (d0 + c) * 16; ulo[k] = c * CV_TB + CV_U + (29 + t3) * 16; ufo[k] = SY_FLAG + xc * 2048 + t3 * 8;
    }
    int pb, o1, o2;
    if (wid == 0) { pb = 4 * (g & 1) + 128 * (g >> 1); o1 = 1; o2 = 2; }
    else if (wid == 1) { pb = g < 2 ? 256 + 128 * g : 256 * g; o1 = g < 2 ? 2 : 4; o2 = g < 2 ? 4 : 128; }
    else { pb = 1024 + 512 * g; o1 = 128; o2 = 256; }
    pb += 8 * r16;
    kto[0] = KG_T + 8 + 2 * min(pb, 2056); kto[1] = KG_T + 8 + 2 * min(pb + o1, 2056); kto[2] = KG_T + 8 + 2 * min(pb + o2, 2056); kto[3] = KG_T + 8 + 2 * min(pb + o1 + o2, 2056);
    const int zc = min(tid / 72, 2), zi = min(tid - 72 * zc, 71);
    zoff = zc * CV_TB + (zi < 12 ? (zi & 3) * CV_KP + (256 + (zi >> 2)) * 16 : CV_U + (zi & 1) * CV_UP + ((zi - 12) >> 1) * 16);
#pragma unroll
    for (int k = 0; k < 11; ++k) asm volatile("" : "+v"(kad[k]));
    asm volatile("" : "+v"(ugo[0]), "+v"(ugo[1]), "+v"(ugo[2]), "+v"(ulo[0]), "+v"(ulo[1]), "+v"(ulo[2]), "+v"(kto[0]), "+v"(kto[1]), "+v"(kto[2]), "+v"(kto[3]), "+v"(zoff), "+v"(ufo[0]), "+v"(ufo[1]), "+v"(ufo[2]));
  }
  const unsigned tro = q4 * 128 + g * 32 + p4 * 8;
  auto chunk = [&](unsigned base, int blk) { return cat(trd(base + tro + blk * 1024), trd(base + tro + blk * 1024 + 512)); };
  if (cv_on) VMCNT(21); else VMCNT(5 + PF0_N);
  pin9(xw);
  if (blockIdx.x == 255) {
    __builtin_amdgcn_raw_buffer_store_b32(0u, rw, SY_CNT + tid * 256, 0, SC1);
    if (tid < 128) __builtin_amdgcn_raw_buffer_store_b32(0u, rw, SY_CNT + (256 + tid) * 256, 0, SC1);
  }
  {
    f32x2 v[9];
    unsigned o[9];
#pragma unroll
    for (int i = 0; i < 9; ++i) v[i] = unpk(xw[i]);
    ln9(v, o);
    set_m0(ldsT + wid * 256);
    sfor<9>([&](auto I) { put_row<1024 * I>(o[I]); });
    if (cv_on || blockIdx.x == 255) VMCNT(0); else VMCNT(2 + PF0_N);
    pin9(pp); pin9(pp + 9);
    asm volatile("" : "+v"(bw), "+v"(bias2), "+v"(ep));
    LDS_BARRIER();
    const unsigned off = WS_UB + tb * (D_ * 16) + tid * 16;
    __builtin_amdgcn_raw_buffer_store_b128(chunk(ldsT, wid), rw, off, 0, SC1);
    __builtin_amdgcn_raw_buffer_store_b128(chunk(ldsT, wid + 4), rw, off + 4096, 0, SC1);
    if (wid == 0) __builtin_amdgcn_raw_buffer_store_b128(chunk(ldsT, 8), rw, off + 8192, 0, SC1);
  }
  const u64 tag64 = (((u64)ep.y << 32) | ep.x) * 0x9E3779B97F4A7C15ull + 0xD1B54A32D192ED03ull;
  const u32x2 tag = {(unsigned)tag64, (unsigned)(tag64 >> 32)};
  TS(1);
  auto signal_u = [&]() {
    VMCNT(0);
    __builtin_amdgcn_s_barrier();
    if (tid < 8) __builtin_amdgcn_raw_buffer_store_b64(tag, rw, SY_FLAG + tid * 2048 + tb * 8, 0, SC1);
  };
  auto await_u = [&]() {
#pragma nounroll
    for (int spin = 0; spin < (1 << 16); ++spin) {
      const u32x4 f0 = __builtin_amdgcn_raw_buffer_load_b128(rw, SY_FLAG + xc * 2048 + lane * 16, 0, SC1);
      const u32x4 f1 = __builtin_amdgcn_raw_buffer_load_b128(rw, SY_FLAG + xc * 2048 + 1024 + lane * 16, 0, SC1);
      const unsigned d = ((f0.x ^ tag.x) | (f0.y ^ tag.y) | (f0.z ^ tag.x) | (f0.w ^ tag.y)) | ((f1.x ^ tag.x) | (f1.y ^ tag.y) | (f1.z ^ tag.x) | (f1.w ^ tag.y));
      if (__all(d == 0u)) break;
      __builtin_amdgcn_s_sleep(POLL_SLEEP);
      asm volatile("" ::: "memory");
    }
  };

  if (cv_on) {
    {
      float zl[3], zv[3];
      f32x2 G0[3];
#pragma unroll
      for (int c = 0; c < 3; ++c) {
        const float dt = rbf(__expf(bflo(pp[6 * c + 4])));
        const float zr = -rbf(__expf(bflo(pp[6 * c]))) * dt, zi = bflo(pp[6 * c + 1]) * dt;
        zl[c] = zr * 1.4426950408889634f; zv[c] = zi * 0.15915494309189535f;
        G0[c] = f32x2{2.0f * (bflo(pp[6 * c + 2]) * dt), -2.0f * (bflo(pp[6 * c + 3]) * dt)};
      }
#define PUT_A(row, v) put_row<B0 + (row) * KG_PITCH>(v)
#define PUT_B(row, v) put_row<B0 + (41 + (row)) * KG_PITCH>(v)
      if (wid < 2) {
        sfor<3>([&](auto C) {
          constexpr int c = C;
          f32x2 E[8], G[6];
          const f32x2 E128 = cexp_p(zl[c], -zv[c], 128.f);
          E[1] = cexp_p(zl[c], -zv[c], 1.f); E[2] = cexp_p(zl[c], -zv[c], 2.f); E[4] = cexp_p(zl[c], -zv[c], 4.f);
          G[0] = G0[c]; G[4] = cmul(cexp_p(zl[c], -zv[c], 512.f), G0[c]);
          G[1] = cmul(G[0], E128); E[3] = cmul(E[1], E[2]); G[5] = cmul(G[4], E128); E[5] = cmul(E[1], E[4]);
          G[2] = cmul(G[1], E128); E[6] = cmul(E[2], E[4]); E[7] = cmul(E[3], E[4]); G[3] = cmul(G[2], E128);
          set_m0(ldsT + c * KG_CH + wid * 256);
          f32x2 v[28], Gs[6];
          sfor<6>([&](auto t) { Gs[t] = f32x2{-G[t].y, G[t].x}; });
          sfor<28>([&](auto r) { if constexpr (kg_e(r) > 0) v[r] = E[kg_e(r)].xx * G[kg_g(r)]; });
          SB0;
          sfor<28>([&](auto r) { if constexpr (kg_e(r) > 0) v[r] = __builtin_elementwise_fma(E[kg_e(r)].yy, Gs[kg_g(r)], v[r]); else v[r] = G[kg_g(r)]; });
          SB0;
          unsigned p[28];
          sfor<29>([&](auto r) { if constexpr (r < 28) p[r] = pkv(v[r]); if constexpr (r > 0) PUT_A(r - 1, p[r - 1]); SB0; });
          if constexpr (c == 0) { signal_u(); TS(2); }
        });
      } else {
        sfor<3>([&](auto C) {
          constexpr int c = C;
          const f32x2 E128 = cexp_p(zl[c], -zv[c], 128.f), E4 = cexp_p(zl[c], -zv[c], 4.f), E8 = cexp_p(zl[c], zv[c], 8.f);
          f32x2 b0 = E8, b1v = cexp_p(zl[c], zv[c], 64.f);
          f32x2 g0 = cmul(cexp_p(zl[c], -zv[c], 768.f), G0[c]), g1 = cmul(cexp_p(zl[c], -zv[c], 1536.f), G0[c]);
          const f32x2 E8s = {-E8.y, E8.x}, E128s = {-E128.y, E128.x}, E4s = {-E4.y, E4.x};
          set_m0(ldsT + c * KG_CH + (wid & 1) * 256);
          PUT_B(0, 0x00003f80u);
          sfor<8>([&](auto j) {
            f32x2 t0, t1, t2, t3, t4;
            if constexpr (j < 6) t0 = b0.xx * E8;
            if constexpr (j < 7) t1 = b1v.xx * E8;
            if constexpr (j < 5) t2 = g0.xx * E128;
            if constexpr (j < 4) t3 = g1.xx * E128;
            if constexpr (j < 2) t4 = g0.xx * E4;
            const unsigned q0 = pkv(b0), q1 = pkv(b1v), q2 = pkv(g0), q3 = pkv(g1);
            SB0;
            if constexpr (j < 2) t4 = __builtin_elementwise_fma(g0.yy, E4s, t4);
            if constexpr (j < 7) PUT_B(j + 1, q0);
            if constexpr (j < 6) b0 = __builtin_elementwise_fma(b0.yy, E8s, t0);
            SB0;
            PUT_B(8 + j, q1);
            if constexpr (j < 7) b1v = __builtin_elementwise_fma(b1v.yy, E8s, t1);
            SB0;
            if constexpr (j < 2) PUT_A(28 + 2 * j, q2); else if constexpr (j < 6) PUT_A(30 + j, q2);
            if constexpr (j < 5) g0 = __builtin_elementwise_fma(g0.yy, E128s, t2);
            SB0;
            if constexpr (j < 5) PUT_A(36 + j, q3);
            if constexpr (j < 4) g1 = __builtin_elementwise_fma(g1.yy, E128s, t3);
            if constexpr (j < 2) PUT_A(29 + 2 * j, pkv(t4));
            SB0;
          });
          if constexpr (c == 0) signal_u();
        });
      }
    }
    u32x2 fl[3];
#pragma unroll
    for (int k = 0; k < 3; ++k) bload2(fl[k], ufo[k], sw);
    LDS_BARRIER();
    TS(3);
    if (wid < 3) {
      const char* ap = smem + min(wid * 16 + r16, 40) * KG_PITCH + g * 16;
      const char* bp = smem + KG_B + r16 * KG_PITCH + g * 16;
      f32x4 a0[3] = {};
      u32x4 fa[2][3][4], fb[2][3][4];
#pragma unroll
      for (int h = 0; h < 2; ++h) {
#pragma unroll
        for (int s = 0; s < 4; ++s)
#pragma unroll
          for (int c = 0; c < 3; ++c) { fa[h][c][s] = *(const u32x4*)(ap + c * KG_CH + (4 * h + s) * 64); fb[h][c][s] = *(const u32x4*)(bp + c * KG_CH + (4 * h + s) * 64); }
        if (h == 1) {
#pragma unroll
          for (int s = 0; s < 4; ++s)
#pragma unroll
            for (int c = 0; c < 3; ++c) a0[c] = mfma16(fa[0][c][s], fb[0][c][s], a0[c]);
#pragma unroll
          for (int k = 0; k < 12; ++k) { __builtin_amdgcn_sched_group_barrier(0x100, 2, 0); __builtin_amdgcn_sched_group_barrier(0x8, 1, 0); }
        }
        SB0;
      }
#pragma unroll
      for (int s = 0; s < 4; ++s)
#pragma unroll
        for (int c = 0; c < 3; ++c) a0[c] = mfma16(fa[1][c][s], fb[1][c][s], a0[c]);
#pragma unroll
      for (int c = 0; c < 3; ++c) {
        const unsigned k01 = pkv(a0[c].xy), k23 = pkv(a0[c].zw);
        *(u16*)(smem + kto[0] + c * KG_TP) = (u16)k01; *(u16*)(smem + kto[1] + c * KG_TP) = (u16)(k01 >> 16);
        *(u16*)(smem + kto[2] + c * KG_TP) = (u16)k23; *(u16*)(smem + kto[3] + c * KG_TP) = (u16)(k23 >> 16);
      }
    } else {
      if (lane < 3) *(u32x2*)(smem + KG_T + lane * KG_TP) = u32x2{0u, 0u};
    }
#pragma nounroll
    for (int spin = 0; spin < (1 << 16); ++spin) {
      asm volatile("s_waitcnt vmcnt(0)" : "+v"(fl[0]), "+v"(fl[1]), "+v"(fl[2]) :: "memory");
      const unsigned d = ((fl[0].x ^ tag.x) | (fl[0].y ^ tag.y)) | ((fl[1].x ^ tag.x) | (fl[1].y ^ tag.y)) | ((fl[2].x ^ tag.x) | (fl[2].y ^ tag.y));
      if (__all(d == 0u)) break;
      __builtin_amdgcn_s_sleep(POLL_SLEEP);
#pragma unroll
      for (int k = 0; k < 3; ++k) bload2r(fl[k], ufo[k], sw);
    }
    VMCNT(0);
    u32x4 ug[3];
#pragma unroll
    for (int k = 0; k < 3; ++k) ug[k] = __builtin_amdgcn_raw_buffer_load_b128(rw, ugo[k], 0, SC1);
    LDS_BARRIER();
    TS(4);
    {
      if (blockIdx.x == 0 && tid == 0) __builtin_amdgcn_raw_buffer_store_b64(tag, rw, SY_EPOCH, 0, SC1);
      u16 kv[3][11];
#pragma unroll
      for (int c = 0; c < 3; ++c)
#pragma unroll
        for (int k = 0; k < 11; ++k) kv[c][k] = *(const LAS u16*)(size_t)(kad[k] + c * KG_TP);
      *(u32x4*)(smem + zoff) = u32x4{0u, 0u, 0u, 0u};
      SB0;
#pragma unroll
      for (int c = 0; c < 3; ++c) {
        unsigned e[5], a[5];
#pragma unroll
        for (int k = 0; k < 5; ++k) { e[k] = kv[c][2 * k] | ((unsigned)kv[c][2 * k + 1] << 16); a[k] = kv[c][2 * k + 1] | ((unsigned)kv[c][2 * k + 2] << 16); }
        char* kp = smem + c * CV_TB + tid * 16;
        *(u32x4*)(kp + 3 * CV_KP) = u32x4{e[0], e[1], e[2], e[3]};
        *(u32x4*)(kp + 2 * CV_KP) = u32x4{a[0], a[1], a[2], a[3]};
        *(u32x4*)(kp + 1 * CV_KP) = u32x4{e[1], e[2], e[3], e[4]};
        *(u32x4*)(kp + 0 * CV_KP) = u32x4{a[1], a[2], a[3], a[4]};
      }
      LDS_BARRIER();
#pragma unroll
      for (int k = 0; k < 3; ++k) {
        char* up = smem + ulo[k];
        *(u32x2*)(up + 8) = ug[k].xy; *(u32x2*)(up + 16) = ug[k].zw;
        *(u32x4*)(up + CV_UP) = ug[k];
      }
    }
    LDS_BARRIER();
    TS(5);
    {
      const char* ap0 = smem + (r16 & 3) * CV_KP + 2 * (2064 - 256 * 7 - 8 * (r16 >> 2) - 8 * g - 64 * wid - 32);
      const char* bp0 = smem + CV_U + (r16 & 1) * CV_UP + 2 * (232 + 32 * (r16 >> 1) - 64 * wid - 32 + 24 - 8 * g);
      char* pw = smem + CV_P + wid * 8192 + ((r16 & 1) + 2 * (g ^ ((r16 >> 1) & 3)) + 8 * (r16 >> 1)) * 16;
      const char* pr = smem + CV_P + (tid >> 5) * 1024 + (2 * ((tid & 3) ^ ((tid >> 2) & 3)) + 8 * ((tid & 31) >> 2)) * 16;
      u32x4 FA[2][8], FB[2][8];
      f32x4 acc[2][8];
      f32x4 py[8];
      unsigned sk[3][4];
      auto rd = [&](int u) {
        const char* ap = ap0 + (u >> 1) * CV_TB + 64 * (1 - (u & 1));
        const char* bp = bp0 + (u >> 1) * CV_TB + 64 * (1 - (u & 1));
#pragma unroll
        for (int k = 0; k < 8; ++k) { FA[u & 1][k] = *(const u32x4*)(ap + 512 * (7 - k)); FB[u & 1][k] = *(const u32x4*)(bp + 512 * k); }
      };
      auto mm = [&](int u, int lo, int hi) {
        f32x4* a = acc[(u >> 1) & 1];
        if (!(u & 1) && lo == 0) {
#pragma unroll
          for (int k = 0; k < 8; ++k) a[k] = f32x4{0.f, 0.f, 0.f, 0.f};
        }
        int n = 0;
#pragma unroll
        for (int m = 0; m < 8; ++m)
#pragma unroll
          for (int o = 0; o <= m; ++o)
            if (m + o < 8) {
              if (n >= lo && n < hi) a[m + o] = mfma16(FA[u & 1][m], FB[u & 1][o], a[m + o]);
              ++n;
              if (o < m) { if (n >= lo && n < hi) a[m + o] = mfma16(FA[u & 1][o], FB[u & 1][m], a[m + o]); ++n; }
            }
      };
      auto wp = [&](int c) {
#pragma unroll
        for (int k = 0; k < 8; ++k) *(f32x4*)(pw + (c & 1) * 32768 + k * 1024) = acc[c & 1][k];
      };
      auto skip = [&](int c) {
        const u32x4 uw = *(const u32x4*)(smem + c * CV_TB + CV_U + CV_UP + (29 + tid) * 16);
        const float dsk = bflo(pp[6 * c + 5]);
#pragma unroll
        for (int k = 0; k < 4; ++k) sk[c][k] = pkv(unpk(uw[k]) * dsk);
      };
      auto rp = [&](int c) {
#pragma unroll
        for (int w = 0; w < 4; ++w) { py[2 * w] = *(const f32x4*)(pr + (c & 1) * 32768 + w * 8192); py[2 * w + 1] = *(const f32x4*)(pr + (c & 1) * 32768 + w * 8192 + 16); }
      };
      auto fin = [&](int c) {
        const f32x4 y0 = (py[0] + py[2]) + (py[4] + py[6]), y1 = (py[1] + py[3]) + (py[5] + py[7]);
        const f32x2 y[4] = {y0.xy, y0.zw, y1.xy, y1.zw};
        u32x4 o;
#pragma unroll
        for (int k = 0; k < 4; ++k) o[k] = pkv(unpk(pkv(y[k])) + unpk(sk[c][k]));
        __builtin_amdgcn_raw_buffer_store_b128(o, rw, WS_AT + (d0 + c) * (L_ * 2) + 16 * tid, 0, SC1);
      };
#define CV_MIX(n, nds, nvalu) do { _Pragma("unroll") for (int k_ = 0; k_ < (n); ++k_) { __builtin_amdgcn_sched_group_barrier(0x8, 1, 0); \
        if (nds) __builtin_amdgcn_sched_group_barrier(0x80, nds, 0); if (nvalu) __builtin_amdgcn_sched_group_barrier(0x2, nvalu, 0); } SB0; } while (0)
      rd(0); SB0;
      mm(0, 0, 36); rd(1); CV_MIX(36, 1, 0);
      mm(1, 0, 20); rd(2); CV_MIX(20, 1, 0);
      { mm(1, 20, 36); skip(0); skip(1); CV_MIX(16, 1, 2); }
      mm(2, 0, 36); wp(0); rd(3); CV_MIX(36, 1, 0);
      LDS_BARRIER();
      mm(3, 0, 12); rp(0); CV_MIX(12, 1, 0);
      mm(3, 12, 36); rd(4); fin(0); CV_MIX(24, 1, 2);
      mm(4, 0, 36); wp(1); rd(5); skip(2); CV_MIX(36, 1, 1);
      LDS_BARRIER();
      mm(5, 0, 12); rp(1); CV_MIX(12, 1, 0);
      mm(5, 12, 36); fin(1); CV_MIX(24, 0, 2);
      wp(2);
      LDS_BARRIER();
      rp(2); SB0;
      fin(2);
    }
    TS(6);
    arrive(rw, 0, (xc * 24 + idx) & 15);
  } else {
    signal_u();
#pragma unroll
    for (int i = 0; i < PF0_N + 2; ++i) keep(pf0[i]);
    warm<10>(sb1, F_ * 2, w1c0, PF0_N * 2048, PF1_END, (idx - 24) * 256 + tid, 8 * 256);
    if (wid == 0) await_u();
    __builtin_amdgcn_s_barrier();
  }
  TS(7);

  const bool g1_on = idx < 27;
  unsigned vb1[4], rb1[4];
#pragma unroll
  for (int j = 0; j < 4; ++j) vb1[j] = (4 * j + g) * (F_ * 2) + ((r16 ^ (g << 2)) << 4) - j * 1024;
#pragma unroll
  for (int m = 0; m < 4; ++m) rb1[m] = ring + 256 * (8 * g + q4) + 64 * (((m + wid) & 3) ^ q4) + 16 * p4;
  const unsigned b1_base = nt1 * 256 + wid * 32 * (F_ * 2);
#define G1_LDB(slot, kb, j) bdma<(slot) * 8192 + ((j) >> 2) * 4096, ((j) & 3) * 1024>(ring, vb1[(j) & 3], sb1, b1_base + (((kb) & ((ABL & 8) ? 1 : 15)) * 128 + ((j) >> 2) * 16) * (F_ * 2))
#define G1_HALF(slot, m, c, h) trd(rb1[m] + (slot) * 8192 + 8 * (c) + 1024 * (h))
  if (g1_on) sfor<32>([&](auto I) { G1_LDB(I / 8, I / 8, I % 8); });

  u32x4 BR[4][8];
  await(rw, 0, xc, 0, 16, 12, wid == 0, lane);
  TS(8);
  {
    const unsigned ao = WS_AT + 2 * tid * (L_ * 2) + tb * 16;
    const u32x4 e0 = __builtin_amdgcn_raw_buffer_load_b128(rw, ao, 0, SC1);
    const u32x4 o0 = __builtin_amdgcn_raw_buffer_load_b128(rw, ao + L_ * 2, 0, SC1);
    u32x4 e1, o1;
    if (wid == 0) {
      e1 = __builtin_amdgcn_raw_buffer_load_b128(rw, lane < 32 ? ao + 512 * (L_ * 2) : OOB, 0, SC1);
      o1 = __builtin_amdgcn_raw_buffer_load_b128(rw, lane < 32 ? ao + 513 * (L_ * 2) : OOB, 0, SC1);
    }
    if (wid == 0) VMCNT(4); else VMCNT(2);
    sfor<32>([&](auto I) { BR[I / 8][I % 8] = cat(G1_HALF(I / 8, (I % 8) >> 1, I & 1, 0), G1_HALF(I / 8, (I % 8) >> 1, I & 1, 1)); if constexpr (I >= 16) pin_a(BR[I / 8][I % 8]); });
    SB0;
    set_m0(ldsT + wid * 256);
    sfor<4>([&](auto K) { put_row<(2 * K) * T_PITCH * 4>(perm_lo(e0[K.value], o0[K.value])); put_row<(2 * K + 1) * T_PITCH * 4>(perm_hi(e0[K.value], o0[K.value])); });
    if (wid == 0 && lane < 32)
      sfor<4>([&](auto K) { put_row<(2 * K) * T_PITCH * 4 + 1024>(perm_lo(e1[K.value], o1[K.value])); put_row<(2 * K + 1) * T_PITCH * 4 + 1024>(perm_hi(e1[K.value], o1[K.value])); });
    LDS_BARRIER();
    TS(9);
    f32x2 v[9];
    unsigned o[9];
#pragma unroll
    for (int i = 0; i < 9; ++i) { o[i] = pkv(unpk(xw[i]) + unpk(T[tok * T_PITCH + 32 * i + ll])); v[i] = unpk(o[i]); }
    sfor<9>([&](auto I) { put_row<B0 + 9216 + 1024 * I>(o[I]); });
    ln9(v, o);
#pragma unroll
    for (int i = 0; i < 9; ++i) v[i] = unpk(o[i]);
    ln9(v, o);
    sfor<9>([&](auto I) { put_row<B0 + 1024 * I>(o[I]); });
    LDS_BARRIER();
    {
      const unsigned off = WS_HF + ((tb >> 2) * 36 + wid * 4 + g) * 1024 + (tb & 3) * 256 + r16 * 16;
      __builtin_amdgcn_raw_buffer_store_b128(chunk(lds0, wid), rw, off, 0, SC1);
      __builtin_amdgcn_raw_buffer_store_b128(chunk(lds0, wid + 4), rw, off + 16384, 0, SC1);
      if (wid == 0) __builtin_amdgcn_raw_buffer_store_b128(chunk(lds0, 8), rw, off + 32768, 0, SC1);
      const unsigned ho = WS_H + (tb * 8 + (lane >> 3)) * (D_ * 2) + (lane & 7) * 16;
      const char* hp = smem + 9216 + lane * 16;
      __builtin_amdgcn_raw_buffer_store_b128(*(const u32x4*)(hp + wid * 1024), rw, ho + wid * 128, 0, SC1);
      __builtin_amdgcn_raw_buffer_store_b128(*(const u32x4*)(hp + (wid + 4) * 1024), rw, ho + (wid + 4) * 128, 0, SC1);
      if (wid == 3) __builtin_amdgcn_raw_buffer_store_b128(*(const u32x4*)(hp + 8192), rw, ho + 1024, 0, SC1);
    }
    TS(10);
    arrive(rw, 1, tb & 15);
    TS(11);
  }

  unsigned vb2[4];
#pragma unroll
  for (int j = 0; j < 4; ++j) vb2[j] = (8 * j + (lane >> 3)) * (L_ * 2) + (((lane & 7) ^ (2 * ((g & 1) | ((j & 1) << 1)))) << 4) - j * 1024;
  const unsigned b2_base = nt2 * 128 + wid * 32 * (L_ * 2);
  const int lead = idx >> 3;
  auto g2_kb = [&](int t) {
    if (G2_LEAD == 0) return t;
    const int tau = t / G2_LEAD, last = 36 / G2_LEAD - 1;
    return t + (tau < last && (tau & 3) == lead ? G2_LEAD : tau > 0 && ((tau - 1) & 3) == lead ? -G2_LEAD : 0);
  };
#define G2_LDB(slot, i, j) bdma<(slot) * 4096, (j) * 1024>(ring, vb2[j], sb2, b2_base + (g2_kb(i) & ((ABL & 8) ? 1 : 63)) * 128 * (L_ * 2))

  if (g1_on) {
    sfor<32>([&](auto I) { G1_LDB(I / 8, 4 + I / 8, I % 8); });
    const unsigned va0 = lane * 16, va1 = lane * 16 + 4096;
    const unsigned a_base = WS_HF + wid * 36864 + mt * 6144;
    u32x4 A[4][6];
    f32x4 acc[4][2][6];
#pragma unroll
    for (int m = 0; m < 4; ++m)
#pragma unroll
      for (int c = 0; c < 2; ++c)
#pragma unroll
        for (int t = 0; t < 6; ++t) { acc[m][c][t] = f32x4{0.f, 0.f, 0.f, 0.f}; pin_af(acc[m][c][t]); }
    await(rw, 1, xc, 0, 16, 16, wid == 0, lane);
    TS(12);
#define G1_LDA(t, f) BLOAD(A[(t) & 3][f], (f) < 4 ? va0 : va1, sw, a_base + (g1_kb(t) & ((ABL & 4) ? 1 : 15)) * (4 * 36864), ((f) & 3) * 1024)
#define G1_WAIT(t, n) wait6<(n)>(A[(t) & 3])
    sfor<18>([&](auto I) { G1_LDA(I / 6, I % 6); });
    u32x2 Bh[2][4];
    G1_WAIT(0, 12);
    TS(13); TC(0);
    sfor<4>([&](auto I) { Bh[0][I] = G1_HALF(0, 0, I >> 1, I & 1); });
    SB0;
    sfor<13>([&](auto TT) {
      constexpr int t = TT;
      constexpr bool R = g1_isR(t);
      constexpr int slot = g1_lidx(t) & 3, nops = g1_ops(t);
      sfor<48>([&](auto NN) {
        constexpr int n = NN, m = n / 12, c = (n / 6) & 1, f = n % 6, i12 = n % 12;
        if constexpr (n == 36) G1_WAIT(t + 1, g1_ops(t - 1) + g1_ops(t));
        if constexpr (!R) MFMA_VV(acc[m][c][f], cat(Bh[m & 1][2 * c], Bh[m & 1][2 * c + 1]), A[t & 3][f]);
        else MFMA_VV(acc[m][c][f], BR[g1_kb(t)][2 * m + c], A[t & 3][f]);
        if constexpr (i12 < 4) {
          if constexpr (m < 3) { if constexpr (!R) { Bh[(m + 1) & 1][i12] = G1_HALF(slot, m + 1, i12 >> 1, i12 & 1); SB0; } }
          else if constexpr (!g1_isR(t + 1)) { Bh[0][i12] = G1_HALF(g1_lidx(t + 1) & 3, t == 12 ? 1 : 0, i12 >> 1, i12 & 1); SB0; }
        } else if constexpr (m < 3) {
          constexpr int pl = m * 8 + i12 - 4, k = (pl * nops + 23) / 24;
          if constexpr (k < nops && (k * 24) / nops == pl && !(ABL & 2)) {
            if constexpr (k < g1_nb(t)) { constexpr int pc = g1_b0(t) + k; G1_LDB((pc >> 3) & 3, 8 + (pc >> 3), pc & 7); }
            else G1_LDA(t + 3, k - g1_nb(t));
          }
        }
      });
    });
    unsigned pf3[PF3_N + 1];
    char* xo = smem + wid * G_WS + lane * 16;
    sfor<4>([&](auto PP) {
      constexpr int ph = PP, m = (ph + 1) & 3;
      sfor<36>([&](auto NN) {
        constexpr int n = NN, t = 13 + n / 12, c = (n / 6) & 1, f = n % 6;
        if constexpr (ph == 0 && n == 12) G1_WAIT(14, 6);
        if constexpr (ph == 0 && n == 24) G1_WAIT(15, 0);
        if constexpr (t == 13) MFMA_VV(acc[m][c][f], cat(Bh[ph & 1][2 * c], Bh[ph & 1][2 * c + 1]), A[t & 3][f]);
        else MFMA_AV(acc[m][c][f], BR[t - 12][2 * m + c], A[t & 3][f]);
        if constexpr (n >= 1 && n < 5 && ph < 3) { Bh[(ph + 1) & 1][n - 1] = G1_HALF(3, (m + 1) & 3, (n - 1) >> 1, (n - 1) & 1); SB0; }
        if constexpr (ph > 0 && n % 3 == 2) { constexpr int k = n / 3; pin_af(acc[ph][k / 6][k % 6]); *(f32x4*)(xo + ((ph - 1) * 12 + k) * 1024) = acc[ph][k / 6][k % 6]; SB0; }
        if constexpr (ph == 0 && n >= 26 && n < 26 + PF3_N)
          touch(pf3[n - 26], slab_off<4>(PF2_ROWS * 4 + ((n - 26) * 27 + idx) * 256 + tid, F_ * 4, L_ * 2, xc * 512), sb2);
      });
    });
    asm volatile("s_nop 7" ::: "memory");
#pragma unroll
    for (int c = 0; c < 2; ++c)
#pragma unroll
      for (int t = 0; t < 6; ++t) pin_af(acc[0][c][t]);
    SB0;
    TS(14); TC(3);
    LDS_BARRIER();
    f32x4 P[3][12];
#pragma unroll
    for (int k = 0; k < 12; ++k)
#pragma unroll
      for (int m = 1; m < 4; ++m) P[m - 1][(k & 1) * 6 + (k >> 1)] = *(const f32x4*)(smem + ((wid - m) & 3) * G_WS + ((m - 1) * 12 + (k & 1) * 6 + (k >> 1)) * 1024 + lane * 16);
    SB0;
    const f32x4 bv[2] = {f32x4{bflo(bw.x), bfhi(bw.x), bflo(bw.y), bfhi(bw.y)}, f32x4{bflo(bw.z), bfhi(bw.z), bflo(bw.w), bfhi(bw.w)}};
#pragma unroll
    for (int c = 0; c < 2; ++c)
#pragma unroll
      for (int t = 0; t < 6; ++t) acc[0][c][t] += bv[c];
    SB0;
    LDS_BARRIER();
    sfor<6>([&](auto TT) {
      constexpr int t = TT;
      sfor<4>([&](auto J) { G2_LDB(t, g2_qstep(t), J.value); });
      u32x4 o;
#pragma unroll
      for (int c = 0; c < 2; ++c) {
        const f32x4 u = (acc[0][c][t] + P[0][c * 6 + t]) + (P[1][c * 6 + t] + P[2][c * 6 + t]);
        const f32x4 z = u * __builtin_elementwise_fma(u * u, (f32x4)(-0.1029432397f), (f32x4)(-2.302208198f));
        const f32x4 den = f32x4{__builtin_amdgcn_exp2f(z.x), __builtin_amdgcn_exp2f(z.y), __builtin_amdgcn_exp2f(z.z), __builtin_amdgcn_exp2f(z.w)} + 1.0f;
        const f32x4 y = u * f32x4{__builtin_amdgcn_rcpf(den.x), __builtin_amdgcn_rcpf(den.y), __builtin_amdgcn_rcpf(den.z), __builtin_amdgcn_rcpf(den.w)};
        o[2 * c] = pkv(y.xy); o[2 * c + 1] = pkv(y.zw);
      }
      __builtin_amdgcn_raw_buffer_store_b128(o, rw, WS_GF + ((nt1 * 4 + wid) * 36 + mt * 6 + t) * 1024 + lane * 16, 0, SC1);
      SB0;
    });
    TS(15);
    sfor<12>([&](auto I) { G2_LDB(6 + I / 4, g2_qstep(6 + I / 4), I % 4); });
    arrive<12>(rw, 2, mt * 2 + (nt1 & 1));
#pragma unroll
    for (int i = 0; i < PF3_N; ++i) keep(pf3[i]);
  } else {
    warm<10>(sb1, F_ * 2, w1c0, PF1_END, L_ * 10, (idx - 27) * 256 + tid, 5 * 256);
    warm<4>(sb2, L_ * 2, xc * 512, 0, PF2_ROWS * 4, (idx - 27) * 256 + tid, 5 * 256);
    TS(15);
    sfor<36>([&](auto I) { G2_LDB(I / 4, g2_qstep(I / 4), I % 4); });
  }

  {
    unsigned rb[4];
#pragma unroll
    for (int c = 0; c < 4; ++c) rb[c] = ring + 128 * (8 * g + q4) + 16 * ((2 * ((c + wid) & 3) + (p4 >> 1)) ^ (2 * ((q4 >> 1) | ((g & 1) << 1)))) + 8 * (p4 & 1);
#define G2_HALF(slot, c, h) trd(rb[c] + (slot) * 4096 + 512 * (h))
    u32x4 W[18][4];
    f32x4 acc[4][6];
    const int m0 = (72 * db) / 96, nsh = 2 * ((72 * db + 71) / 96 - m0 + 1);
    unsigned early = 0;
    sfor<6>([&](auto K) {
      if constexpr (K == 4) { if (wid == 0) VMCNT(25); else VMCNT(24); } else VMCNT(24);
      sfor<12>([&](auto I) { constexpr int q = 3 * K + I / 4; W[q][I % 4] = cat(G2_HALF(q % 9, I % 4, 0), G2_HALF(q % 9, I % 4, 1)); });
      asm volatile("s_waitcnt lgkmcnt(0)" ::: "memory");
      SB0;
      if constexpr (K == 3) { if (wid == 0) poll_issue(early, cnt_off(2, xc, 2 * m0, nsh, lane), sw); }
      sfor<12>([&](auto I) {
        constexpr int q = 3 * K + 9 + I / 4;
        G2_LDB(q % 9, g2_qstep(q), I % 4);
        if constexpr (3 * K + I / 4 >= 8) pin_a(W[3 * K + I / 4][I % 4]);
        if constexpr (K < 2) { acc[2 * K + I / 6][I % 6] = f32x4{0.f, 0.f, 0.f, 0.f}; pin_af(acc[2 * K + I / 6][I % 6]); }
      });
    });
    asm volatile("" : "+v"(early));
    await(rw, 2, xc, 2 * m0, nsh, 18, wid == 0 && !__all(early == (lane < nsh ? 18u : 0u)), lane);
    TS(16);
    const int rt0 = 9 * (db >> 1) + 5 * odd, rth = 9 * (db >> 1) + 4;
    const unsigned va = rt0 * 1024 + lane * 16;
    const unsigned vh = rth * 1024 + ((lane & 48) + (lane & 7) + 8 * odd) * 16 + ((lane >> 3) & 1) * (4 * 36864);
    const unsigned a_base = WS_GF + wid * 36864;
    u32x4 A[36][4], Ah[18];
    auto lda = [&](auto J) {
      constexpr int p = J / 9, r = J % 9, t = 2 * p + (r >= 5), f = (r + 3) & 3;
      const unsigned so = a_base + (g2_kb(t) & ((ABL & 4) ? 1 : 63)) * (4 * 36864);
      if constexpr (r == 0) BLOAD(Ah[p], vh, sw, so, 0); else BLOAD(A[t][f], va, sw, so, f * 1024);
    };
    sfor<g2_aend(2)>(lda);
    const int s = nt2 * 64 + wid * 16 + r16;
    const int dfull = rt0 * 16 + 4 * g, dhalf = rth * 16 + 8 * odd + 4 * (g & 1);
    u32x2 hw[5];
    u32x2 Bh[8];
    wait5<g2_aend(2) - g2_aend(0)>(A[0], Ah[0]);
    TS(17); TC(4);
    sfor<36>([&](auto TT) {
      constexpr int t = TT;
      constexpr bool R = g2_isR(t);
      sfor<20>([&](auto NN) {
        constexpr int n = NN, c = n / 5, f = n % 5, op = G2S.op[t][n];
        if constexpr (n == G2_WP && t < 35) wait5<G2S.wait[t]>(A[t + 1], Ah[(t + 1) >> 1]);
        f32x4& ac = acc[c][f < 4 ? f : 4 + (t & 1)];
        const u32x4& av = f < 4 ? A[t][f & 3] : Ah[t >> 1];
        if constexpr (!R) MFMA_VV(ac, av, cat(Bh[2 * c], Bh[2 * c + 1]));
        else if constexpr (g2_ridx(t) >= 8) MFMA_VA(ac, av, W[g2_ridx(t)][c]);
        else MFMA_VV(ac, av, W[g2_ridx(t)][c]);
        if constexpr (!R && n < 4) { Bh[4 + n] = G2_HALF(g2_slot(t), 2 + (n >> 1), n & 1); SB0; }
        if constexpr (n >= G2_WP && n < G2_WP + 4 && t < 35 && !g2_isR(t + 1)) { Bh[n - G2_WP] = G2_HALF(g2_slot(t + 1), (n - G2_WP) >> 1, (n - G2_WP) & 1); SB0; }
        if constexpr (op >= 3000) bload2(hw[op - 3000], WS_H + (s * D_ + (op < 3004 ? dfull + 16 * (op - 3000) : dhalf)) * 2, sw);
        else if constexpr (op >= 2000) G2_LDB((op - 2000) >> 2, 27 + ((op - 2000) >> 2), op & 3);
        else if constexpr (op >= 1000) lda(std::integral_constant<int, op - 1000>{});
      });
    });
    asm volatile("s_nop 7\n\ts_waitcnt vmcnt(0)" : "+v"(hw[0]), "+v"(hw[1]), "+v"(hw[2]), "+v"(hw[3]), "+v"(hw[4]) :: "memory");
#pragma unroll
    for (int c = 0; c < 4; ++c)
#pragma unroll
      for (int t = 0; t < 6; ++t) asm volatile("" : "+a"(acc[c][t]));
    SB0;
    TS(18); TC(7);
    const float bias = bflo(bias2);
    {
      char* o = smem + wid * G_WS + lane * 16;
#pragma unroll
      for (int c = 0; c < 4; ++c) {
        if (c > 0) {
#pragma unroll
          for (int t = 0; t < 4; ++t) *(f32x4*)(o + (c * 5 + t) * 1024) = acc[c][t];
        }
        *(f32x4*)(o + (c * 5 + 4) * 1024) = g < 2 ? acc[c][4] : acc[c][5];
      }
    }
    LDS_BARRIER();
    f32x4 Q[3][4], Hh[8];
#pragma unroll
    for (int t = 0; t < 4; ++t)
#pragma unroll
      for (int c = 1; c < 4; ++c) Q[c - 1][t] = *(const f32x4*)(smem + ((wid - c) & 3) * G_WS + (c * 5 + t) * 1024 + lane * 16);
#pragma unroll
    for (int c = 0; c < 4; ++c) {
      const char* sp = smem + ((wid - c) & 3) * G_WS + (c * 5 + 4) * 1024 + (lane & 31) * 16;
      Hh[2 * c] = *(const f32x4*)(sp); Hh[2 * c + 1] = *(const f32x4*)(sp + 512);
    }
    SB0;
#pragma unroll
    for (int t = 0; t < 4; ++t) acc[0][t] = (acc[0][t] + Q[0][t]) + (Q[1][t] + Q[2][t]);
    const f32x4 yh = ((Hh[0] + Hh[1]) + (Hh[2] + Hh[3])) + ((Hh[4] + Hh[5]) + (Hh[6] + Hh[7]));
#pragma unroll
    for (int t = 0; t < 5; ++t) {
      const f32x4 y = t < 4 ? acc[0][t & 3] : yh;
      u32x2 o;
      o.x = pkv(unpk(hw[t].x) + unpk(pkv(unpk(pkv(y.xy)) + bias)));
      o.y = pkv(unpk(hw[t].y) + unpk(pkv(unpk(pkv(y.zw)) + bias)));
      if (t < 4) *(u32x2*)(out + s * D_ + dfull + 16 * t) = o;
      else if (g < 2) *(u32x2*)(out + s * D_ + dhalf) = o;
    }
    TS(19);
  }
  TSOUT();
  asm volatile(".Lk_all_end:");
}

extern "C" void kernel_launch(void* const* d_in, const int* in_sizes, int n_in,
                              void* d_out, int out_size, void* d_ws, size_t ws_size,
                              hipStream_t stream) {
  k_all<<<256, 256, 0, stream>>>((const u16*)d_in[0], (const u16*)d_in[1], (const u16*)d_in[2], (const u16*)d_in[3], (const u16*)d_in[4],
                                 (const u16*)d_in[5], (const u16*)d_in[6], (const u16*)d_in[8], (const u16*)d_in[9], (const u16*)d_in[10],
                                 (const u16*)d_in[11], (char*)d_ws, (u16*)d_out);
}
```

```cpp
#include <hip/hip_runtime.h>
#include <stdint.h>
#include <utility>

typedef unsigned short u16;
typedef unsigned long long u64;
typedef __attribute__((ext_vector_type(4))) short s16x4;
typedef __attribute__((ext_vector_type(2))) unsigned short u16x2;
typedef __attribute__((ext_vector_type(8))) __bf16 bf16x8;
typedef __attribute__((ext_vector_type(2))) __bf16 bf16x2;
typedef __attribute__((ext_vector_type(4))) float f32x4;
typedef __attribute__((ext_vector_type(2))) float f32x2;
typedef __attribute__((ext_vector_type(4))) unsigned u32x4;
typedef __attribute__((ext_vector_type(2))) unsigned u32x2;
typedef __attribute__((ext_vector_type(4))) int i32x4;
typedef __amdgpu_buffer_rsrc_t rsrc_t;
#define LAS __attribute__((address_space(3)))
#define DI __device__ __forceinline__

#define TSDECL
#define TS(i)
#define TC(i)
#define TSOUT()

#define PF0_N 0
#define G2_LEAD 4
#define PF1_END 14336
#define PF1_BEG 0
#define RW_POL ""
#define PF_WIN 3
#define PFS 0
#define PFN 4
#define PF2_FLAT 0
#define PFG 0
#define PFG_T0 1
#define PFG_DT 3
#define PF1_END2 PF1_END
#define PF1_NT 0
#define PF2_ROWS 3200
#define PF2_NT PF2_ROWS
#define PFR_NT 0
#define PF3_N 1
#define ABL 0
#define SPARE_SLEEP 16
#define GATHER_AT 24
#define G1_PRE 32
#define NW1 12
#define NW2 8
#define CVA 0
#define POLL_SLEEP 4

constexpr int L_ = 2048, D_ = 576, N_ = 128, F_ = 4608;
constexpr unsigned SZ = L_ * D_ * 2;
constexpr unsigned WS_UB = 0, WS_AT = SZ, WS_H = 2 * SZ, WS_HF = 3 * SZ, WS_GF = 4 * SZ, WS_SY = 7 * SZ, WS_BYTES = 7 * SZ + 131072;
constexpr unsigned SY_EPOCH = WS_SY, SY_FLAG = WS_SY + 4096, SY_CNT = WS_SY + 32768;
constexpr unsigned OOB = 0x7ff00000u;
constexpr int SC1 = 16;
constexpr unsigned ROWB = L_ * 2;
constexpr int NMARK = 6;

template <class Fn, int... I> DI void sfor_(Fn&& f, std::integer_sequence<int, I...>) { (f(std::integral_constant<int, I>{}), ...); }
template <int N, class Fn> DI void sfor(Fn&& f) { sfor_(f, std::make_integer_sequence<int, N>{}); }

DI float bflo(unsigned w) { return __uint_as_float(w << 16); }
DI float bfhi(unsigned w) { return __uint_as_float(w & 0xffff0000u); }
DI f32x2 unpk(unsigned w) { return f32x2{bflo(w), bfhi(w)}; }
DI float bf2f(u16 v) { return __uint_as_float(((unsigned)v) << 16); }
DI unsigned pkv(f32x2 v) { return __builtin_bit_cast(unsigned, __builtin_convertvector(v, bf16x2)); }
DI unsigned pk2(float lo, float hi) { return pkv(f32x2{lo, hi}); }
DI float rbf(float f) { return bflo(pk2(f, f)); }
DI unsigned perm_lo(unsigned a, unsigned b) { return __builtin_amdgcn_perm(b, a, 0x05040100u); }
DI unsigned perm_hi(unsigned a, unsigned b) { return __builtin_amdgcn_perm(b, a, 0x07060302u); }
DI f32x4 mfma16(u32x4 a, u32x4 b, f32x4 c) { return __builtin_amdgcn_mfma_f32_16x16x32_bf16(__builtin_bit_cast(bf16x8, a), __builtin_bit_cast(bf16x8, b), c, 0, 0, 0); }
DI rsrc_t mk_rsrc(const void* p, unsigned bytes) { return __builtin_amdgcn_make_buffer_rsrc((void*)p, 0, bytes, 0x00020000); }
DI i32x4 mk_srd(const void* p, unsigned bytes) {
  const u64 a = (u64)p;
  i32x4 r; r.x = (int)(unsigned)a; r.y = (int)((unsigned)(a >> 32) & 0xffffu); r.z = (int)bytes; r.w = 0x00020000; return r;
}
#define LDS_BARRIER() do { asm volatile("s_waitcnt lgkmcnt(0)" ::: "memory"); __builtin_amdgcn_s_barrier(); asm volatile("" ::: "memory"); } while (0)
template <int IMM> DI void bload_(u32x4& dst, unsigned voff, i32x4 srd, unsigned soff) {
  asm volatile("buffer_load_dwordx4 %0, %1, %2, %3 offen offset:%4 sc1" : "=v"(dst) : "v"(voff), "s"(srd), "s"(soff), "i"(IMM) : "memory");
}
template <int IMM> DI void bload_r(u32x4& dst, unsigned voff, i32x4 srd, unsigned soff) {
  asm volatile("buffer_load_dwordx4 %0, %1, %2, %3 offen offset:%4 sc1" : "+v"(dst) : "v"(voff), "s"(srd), "s"(soff), "i"(IMM) : "memory");
}
template <int M0OFF, int IMM> DI void bdma(unsigned ldsbase, unsigned voff, i32x4 srd, unsigned soff) {
  if constexpr (IMM == 0) asm volatile("s_add_u32 m0, %0, %1\n\ts_nop 0\n\tbuffer_load_dwordx4 %2, %3, %4 offen lds" :: "s"(ldsbase), "i"(M0OFF), "v"(voff), "s"(srd), "s"(soff) : "memory", "scc");
  else asm volatile("buffer_load_dwordx4 %0, %1, %2 offen offset:%3 lds" :: "v"(voff), "s"(srd), "s"(soff), "i"(IMM) : "memory");
}
template <int OFF> DI void put_row(unsigned v) { asm volatile("ds_write_addtid_b32 %0 offset:%1" :: "v"(v), "i"(OFF) : "memory"); }
DI void keep(unsigned v) { asm volatile("" :: "v"(v)); }
DI void bload2(u32x2& dst, unsigned voff, i32x4 srd) { asm volatile("buffer_load_dwordx2 %0, %1, %2, 0 offen sc1" : "=v"(dst) : "v"(voff), "s"(srd) : "memory"); }
DI void bload2r(u32x2& dst, unsigned voff, i32x4 srd) { asm volatile("buffer_load_dwordx2 %0, %1, %2, 0 offen sc1" : "+v"(dst) : "v"(voff), "s"(srd) : "memory"); }
DI void bstore(const u32x4& v, unsigned voff, i32x4 srd) { asm volatile("buffer_store_dwordx4 %0, %1, %2, 0 offen sc1\n\ts_nop 1" :: "v"(v), "v"(voff), "s"(srd) : "memory"); }
DI unsigned umax3(unsigned a, unsigned b, unsigned c) { return max(max(a, b), c); }
DI unsigned umax4(const u32x4& a, unsigned m) { return umax3(umax3(a.x, a.y, a.z), a.w, m); }
template <int IMM> DI void bload_a(u32x4& dst, unsigned voff, i32x4 srd, unsigned soff) {
  asm volatile("buffer_load_dwordx4 %0, %1, %2, %3 offen offset:%4" RW_POL : "=a"(dst) : "v"(voff), "s"(srd), "s"(soff), "i"(IMM));
}
template <int OFF> DI void lds_put_a(unsigned addr, const u32x4& v) { asm volatile("ds_write_b128 %0, %1 offset:%2" :: "v"(addr), "a"(v), "i"(OFF) : "memory"); }
DI void pin_af(f32x4& v) { asm volatile("" : "+a"(v)); }
#define BLOAD(dst, voff, srd, soff, imm) bload_<(imm)>(dst, voff, srd, soff)
#define SB0 __builtin_amdgcn_sched_barrier(0)
#define MFMA_STR "v_mfma_f32_16x16x32_bf16 %0, %1, %2, %0"
DI void MFMA_VV(f32x4& acc, const u32x4& a, const u32x4& b) { asm volatile(MFMA_STR : "+a"(acc) : "v"(a), "v"(b)); }
DI void MFMA_V(f32x4& acc, const u32x4& a, const u32x4& b) { asm volatile("v_mfma_f32_16x16x32_bf16 %0, %1, %2, %0" : "+v"(acc) : "v"(a), "v"(b)); }
DI void MFMA_V0(f32x4& acc, const u32x4& a, const u32x4& b) { asm volatile("v_mfma_f32_16x16x32_bf16 %0, %1, %2, 0" : "=&v"(acc) : "v"(a), "v"(b)); }
DI void pin_vf(f32x4& v) { asm volatile("" : "+v"(v)); }
DI void fadd(float& a, float b) { asm volatile("v_add_f32 %0, %0, %1" : "+v"(a) : "v"(b)); }
DI void MFMA_VV0(f32x4& acc, const u32x4& a, const u32x4& b) { asm volatile("v_mfma_f32_16x16x32_bf16 %0, %1, %2, 0" : "=a"(acc) : "v"(a), "v"(b)); }
DI void MFMA_AV(f32x4& acc, const u32x4& a, const u32x4& b) { asm volatile(MFMA_STR : "+a"(acc) : "a"(a), "v"(b)); }
DI void MFMA_VA(f32x4& acc, const u32x4& a, const u32x4& b) { asm volatile(MFMA_STR : "+a"(acc) : "v"(a), "a"(b)); }
template <int IMM> DI void gld32(unsigned& d, unsigned voff, const void* base) { asm volatile("global_load_dword %0, %1, %2 offset:%3" : "=v"(d) : "v"(voff), "s"(base), "i"(IMM) : "memory"); }
template <int IMM> DI void gld16(unsigned& d, unsigned voff, const void* base) { asm volatile("global_load_ushort %0, %1, %2 offset:%3" : "=v"(d) : "v"(voff), "s"(base), "i"(IMM) : "memory"); }
DI void gld128(u32x4& d, unsigned voff, const void* base) { asm volatile("global_load_dwordx4 %0, %1, %2" : "=v"(d) : "v"(voff), "s"(base) : "memory"); }
DI void set_m0(unsigned v) { asm volatile("s_mov_b32 m0, %0\n\ts_nop 0" :: "s"(v) : "memory"); }
template <int N> DI void wait5(u32x4* a, u32x4& h) { asm volatile("s_waitcnt vmcnt(%5)" : "+v"(a[0]), "+v"(a[1]), "+v"(a[2]), "+v"(a[3]), "+v"(h) : "i"(N) : "memory"); }
template <int N> DI void wait9(unsigned* a) { asm volatile("s_waitcnt vmcnt(%9)" : "+v"(a[0]), "+v"(a[1]), "+v"(a[2]), "+v"(a[3]), "+v"(a[4]), "+v"(a[5]), "+v"(a[6]), "+v"(a[7]), "+v"(a[8]) : "i"(N) : "memory"); }
DI void pin9(unsigned* a) { asm volatile("" : "+v"(a[0]), "+v"(a[1]), "+v"(a[2]), "+v"(a[3]), "+v"(a[4]), "+v"(a[5]), "+v"(a[6]), "+v"(a[7]), "+v"(a[8])); }
DI void pin_a(u32x4& v) { asm volatile("" : "+a"(v)); }
template <int N> DI void wait6(u32x4* a) { asm volatile("s_waitcnt vmcnt(%6)" : "+v"(a[0]), "+v"(a[1]), "+v"(a[2]), "+v"(a[3]), "+v"(a[4]), "+v"(a[5]) : "i"(N) : "memory"); }
#define VMCNT(n) asm volatile("s_waitcnt vmcnt(%0)" :: "i"(n) : "memory")
DI u32x2 trd(unsigned addr) { return __builtin_bit_cast(u32x2, __builtin_amdgcn_ds_read_tr16_b64_v4i16((LAS s16x4*)(size_t)addr)); }
DI u32x4 cat(u32x2 a, u32x2 b) { return u32x4{a.x, a.y, b.x, b.y}; }

template <int CTRL> DI float dpp_add(float v) { return v + __builtin_bit_cast(float, __builtin_amdgcn_update_dpp(0, __builtin_bit_cast(int, v), CTRL, 0xf, 0xf, true)); }
DI float half_sum(float v) {
  v = dpp_add<0xB1>(v); v = dpp_add<0x4E>(v); v = dpp_add<0x141>(v); v = dpp_add<0x140>(v);
  float w = v;
  asm volatile("s_nop 1\n\tv_permlane16_swap_b32 %0, %1" : "+v"(v), "+v"(w));
  return v + w;
}
template <int K> using IC = std::integral_constant<int, K>;
DI void ln9(const f32x2* v, unsigned* o) {
  const f32x2 s = ((v[0] + v[1]) + (v[2] + v[3])) + ((v[4] + v[5]) + (v[6] + v[7])) + v[8];
  const float mean = half_sum(s.x + s.y) * (1.0f / D_);
  f32x2 q0 = {0.f, 0.f}, q1 = {0.f, 0.f};
#pragma unroll
  for (int i = 0; i < 9; ++i) { const f32x2 t = v[i] - mean; if (i & 1) q1 = __builtin_elementwise_fma(t, t, q1); else q0 = __builtin_elementwise_fma(t, t, q0); }
  q0 += q1;
  const float var = half_sum(q0.x + q0.y) * (1.0f / D_);
  const float mb = rbf(mean);
  const float sd = rbf(__builtin_amdgcn_sqrtf(rbf(rbf(var) + 1e-5f)));
  const float inv = __builtin_amdgcn_rcpf(sd);
#pragma unroll
  for (int i = 0; i < 9; ++i) o[i] = pkv(unpk(pkv(v[i] - mb)) * inv);
}
DI f32x2 cmul(f32x2 a, f32x2 b) { const f32x2 bs = {-b.y, b.x}; return __builtin_elementwise_fma(a.yy, bs, a.xx * b); }
DI f32x2 cexp_p(float zl, float zv, float p) {
  const float m = __builtin_amdgcn_exp2f(zl * p);
  const float rev = __builtin_amdgcn_fractf(zv * p);
  return f32x2{__builtin_amdgcn_cosf(rev), __builtin_amdgcn_sinf(rev)} * m;
}
template <int LINES> DI unsigned slab_off(unsigned e, unsigned e1, unsigned pitch, unsigned c0) {
  const unsigned r = e / LINES;
  return e < e1 ? r * pitch + c0 + (e - r * LINES) * 128 : OOB;
}
template <int NT = 0> DI void touch(unsigned& sink, unsigned off, i32x4 srd) {
  if constexpr (NT) asm volatile("buffer_load_dword %0, %1, %2, 0 offen nt" : "=v"(sink) : "v"(off), "s"(srd) : "memory");
  else asm volatile("buffer_load_dword %0, %1, %2, 0 offen" : "=v"(sink) : "v"(off), "s"(srd) : "memory");
}
template <int LINES, int WIN = PF_WIN, int NT = 0> DI void warm(unsigned& v, i32x4 srd, unsigned pitch, unsigned c0, unsigned e0, unsigned e1, unsigned idx, unsigned nthr) {
#pragma nounroll
  for (unsigned e = e0 + idx; e < e1 + idx; e += nthr) {
    const unsigned off = slab_off<LINES>(e, e1, pitch, c0);
    if constexpr (NT) asm volatile("s_waitcnt vmcnt(%3)\n\tbuffer_load_dword %0, %1, %2, 0 offen nt" : "+v"(v) : "v"(off), "s"(srd), "i"(WIN) : "memory");
    else asm volatile("s_waitcnt vmcnt(%3)\n\tbuffer_load_dword %0, %1, %2, 0 offen" : "+v"(v) : "v"(off), "s"(srd), "i"(WIN) : "memory");
  }
}

template <int YOUNGER = 0> DI void arrive(rsrc_t rw, int ho, int shard) {
  VMCNT(YOUNGER);
  __builtin_amdgcn_s_barrier();
  if (threadIdx.x < 8) __builtin_amdgcn_raw_ptr_buffer_atomic_add_i32(1, rw, SY_CNT + ((ho * 8 + threadIdx.x) * 16 + shard) * 256, 0, SC1);
}
DI unsigned cnt_off(int ho, int rep, int s0, int ns, int lane) { return lane < ns ? SY_CNT + ((ho * 8 + rep) * 16 + s0 + lane) * 256 : OOB; }
DI void poll_issue(unsigned& f, unsigned off, i32x4 srd) { asm volatile("buffer_load_dword %0, %1, %2, 0 offen sc1" : "+v"(f) : "v"(off), "s"(srd) : "memory"); }
DI void poll_issue_plain(unsigned& f, unsigned off, i32x4 srd) { asm volatile("buffer_load_dword %0, %1, %2, 0 offen" : "+v"(f) : "v"(off), "s"(srd) : "memory"); }
DI void add_one(unsigned one, unsigned off, i32x4 srd) { asm volatile("buffer_atomic_add %0, %1, %2, 0 offen sc1" :: "v"(one), "v"(off), "s"(srd) : "memory"); }
DI void spin_cnt(rsrc_t rw, unsigned off, unsigned w) {
#pragma nounroll
  for (int spin = 0; spin < (1 << 16); ++spin) {
    const unsigned f = __builtin_amdgcn_raw_buffer_load_b32(rw, off, 0, SC1);
    if (__all(f == w)) break;
    __builtin_amdgcn_s_sleep(POLL_SLEEP);
    asm volatile("" ::: "memory");
  }
}
DI void await(rsrc_t rw, int ho, int rep, int s0, int ns, unsigned want, bool poller, int lane) {
  if (poller) spin_cnt(rw, cnt_off(ho, rep, s0, ns, lane), lane < ns ? want : 0u);
  asm volatile("" ::: "memory");
  __builtin_amdgcn_s_barrier();
  asm volatile("" ::: "memory");
}

constexpr int KG_PITCH = 528;
constexpr int KG_B = 41 * KG_PITCH, KG_CH = 57 * KG_PITCH;
constexpr int KG_T = 3 * KG_CH, KG_TP = 2072 * 2;
constexpr int CV_KP = 259 * 16 + 16;
constexpr int CV_U = 4 * CV_KP;
constexpr int CV_UP = (29 + 256) * 16 + 64 + 16;
constexpr int CV_TB = CV_U + 2 * CV_UP;
constexpr int CV_P = 3 * CV_TB;
constexpr int G_WS = 36864;
constexpr int B0 = 16384;
constexpr int t2_off(int i) { return i < 7 ? 9216 + 1024 * i : B0 + 32768 + 1024 * (i - 7); }
constexpr int K_LDS = B0 + 4 * G_WS;
static_assert(CV_KP % 256 == 64 && CV_UP % 64 == 32 && CV_TB % 16 == 0 && KG_CH % 16 == 0 && CV_P + 65536 <= 4 * G_WS && KG_T >= CV_P && KG_T + 3 * KG_TP <= 4 * G_WS &&
              K_LDS <= 163840, "");

constexpr int kg_e(int r) { return r < 16 ? r & 7 : r < 24 ? 2 * (r & 3) : 4 * (r & 1); }
constexpr int kg_g(int r) { return r < 16 ? r >> 3 : r < 24 ? 2 + ((r - 16) >> 2) : 4 + ((r - 24) >> 1); }

constexpr int cv_ab(int j) {
  int n = 0;
  for (int m = 0; m < 8; ++m)
    for (int o = 0; o <= m && m + o < 8; ++o) { if (n++ == j) return 8 * m + o; if (o < m && n++ == j) return 8 * o + m; }
  return 0;
}
constexpr int cv_a(int j) { return cv_ab(j) >> 3; }
constexpr int cv_b(int j) { return cv_ab(j) & 7; }
constexpr bool cv_first(int j) { for (int i = 0; i < j; ++i) if (cv_a(i) + cv_b(i) == cv_a(j) + cv_b(j)) return false; return true; }
constexpr int cv_done(int j) { return j == 8 ? 0 : j == 10 ? 1 : j == 12 ? 2 : j == 15 ? 3 : j == 33 ? 4 : j == 34 ? 5 : -1; }

constexpr bool g2_isR(int t) { return t < 27 && t % 3 != 2; }
constexpr int g2_ridx(int t) { return t - t / 3; }
constexpr int g2_slot(int t) { return t < 27 ? t / 3 : t - 27; }
constexpr int g2_qstep(int q) { return q < 18 ? q + q / 2 : 3 * (q - 18) + 2; }
#define G2_DMAX 5
constexpr int G2_WP = 14;
constexpr int g2_aend(int t) { return t > 35 ? 162 : 9 * (t / 2) + ((t & 1) ? 9 : 5); }
struct G2Sched { short op[36][20]; short wait[36]; };
constexpr G2Sched g2_make() {
  G2Sched s{};
  int apos[162] = {}, bpos[36] = {}, seq = g2_aend(2), na = g2_aend(2);
  for (int i = 0; i < na; ++i) apos[i] = i;
  for (int t = 0; t < 36; ++t) {
    int list[20] = {}, n = 0;
    if (t >= 3 && t < 30) { const int p = 4 * ((t - 3) / 3) + (t % 3 == 0 ? 0 : t % 3 + 1); list[n++] = 2000 + p; if (t % 3 == 0) list[n++] = 2000 + p + 1; }
    const int d = 3 + (t + 1) / 2 < G2_DMAX ? 3 + (t + 1) / 2 : G2_DMAX;
    while (na < g2_aend(t + d) && n < 9) list[n++] = 1000 + na++;
    if (t == 33) for (int i = 0; i < 5; ++i) list[n++] = 3000 + i;
    if (t == 23) list[n++] = 4000;
    for (int m = 0, k = 0; m < 20; ++m) {
      if (m == G2_WP && t < 35) {
        int need = apos[g2_aend(t + 1) - 1];
        if (t + 1 >= 27 && bpos[4 * (t + 1 - 27) + 3] > need) need = bpos[4 * (t + 1 - 27) + 3];
        s.wait[t] = (short)(seq - 1 - need);
      }
      if (k < n && m == (k * 20) / n) {
        const int o = list[k++];
        s.op[t][m] = (short)o;
        if (o >= 2000 && o < 3000) bpos[o - 2000] = seq; else if (o < 2000) apos[o - 1000] = seq;
        ++seq;
      }
    }
  }
  return s;
}
constexpr G2Sched G2S = g2_make();

constexpr bool g1_isR(int t) { return t == 2 || t == 5; }
constexpr int g1_lidx(int t) { return t < 2 ? t : t < 5 ? t - 1 : t - 2; }
constexpr int g1_kb(int t) { return t == 2 ? 0 : t == 5 ? 1 : t >= 14 ? t - 12 : 4 + g1_lidx(t); }
constexpr int g1_nb(int t) { return (t < 1 || t > 10) ? 0 : t >= 7 ? 8 : ((t - 1) % 3 == 2 ? 4 : 6); }
constexpr int g1_b0(int t) { return t >= 7 ? 32 + 8 * (t - 7) : 16 * ((t - 1) / 3) + 6 * ((t - 1) % 3); }
constexpr int g1_pf(int t) { return PFG && t >= PFG_T0 && (t - PFG_T0) % PFG_DT == 0 && (t - PFG_T0) / PFG_DT < 4; }
constexpr int g1_ops(int t) { return t < 0 ? 6 : g1_nb(t) + 6; }
constexpr int g1_all(int t) { return g1_ops(t) + g1_pf(t); }

__global__ __launch_bounds__(256, 1) void k_all(const u16* __restrict__ x, const u16* __restrict__ logA, const u16* __restrict__ Aim,
                                                const u16* __restrict__ Cre, const u16* __restrict__ Cim, const u16* __restrict__ logdt,
                                                const u16* __restrict__ Dsk, const u16* __restrict__ w1, const u16* __restrict__ b1,
                                                const u16* __restrict__ w2, const u16* __restrict__ b2, char* __restrict__ ws, u16* __restrict__ out) {
  __shared__ __attribute__((aligned(16))) char smem_[K_LDS];
  char* const smem = smem_ + B0;
  const int tid = threadIdx.x, lane = tid & 63, wid = __builtin_amdgcn_readfirstlane(tid >> 6);
  const int xc = blockIdx.x & 7, idx = blockIdx.x >> 3;
  TSDECL;
  TS(0);
  const rsrc_t rw = mk_rsrc(ws, WS_BYTES);
  const i32x4 sw = mk_srd(ws, WS_BYTES), sb1 = mk_srd(w1, L_ * F_ * 2), sb2 = mk_srd(w2, L_ * F_ * 2);
  const unsigned ldsT = (unsigned)(size_t)smem_, lds0 = ldsT + B0, ring = lds0 + wid * G_WS;
  const int g = lane >> 4, r16 = lane & 15, q4 = r16 >> 2, p4 = lane & 3;
  const int tj = xc * 3 + (idx >> 3), tg0 = tj + 4 * min(tj >> 2, 1), tg1 = xc + 4 + 20 * (xc >> 2);
  const int tgr = tg0 + (tg1 - tg0) * ((idx + 8) >> 5);
  const int tb = tgr * 8 + (idx & 7);
  const int tok = wid * 2 + (lane >> 5), ll = lane & 31;
  const bool cv_on = idx < 24;
  const int d0 = xc * 72 + idx * 3;
  const unsigned w1c0 = ((27 * xc) / 6) * 256;

  const int lin1 = xc * 27 + idx, mt = lin1 % 6, nt1 = min(lin1 / 6, 35);
  const int db = idx & 7, nt2 = xc * 4 + (idx >> 3), odd = db & 1;
  unsigned xw[9], pp[18], bias2, pf0[PF0_N + 2];
  u32x2 ep;
  u32x4 bw;
  sfor<9>([&](auto I) { gld32<128 * I>(xw[I], ((tb * 8 + tok) * (D_ / 2) + ll) * 4, x); });
  bload2(ep, SY_EPOCH, sw);
  gld128(bw, (nt1 * 128 + wid * 32 + 8 * g) * 2, b1);
  gld16<0>(bias2, (nt2 * 64 + wid * 16 + r16) * 2, b2);
  if (cv_on) {
    const unsigned o = (d0 * N_ + (wid & 1) * 64 + lane) * 2;
    sfor<3>([&](auto C) {
      gld16<256 * C>(pp[6 * C], o, logA); gld16<256 * C>(pp[6 * C + 1], o, Aim); gld16<256 * C>(pp[6 * C + 2], o, Cre); gld16<256 * C>(pp[6 * C + 3], o, Cim);
      gld16<2 * C>(pp[6 * C + 4], d0 * 2, logdt); gld16<2 * C>(pp[6 * C + 5], d0 * 2, Dsk);
    });
  } else {
    u64 pc; unsigned len;
    asm volatile("s_getpc_b64 %0\n.Lk_all_pc:\n\ts_mov_b32 %1, .Lk_all_end-.Lk_all_pc" : "=s"(pc), "=s"(len));
    const i32x4 sc = mk_srd((const void*)pc, idx == 31 ? len : 0u);
    touch(pf0[PF0_N], tid * 128, sc); touch(pf0[PF0_N + 1], (256 + tid) * 128, sc);
#pragma unroll
    for (int i = 0; i < PF0_N; ++i) touch(pf0[i], slab_off<10>(PF1_BEG + (i * 8 + idx - 24) * 256 + tid, L_ * 10, F_ * 2, w1c0), sb1);
  }
  unsigned kad[11], ugo[3], ulo[3], ufo[3], kto[4], zoff;
  {
#pragma unroll
    for (int k = 0; k < 6; ++k) {
      const int j0 = 2047 - (8 * tid + 2 * k);
      const unsigned pr = pk2((float)j0, (float)(j0 - 1));
      kad[2 * k] = lds0 + KG_T + 8 + 2 * (int)bflo(pr);
      if (k < 5) kad[2 * k + 1] = lds0 + KG_T + 8 + 2 * (int)bfhi(pr);
    }
#pragma unroll
    for (int k = 0; k < 3; ++k) {
      const unsigned i = tid + 256 * k, t3 = i / 3, c = i - 3 * t3;
      ugo[k] = WS_UB + t3 * (D_ * 16) + (d0 + c) * 16; ulo[k] = c * CV_TB + CV_U + (29 + t3) * 16; ufo[k] = SY_FLAG + xc * 2048 + t3 * 8;
    }
    int pb, o1, o2;
    if (wid == 0) { pb = 4 * (g & 1) + 128 * (g >> 1); o1 = 1; o2 = 2; }
    else if (wid == 1) { pb = g < 2 ? 256 + 128 * g : 256 * g; o1 = g < 2 ? 2 : 4; o2 = g < 2 ? 4 : 128; }
    else { pb = 1024 + 512 * g; o1 = 128; o2 = 256; }
    pb += 8 * r16;
    kto[0] = KG_T + 8 + 2 * min(pb, 2056); kto[1] = KG_T + 8 + 2 * min(pb + o1, 2056); kto[2] = KG_T + 8 + 2 * min(pb + o2, 2056); kto[3] = KG_T + 8 + 2 * min(pb + o1 + o2, 2056);
    const int zc = min(tid / 72, 2), zi = min(tid - 72 * zc, 71);
    zoff = zc * CV_TB + (zi < 12 ? (zi & 3) * CV_KP + (256 + (zi >> 2)) * 16 : CV_U + (zi & 1) * CV_UP + ((zi - 12) >> 1) * 16);
#pragma unroll
    for (int k = 0; k < 11; ++k) asm volatile("" : "+v"(kad[k]));
    asm volatile("" : "+v"(ugo[0]), "+v"(ugo[1]), "+v"(ugo[2]), "+v"(ulo[0]), "+v"(ulo[1]), "+v"(ulo[2]), "+v"(kto[0]), "+v"(kto[1]), "+v"(kto[2]), "+v"(kto[3]), "+v"(zoff), "+v"(ufo[0]), "+v"(ufo[1]), "+v"(ufo[2]));
  }
  const unsigned tro = q4 * 128 + g * 32 + p4 * 8;
  auto chunk = [&](unsigned base, int blk) { return cat(trd(base + tro + blk * 1024), trd(base + tro + blk * 1024 + 512)); };
  if (cv_on) VMCNT(21); else VMCNT(5 + PF0_N);
  pin9(xw);
  if (blockIdx.x == 255) {
    __builtin_amdgcn_raw_buffer_store_b32(0u, rw, SY_CNT + tid * 256, 0, SC1);
    if (tid < 128) __builtin_amdgcn_raw_buffer_store_b32(0u, rw, SY_CNT + (256 + tid) * 256, 0, SC1);
  }
  {
    f32x2 v[9];
    unsigned o[9];
#pragma unroll
    for (int i = 0; i < 9; ++i) v[i] = unpk(xw[i]);
    ln9(v, o);
    set_m0(ldsT + wid * 256);
    sfor<9>([&](auto I) { put_row<1024 * I>(o[I]); });
    if (cv_on || blockIdx.x == 255) VMCNT(0); else VMCNT(2 + PF0_N);
    pin9(pp); pin9(pp + 9);
    asm volatile("" : "+v"(bw), "+v"(bias2), "+v"(ep));
    LDS_BARRIER();
    const unsigned off = WS_UB + tb * (D_ * 16) + tid * 16;
    __builtin_amdgcn_raw_buffer_store_b128(chunk(ldsT, wid), rw, off, 0, SC1);
    __builtin_amdgcn_raw_buffer_store_b128(chunk(ldsT, wid + 4), rw, off + 4096, 0, SC1);
    if (wid == 0) __builtin_amdgcn_raw_buffer_store_b128(chunk(ldsT, 8), rw, off + 8192, 0, SC1);
  }
  const u64 tag64 = (((u64)ep.y << 32) | ep.x) * 0x9E3779B97F4A7C15ull + 0xD1B54A32D192ED03ull;
  const u32x2 tag = {(unsigned)tag64, (unsigned)(tag64 >> 32)};
  TS(1);
  unsigned sink = 0;
  auto signal_u = [&]() {
    __builtin_amdgcn_s_barrier();
    if (tid < 8) __builtin_amdgcn_raw_buffer_store_b64(tag, rw, SY_FLAG + tid * 2048 + tb * 8, 0, SC1);
  };
  auto await_u = [&]() {
#pragma nounroll
    for (int spin = 0; spin < (1 << 16); ++spin) {
      const u32x4 f0 = __builtin_amdgcn_raw_buffer_load_b128(rw, SY_FLAG + xc * 2048 + lane * 16, 0, SC1);
      const u32x4 f1 = __builtin_amdgcn_raw_buffer_load_b128(rw, SY_FLAG + xc * 2048 + 1024 + lane * 16, 0, SC1);
      const unsigned d = ((f0.x ^ tag.x) | (f0.y ^ tag.y) | (f0.z ^ tag.x) | (f0.w ^ tag.y)) | ((f1.x ^ tag.x) | (f1.y ^ tag.y) | (f1.z ^ tag.x) | (f1.w ^ tag.y));
      if (__all(d == 0u)) break;
      __builtin_amdgcn_s_sleep(POLL_SLEEP);
      asm volatile("" ::: "memory");
    }
  };

  const bool g1_on = idx < 27;
  unsigned vb1[4], rb1[4];
#pragma unroll
  for (int j = 0; j < 4; ++j) vb1[j] = ((4 * j + g) * (F_ * 2) + ((r16 ^ (g << 2)) << 4) - j * 1024) | (OOB * ((idx + 5) >> 5));
#pragma unroll
  for (int m = 0; m < 4; ++m) rb1[m] = ring + 256 * (8 * g + q4) + 64 * (((m + wid) & 3) ^ q4) + 16 * p4;
  const unsigned b1_base = nt1 * 256 + wid * 32 * (F_ * 2);
#define G1_LDB(slot, kb, j) bdma<(slot) * 8192 + ((j) >> 2) * 4096, ((j) & 3) * 1024>(ring, vb1[(j) & 3], sb1, b1_base + (((kb) & ((ABL & 8) ? 1 : 15)) * 128 + ((j) >> 2) * 16) * (F_ * 2))
#define G1_HALF(slot, m, c, h) trd(rb1[m] + (slot) * 8192 + 8 * (c) + 1024 * (h))
  const unsigned ao = WS_AT + 2 * tid * (L_ * 2) + tb * 16, ao1 = tid < 32 ? ao + 512 * (L_ * 2) : OOB;
  u32x4 ga[4];
  auto gather = [&]() { BLOAD(ga[0], ao, sw, 0u, 0); BLOAD(ga[1], ao, sw, ROWB, 0); BLOAD(ga[2], ao1, sw, 0u, 0); BLOAD(ga[3], ao1, sw, ROWB, 0); };

  if (cv_on) {
    {
      float zl[3], zv[3];
      f32x2 G0[3];
#pragma unroll
      for (int c = 0; c < 3; ++c) {
        const float dt = rbf(__expf(bflo(pp[6 * c + 4])));
        const float zr = -rbf(__expf(bflo(pp[6 * c]))) * dt, zi = bflo(pp[6 * c + 1]) * dt;
        zl[c] = zr * 1.4426950408889634f; zv[c] = zi * 0.15915494309189535f;
        G0[c] = f32x2{2.0f * (bflo(pp[6 * c + 2]) * dt), -2.0f * (bflo(pp[6 * c + 3]) * dt)};
      }
#define PUT_A(row, v) put_row<B0 + (row) * KG_PITCH>(v)
#define PUT_B(row, v) put_row<B0 + (41 + (row)) * KG_PITCH>(v)
      if (wid < 2) {
        sfor<3>([&](auto C) {
          constexpr int c = C;
          f32x2 E[8], G[6];
          const f32x2 E128 = cexp_p(zl[c], -zv[c], 128.f);
          E[1] = cexp_p(zl[c], -zv[c], 1.f); E[2] = cexp_p(zl[c], -zv[c], 2.f); E[4] = cexp_p(zl[c], -zv[c], 4.f);
          G[0] = G0[c]; G[4] = cmul(cexp_p(zl[c], -zv[c], 512.f), G0[c]);
          G[1] = cmul(G[0], E128); E[3] = cmul(E[1], E[2]); G[5] = cmul(G[4], E128); E[5] = cmul(E[1], E[4]);
          G[2] = cmul(G[1], E128); E[6] = cmul(E[2], E[4]); E[7] = cmul(E[3], E[4]); G[3] = cmul(G[2], E128);
          set_m0(ldsT + c * KG_CH + wid * 256);
          f32x2 v[28], Gs[6];
          sfor<6>([&](auto t) { Gs[t] = f32x2{-G[t].y, G[t].x}; });
          sfor<28>([&](auto r) { if constexpr (kg_e(r) > 0) v[r] = E[kg_e(r)].xx * G[kg_g(r)]; });
          SB0;
          sfor<28>([&](auto r) { if constexpr (kg_e(r) > 0) v[r] = __builtin_elementwise_fma(E[kg_e(r)].yy, Gs[kg_g(r)], v[r]); else v[r] = G[kg_g(r)]; });
          SB0;
          unsigned p[28];
          sfor<29>([&](auto r) { if constexpr (r < 28) p[r] = pkv(v[r]); if constexpr (r > 0) PUT_A(r - 1, p[r - 1]); SB0; });
          if constexpr (c == 0) { VMCNT(0); signal_u(); TS(2); }
        });
      } else {
        sfor<3>([&](auto C) {
          constexpr int c = C;
          const f32x2 E128 = cexp_p(zl[c], -zv[c], 128.f), E4 = cexp_p(zl[c], -zv[c], 4.f), E8 = cexp_p(zl[c], zv[c], 8.f);
          f32x2 b0 = E8, b1v = cexp_p(zl[c], zv[c], 64.f);
          f32x2 g0 = cmul(cexp_p(zl[c], -zv[c], 768.f), G0[c]), g1 = cmul(cexp_p(zl[c], -zv[c], 1536.f), G0[c]);
          const f32x2 E8s = {-E8.y, E8.x}, E128s = {-E128.y, E128.x}, E4s = {-E4.y, E4.x};
          set_m0(ldsT + c * KG_CH + (wid & 1) * 256);
          PUT_B(0, 0x00003f80u);
          sfor<8>([&](auto j) {
            f32x2 t0, t1, t2, t3, t4;
            if constexpr (j < 6) t0 = b0.xx * E8;
            if constexpr (j < 7) t1 = b1v.xx * E8;
            if constexpr (j < 5) t2 = g0.xx * E128;
            if constexpr (j < 4) t3 = g1.xx * E128;
            if constexpr (j < 2) t4 = g0.xx * E4;
            const unsigned q0 = pkv(b0), q1 = pkv(b1v), q2 = pkv(g0), q3 = pkv(g1);
            SB0;
            if constexpr (j < 2) t4 = __builtin_elementwise_fma(g0.yy, E4s, t4);
            if constexpr (j < 7) PUT_B(j + 1, q0);
            if constexpr (j < 6) b0 = __builtin_elementwise_fma(b0.yy, E8s, t0);
            SB0;
            PUT_B(8 + j, q1);
            if constexpr (j < 7) b1v = __builtin_elementwise_fma(b1v.yy, E8s, t1);
            SB0;
            if constexpr (j < 2) PUT_A(28 + 2 * j, q2); else if constexpr (j < 6) PUT_A(30 + j, q2);
            if constexpr (j < 5) g0 = __builtin_elementwise_fma(g0.yy, E128s, t2);
            SB0;
            if constexpr (j < 5) PUT_A(36 + j, q3);
            if constexpr (j < 4) g1 = __builtin_elementwise_fma(g1.yy, E128s, t3);
            if constexpr (j < 2) PUT_A(29 + 2 * j, pkv(t4));
            SB0;
          });
          if constexpr (c == 0) { VMCNT(0); signal_u(); }
        });
      }
    }
    u32x2 fl[3];
#pragma unroll
    for (int k = 0; k < 3; ++k) bload2(fl[k], ufo[k], sw);
    LDS_BARRIER();
    TS(3);
    if (wid < 3) {
      const char* ap = smem + min(wid * 16 + r16, 40) * KG_PITCH + g * 16;
      const char* bp = smem + KG_B + r16 * KG_PITCH + g * 16;
      f32x4 a0[3] = {};
      u32x4 fa[2][3][4], fb[2][3][4];
#pragma unroll
      for (int h = 0; h < 2; ++h) {
#pragma unroll
        for (int s = 0; s < 4; ++s)
#pragma unroll
          for (int c = 0; c < 3; ++c) { fa[h][c][s] = *(const u32x4*)(ap + c * KG_CH + (4 * h + s) * 64); fb[h][c][s] = *(const u32x4*)(bp + c * KG_CH + (4 * h + s) * 64); }
        if (h == 1) {
#pragma unroll
          for (int s = 0; s < 4; ++s)
#pragma unroll
            for (int c = 0; c < 3; ++c) a0[c] = mfma16(fa[0][c][s], fb[0][c][s], a0[c]);
#pragma unroll
          for (int k = 0; k < 12; ++k) { __builtin_amdgcn_sched_group_barrier(0x100, 2, 0); __builtin_amdgcn_sched_group_barrier(0x8, 1, 0); }
        }
        SB0;
      }
#pragma unroll
      for (int s = 0; s < 4; ++s)
#pragma unroll
        for (int c = 0; c < 3; ++c) a0[c] = mfma16(fa[1][c][s], fb[1][c][s], a0[c]);
#pragma unroll
      for (int c = 0; c < 3; ++c) {
        a0[c].x += tid == 0 ? bflo(pp[6 * c + 5]) : 0.f;
        const unsigned k01 = pkv(a0[c].xy), k23 = pkv(a0[c].zw);
        *(u16*)(smem + kto[0] + c * KG_TP) = (u16)k01; *(u16*)(smem + kto[1] + c * KG_TP) = (u16)(k01 >> 16);
        *(u16*)(smem + kto[2] + c * KG_TP) = (u16)k23; *(u16*)(smem + kto[3] + c * KG_TP) = (u16)(k23 >> 16);
      }
    } else {
      if (lane < 3) *(u32x2*)(smem + KG_T + lane * KG_TP) = u32x2{0u, 0u};
    }
#pragma nounroll
    for (int spin = 0; spin < (1 << 16); ++spin) {
      asm volatile("s_waitcnt vmcnt(0)" : "+v"(fl[0]), "+v"(fl[1]), "+v"(fl[2]) :: "memory");
      const unsigned d = ((fl[0].x ^ tag.x) | (fl[0].y ^ tag.y)) | ((fl[1].x ^ tag.x) | (fl[1].y ^ tag.y)) | ((fl[2].x ^ tag.x) | (fl[2].y ^ tag.y));
      if (__all(d == 0u)) break;
      __builtin_amdgcn_s_sleep(POLL_SLEEP);
#pragma unroll
      for (int k = 0; k < 3; ++k) bload2r(fl[k], ufo[k], sw);
    }
    VMCNT(0);
    u32x4 ug[3];
#pragma unroll
    for (int k = 0; k < 3; ++k) BLOAD(ug[k], ugo[k], sw, 0u, 0);
    u32x4 RW[32];
    const u32x4 none = {~0u, ~0u, ~0u, ~0u};
    const unsigned mk0 = WS_AT + d0 * (L_ * 2) + 16 * tid;
    auto side = [&](auto K0, auto N) {
      SB0;
      sfor<N>([&](auto J) {
        constexpr int k = K0 + J, i = k - NMARK;
        if constexpr (k < 3) bstore(none, mk0 + k * 4096, sw);
        else if constexpr (k < 6) bstore(none, mk0 + (WS_HF - WS_AT) + (k - 3) * 4096, sw);
        else if constexpr (i < 32) bload_a<(i & 3) * 1024>(RW[i], vb1[i & 3], sb1, b1_base + ((i >> 3) * 128 + ((i >> 2) & 1) * 16) * (F_ * 2));
      });
      SB0;
    };
#define SIDE(k0, n) side(IC<(k0)>{}, IC<(n)>{})
    SIDE(0, 3);
    LDS_BARRIER();
    TS(4);
    {
      if (blockIdx.x == 0 && tid == 0) __builtin_amdgcn_raw_buffer_store_b64(tag, rw, SY_EPOCH, 0, SC1);
      u16 kv[3][11];
#pragma unroll
      for (int c = 0; c < 3; ++c)
#pragma unroll
        for (int k = 0; k < 11; ++k) kv[c][k] = *(const LAS u16*)(size_t)(kad[k] + c * KG_TP);
      *(u32x4*)(smem + zoff) = u32x4{0u, 0u, 0u, 0u};
      SIDE(3, 2);
      sfor<3>([&](auto C) {
        constexpr int c = C;
        unsigned e[5], a[5];
#pragma unroll
        for (int k = 0; k < 5; ++k) { e[k] = kv[c][2 * k] | ((unsigned)kv[c][2 * k + 1] << 16); a[k] = kv[c][2 * k + 1] | ((unsigned)kv[c][2 * k + 2] << 16); }
        char* kp = smem + c * CV_TB + tid * 16;
        *(u32x4*)(kp + 3 * CV_KP) = u32x4{e[0], e[1], e[2], e[3]};
        *(u32x4*)(kp + 2 * CV_KP) = u32x4{a[0], a[1], a[2], a[3]};
        *(u32x4*)(kp + 1 * CV_KP) = u32x4{e[1], e[2], e[3], e[4]};
        *(u32x4*)(kp + 0 * CV_KP) = u32x4{a[1], a[2], a[3], a[4]};
        SIDE(5 + 2 * c, 2);
      });
      LDS_BARRIER();
      asm volatile("s_waitcnt vmcnt(11)" : "+v"(ug[0]), "+v"(ug[1]), "+v"(ug[2]) :: "memory");
#pragma unroll
      for (int k = 0; k < 3; ++k) {
        char* up = smem + ulo[k];
        *(u32x2*)(up + 8) = ug[k].xy; *(u32x2*)(up + 16) = ug[k].zw;
        *(u32x4*)(up + CV_UP) = ug[k];
      }
    }
    LDS_BARRIER();
    TS(5);
    {
      const char* ap0 = smem + (r16 & 3) * CV_KP + 2 * (2064 - 256 * 7 - 8 * (r16 >> 2) - 8 * g - 64 * wid - 32);
      const char* bp0 = smem + CV_U + (r16 & 1) * CV_UP + 2 * (232 + 32 * (r16 >> 1) - 64 * wid - 32 + 24 - 8 * g);
      char* pw = smem + CV_P + wid * 8192 + ((r16 & 1) + 2 * (g ^ ((r16 >> 1) & 3)) + 8 * (r16 >> 1)) * 16;
      unsigned pr = lds0 + CV_P + (tid >> 5) * 1024 + (2 * ((tid & 3) ^ ((tid >> 2) & 3)) + 8 * ((tid & 31) >> 2)) * 16;
      unsigned c0a = tid < 8 ? SY_CNT + (tid * 16 + ((xc * 24 + idx) & 15)) * 256 : OOB, c0p = cnt_off(0, xc, 0, 16, tid), one = 1;
      u32x4 FA[2][8], FB[2][8], ov;
      f32x4 acc[2][8], py[8];
      auto rd = [&](int u, int j) {
        const int o = (u >> 1) * CV_TB + 64 * (1 - (u & 1)), k = j >> 1;
        if ((CVA & 2) && u > 1) return;
        if (j & 1) FB[u & 1][k] = *(const u32x4*)(bp0 + o + 512 * k); else FA[u & 1][k] = *(const u32x4*)(ap0 + o + 512 * (7 - k));
      };
      auto wp = [&](int c, int k) { if ((CVA & 4) && c < 2) return; pin_vf(acc[c & 1][k]); *(f32x4*)(pw + (c & 1) * 32768 + k * 1024) = acc[c & 1][k]; };
      auto rp = [&](int c, int j) { py[j] = *(const LAS f32x4*)(size_t)(pr + (c & 1) * 32768 + (j >> 1) * 8192 + (j & 1) * 16); };
      auto fin = [&](int c, int j) {
        if (j < 12) {
          const int d = j < 8 ? (j & 1) + (j & 4) : j & 1, e = j & 2, sft = j < 8 ? 2 : 4;
          float x0 = py[d][e], x1 = py[d][e + 1];
          fadd(x0, py[d + sft][e]); fadd(x1, py[d + sft][e + 1]);
          py[d][e] = x0; py[d][e + 1] = x1;
        } else if (j < 14) { ov[j - 12] = pkv(j & 1 ? py[0].zw : py[0].xy); ov[j - 10] = pkv(j & 1 ? py[1].zw : py[1].xy); asm volatile("" : "+v"(ov)); }
        else if (j < 16) { ov[j - 14] = min(ov[j - 14], 0xfffffffeu); ov[j - 12] = min(ov[j - 12], 0xfffffffeu); asm volatile("" : "+v"(ov)); }
        else __builtin_amdgcn_raw_buffer_store_b128(ov, rw, mk0 + c * 4096, 0, SC1);
      };
      sfor<16>([&](auto J) { rd(0, J); });
      SB0;
      asm volatile("" : "+v"(pr), "+v"(c0a), "+v"(c0p), "+v"(one));
      SIDE(11, 1);
      unsigned pf = 0;
      sfor<216>([&](auto NN) {
        constexpr int n = NN, u = n / 36, j = n % 36, c1 = cv_a(j), c2 = cv_b(j);
        if constexpr (n == 108) {
          VMCNT(12 + 108 / 5 - NMARK);
          LDS_BARRIER();
          add_one(one, c0a, sw);
          TS(21);
        }
        if constexpr (n == 180) {
          LDS_BARRIER();
          TS(22);
        }
        if constexpr (!(u & 1) && cv_first(j)) MFMA_V0(acc[(u >> 1) & 1][c1 + c2], FA[u & 1][c1], FB[u & 1][c2]);
        else MFMA_V(acc[(u >> 1) & 1][c1 + c2], FA[u & 1][c1], FB[u & 1][c2]);
        if constexpr (u != 3 && u != 5) {
          if constexpr (!(j & 1)) { if constexpr (j < 32) rd(u + 1, j / 2); }
          else if constexpr (u >= 2 && j >= 3 && j < 19) wp(u / 2 - 1, (j - 3) / 2);
        } else {
          if constexpr (j < 8) rp(u / 2 - 1, j);
          if constexpr (u == 3 && j < 16) rd(4, j);
          if constexpr (j >= 16 && j < 33) fin(u / 2 - 1, j - 16);
          if constexpr (u == 5 && cv_done(j) >= 0) wp(2, cv_done(j));
        }
        if constexpr (n % 5 == 4 && !(CVA & 1)) SIDE(12 + n / 5, 1);
        if constexpr (n == 191) poll_issue(pf, c0p, sw);
        SB0;
      });
      wp(2, 6);
      asm volatile("s_nop 7" ::: "memory");
      wp(2, 7);
      LDS_BARRIER();
      TS(23);
      sfor<8>([&](auto J) { rp(2, J); });
      SB0;
      py[0] = (py[0] + py[2]) + (py[4] + py[6]); py[1] = (py[1] + py[3]) + (py[5] + py[7]);
      sfor<5>([&](auto J) { fin(2, 12 + J); });
      TS(6);
      asm volatile("s_waitcnt vmcnt(2)" : "+v"(pf) :: "memory");
      await(rw, 0, xc, 0, 16, 12, wid == 0 && !__all(pf == (lane < 16 ? 12u : 0u)), lane);
      sfor<32>([&](auto I) { if constexpr (I == GATHER_AT) gather(); lds_put_a<I * 1024>(ring + lane * 16, RW[I]); });
    }
  } else {
    warm<10, 63>(sink, sb1, F_ * 2, w1c0, PF1_BEG + PF0_N * 2048, PF1_BEG + (PF0_N + PFS) * 2048, (idx - 24) * 256 + tid, 8 * 256);
    VMCNT(PFS);
    signal_u();
#pragma unroll
    for (int i = 0; i < PF0_N + 2; ++i) keep(pf0[i]);
    warm<10, PF_WIN, 1>(sink, sb1, F_ * 2, w1c0, 0, PF1_NT, (idx - 24) * 256 + tid, 8 * 256);
    warm<10>(sink, sb1, F_ * 2, w1c0, PF1_NT + PF1_BEG + (PF0_N + PFS) * 2048, PF1_END, (idx - 24) * 256 + tid, 8 * 256);
    if (g1_on) sfor<32>([&](auto I) { G1_LDB(I / 8, I / 8, I % 8); });
    else warm<10>(sink, sb1, F_ * 2, w1c0, PF1_END, PF1_END2, (idx - 27) * 256 + tid, 5 * 256);
    if (wid == 0) await_u();
    await(rw, 0, xc, 0, 16, 12, wid == 0, lane);
  }
  TS(7);

  u32x4 BR[4][8];
  TS(8);
  {
    if (!cv_on) {
#pragma nounroll
      for (int spin = 0; spin < (1 << 16); ++spin) {
        const unsigned f = __builtin_amdgcn_raw_buffer_load_b32(rw, WS_AT + (9 * lane + 2) * (L_ * 2) + tb * 16, 0, SC1);
        if (__all(f != ~0u)) break;
        __builtin_amdgcn_s_sleep(SPARE_SLEEP);
        asm volatile("" ::: "memory");
      }
      gather();
    }
    sfor<32>([&](auto J) { constexpr int I = (J + 16) & 31; BR[I / 8][I % 8] = cat(G1_HALF(I / 8, (I % 8) >> 1, I & 1, 0), G1_HALF(I / 8, (I % 8) >> 1, I & 1, 1)); });
    SB0;
    sfor<16>([&](auto I) { pin_a(BR[2 + I / 8][I % 8]); });
    SB0;
    auto ride = [&](auto K0, auto K1) { SB0; sfor<K1 - K0>([&](auto J) { constexpr int p = K0 + J; G1_LDB(p / 8, 4 + p / 8, p % 8); }); SB0; };
    asm volatile("s_waitcnt lgkmcnt(0)" ::: "memory");
    ride(IC<0>{}, IC<NW1>{});
    VMCNT(NW1);
#pragma nounroll
    for (int spin = 0; spin < (1 << 16); ++spin) {
      asm volatile("" : "+v"(ga[0]), "+v"(ga[1]), "+v"(ga[2]), "+v"(ga[3]) :: "memory");
      if (__all(umax4(ga[0], umax4(ga[1], umax4(ga[2], umax4(ga[3], 0u)))) != ~0u)) break;
      __builtin_amdgcn_s_sleep(POLL_SLEEP);
      bload_r<0>(ga[0], ao, sw, 0u); bload_r<0>(ga[1], ao, sw, ROWB); bload_r<0>(ga[2], ao1, sw, 0u); bload_r<0>(ga[3], ao1, sw, ROWB);
      VMCNT(0);
    }
    asm volatile("" : "+v"(ga[0]), "+v"(ga[1]), "+v"(ga[2]), "+v"(ga[3]) :: "memory");
    if (!g1_on) warm<10, 63, PFR_NT>(sink, sb1, F_ * 2, w1c0, PF1_END2, min(PF1_END2 + PFN * 1280, L_ * 10), (idx - 27) * 256 + tid, 5 * 256);
    {
      unsigned* tp = (unsigned*)(smem_ + (tid >> 5) * 1024 + ll * 4);
#pragma unroll
      for (int k = 0; k < 4; ++k) { tp[64 * k] = perm_lo(ga[0][k], ga[1][k]); tp[64 * k + 32] = perm_hi(ga[0][k], ga[1][k]); }
      if (tid < 32) {
#pragma unroll
        for (int k = 0; k < 4; ++k) { tp[2048 + 64 * k] = perm_lo(ga[2][k], ga[3][k]); tp[2048 + 64 * k + 32] = perm_hi(ga[2][k], ga[3][k]); }
      }
    }
    LDS_BARRIER();
    TS(9);
    f32x2 v[9];
    unsigned o[9];
#pragma unroll
    for (int i = 0; i < 9; ++i) { o[i] = pkv(unpk(xw[i]) + unpk(*(const unsigned*)(smem_ + 1024 * i + tid * 4))); v[i] = unpk(o[i]); }
    set_m0(ldsT + wid * 256);
    sfor<9>([&](auto I) { put_row<1024 * I>(o[I]); });
    ride(IC<NW1>{}, IC<NW1 + NW2 / 2>{});
    ln9(v, o);
#pragma unroll
    for (int i = 0; i < 9; ++i) v[i] = unpk(o[i]);
    ride(IC<NW1 + NW2 / 2>{}, IC<NW1 + NW2>{});
    ln9(v, o);
    set_m0(ldsT + wid * 256);
    sfor<9>([&](auto I) { put_row<t2_off(I)>(__builtin_bit_cast(unsigned, __builtin_elementwise_min(__builtin_bit_cast(u16x2, o[I]), u16x2{0xfffe, 0xfffe}))); });
    LDS_BARRIER();
    {
      const unsigned off = WS_HF + ((tb >> 2) * 36 + wid * 4 + g) * 1024 + (tb & 3) * 256 + r16 * 16;
      __builtin_amdgcn_raw_buffer_store_b128(chunk(ldsT + 9216, wid), rw, off, 0, SC1);
      __builtin_amdgcn_raw_buffer_store_b128(chunk(ldsT + (wid == 3 ? t2_off(7) - 7168 : 9216), wid + 4), rw, off + 16384, 0, SC1);
      if (wid == 0) __builtin_amdgcn_raw_buffer_store_b128(chunk(ldsT + t2_off(8), 0), rw, off + 32768, 0, SC1);
      const unsigned ho = WS_H + (tb * 8 + (lane >> 3)) * (D_ * 2) + (lane & 7) * 16;
      const char* hp = smem_ + lane * 16;
      __builtin_amdgcn_raw_buffer_store_b128(*(const u32x4*)(hp + wid * 1024), rw, ho + wid * 128, 0, SC1);
      __builtin_amdgcn_raw_buffer_store_b128(*(const u32x4*)(hp + (wid + 4) * 1024), rw, ho + (wid + 4) * 128, 0, SC1);
      if (wid == 3) __builtin_amdgcn_raw_buffer_store_b128(*(const u32x4*)(hp + 8192), rw, ho + 1024, 0, SC1);
    }
    TS(10);
  }

  unsigned vb2[4];
#pragma unroll
  for (int j = 0; j < 4; ++j) vb2[j] = (8 * j + (lane >> 3)) * (L_ * 2) + (((lane & 7) ^ (2 * ((g & 1) | ((j & 1) << 1)))) << 4) - j * 1024;
  const unsigned b2_base = nt2 * 128 + wid * 32 * (L_ * 2);
  const int lead = idx >> 3;
  auto g2_kb = [&](int t) {
    if (G2_LEAD == 0) return t;
    const int tau = t / G2_LEAD, last = 36 / G2_LEAD - 1;
    return t + (tau < last && (tau & 3) == lead ? G2_LEAD : tau > 0 && ((tau - 1) & 3) == lead ? -G2_LEAD : 0);
  };
#define G2_LDB(slot, i, j) bdma<(slot) * 4096, (j) * 1024>(ring, vb2[j], sb2, b2_base + (g2_kb(i) & ((ABL & 8) ? 1 : 63)) * 128 * (L_ * 2))

  if (g1_on) {
    sfor<G1_PRE - NW1 - NW2>([&](auto J) { constexpr int I = NW1 + NW2 + J; G1_LDB(I / 8, 4 + I / 8, I % 8); });
    TS(11);
    const unsigned va0 = lane * 16, va1 = lane * 16 + 4096;
    const unsigned a_base = WS_HF + wid * 36864 + mt * 6144;
    u32x4 A[4][6];
    f32x4 acc[4][2][6];
#define G1_AOFF(t) (a_base + (g1_kb(t) & ((ABL & 4) ? 1 : 15)) * (4 * 36864))
#define G1_LDA(t, f) BLOAD(A[(t) & 3][f], (f) < 4 ? va0 : va1, sw, G1_AOFF(t), ((f) & 3) * 1024)
#define G1_WAIT(t, n) wait6<(n)>(A[(t) & 3])
    unsigned mx, my, pfg = 0;
    const unsigned pfgo = slab_off<4>(PF2_ROWS * 4 + idx * 256 + tid, F_ * 4, L_ * 2, xc * 512);
    auto scan = [&](auto T, auto K) {
      const u32x4& a = A[T & 3][K >> 1];
      if constexpr (K & 1) { my = K == 1 ? max(a.z, a.w) : umax3(my, a.z, a.w); asm volatile("" : "+v"(my)); }
      else { mx = K == 0 ? max(a.x, a.y) : umax3(mx, a.x, a.y); asm volatile("" : "+v"(mx)); }
      SB0;
    };
    auto check = [&](auto T) {
      if (__builtin_expect(__builtin_amdgcn_ballot_w64(max(mx, my) == ~0u) != 0, 0)) {
#pragma nounroll
        for (int spin = 0; spin < (1 << 16); ++spin) {
          __builtin_amdgcn_s_sleep(POLL_SLEEP);
          sfor<18>([&](auto I) { constexpr int t = T + I / 6, f = I % 6; if constexpr (t < 16) bload_r<(f & 3) * 1024>(A[t & 3][f], f < 4 ? va0 : va1, sw, G1_AOFF(t)); });
          G1_WAIT(T, 0);
          sfor<12>([&](auto K) { scan(T, K); });
          if (__builtin_amdgcn_ballot_w64(max(mx, my) == ~0u) == 0) break;
        }
      }
    };
    sfor<18>([&](auto I) { G1_LDA(I / 6, I % 6); });
    sfor<32 - G1_PRE>([&](auto J) { constexpr int I = G1_PRE + J; G1_LDB(I / 8, 4 + I / 8, I % 8); });
    TS(12);
    u32x2 Bh[2][4];
    G1_WAIT(0, 12 + 32 - G1_PRE);
    sfor<12>([&](auto K) { scan(IC<0>{}, K); });
    check(IC<0>{});
    TS(13); TC(0);
    sfor<4>([&](auto I) { Bh[0][I] = G1_HALF(0, 0, I >> 1, I & 1); });
    SB0;
    sfor<13>([&](auto TT) {
      constexpr int t = TT;
      constexpr bool R = g1_isR(t);
      constexpr int slot = g1_lidx(t) & 3, nops = g1_ops(t);
      sfor<48>([&](auto NN) {
        constexpr int n = NN, m = n / 12, c = (n / 6) & 1, f = n % 6, i12 = n % 12;
        if constexpr (n == 36) G1_WAIT(t + 1, g1_all(t - 1) + g1_all(t) + (t < 2 ? 32 - G1_PRE : 0));
        if constexpr (n == 1 && g1_pf(t)) poll_issue_plain(pfg, ((PFG == 1 ? wid : g) == (t - PFG_T0) / PFG_DT) ? pfgo : OOB, sb2);
        if constexpr (t == 0) MFMA_VV0(acc[m][c][f], cat(Bh[m & 1][2 * c], Bh[m & 1][2 * c + 1]), A[t & 3][f]);
        else if constexpr (!R) MFMA_VV(acc[m][c][f], cat(Bh[m & 1][2 * c], Bh[m & 1][2 * c + 1]), A[t & 3][f]);
        else MFMA_VV(acc[m][c][f], BR[g1_kb(t)][2 * m + c], A[t & 3][f]);
        if constexpr (i12 < 4) {
          if constexpr (m < 3) { if constexpr (!R) { Bh[(m + 1) & 1][i12] = G1_HALF(slot, m + 1, i12 >> 1, i12 & 1); SB0; } }
          else if constexpr (!g1_isR(t + 1)) { Bh[0][i12] = G1_HALF(g1_lidx(t + 1) & 3, t == 12 ? 1 : 0, i12 >> 1, i12 & 1); SB0; }
        } else if constexpr (m < 3) {
          constexpr int pl = m * 8 + i12 - 4, k = (pl * nops + 23) / 24;
          if constexpr (k < nops && (k * 24) / nops == pl && !(ABL & 2)) {
            if constexpr (k < g1_nb(t)) { constexpr int pc = g1_b0(t) + k; G1_LDB((pc >> 3) & 3, 8 + (pc >> 3), pc & 7); }
            else G1_LDA(t + 3, k - g1_nb(t));
          }
        }
        if constexpr (n >= 36) scan(IC<t + 1>{}, IC<n - 36>{});
      });
      check(IC<t + 1>{});
    });
    unsigned pf3[PF3_N + 1];
    char* xo = smem + wid * G_WS + lane * 16;
    sfor<4>([&](auto PP) {
      constexpr int ph = PP, m = (ph + 1) & 3;
      sfor<36>([&](auto NN) {
        constexpr int n = NN, t = 13 + n / 12, c = (n / 6) & 1, f = n % 6;
        if constexpr (ph == 0 && n == 0) G1_WAIT(14, 6);
        if constexpr (ph == 0 && n == 12) { check(IC<14>{}); G1_WAIT(15, 0); }
        if constexpr (ph == 0 && n == 24) { check(IC<15>{}); keep(pfg); }
        if constexpr (t == 13) MFMA_VV(acc[m][c][f], cat(Bh[ph & 1][2 * c], Bh[ph & 1][2 * c + 1]), A[t & 3][f]);
        else MFMA_AV(acc[m][c][f], BR[t - 12][2 * m + c], A[t & 3][f]);
        if constexpr (n >= 1 && n < 5 && ph < 3) { Bh[(ph + 1) & 1][n - 1] = G1_HALF(3, (m + 1) & 3, (n - 1) >> 1, (n - 1) & 1); SB0; }
        if constexpr (ph == 0 && n < 24) scan(IC<14 + n / 12>{}, IC<n % 12>{});
        if constexpr (ph > 0 && n % 3 == 2) { constexpr int k = n / 3; pin_af(acc[ph][k / 6][k % 6]); *(f32x4*)(xo + ((ph - 1) * 12 + k) * 1024) = acc[ph][k / 6][k % 6]; SB0; }
        if constexpr (ph == 0 && n >= 26 && n < 26 + PF3_N)
          touch(pf3[n - 26], slab_off<4>(PF2_ROWS * 4 + ((n - 26 + (PFG ? 1 : 0)) * 27 + idx) * 256 + tid, F_ * 4, L_ * 2, xc * 512), sb2);
      });
    });
    asm volatile("s_nop 7" ::: "memory");
#pragma unroll
    for (int c = 0; c < 2; ++c)
#pragma unroll
      for (int t = 0; t < 6; ++t) pin_af(acc[0][c][t]);
    SB0;
    TS(14); TC(3);
    LDS_BARRIER();
    if (tid < 8) __builtin_amdgcn_raw_ptr_buffer_atomic_add_i32(1, rw, SY_CNT + ((8 + tid) * 16 + (tb & 15)) * 256, 0, SC1);
    f32x4 P[3][12];
#pragma unroll
    for (int k = 0; k < 12; ++k)
#pragma unroll
      for (int m = 1; m < 4; ++m) P[m - 1][(k & 1) * 6 + (k >> 1)] = *(const f32x4*)(smem + ((wid - m) & 3) * G_WS + ((m - 1) * 12 + (k & 1) * 6 + (k >> 1)) * 1024 + lane * 16);
    SB0;
    const f32x4 bv[2] = {f32x4{bflo(bw.x), bfhi(bw.x), bflo(bw.y), bfhi(bw.y)}, f32x4{bflo(bw.z), bfhi(bw.z), bflo(bw.w), bfhi(bw.w)}};
#pragma unroll
    for (int c = 0; c < 2; ++c)
#pragma unroll
      for (int t = 0; t < 6; ++t) acc[0][c][t] += bv[c];
    SB0;
    LDS_BARRIER();
    sfor<6>([&](auto TT) {
      constexpr int t = TT;
      sfor<4>([&](auto J) { G2_LDB(t, g2_qstep(t), J.value); });
      u32x4 o;
#pragma unroll
      for (int c = 0; c < 2; ++c) {
        const f32x4 u = (acc[0][c][t] + P[0][c * 6 + t]) + (P[1][c * 6 + t] + P[2][c * 6 + t]);
        const f32x4 z = u * __builtin_elementwise_fma(u * u, (f32x4)(-0.1029432397f), (f32x4)(-2.302208198f));
        const f32x4 den = f32x4{__builtin_amdgcn_exp2f(z.x), __builtin_amdgcn_exp2f(z.y), __builtin_amdgcn_exp2f(z.z), __builtin_amdgcn_exp2f(z.w)} + 1.0f;
        const f32x4 y = u * f32x4{__builtin_amdgcn_rcpf(den.x), __builtin_amdgcn_rcpf(den.y), __builtin_amdgcn_rcpf(den.z), __builtin_amdgcn_rcpf(den.w)};
        o[2 * c] = pkv(y.xy); o[2 * c + 1] = pkv(y.zw);
      }
      __builtin_amdgcn_raw_buffer_store_b128(o, rw, WS_GF + ((nt1 * 4 + wid) * 36 + mt * 6 + t) * 1024 + lane * 16, 0, SC1);
      SB0;
    });
    TS(15);
    sfor<12>([&](auto I) { G2_LDB(6 + I / 4, g2_qstep(6 + I / 4), I % 4); });
    arrive<12>(rw, 2, mt * 2 + (nt1 & 1));
#pragma unroll
    for (int i = 0; i < PF3_N; ++i) keep(pf3[i]);
  } else {
    unsigned sink2 = 0;
    VMCNT(4);
    keep(sink);
    warm<10, PF_WIN, PFR_NT>(sink2, sb1, F_ * 2, w1c0, PF1_END2 + PFN * 1280, L_ * 10, (idx - 27) * 256 + tid, 5 * 256);
    if (PF2_FLAT) warm<32>(sink2, sb2, L_ * 2, 0, 0, PF2_ROWS * 32, (xc * 5 + idx - 27) * 256 + tid, 40 * 256);
    else { warm<4>(sink2, sb2, L_ * 2, xc * 512, 0, PF2_NT * 4, (idx - 27) * 256 + tid, 5 * 256); warm<4, PF_WIN, 1>(sink2, sb2, L_ * 2, xc * 512, PF2_NT * 4, PF2_ROWS * 4, (idx - 27) * 256 + tid, 5 * 256); }
    arrive<PF_WIN + 1>(rw, 1, tb & 15);
    TS(15);
    sfor<36>([&](auto I) { G2_LDB(I / 4, g2_qstep(I / 4), I % 4); });
    VMCNT(36);
    keep(sink2);
  }

  {
    unsigned rb[4];
#pragma unroll
    for (int c = 0; c < 4; ++c) rb[c] = ring + 128 * (8 * g + q4) + 16 * ((2 * ((c + wid) & 3) + (p4 >> 1)) ^ (2 * ((q4 >> 1) | ((g & 1) << 1)))) + 8 * (p4 & 1);
#define G2_HALF(slot, c, h) trd(rb[c] + (slot) * 4096 + 512 * (h))
    u32x4 W[18][4];
    f32x4 acc[4][6];
    const int m0 = (72 * db) / 96, nsh = 2 * ((72 * db + 71) / 96 - m0 + 1);
    unsigned early = 0;
    sfor<6>([&](auto K) {
      if constexpr (K == 4) { if (wid == 0) VMCNT(25); else VMCNT(24); } else VMCNT(24);
      sfor<12>([&](auto I) { constexpr int q = 3 * K + I / 4; W[q][I % 4] = cat(G2_HALF(q % 9, I % 4, 0), G2_HALF(q % 9, I % 4, 1)); });
      asm volatile("s_waitcnt lgkmcnt(0)" ::: "memory");
      SB0;
      if constexpr (K == 3) { if (wid == 0) poll_issue(early, cnt_off(2, xc, 2 * m0, nsh, lane), sw); }
      sfor<12>([&](auto I) {
        constexpr int q = 3 * K + 9 + I / 4;
        G2_LDB(q % 9, g2_qstep(q), I % 4);
        if constexpr (3 * K + I / 4 >= 8) pin_a(W[3 * K + I / 4][I % 4]);
        if constexpr (K < 2) { acc[2 * K + I / 6][I % 6] = f32x4{0.f, 0.f, 0.f, 0.f}; pin_af(acc[2 * K + I / 6][I % 6]); }
      });
    });
    asm volatile("" : "+v"(early));
    await(rw, 2, xc, 2 * m0, nsh, 18, wid == 0 && !__all(early == (lane < nsh ? 18u : 0u)), lane);
    TS(16);
    const int rt0 = 9 * (db >> 1) + 5 * odd, rth = 9 * (db >> 1) + 4;
    const unsigned va = rt0 * 1024 + lane * 16;
    const unsigned vh = rth * 1024 + ((lane & 48) + (lane & 7) + 8 * odd) * 16 + ((lane >> 3) & 1) * (4 * 36864);
    const unsigned a_base = WS_GF + wid * 36864;
    u32x4 A[36][4], Ah[18];
    auto lda = [&](auto J) {
      constexpr int p = J / 9, r = J % 9, t = 2 * p + (r >= 5), f = (r + 3) & 3;
      const unsigned so = a_base + (g2_kb(t) & ((ABL & 4) ? 1 : 63)) * (4 * 36864);
      if constexpr (r == 0) BLOAD(Ah[p], vh, sw, so, 0); else BLOAD(A[t][f], va, sw, so, f * 1024);
    };
    sfor<g2_aend(2)>(lda);
    const int s = nt2 * 64 + wid * 16 + r16;
    const int dfull = rt0 * 16 + 4 * g, dhalf = rth * 16 + 8 * odd + 4 * (g & 1);
    u32x2 hw[5];
    unsigned hpoll = 0;
    u32x2 Bh[8];
    wait5<g2_aend(2) - g2_aend(0)>(A[0], Ah[0]);
    TS(17); TC(4);
    sfor<36>([&](auto TT) {
      constexpr int t = TT;
      constexpr bool R = g2_isR(t);
      sfor<20>([&](auto NN) {
        constexpr int n = NN, c = n / 5, f = n % 5, op = G2S.op[t][n];
        if constexpr (n == G2_WP && t < 35) wait5<G2S.wait[t]>(A[t + 1], Ah[(t + 1) >> 1]);
        f32x4& ac = acc[c][f < 4 ? f : 4 + (t & 1)];
        const u32x4& av = f < 4 ? A[t][f & 3] : Ah[t >> 1];
        if constexpr (!R) MFMA_VV(ac, av, cat(Bh[2 * c], Bh[2 * c + 1]));
        else if constexpr (g2_ridx(t) >= 8) MFMA_VA(ac, av, W[g2_ridx(t)][c]);
        else MFMA_VV(ac, av, W[g2_ridx(t)][c]);
        if constexpr (!R && n < 4) { Bh[4 + n] = G2_HALF(g2_slot(t), 2 + (n >> 1), n & 1); SB0; }
        if constexpr (n >= G2_WP && n < G2_WP + 4 && t < 35 && !g2_isR(t + 1)) { Bh[n - G2_WP] = G2_HALF(g2_slot(t + 1), (n - G2_WP) >> 1, (n - G2_WP) & 1); SB0; }
        if constexpr (op == 3000) {
          asm volatile("" : "+v"(hpoll));
          if (__builtin_expect(__builtin_amdgcn_ballot_w64(hpoll != (lane < 16 ? 16u : 0u)) != 0, 0)) spin_cnt(rw, cnt_off(1, xc, 0, 16, lane), lane < 16 ? 16u : 0u);
        }
        if constexpr (op == 4000) poll_issue(hpoll, cnt_off(1, xc, 0, 16, lane), sw);
        else if constexpr (op >= 3000) bload2(hw[op - 3000], WS_H + (s * D_ + (op < 3004 ? dfull + 16 * (op - 3000) : dhalf)) * 2, sw);
        else if constexpr (op >= 2000) G2_LDB((op - 2000) >> 2, 27 + ((op - 2000) >> 2), op & 3);
        else if constexpr (op >= 1000) lda(std::integral_constant<int, op - 1000>{});
      });
    });
    asm volatile("s_nop 7\n\ts_waitcnt vmcnt(0)" : "+v"(hw[0]), "+v"(hw[1]), "+v"(hw[2]), "+v"(hw[3]), "+v"(hw[4]) :: "memory");
#pragma unroll
    for (int c = 0; c < 4; ++c)
#pragma unroll
      for (int t = 0; t < 6; ++t) asm volatile("" : "+a"(acc[c][t]));
    SB0;
    TS(18); TC(7);
    const float bias = bflo(bias2);
    {
      char* o = smem + wid * G_WS + lane * 16;
#pragma unroll
      for (int c = 0; c < 4; ++c) {
        if (c > 0) {
#pragma unroll
          for (int t = 0; t < 4; ++t) *(f32x4*)(o + (c * 5 + t) * 1024) = acc[c][t];
        }
        *(f32x4*)(o + (c * 5 + 4) * 1024) = g < 2 ? acc[c][4] : acc[c][5];
      }
    }
    LDS_BARRIER();
    f32x4 Q[3][4], Hh[8];
#pragma unroll
    for (int t = 0; t < 4; ++t)
#pragma unroll
      for (int c = 1; c < 4; ++c) Q[c - 1][t] = *(const f32x4*)(smem + ((wid - c) & 3) * G_WS + (c * 5 + t) * 1024 + lane * 16);
#pragma unroll
    for (int c = 0; c < 4; ++c) {
      const char* sp = smem + ((wid - c) & 3) * G_WS + (c * 5 + 4) * 1024 + (lane & 31) * 16;
      Hh[2 * c] = *(const f32x4*)(sp); Hh[2 * c + 1] = *(const f32x4*)(sp + 512);
    }
    SB0;
#pragma unroll
    for (int t = 0; t < 4; ++t) acc[0][t] = (acc[0][t] + Q[0][t]) + (Q[1][t] + Q[2][t]);
    const f32x4 yh = ((Hh[0] + Hh[1]) + (Hh[2] + Hh[3])) + ((Hh[4] + Hh[5]) + (Hh[6] + Hh[7]));
#pragma unroll
    for (int t = 0; t < 5; ++t) {
      const f32x4 y = t < 4 ? acc[0][t & 3] : yh;
      u32x2 o;
      o.x = pkv(unpk(hw[t].x) + unpk(pkv(unpk(pkv(y.xy)) + bias)));
      o.y = pkv(unpk(hw[t].y) + unpk(pkv(unpk(pkv(y.zw)) + bias)));
      if (t < 4) *(u32x2*)(out + s * D_ + dfull + 16 * t) = o;
      else if (g < 2) *(u32x2*)(out + s * D_ + dhalf) = o;
    }
    TS(19);
  }
  TSOUT();
  asm volatile(".Lk_all_end:");
}

extern "C" void kernel_launch(void* const* d_in, const int* in_sizes, int n_in,
                              void* d_out, int out_size, void* d_ws, size_t ws_size,
                              hipStream_t stream) {
  k_all<<<256, 256, 0, stream>>>((const u16*)d_in[0], (const u16*)d_in[1], (const u16*)d_in[2], (const u16*)d_in[3], (const u16*)d_in[4],
                                 (const u16*)d_in[5], (const u16*)d_in[6], (const u16*)d_in[8], (const u16*)d_in[9], (const u16*)d_in[10],
                                 (const u16*)d_in[11], (char*)d_ws, (u16*)d_out);
}
```

```cpp
#include <hip/hip_runtime.h>
#include <stdint.h>
#include <utility>

typedef unsigned short u16;
typedef unsigned long long u64;
typedef __attribute__((ext_vector_type(4))) short s16x4;
typedef __attribute__((ext_vector_type(2))) unsigned short u16x2;
typedef __attribute__((ext_vector_type(8))) __bf16 bf16x8;
typedef __attribute__((ext_vector_type(2))) __bf16 bf16x2;
typedef __attribute__((ext_vector_type(4))) float f32x4;
typedef __attribute__((ext_vector_type(2))) float f32x2;
typedef __attribute__((ext_vector_type(4))) unsigned u32x4;
typedef __attribute__((ext_vector_type(2))) unsigned u32x2;
typedef __attribute__((ext_vector_type(4))) int i32x4;
typedef __amdgpu_buffer_rsrc_t rsrc_t;
#define LAS __attribute__((address_space(3)))
#define DI __device__ __forceinline__

#define TSDECL
#define TS(i)
#define TC(i)
#define TSOUT()

#define G2_LEAD 4
#define PF_WIN 1
#define PF1_A 3
#define PF0 0
#define PF2_ORD 1
#define PFK 0
#define PFK_POL ""
#define G1_ROTM 5
#define G1_ROTS 0
#define PF1_B 2
#define PF1_C 3
#define PFN 3
#define PF3_R0 1600
#define PF3_X 0
#define PF2_END F_
#define PF3_T0 530
#define PF3_DT 70
#define PF2_DON 1280
#define W1L 9
#define SWAP1 1
#define SWAP2 0
#define ABL 0
#define SPARE_SLEEP 16
#define GATHER_AT 12
#define G1_DELAY 8
#define G1_A0 2
#define POLL_SLEEP 4

constexpr int L_ = 2048, D_ = 576, N_ = 128, F_ = 4608;
constexpr unsigned SZ = L_ * D_ * 2;
constexpr unsigned WS_UB = 0, WS_AT = SZ, WS_H = 2 * SZ, WS_HF = 3 * SZ, WS_GF = 4 * SZ, WS_SY = 7 * SZ, WS_BYTES = 7 * SZ + 131072;
constexpr unsigned SY_EPOCH = WS_SY, SY_FLAG = WS_SY + 4096, SY_CNT = WS_SY + 32768;
constexpr unsigned OOB = 0x7ff00000u;
constexpr int SC1 = 16;
constexpr unsigned ROWB = L_ * 2;
constexpr int NMARK = 6;

template <class Fn, int... I> DI void sfor_(Fn&& f, std::integer_sequence<int, I...>) { (f(std::integral_constant<int, I>{}), ...); }
template <int N, class Fn> DI void sfor(Fn&& f) { sfor_(f, std::make_integer_sequence<int, N>{}); }

DI float bflo(unsigned w) { return __uint_as_float(w << 16); }
DI float bfhi(unsigned w) { return __uint_as_float(w & 0xffff0000u); }
DI f32x2 unpk(unsigned w) { return f32x2{bflo(w), bfhi(w)}; }
DI float bf2f(u16 v) { return __uint_as_float(((unsigned)v) << 16); }
DI unsigned pkv(f32x2 v) { return __builtin_bit_cast(unsigned, __builtin_convertvector(v, bf16x2)); }
DI unsigned pk2(float lo, float hi) { return pkv(f32x2{lo, hi}); }
DI float rbf(float f) { return bflo(pk2(f, f)); }
DI unsigned perm_lo(unsigned a, unsigned b) { return __builtin_amdgcn_perm(b, a, 0x05040100u); }
DI unsigned perm_hi(unsigned a, unsigned b) { return __builtin_amdgcn_perm(b, a, 0x07060302u); }
DI f32x4 mfma16(u32x4 a, u32x4 b, f32x4 c) { return __builtin_amdgcn_mfma_f32_16x16x32_bf16(__builtin_bit_cast(bf16x8, a), __builtin_bit_cast(bf16x8, b), c, 0, 0, 0); }
DI rsrc_t mk_rsrc(const void* p, unsigned bytes) { return __builtin_amdgcn_make_buffer_rsrc((void*)p, 0, bytes, 0x00020000); }
DI i32x4 mk_srd(const void* p, unsigned bytes) {
  const u64 a = (u64)p;
  i32x4 r; r.x = (int)(unsigned)a; r.y = (int)((unsigned)(a >> 32) & 0xffffu); r.z = (int)bytes; r.w = 0x00020000; return r;
}
#define LDS_BARRIER() do { asm volatile("s_waitcnt lgkmcnt(0)" ::: "memory"); __builtin_amdgcn_s_barrier(); asm volatile("" ::: "memory"); } while (0)
template <int IMM> DI void bload_(u32x4& dst, unsigned voff, i32x4 srd, unsigned soff) {
  asm volatile("buffer_load_dwordx4 %0, %1, %2, %3 offen offset:%4 sc1" : "=v"(dst) : "v"(voff), "s"(srd), "s"(soff), "i"(IMM) : "memory");
}
template <int IMM> DI void bload_r(u32x4& dst, unsigned voff, i32x4 srd, unsigned soff) {
  asm volatile("buffer_load_dwordx4 %0, %1, %2, %3 offen offset:%4 sc1" : "+v"(dst) : "v"(voff), "s"(srd), "s"(soff), "i"(IMM) : "memory");
}
template <int M0OFF, int IMM> DI void bdma(unsigned ldsbase, unsigned voff, i32x4 srd, unsigned soff) {
  if constexpr (IMM == 0) asm volatile("s_add_u32 m0, %0, %1\n\ts_nop 0\n\tbuffer_load_dwordx4 %2, %3, %4 offen lds" :: "s"(ldsbase), "i"(M0OFF), "v"(voff), "s"(srd), "s"(soff) : "memory", "scc");
  else asm volatile("buffer_load_dwordx4 %0, %1, %2 offen offset:%3 lds" :: "v"(voff), "s"(srd), "s"(soff), "i"(IMM) : "memory");
}
template <int OFF> DI void put_row(unsigned v) { asm volatile("ds_write_addtid_b32 %0 offset:%1" :: "v"(v), "i"(OFF) : "memory"); }
DI void keep(unsigned v) { asm volatile("" :: "v"(v)); }
DI void bload2(u32x2& dst, unsigned voff, i32x4 srd) { asm volatile("buffer_load_dwordx2 %0, %1, %2, 0 offen sc1" : "=v"(dst) : "v"(voff), "s"(srd) : "memory"); }
DI void bload2r(u32x2& dst, unsigned voff, i32x4 srd) { asm volatile("buffer_load_dwordx2 %0, %1, %2, 0 offen sc1" : "+v"(dst) : "v"(voff), "s"(srd) : "memory"); }
DI void bstore(const u32x4& v, unsigned voff, i32x4 srd) { asm volatile("buffer_store_dwordx4 %0, %1, %2, 0 offen sc1\n\ts_nop 1" :: "v"(v), "v"(voff), "s"(srd) : "memory"); }
DI unsigned umax3(unsigned a, unsigned b, unsigned c) { return max(max(a, b), c); }
DI unsigned umax4(const u32x4& a, unsigned m) { return umax3(umax3(a.x, a.y, a.z), a.w, m); }
template <int I> DI void rw_load(u32x4& dst, unsigned voff, i32x4 srd, unsigned soff);
template <int I> DI void rw_put(unsigned addr, const u32x4& v);
#define RW_IO(i, lo, hi) \
  template <> DI void rw_load<i>(u32x4& dst, unsigned voff, i32x4 srd, unsigned soff) { \
    asm volatile("buffer_load_dwordx4 %0, %1, %2, %3 offen offset:%4" : "={a[" #lo ":" #hi "]}"(dst) : "v"(voff), "s"(srd), "s"(soff), "i"((i & 3) * 1024)); } \
  template <> DI void rw_put<i>(unsigned addr, const u32x4& v) { \
    asm volatile("ds_write_b128 %0, %1 offset:%2" :: "v"(addr), "{a[" #lo ":" #hi "]}"(v), "i"(i < 16 ? 16384 + i * 1024 : (i & 7) * 1024) : "memory"); }
RW_IO(0, 128, 131) RW_IO(1, 132, 135) RW_IO(2, 136, 139) RW_IO(3, 140, 143) RW_IO(4, 144, 147) RW_IO(5, 148, 151) RW_IO(6, 152, 155) RW_IO(7, 156, 159)
RW_IO(8, 160, 163) RW_IO(9, 164, 167) RW_IO(10, 168, 171) RW_IO(11, 172, 175) RW_IO(12, 176, 179) RW_IO(13, 180, 183) RW_IO(14, 184, 187) RW_IO(15, 188, 191)
RW_IO(16, 192, 195) RW_IO(17, 196, 199) RW_IO(18, 200, 203) RW_IO(19, 204, 207) RW_IO(20, 208, 211) RW_IO(21, 212, 215) RW_IO(22, 216, 219) RW_IO(23, 220, 223)
RW_IO(24, 224, 227) RW_IO(25, 228, 231) RW_IO(26, 232, 235) RW_IO(27, 236, 239) RW_IO(28, 240, 243) RW_IO(29, 244, 247) RW_IO(30, 248, 251) RW_IO(31, 252, 255)
DI void pin_af(f32x4& v) { asm volatile("" : "+a"(v)); }
#define BLOAD(dst, voff, srd, soff, imm) bload_<(imm)>(dst, voff, srd, soff)
#define SB0 __builtin_amdgcn_sched_barrier(0)
#define MFMA_STR "v_mfma_f32_16x16x32_bf16 %0, %1, %2, %0"
DI void MFMA_VV(f32x4& acc, const u32x4& a, const u32x4& b) { asm volatile(MFMA_STR : "+a"(acc) : "v"(a), "v"(b)); }
DI void MFMA_V(f32x4& acc, const u32x4& a, const u32x4& b) { asm volatile("v_mfma_f32_16x16x32_bf16 %0, %1, %2, %0" : "+v"(acc) : "v"(a), "v"(b)); }
DI void MFMA_V0(f32x4& acc, const u32x4& a, const u32x4& b) { asm volatile("v_mfma_f32_16x16x32_bf16 %0, %1, %2, 0" : "=&v"(acc) : "v"(a), "v"(b)); }
DI void pin_vf(f32x4& v) { asm volatile("" : "+v"(v)); }
DI void fadd(float& a, float b) { asm volatile("v_add_f32 %0, %0, %1" : "+v"(a) : "v"(b)); }
DI void MFMA_VV0(f32x4& acc, const u32x4& a, const u32x4& b) { asm volatile("v_mfma_f32_16x16x32_bf16 %0, %1, %2, 0" : "=a"(acc) : "v"(a), "v"(b)); }
DI void MFMA_AV(f32x4& acc, const u32x4& a, const u32x4& b) { asm volatile(MFMA_STR : "+a"(acc) : "a"(a), "v"(b)); }
DI void MFMA_VA(f32x4& acc, const u32x4& a, const u32x4& b) { asm volatile(MFMA_STR : "+a"(acc) : "v"(a), "a"(b)); }
template <int IMM> DI void gld32(unsigned& d, unsigned voff, const void* base) { asm volatile("global_load_dword %0, %1, %2 offset:%3" : "=v"(d) : "v"(voff), "s"(base), "i"(IMM) : "memory"); }
template <int IMM> DI void gld16(unsigned& d, unsigned voff, const void* base) { asm volatile("global_load_ushort %0, %1, %2 offset:%3" : "=v"(d) : "v"(voff), "s"(base), "i"(IMM) : "memory"); }
DI void gld128(u32x4& d, unsigned voff, const void* base) { asm volatile("global_load_dwordx4 %0, %1, %2" : "=v"(d) : "v"(voff), "s"(base) : "memory"); }
DI void set_m0(unsigned v) { asm volatile("s_mov_b32 m0, %0\n\ts_nop 0" :: "s"(v) : "memory"); }
template <int N> DI void wait5(u32x4* a, u32x4& h) { asm volatile("s_waitcnt vmcnt(%5)" : "+v"(a[0]), "+v"(a[1]), "+v"(a[2]), "+v"(a[3]), "+v"(h) : "i"(N) : "memory"); }
template <int N> DI void wait9(unsigned* a) { asm volatile("s_waitcnt vmcnt(%9)" : "+v"(a[0]), "+v"(a[1]), "+v"(a[2]), "+v"(a[3]), "+v"(a[4]), "+v"(a[5]), "+v"(a[6]), "+v"(a[7]), "+v"(a[8]) : "i"(N) : "memory"); }
DI void pin9(unsigned* a) { asm volatile("" : "+v"(a[0]), "+v"(a[1]), "+v"(a[2]), "+v"(a[3]), "+v"(a[4]), "+v"(a[5]), "+v"(a[6]), "+v"(a[7]), "+v"(a[8])); }
DI void pin_a(u32x4& v) { asm volatile("" : "+a"(v)); }
template <int N> DI void wait6(u32x4* a) { asm volatile("s_waitcnt vmcnt(%6)" : "+v"(a[0]), "+v"(a[1]), "+v"(a[2]), "+v"(a[3]), "+v"(a[4]), "+v"(a[5]) : "i"(N) : "memory"); }
#define VMCNT(n) asm volatile("s_waitcnt vmcnt(%0)" :: "i"(n) : "memory")
DI u32x2 trd(unsigned addr) { return __builtin_bit_cast(u32x2, __builtin_amdgcn_ds_read_tr16_b64_v4i16((LAS s16x4*)(size_t)addr)); }
DI u32x4 cat(u32x2 a, u32x2 b) { return u32x4{a.x, a.y, b.x, b.y}; }

template <int CTRL> DI float dpp_add(float v) { return v + __builtin_bit_cast(float, __builtin_amdgcn_update_dpp(0, __builtin_bit_cast(int, v), CTRL, 0xf, 0xf, true)); }
DI float half_sum(float v) {
  v = dpp_add<0xB1>(v); v = dpp_add<0x4E>(v); v = dpp_add<0x141>(v); v = dpp_add<0x140>(v);
  float w = v;
  asm volatile("s_nop 1\n\tv_permlane16_swap_b32 %0, %1" : "+v"(v), "+v"(w));
  return v + w;
}
template <int K> using IC = std::integral_constant<int, K>;
template <class Fn> DI void ln9(const f32x2* v, unsigned* o, Fn&& hook) {
  const f32x2 s = ((v[0] + v[1]) + (v[2] + v[3])) + ((v[4] + v[5]) + (v[6] + v[7])) + v[8];
  float a = s.x + s.y;
  hook(IC<0>{}, a);
  float mean = half_sum(a) * (1.0f / D_);
  hook(IC<1>{}, mean);
  f32x2 q0 = {0.f, 0.f}, q1 = {0.f, 0.f};
#pragma unroll
  for (int i = 0; i < 9; ++i) { const f32x2 t = v[i] - mean; if (i & 1) q1 = __builtin_elementwise_fma(t, t, q1); else q0 = __builtin_elementwise_fma(t, t, q0); }
  q0 += q1;
  a = q0.x + q0.y;
  hook(IC<2>{}, a);
  float var = half_sum(a) * (1.0f / D_);
  hook(IC<3>{}, var);
  const float mb = rbf(mean);
  const float sd = rbf(__builtin_amdgcn_sqrtf(rbf(rbf(var) + 1e-5f)));
  float inv = __builtin_amdgcn_rcpf(sd);
  f32x2 w[9];
#pragma unroll
  for (int i = 0; i < 9; ++i) w[i] = v[i] - mb;
  hook(IC<4>{}, inv);
#pragma unroll
  for (int i = 0; i < 9; ++i) o[i] = pkv(w[i]);
  SB0;
#pragma unroll
  for (int i = 0; i < 5; ++i) w[i] = unpk(o[i]);
  hook(IC<5>{}, inv);
#pragma unroll
  for (int i = 5; i < 9; ++i) w[i] = unpk(o[i]);
  SB0;
#pragma unroll
  for (int i = 0; i < 9; ++i) w[i] *= inv;
  hook(IC<6>{}, inv);
#pragma unroll
  for (int i = 0; i < 9; ++i) o[i] = pkv(w[i]);
  hook(IC<7>{}, inv);
}
DI f32x2 cmul(f32x2 a, f32x2 b) { const f32x2 bs = {-b.y, b.x}; return __builtin_elementwise_fma(a.yy, bs, a.xx * b); }
DI f32x2 cexp_p(float zl, float zv, float p) {
  const float m = __builtin_amdgcn_exp2f(zl * p);
  const float rev = __builtin_amdgcn_fractf(zv * p);
  return f32x2{__builtin_amdgcn_cosf(rev), __builtin_amdgcn_sinf(rev)} * m;
}
constexpr u64 G1_STEP = 0xDCBA98764310FE52ull;
DI unsigned g1_blk(unsigned q, unsigned strip) {
  const unsigned t = (unsigned)(G1_STEP >> (4 * q)) & 15u, k = t + (strip % G1_ROTM) * G1_ROTS;
  return t >= 12 ? t : k >= 12 ? k - 12 : k;
}
template <int LINES> DI unsigned slab_off(unsigned e, unsigned e1, unsigned pitch, unsigned c0) {
  unsigned r = e / LINES;
  const unsigned c = c0 + (e - r * LINES) * 128;
  if (LINES >= 9) r = g1_blk((r >> 7) & 15, c >> 8) * 128 + (r & 127);
  return e < e1 ? r * pitch + c : OOB;
}
DI void touch_nt(unsigned& sink, unsigned off, i32x4 srd) { asm volatile("buffer_load_dword %0, %1, %2, 0 offen" PFK_POL : "+v"(sink) : "v"(off), "s"(srd) : "memory"); }
DI void touch(unsigned& sink, unsigned off, i32x4 srd) { asm volatile("buffer_load_dword %0, %1, %2, 0 offen" : "=v"(sink) : "v"(off), "s"(srd) : "memory"); }
template <int LINES, int WIN = PF_WIN> DI void warm(unsigned lds, i32x4 srd, unsigned pitch, unsigned c0, unsigned e0, unsigned e1, unsigned idx, unsigned nthr) {
  set_m0(lds);
#pragma nounroll
  for (unsigned e = e0 + idx; e < e1 + idx; e += nthr) {
    const unsigned off = slab_off<LINES>(e, e1, pitch, c0);
    asm volatile("s_waitcnt vmcnt(%2)\n\tbuffer_load_dword %0, %1, 0 offen lds" :: "v"(off), "s"(srd), "i"(WIN) : "memory");
  }
}

template <int YOUNGER = 0> DI void arrive(rsrc_t rw, int ho, int shard) {
  VMCNT(YOUNGER);
  __builtin_amdgcn_s_barrier();
  if (threadIdx.x < 8) __builtin_amdgcn_raw_ptr_buffer_atomic_add_i32(1, rw, SY_CNT + ((ho * 8 + threadIdx.x) * 16 + shard) * 256, 0, SC1);
}
DI unsigned cnt_off(int ho, int rep, int s0, int ns, int lane) { return lane < ns ? SY_CNT + ((ho * 8 + rep) * 16 + s0 + lane) * 256 : OOB; }
DI void poll_issue(unsigned& f, unsigned off, i32x4 srd) { asm volatile("buffer_load_dword %0, %1, %2, 0 offen sc1" : "+v"(f) : "v"(off), "s"(srd) : "memory"); }
DI void add_one(unsigned one, unsigned off, i32x4 srd) { asm volatile("buffer_atomic_add %0, %1, %2, 0 offen sc1" :: "v"(one), "v"(off), "s"(srd) : "memory"); }
DI void spin_cnt(rsrc_t rw, unsigned off, unsigned w) {
#pragma nounroll
  for (int spin = 0; spin < (1 << 16); ++spin) {
    const unsigned f = __builtin_amdgcn_raw_buffer_load_b32(rw, off, 0, SC1);
    if (__all(f == w)) break;
    __builtin_amdgcn_s_sleep(POLL_SLEEP);
    asm volatile("" ::: "memory");
  }
}
DI void await(rsrc_t rw, int ho, int rep, int s0, int ns, unsigned want, bool poller, int lane) {
  if (poller) spin_cnt(rw, cnt_off(ho, rep, s0, ns, lane), lane < ns ? want : 0u);
  asm volatile("" ::: "memory");
  __builtin_amdgcn_s_barrier();
  asm volatile("" ::: "memory");
}

constexpr int KG_PITCH = 528;
constexpr int KG_B = 41 * KG_PITCH, KG_CH = 57 * KG_PITCH;
constexpr int KG_T = 3 * KG_CH, KG_TP = 2072 * 2;
constexpr int CV_KP = 259 * 16 + 16;
constexpr int CV_U = 4 * CV_KP;
constexpr int CV_UP = (29 + 256) * 16 + 64 + 16;
constexpr int CV_TB = CV_U + 2 * CV_UP;
constexpr int CV_P = 3 * CV_TB;
constexpr int G_WS = 36864;
constexpr int B0 = 16384;
constexpr int t2_off(int i) { return i < 7 ? 9216 + 1024 * i : B0 + 32768 + 1024 * (i - 7); }
constexpr int K_LDS = B0 + 4 * G_WS;
static_assert(CV_KP % 256 == 64 && CV_UP % 64 == 32 && CV_TB % 16 == 0 && KG_CH % 16 == 0 && CV_P + 65536 <= 4 * G_WS && KG_T >= CV_P && KG_T + 3 * KG_TP <= 4 * G_WS &&
              K_LDS <= 163840, "");

constexpr int kg_e(int r) { return r < 16 ? r & 7 : r < 24 ? 2 * (r & 3) : 4 * (r & 1); }
constexpr int kg_g(int r) { return r < 16 ? r >> 3 : r < 24 ? 2 + ((r - 16) >> 2) : 4 + ((r - 24) >> 1); }

constexpr int cv_ab(int j) {
  int n = 0;
  for (int m = 0; m < 8; ++m)
    for (int o = 0; o <= m && m + o < 8; ++o) { if (n++ == j) return 8 * m + o; if (o < m && n++ == j) return 8 * o + m; }
  return 0;
}
constexpr int cv_a(int j) { return cv_ab(j) >> 3; }
constexpr int cv_b(int j) { return cv_ab(j) & 7; }
constexpr bool cv_first(int j) { for (int i = 0; i < j; ++i) if (cv_a(i) + cv_b(i) == cv_a(j) + cv_b(j)) return false; return true; }
constexpr int cv_done(int j) { return j == 8 ? 0 : j == 10 ? 1 : j == 12 ? 2 : j == 15 ? 3 : j == 33 ? 4 : j == 34 ? 5 : -1; }

constexpr bool g2_isR(int t) { return t < 27 && t % 3 != 2; }
constexpr int g2_ridx(int t) { return t - t / 3; }
constexpr int g2_slot(int t) { return t < 27 ? t / 3 : t - 27; }
constexpr int g2_qstep(int q) { return q < 18 ? q + q / 2 : 3 * (q - 18) + 2; }
#define G2_TEND 29
#define G2_TH 32
#define G2_DMAX 4
constexpr int G2_WP = 14;
constexpr int g2_aend(int t) { return t > 35 ? 162 : 9 * (t / 2) + ((t & 1) ? 9 : 5); }
struct G2Sched { short op[36][20]; short wait[36]; };
constexpr G2Sched g2_make() {
  G2Sched s{};
  int apos[162] = {}, bpos[36] = {}, seq = g2_aend(2), na = g2_aend(2);
  for (int i = 0; i < na; ++i) apos[i] = i;
  for (int t = 0; t < 36; ++t) {
    int list[20] = {}, n = 0;
    if (t >= 3 && t < 30) { const int p = 4 * ((t - 3) / 3) + (t % 3 == 0 ? 0 : t % 3 + 1); list[n++] = 2000 + p; if (t % 3 == 0) list[n++] = 2000 + p + 1; }
    const int d = 3 + (t + 1) / 2 < G2_DMAX ? 3 + (t + 1) / 2 : t >= G2_TEND ? G2_DMAX + 1 : G2_DMAX;
    while (na < g2_aend(t + d) && n < 9) list[n++] = 1000 + na++;
    if (t == G2_TH) for (int i = 0; i < 5; ++i) list[n++] = 3000 + i;
    if (t == 20) list[n++] = 4000;
    for (int m = 0, k = 0; m < 20; ++m) {
      if (m == (t < 33 ? G2_WP : 0) && t < 35) {
        int need = apos[g2_aend(t + 1) - 1];
        const int tw = t >= 32 ? 35 : t + 1;
        if (tw >= 27 && bpos[4 * (tw - 27) + 3] > need) need = bpos[4 * (tw - 27) + 3];
        s.wait[t] = (short)(seq - 1 - need);
      }
      if (k < n && m == (k * 20) / n) {
        const int o = list[k++];
        s.op[t][m] = (short)o;
        if (o >= 2000 && o < 3000) bpos[o - 2000] = seq; else if (o < 2000) apos[o - 1000] = seq;
        ++seq;
      }
    }
  }
  return s;
}
constexpr G2Sched G2S = g2_make();

constexpr bool g1_isR(int t) { return t == 2 || t == 5; }
constexpr int g1_lidx(int t) { return t < 2 ? t : t < 5 ? t - 1 : t - 2; }
constexpr int g1_kb(int t) { return t == 2 ? 0 : t == 5 ? 1 : t >= 14 ? t - 12 : 4 + g1_lidx(t); }
constexpr int g1_nb(int t) { return (t < 1 || t > 10) ? 0 : t >= 7 ? 8 : ((t - 1) % 3 == 2 ? 4 : 6); }
constexpr int g1_b0(int t) { return t >= 7 ? 32 + 8 * (t - 7) : 16 * ((t - 1) / 3) + 6 * ((t - 1) % 3); }
constexpr int g1_ops(int t) { return t == 0 ? 6 * (4 - G1_A0) : g1_nb(t) + 6; }
constexpr int PF1_E1 = (PF1_A + PF1_B) * 2048, PF1_E2 = PF1_E1 + PF1_C * 1280;

__global__ __launch_bounds__(256, 1) void k_all(const u16* __restrict__ x, const u16* __restrict__ logA, const u16* __restrict__ Aim,
                                                const u16* __restrict__ Cre, const u16* __restrict__ Cim, const u16* __restrict__ logdt,
                                                const u16* __restrict__ Dsk, const u16* __restrict__ w1, const u16* __restrict__ b1,
                                                const u16* __restrict__ w2, const u16* __restrict__ b2, char* __restrict__ ws, u16* __restrict__ out) {
  __shared__ __attribute__((aligned(16))) char smem_[K_LDS];
  char* const smem = smem_ + B0;
  const int tid = threadIdx.x, lane = tid & 63, wid = __builtin_amdgcn_readfirstlane(tid >> 6);
  const int xc = blockIdx.x & 7, idx = blockIdx.x >> 3;
  TSDECL;
  TS(0);
  const rsrc_t rw = mk_rsrc(ws, WS_BYTES);
  const i32x4 sw = mk_srd(ws, WS_BYTES), sb1 = mk_srd(w1, L_ * F_ * 2), sb2 = mk_srd(w2, L_ * F_ * 2);
  const unsigned ldsT = (unsigned)(size_t)smem_, lds0 = ldsT + B0, ring = lds0 + wid * G_WS;
  const int g = lane >> 4, r16 = lane & 15, q4 = r16 >> 2, p4 = lane & 3;
  const int tb = xc * 24 + idx + (168 - 16 * xc) * ((idx + 8) >> 5);
  const int tok = wid * 2 + (lane >> 5), ll = lane & 31;
  const bool cv_on = idx < 24;
  const int d0 = xc * 72 + idx * 3;
  const unsigned w1c0 = W1L == 10 ? ((27 * xc) / 6) * 256 : xc * 1152;

  const int lin1 = xc * 27 + (SWAP1 && !(xc & 1) ? (SWAP1 == 1 ? max(26 - idx, 0) : (idx + 16) % 27) : idx), mt = lin1 % 6, nt1 = min(lin1 / 6, 35);
  const int db = idx & 7, lead = (idx >> 3) ^ (SWAP2 && idx >= 16 && db < 3), nt2 = xc * 4 + lead, odd = db & 1;
  unsigned xw[9], pp[18], bias2, pf0[3];
  u32x2 ep;
  u32x4 bw;
  sfor<9>([&](auto I) { gld32<128 * I>(xw[I], ((tb * 8 + tok) * (D_ / 2) + ll) * 4, x); });
  bload2(ep, SY_EPOCH, sw);
  gld128(bw, (nt1 * 128 + wid * 32 + 8 * g) * 2, b1);
  gld16<0>(bias2, (nt2 * 64 + wid * 16 + r16) * 2, b2);
  if (cv_on) {
    const unsigned o = (d0 * N_ + (wid & 1) * 64 + lane) * 2;
    sfor<3>([&](auto C) {
      gld16<256 * C>(pp[6 * C], o, logA); gld16<256 * C>(pp[6 * C + 1], o, Aim); gld16<256 * C>(pp[6 * C + 2], o, Cre); gld16<256 * C>(pp[6 * C + 3], o, Cim);
      gld16<2 * C>(pp[6 * C + 4], d0 * 2, logdt); gld16<2 * C>(pp[6 * C + 5], d0 * 2, Dsk);
    });
  } else {
    u64 pc; unsigned len;
    asm volatile("s_getpc_b64 %0\n.Lk_all_pc:\n\ts_mov_b32 %1, .Lk_all_end-.Lk_all_pc" : "=s"(pc), "=s"(len));
    const i32x4 sc = mk_srd((const void*)pc, idx == 31 ? len : 0u);
    touch(pf0[0], tid * 128, sc); touch(pf0[1], (256 + tid) * 128, sc);
    touch(pf0[2], slab_off<W1L>((idx - 24) * 256 + tid, PF0 * 2048, F_ * 2, w1c0), sb1);
  }
  unsigned kad[11], ugo[3], ulo[3], ufo[3], kto[4], zoff;
  {
#pragma unroll
    for (int k = 0; k < 6; ++k) {
      const int j0 = 2047 - (8 * tid + 2 * k);
      const unsigned pr = pk2((float)j0, (float)(j0 - 1));
      kad[2 * k] = lds0 + KG_T + 8 + 2 * (int)bflo(pr);
      if (k < 5) kad[2 * k + 1] = lds0 + KG_T + 8 + 2 * (int)bfhi(pr);
    }
#pragma unroll
    for (int k = 0; k < 3; ++k) {
      const unsigned i = tid + 256 * k, t3 = i / 3, c = i - 3 * t3;
      ugo[k] = WS_UB + t3 * (D_ * 16) + (d0 + c) * 16; ulo[k] = c * CV_TB + CV_U + (29 + t3) * 16; ufo[k] = SY_FLAG + xc * 2048 + t3 * 8;
    }
    int pb, o1, o2;
    if (wid == 0) { pb = 4 * (g & 1) + 128 * (g >> 1); o1 = 1; o2 = 2; }
    else if (wid == 1) { pb = g < 2 ? 256 + 128 * g : 256 * g; o1 = g < 2 ? 2 : 4; o2 = g < 2 ? 4 : 128; }
    else { pb = 1024 + 512 * g; o1 = 128; o2 = 256; }
    pb += 8 * r16;
    kto[0] = KG_T + 8 + 2 * min(pb, 2056); kto[1] = KG_T + 8 + 2 * min(pb + o1, 2056); kto[2] = KG_T + 8 + 2 * min(pb + o2, 2056); kto[3] = KG_T + 8 + 2 * min(pb + o1 + o2, 2056);
    const int zc = min(tid / 72, 2), zi = min(tid - 72 * zc, 71);
    zoff = zc * CV_TB + (zi < 12 ? (zi & 3) * CV_KP + (256 + (zi >> 2)) * 16 : CV_U + (zi & 1) * CV_UP + ((zi - 12) >> 1) * 16);
#pragma unroll
    for (int k = 0; k < 11; ++k) asm volatile("" : "+v"(kad[k]));
    asm volatile("" : "+v"(ugo[0]), "+v"(ugo[1]), "+v"(ugo[2]), "+v"(ulo[0]), "+v"(ulo[1]), "+v"(ulo[2]), "+v"(kto[0]), "+v"(kto[1]), "+v"(kto[2]), "+v"(kto[3]), "+v"(zoff), "+v"(ufo[0]), "+v"(ufo[1]), "+v"(ufo[2]));
  }
  const unsigned tro = q4 * 128 + g * 32 + p4 * 8;
  auto chunk = [&](unsigned base, int blk) { return cat(trd(base + tro + blk * 1024), trd(base + tro + blk * 1024 + 512)); };
  if (cv_on) VMCNT(21); else VMCNT(6);
  pin9(xw);
  if (blockIdx.x == 255) {
    __builtin_amdgcn_raw_buffer_store_b32(0u, rw, SY_CNT + tid * 256, 0, SC1);
    if (tid < 128) __builtin_amdgcn_raw_buffer_store_b32(0u, rw, SY_CNT + (256 + tid) * 256, 0, SC1);
  }
  {
    f32x2 v[9];
    unsigned o[9];
#pragma unroll
    for (int i = 0; i < 9; ++i) v[i] = unpk(xw[i]);
    ln9(v, o, [](auto, float&) { SB0; });
    set_m0(ldsT + wid * 256);
    sfor<9>([&](auto I) { put_row<1024 * I>(o[I]); });
    if (cv_on || blockIdx.x == 255) VMCNT(0); else VMCNT(3);
    pin9(pp); pin9(pp + 9);
    asm volatile("" : "+v"(bw), "+v"(bias2), "+v"(ep));
    LDS_BARRIER();
    const unsigned off = WS_UB + tb * (D_ * 16) + tid * 16;
    __builtin_amdgcn_raw_buffer_store_b128(chunk(ldsT, wid), rw, off, 0, SC1);
    __builtin_amdgcn_raw_buffer_store_b128(chunk(ldsT, wid + 4), rw, off + 4096, 0, SC1);
    if (wid == 0) __builtin_amdgcn_raw_buffer_store_b128(chunk(ldsT, 8), rw, off + 8192, 0, SC1);
  }
  const u64 tag64 = (((u64)ep.y << 32) | ep.x) * 0x9E3779B97F4A7C15ull + 0xD1B54A32D192ED03ull;
  const u32x2 tag = {(unsigned)tag64, (unsigned)(tag64 >> 32)};
  TS(1);
  const unsigned sink = ring + 35 * 1024;
  auto signal_u = [&]() {
    __builtin_amdgcn_s_barrier();
    if (tid < 8) __builtin_amdgcn_raw_buffer_store_b64(tag, rw, SY_FLAG + tid * 2048 + tb * 8, 0, SC1);
  };
  auto await_u = [&]() {
#pragma nounroll
    for (int spin = 0; spin < (1 << 16); ++spin) {
      const u32x4 f0 = __builtin_amdgcn_raw_buffer_load_b128(rw, SY_FLAG + xc * 2048 + lane * 16, 0, SC1);
      const u32x4 f1 = __builtin_amdgcn_raw_buffer_load_b128(rw, SY_FLAG + xc * 2048 + 1024 + lane * 16, 0, SC1);
      const unsigned d = ((f0.x ^ tag.x) | (f0.y ^ tag.y) | (f0.z ^ tag.x) | (f0.w ^ tag.y)) | ((f1.x ^ tag.x) | (f1.y ^ tag.y) | (f1.z ^ tag.x) | (f1.w ^ tag.y));
      if (__all(d == 0u)) break;
      __builtin_amdgcn_s_sleep(POLL_SLEEP);
      asm volatile("" ::: "memory");
    }
  };

  const bool g1_on = idx < 27;
  unsigned vb1[4], rb1[4];
#pragma unroll
  for (int j = 0; j < 4; ++j) vb1[j] = ((4 * j + g) * (F_ * 2) + ((r16 ^ (g << 2)) << 4) - j * 1024) | (OOB * ((idx + 5) >> 5));
#pragma unroll
  for (int m = 0; m < 4; ++m) rb1[m] = ring + 256 * (8 * g + q4) + 64 * (((m + wid) & 3) ^ q4) + 16 * p4;
  const unsigned b1_base = nt1 * 256 + wid * 32 * (F_ * 2);
#define G1_LDB(slot, kb, j) bdma<(slot) * 8192 + ((j) >> 2) * 4096, ((j) & 3) * 1024>(ring, vb1[(j) & 3], sb1, b1_base + (g1_blk(kb, nt1) * 128 + ((j) >> 2) * 16) * (F_ * 2))
#define G1_HALF(slot, m, c, h) trd(rb1[m] + (slot) * 8192 + 8 * (c) + 1024 * (h))
  const unsigned ao = WS_AT + 2 * tid * (L_ * 2) + tb * 16, ao1 = tid < 32 ? ao + 512 * (L_ * 2) : OOB;
  u32x4 ga[4], RW[32];
#define G1_LDR(i) rw_load<(i)>(RW[i], vb1[(i) & 3], sb1, b1_base + (g1_blk((i) >> 3, nt1) * 128 + (((i) >> 2) & 1) * 16) * (F_ * 2))
  auto gather = [&]() { bload_r<0>(ga[0], ao, sw, 0u); bload_r<0>(ga[1], ao, sw, ROWB); bload_r<0>(ga[2], ao1, sw, 0u); bload_r<0>(ga[3], ao1, sw, ROWB); };

  if (cv_on) {
    unsigned pfk = 0;
    {
      float zl[3], zv[3];
      const unsigned pfko = slab_off<W1L>(idx * 256 + tid, 6144, F_ * 2, w1c0);
      f32x2 G0[3];
#pragma unroll
      for (int c = 0; c < 3; ++c) {
        const float dt = rbf(__expf(bflo(pp[6 * c + 4])));
        const float zr = -rbf(__expf(bflo(pp[6 * c]))) * dt, zi = bflo(pp[6 * c + 1]) * dt;
        zl[c] = zr * 1.4426950408889634f; zv[c] = zi * 0.15915494309189535f;
        G0[c] = f32x2{2.0f * (bflo(pp[6 * c + 2]) * dt), -2.0f * (bflo(pp[6 * c + 3]) * dt)};
      }
#define PUT_A(row, v) put_row<B0 + (row) * KG_PITCH>(v)
#define PUT_B(row, v) put_row<B0 + (41 + (row)) * KG_PITCH>(v)
      if (wid < 2) {
        sfor<3>([&](auto C) {
          constexpr int c = C;
          f32x2 E[8], G[6];
          const f32x2 E128 = cexp_p(zl[c], -zv[c], 128.f);
          E[1] = cexp_p(zl[c], -zv[c], 1.f); E[2] = cexp_p(zl[c], -zv[c], 2.f); E[4] = cexp_p(zl[c], -zv[c], 4.f);
          G[0] = G0[c]; G[4] = cmul(cexp_p(zl[c], -zv[c], 512.f), G0[c]);
          G[1] = cmul(G[0], E128); E[3] = cmul(E[1], E[2]); G[5] = cmul(G[4], E128); E[5] = cmul(E[1], E[4]);
          G[2] = cmul(G[1], E128); E[6] = cmul(E[2], E[4]); E[7] = cmul(E[3], E[4]); G[3] = cmul(G[2], E128);
          set_m0(ldsT + c * KG_CH + wid * 256);
          f32x2 v[28], Gs[6];
          sfor<6>([&](auto t) { Gs[t] = f32x2{-G[t].y, G[t].x}; });
          sfor<28>([&](auto r) { if constexpr (kg_e(r) > 0) v[r] = E[kg_e(r)].xx * G[kg_g(r)]; });
          SB0;
          sfor<28>([&](auto r) { if constexpr (kg_e(r) > 0) v[r] = __builtin_elementwise_fma(E[kg_e(r)].yy, Gs[kg_g(r)], v[r]); else v[r] = G[kg_g(r)]; });
          SB0;
          unsigned p[28];
          sfor<29>([&](auto r) { if constexpr (r < 28) p[r] = pkv(v[r]); if constexpr (r > 0) PUT_A(r - 1, p[r - 1]); SB0; });
          if constexpr (c == 0) { VMCNT(0); signal_u(); if (PFK) touch_nt(pfk, pfko, sb1); TS(2); }
        });
      } else {
        sfor<3>([&](auto C) {
          constexpr int c = C;
          const f32x2 E128 = cexp_p(zl[c], -zv[c], 128.f), E4 = cexp_p(zl[c], -zv[c], 4.f), E8 = cexp_p(zl[c], zv[c], 8.f);
          f32x2 b0 = E8, b1v = cexp_p(zl[c], zv[c], 64.f);
          f32x2 g0 = cmul(cexp_p(zl[c], -zv[c], 768.f), G0[c]), g1 = cmul(cexp_p(zl[c], -zv[c], 1536.f), G0[c]);
          const f32x2 E8s = {-E8.y, E8.x}, E128s = {-E128.y, E128.x}, E4s = {-E4.y, E4.x};
          set_m0(ldsT + c * KG_CH + (wid & 1) * 256);
          PUT_B(0, 0x00003f80u);
          sfor<8>([&](auto j) {
            f32x2 t0, t1, t2, t3, t4;
            if constexpr (j < 6) t0 = b0.xx * E8;
            if constexpr (j < 7) t1 = b1v.xx * E8;
            if constexpr (j < 5) t2 = g0.xx * E128;
            if constexpr (j < 4) t3 = g1.xx * E128;
            if constexpr (j < 2) t4 = g0.xx * E4;
            const unsigned q0 = pkv(b0), q1 = pkv(b1v), q2 = pkv(g0), q3 = pkv(g1);
            SB0;
            if constexpr (j < 2) t4 = __builtin_elementwise_fma(g0.yy, E4s, t4);
            if constexpr (j < 7) PUT_B(j + 1, q0);
            if constexpr (j < 6) b0 = __builtin_elementwise_fma(b0.yy, E8s, t0);
            SB0;
            PUT_B(8 + j, q1);
            if constexpr (j < 7) b1v = __builtin_elementwise_fma(b1v.yy, E8s, t1);
            SB0;
            if constexpr (j < 2) PUT_A(28 + 2 * j, q2); else if constexpr (j < 6) PUT_A(30 + j, q2);
            if constexpr (j < 5) g0 = __builtin_elementwise_fma(g0.yy, E128s, t2);
            SB0;
            if constexpr (j < 5) PUT_A(36 + j, q3);
            if constexpr (j < 4) g1 = __builtin_elementwise_fma(g1.yy, E128s, t3);
            if constexpr (j < 2) PUT_A(29 + 2 * j, pkv(t4));
            SB0;
          });
          if constexpr (c == 0) { VMCNT(0); signal_u(); if (PFK) touch_nt(pfk, pfko, sb1); }
        });
      }
    }
    u32x2 fl[3];
#pragma unroll
    for (int k = 0; k < 3; ++k) bload2(fl[k], ufo[k], sw);
    LDS_BARRIER();
    TS(3);
    if (wid < 3) {
      const char* ap = smem + min(wid * 16 + r16, 40) * KG_PITCH + g * 16;
      const char* bp = smem + KG_B + r16 * KG_PITCH + g * 16;
      f32x4 a0[3] = {};
      u32x4 fa[2][3][4], fb[2][3][4];
#pragma unroll
      for (int h = 0; h < 2; ++h) {
#pragma unroll
        for (int s = 0; s < 4; ++s)
#pragma unroll
          for (int c = 0; c < 3; ++c) { fa[h][c][s] = *(const u32x4*)(ap + c * KG_CH + (4 * h + s) * 64); fb[h][c][s] = *(const u32x4*)(bp + c * KG_CH + (4 * h + s) * 64); }
        if (h == 1) {
#pragma unroll
          for (int s = 0; s < 4; ++s)
#pragma unroll
            for (int c = 0; c < 3; ++c) a0[c] = mfma16(fa[0][c][s], fb[0][c][s], a0[c]);
#pragma unroll
          for (int k = 0; k < 12; ++k) { __builtin_amdgcn_sched_group_barrier(0x100, 2, 0); __builtin_amdgcn_sched_group_barrier(0x8, 1, 0); }
        }
        SB0;
      }
#pragma unroll
      for (int s = 0; s < 4; ++s)
#pragma unroll
        for (int c = 0; c < 3; ++c) a0[c] = mfma16(fa[1][c][s], fb[1][c][s], a0[c]);
#pragma unroll
      for (int c = 0; c < 3; ++c) {
        a0[c].x += tid == 0 ? bflo(pp[6 * c + 5]) : 0.f;
        const unsigned k01 = pkv(a0[c].xy), k23 = pkv(a0[c].zw);
        *(u16*)(smem + kto[0] + c * KG_TP) = (u16)k01; *(u16*)(smem + kto[1] + c * KG_TP) = (u16)(k01 >> 16);
        *(u16*)(smem + kto[2] + c * KG_TP) = (u16)k23; *(u16*)(smem + kto[3] + c * KG_TP) = (u16)(k23 >> 16);
      }
    } else {
      if (lane < 3) *(u32x2*)(smem + KG_T + lane * KG_TP) = u32x2{0u, 0u};
    }
#pragma nounroll
    for (int spin = 0; spin < (1 << 16); ++spin) {
      asm volatile("s_waitcnt vmcnt(0)" : "+v"(fl[0]), "+v"(fl[1]), "+v"(fl[2]) :: "memory");
      const unsigned d = ((fl[0].x ^ tag.x) | (fl[0].y ^ tag.y)) | ((fl[1].x ^ tag.x) | (fl[1].y ^ tag.y)) | ((fl[2].x ^ tag.x) | (fl[2].y ^ tag.y));
      if (__all(d == 0u)) break;
      __builtin_amdgcn_s_sleep(POLL_SLEEP);
#pragma unroll
      for (int k = 0; k < 3; ++k) bload2r(fl[k], ufo[k], sw);
    }
    VMCNT(0);
    keep(pfk);
    u32x4 ug[3];
#pragma unroll
    for (int k = 0; k < 3; ++k) BLOAD(ug[k], ugo[k], sw, 0u, 0);
    const u32x4 none = {~0u, ~0u, ~0u, ~0u};
    const unsigned mk0 = WS_AT + d0 * (L_ * 2) + 16 * tid;
    auto side = [&](auto K0, auto N) {
      SB0;
      sfor<N>([&](auto J) {
        constexpr int k = K0 + J, i = k - NMARK;
        if constexpr (k < 3) bstore(none, mk0 + k * 4096, sw);
        else if constexpr (k < 6) bstore(none, mk0 + (WS_HF - WS_AT) + (k - 3) * 4096, sw);
        else if constexpr (i < 32) G1_LDR(i);
      });
      SB0;
    };
#define SIDE(k0, n) side(IC<(k0)>{}, IC<(n)>{})
    SIDE(0, 3);
    LDS_BARRIER();
    TS(4);
    {
      if (blockIdx.x == 0 && tid == 0) __builtin_amdgcn_raw_buffer_store_b64(tag, rw, SY_EPOCH, 0, SC1);
      u16 kv[3][11];
#pragma unroll
      for (int c = 0; c < 3; ++c)
#pragma unroll
        for (int k = 0; k < 11; ++k) kv[c][k] = *(const LAS u16*)(size_t)(kad[k] + c * KG_TP);
      *(u32x4*)(smem + zoff) = u32x4{0u, 0u, 0u, 0u};
      SIDE(3, 2);
      sfor<3>([&](auto C) {
        constexpr int c = C;
        unsigned e[5], a[5];
#pragma unroll
        for (int k = 0; k < 5; ++k) { e[k] = kv[c][2 * k] | ((unsigned)kv[c][2 * k + 1] << 16); a[k] = kv[c][2 * k + 1] | ((unsigned)kv[c][2 * k + 2] << 16); }
        char* kp = smem + c * CV_TB + tid * 16;
        *(u32x4*)(kp + 3 * CV_KP) = u32x4{e[0], e[1], e[2], e[3]};
        *(u32x4*)(kp + 2 * CV_KP) = u32x4{a[0], a[1], a[2], a[3]};
        *(u32x4*)(kp + 1 * CV_KP) = u32x4{e[1], e[2], e[3], e[4]};
        *(u32x4*)(kp + 0 * CV_KP) = u32x4{a[1], a[2], a[3], a[4]};
        SIDE(5 + 2 * c, 2);
      });
      LDS_BARRIER();
      asm volatile("s_waitcnt vmcnt(11)" : "+v"(ug[0]), "+v"(ug[1]), "+v"(ug[2]) :: "memory");
#pragma unroll
      for (int k = 0; k < 3; ++k) {
        char* up = smem + ulo[k];
        *(u32x2*)(up + 8) = ug[k].xy; *(u32x2*)(up + 16) = ug[k].zw;
        *(u32x4*)(up + CV_UP) = ug[k];
      }
    }
    LDS_BARRIER();
    TS(5);
    {
      const char* ap0 = smem + (r16 & 3) * CV_KP + 2 * (2064 - 256 * 7 - 8 * (r16 >> 2) - 8 * g - 64 * wid - 32);
      const char* bp0 = smem + CV_U + (r16 & 1) * CV_UP + 2 * (232 + 32 * (r16 >> 1) - 64 * wid - 32 + 24 - 8 * g);
      char* pw = smem + CV_P + wid * 8192 + ((r16 & 1) + 2 * (g ^ ((r16 >> 1) & 3)) + 8 * (r16 >> 1)) * 16;
      unsigned pr = lds0 + CV_P + (tid >> 5) * 1024 + (2 * ((tid & 3) ^ ((tid >> 2) & 3)) + 8 * ((tid & 31) >> 2)) * 16;
      unsigned c0a = tid < 8 ? SY_CNT + (tid * 16 + ((xc * 24 + idx) & 15)) * 256 : OOB, c0p = cnt_off(0, xc, 0, 16, tid), one = 1;
      u32x4 FA[2][8], FB[2][8], ov;
      f32x4 acc[2][8], py[8];
      auto rd = [&](int u, int j) {
        const int o = (u >> 1) * CV_TB + 64 * (1 - (u & 1)), k = j >> 1;
        if (j & 1) FB[u & 1][k] = *(const u32x4*)(bp0 + o + 512 * k); else FA[u & 1][k] = *(const u32x4*)(ap0 + o + 512 * (7 - k));
      };
      auto wp = [&](int c, int k) { pin_vf(acc[c & 1][k]); *(f32x4*)(pw + (c & 1) * 32768 + k * 1024) = acc[c & 1][k]; };
      auto rp = [&](int c, int j) { py[j] = *(const LAS f32x4*)(size_t)(pr + (c & 1) * 32768 + (j >> 1) * 8192 + (j & 1) * 16); };
      auto fin = [&](int c, int j) {
        if (j < 12) {
          const int d = j < 8 ? (j & 1) + (j & 4) : j & 1, e = j & 2, sft = j < 8 ? 2 : 4;
          float x0 = py[d][e], x1 = py[d][e + 1];
          fadd(x0, py[d + sft][e]); fadd(x1, py[d + sft][e + 1]);
          py[d][e] = x0; py[d][e + 1] = x1;
        } else if (j < 14) { ov[j - 12] = pkv(j & 1 ? py[0].zw : py[0].xy); ov[j - 10] = pkv(j & 1 ? py[1].zw : py[1].xy); asm volatile("" : "+v"(ov)); }
        else if (j < 16) { ov[j - 14] = min(ov[j - 14], 0xfffffffeu); ov[j - 12] = min(ov[j - 12], 0xfffffffeu); asm volatile("" : "+v"(ov)); }
        else __builtin_amdgcn_raw_buffer_store_b128(ov, rw, mk0 + c * 4096, 0, SC1);
      };
      sfor<16>([&](auto J) { rd(0, J); });
      SB0;
      asm volatile("" : "+v"(pr), "+v"(c0a), "+v"(c0p), "+v"(one));
      SIDE(11, 1);
      unsigned pf = 0;
      sfor<216>([&](auto NN) {
        constexpr int n = NN, u = n / 36, j = n % 36, c1 = cv_a(j), c2 = cv_b(j);
        if constexpr (n == 108) {
          VMCNT(12 + 108 / 5 - NMARK);
          LDS_BARRIER();
          add_one(one, c0a, sw);
          TS(21);
        }
        if constexpr (n == 180) {
          LDS_BARRIER();
          TS(22);
        }
        if constexpr (!(u & 1) && cv_first(j)) MFMA_V0(acc[(u >> 1) & 1][c1 + c2], FA[u & 1][c1], FB[u & 1][c2]);
        else MFMA_V(acc[(u >> 1) & 1][c1 + c2], FA[u & 1][c1], FB[u & 1][c2]);
        if constexpr (u != 3 && u != 5) {
          if constexpr (!(j & 1)) { if constexpr (j < 32) rd(u + 1, j / 2); }
          else if constexpr (u >= 2 && j >= 3 && j < 19) wp(u / 2 - 1, (j - 3) / 2);
        } else {
          if constexpr (j < 8) rp(u / 2 - 1, j);
          if constexpr (u == 3 && j < 16) rd(4, j);
          if constexpr (j >= 16 && j < 33) fin(u / 2 - 1, j - 16);
          if constexpr (u == 5 && cv_done(j) >= 0) wp(2, cv_done(j));
        }
        if constexpr (n % 5 == 4) SIDE(12 + n / 5, 1);
        if constexpr (n == 191) poll_issue(pf, c0p, sw);
        SB0;
      });
      wp(2, 6);
      asm volatile("s_nop 7" ::: "memory");
      wp(2, 7);
      LDS_BARRIER();
      TS(23);
      sfor<8>([&](auto J) { rp(2, J); });
      SB0;
      py[0] = (py[0] + py[2]) + (py[4] + py[6]); py[1] = (py[1] + py[3]) + (py[5] + py[7]);
      sfor<5>([&](auto J) { fin(2, 12 + J); });
      TS(6);
      asm volatile("s_waitcnt vmcnt(2)" : "+v"(pf) :: "memory");
      await(rw, 0, xc, 0, 16, 12, wid == 0 && !__all(pf == (lane < 16 ? 12u : 0u)), lane);
    }
  } else {
    VMCNT(0);
    signal_u();
    keep(pf0[0]); keep(pf0[1]); keep(pf0[2]);
    warm<W1L>(sink, sb1, F_ * 2, w1c0, (PF0 + 3 * PFK) * 2048, PF1_A * 2048, (idx - 24) * 256 + tid, 2048);
    if (wid == 0) await_u();
    warm<W1L>(sink, sb1, F_ * 2, w1c0, PF1_A * 2048, PF1_E1, (idx - 24) * 256 + tid, 2048);
    if (g1_on) { sfor<32>([&](auto I) { G1_LDR(I); }); VMCNT(0); }
    else warm<W1L>(sink, sb1, F_ * 2, w1c0, PF1_E1, PF1_E2, (idx - 27) * 256 + tid, 1280);
  }
  TS(7);

  u32x4 BR[4][8];
  TS(8);
  {
    auto ride = [&](auto P) { SB0; G1_LDB(P / 8, 4 + P / 8, P % 8); SB0; };
    asm volatile("" : "=v"(ga[0]), "=v"(ga[1]), "=v"(ga[2]), "=v"(ga[3]));
    sfor<24>([&](auto I) {
      constexpr int k = I / 8 - 1, j = I % 8;
      if constexpr (I == GATHER_AT) { if (cv_on) gather(); }
      if constexpr (!(I & 1)) ride(IC<I / 2>{});
      if constexpr (I >= 8) { BR[k][j] = cat(G1_HALF(2 + k, j >> 1, j & 1, 0), G1_HALF(2 + k, j >> 1, j & 1, 1)); SB0; }
      if constexpr (I < 16) rw_put<I>(ring + lane * 16, RW[I]);
    });
    sfor<4>([&](auto I) { ride(IC<12 + I>{}); });
    if (cv_on) VMCNT(16 - (GATHER_AT + 1) / 2);
    else {
      if (wid == 0) {
#pragma nounroll
        for (int spin = 0; spin < (1 << 16); ++spin) {
          const unsigned c = __builtin_amdgcn_raw_buffer_load_b32(rw, cnt_off(0, xc, 0, 16, lane), 0, SC1);
          const unsigned f = __builtin_amdgcn_raw_buffer_load_b32(rw, WS_AT + (9 * lane + 2) * (L_ * 2) + tb * 16, 0, SC1);
          if (__all(c == (lane < 16 ? 12u : 0u) && f != ~0u)) break;
          __builtin_amdgcn_s_sleep(SPARE_SLEEP);
          asm volatile("" ::: "memory");
        }
      }
      asm volatile("" ::: "memory");
      __builtin_amdgcn_s_barrier();
      gather();
      if (g1_on) VMCNT(0);
      else { warm<W1L, 63>(sink, sb1, F_ * 2, w1c0, PF1_E2, PF1_E2 + PFN * 1280, (idx - 27) * 256 + tid, 5 * 256); VMCNT(PFN); }
    }
    static_assert(PF1_E2 + PFN * 1280 <= L_ * W1L, "");
#pragma nounroll
    for (int spin = 0; spin < (1 << 16); ++spin) {
      asm volatile("" : "+v"(ga[0]), "+v"(ga[1]), "+v"(ga[2]), "+v"(ga[3]) :: "memory");
      if (__all(umax4(ga[0], umax4(ga[1], umax4(ga[2], umax4(ga[3], 0u)))) != ~0u)) break;
      __builtin_amdgcn_s_sleep(POLL_SLEEP);
      gather();
      VMCNT(0);
    }
    asm volatile("" : "+v"(ga[0]), "+v"(ga[1]), "+v"(ga[2]), "+v"(ga[3]) :: "memory");
    {
      unsigned* tp = (unsigned*)(smem_ + (tid >> 5) * 1024 + ll * 4);
#pragma unroll
      for (int k = 0; k < 4; ++k) { tp[64 * k] = perm_lo(ga[0][k], ga[1][k]); tp[64 * k + 32] = perm_hi(ga[0][k], ga[1][k]); }
      if (tid < 32) {
#pragma unroll
        for (int k = 0; k < 4; ++k) { tp[2048 + 64 * k] = perm_lo(ga[2][k], ga[3][k]); tp[2048 + 64 * k + 32] = perm_hi(ga[2][k], ga[3][k]); }
      }
    }
    LDS_BARRIER();
    TS(9);
    f32x2 v[9];
    unsigned o[9];
#pragma unroll
    for (int i = 0; i < 9; ++i) { o[i] = pkv(unpk(xw[i]) + unpk(*(const unsigned*)(smem_ + 1024 * i + tid * 4))); v[i] = unpk(o[i]); }
    set_m0(ldsT + wid * 256);
    sfor<9>([&](auto I) { put_row<1024 * I>(o[I]); });
    ln9(v, o, [&](auto K, float& x) { asm volatile("" : "+v"(x)); ride(IC<16 + K>{}); });
#pragma unroll
    for (int i = 0; i < 9; ++i) v[i] = unpk(o[i]);
    ln9(v, o, [&](auto K, float& x) { asm volatile("" : "+v"(x)); ride(IC<24 + K>{}); });
    set_m0(ldsT + wid * 256);
    sfor<9>([&](auto I) { put_row<t2_off(I)>(__builtin_bit_cast(unsigned, __builtin_elementwise_min(__builtin_bit_cast(u16x2, o[I]), u16x2{0xfffe, 0xfffe}))); });
    LDS_BARRIER();
    {
      const unsigned off = WS_HF + ((tb >> 2) * 36 + wid * 4 + g) * 1024 + (tb & 3) * 256 + r16 * 16;
      __builtin_amdgcn_raw_buffer_store_b128(chunk(ldsT + 9216, wid), rw, off, 0, SC1);
      __builtin_amdgcn_raw_buffer_store_b128(chunk(ldsT + (wid == 3 ? t2_off(7) - 7168 : 9216), wid + 4), rw, off + 16384, 0, SC1);
      if (wid == 0) __builtin_amdgcn_raw_buffer_store_b128(chunk(ldsT + t2_off(8), 0), rw, off + 32768, 0, SC1);
      const unsigned ho = WS_H + (tb * 8 + (lane >> 3)) * (D_ * 2) + (lane & 7) * 16;
      const char* hp = smem_ + lane * 16;
      __builtin_amdgcn_raw_buffer_store_b128(*(const u32x4*)(hp + wid * 1024), rw, ho + wid * 128, 0, SC1);
      __builtin_amdgcn_raw_buffer_store_b128(*(const u32x4*)(hp + (wid + 4) * 1024), rw, ho + (wid + 4) * 128, 0, SC1);
      if (wid == 3) __builtin_amdgcn_raw_buffer_store_b128(*(const u32x4*)(hp + 8192), rw, ho + 1024, 0, SC1);
    }
    TS(10);
  }

  unsigned vb2[4];
#pragma unroll
  for (int j = 0; j < 4; ++j) vb2[j] = (8 * j + (lane >> 3)) * (L_ * 2) + (((lane & 7) ^ (2 * ((g & 1) | ((j & 1) << 1)))) << 4) - j * 1024;
  const unsigned b2_base = nt2 * 128 + wid * 32 * (L_ * 2);
  auto g2_kb = [&](int t) {
    if (G2_LEAD == 0) return t;
    const int tau = t / G2_LEAD, last = 36 / G2_LEAD - 1;
    return t + (tau < last && (tau & 3) == lead ? G2_LEAD : tau > 0 && ((tau - 1) & 3) == lead ? -G2_LEAD : 0);
  };
#define G2_LDB(slot, i, j) bdma<(slot) * 4096, (j) * 1024>(ring, vb2[j], sb2, b2_base + (g2_kb(i) & ((ABL & 8) ? 1 : 63)) * 128 * (L_ * 2))

  if (g1_on) {
    __builtin_amdgcn_s_sleep(G1_DELAY);
    TS(11);
    const unsigned va0 = lane * 16, va1 = lane * 16 + 4096;
    const unsigned a_base = WS_HF + wid * 36864 + mt * 6144;
    u32x4 A[4][6];
    f32x4 acc[4][2][6];
#define G1_AOFF(t) (a_base + g1_blk(g1_kb(t), nt1) * (4 * 36864))
#define G1_LDA(t, f) BLOAD(A[(t) & 3][f], (f) < 4 ? va0 : va1, sw, G1_AOFF(t), ((f) & 3) * 1024)
#define G1_WAIT(t, n) wait6<(n)>(A[(t) & 3])
    unsigned mx, my, pf3 = 0;
    auto late = [&](auto GG) {
      if constexpr (GG >= PF3_T0 && (GG - PF3_T0) % PF3_DT == 0 && (GG - PF3_T0) / PF3_DT < 4 + PF3_X) {
        constexpr int k = (GG - PF3_T0) / PF3_DT;
        if (wid == (k & 3)) touch_nt(pf3, slab_off<4>((PF3_R0 + 432 * k) * 4 + idx * 64 + lane, F_ * 4, L_ * 2, xc * 512), sb2);
      }
    };
    auto scan = [&](auto T, auto K) {
      const u32x4& a = A[T & 3][K >> 1];
      if constexpr (K & 1) { my = K == 1 ? max(a.z, a.w) : umax3(my, a.z, a.w); asm volatile("" : "+v"(my)); }
      else { mx = K == 0 ? max(a.x, a.y) : umax3(mx, a.x, a.y); asm volatile("" : "+v"(mx)); }
      SB0;
    };
    auto check = [&](auto T) {
      if (__builtin_expect(__builtin_amdgcn_ballot_w64(max(mx, my) == ~0u) != 0, 0)) {
#pragma nounroll
        for (int spin = 0; spin < (1 << 16); ++spin) {
          __builtin_amdgcn_s_sleep(POLL_SLEEP);
          sfor<18>([&](auto I) { constexpr int t = T + I / 6, f = I % 6; if constexpr (t < 16 && (T > 0 || t < G1_A0)) bload_r<(f & 3) * 1024>(A[t & 3][f], f < 4 ? va0 : va1, sw, G1_AOFF(t)); });
          G1_WAIT(T, 0);
          sfor<12>([&](auto K) { scan(T, K); });
          if (__builtin_amdgcn_ballot_w64(max(mx, my) == ~0u) == 0) break;
        }
      }
    };
    sfor<6 * G1_A0>([&](auto I) { G1_LDA(I / 6, I % 6); });
    TS(12);
    u32x2 Bh[2][4], Th[2];
    G1_WAIT(0, 6 * G1_A0 - 6);
    sfor<12>([&](auto K) { scan(IC<0>{}, K); });
    check(IC<0>{});
    TS(13); TC(0);
    sfor<4>([&](auto I) { Bh[0][I] = G1_HALF(0, 0, I >> 1, I & 1); });
    SB0;
    sfor<13>([&](auto TT) {
      constexpr int t = TT;
      constexpr bool R = g1_isR(t);
      constexpr int slot = g1_lidx(t) & 3, nops = g1_ops(t);
      sfor<48>([&](auto NN) {
        constexpr int n = NN, m = n / 12, c = (n / 6) & 1, f = n % 6, i12 = n % 12;
        if constexpr (n == 36) G1_WAIT(t + 1, t == 0 ? 12 : t == 1 ? 6 + g1_ops(1) : g1_ops(t - 1) + g1_ops(t));
        if constexpr (t == 0) MFMA_VV0(acc[m][c][f], cat(Bh[m & 1][2 * c], Bh[m & 1][2 * c + 1]), A[t & 3][f]);
        else if constexpr (!R) MFMA_VV(acc[m][c][f], cat(Bh[m & 1][2 * c], Bh[m & 1][2 * c + 1]), A[t & 3][f]);
        else MFMA_VV(acc[m][c][f], BR[g1_kb(t)][2 * m + c], A[t & 3][f]);
        if constexpr (i12 < 4) {
          if constexpr (m < 3) { if constexpr (!R) { Bh[(m + 1) & 1][i12] = G1_HALF(slot, m + 1, i12 >> 1, i12 & 1); SB0; } }
          else if constexpr (!g1_isR(t + 1)) { Bh[0][i12] = G1_HALF(g1_lidx(t + 1) & 3, t == 12 ? 1 : 0, i12 >> 1, i12 & 1); SB0; }
        } else if constexpr (m < 3) {
          constexpr int pl = m * 8 + i12 - 4, k = (pl * nops + 23) / 24;
          if constexpr (k < nops && (k * 24) / nops == pl && !(ABL & 2)) {
            if constexpr (k < g1_nb(t)) { constexpr int pc = g1_b0(t) + k; G1_LDB((pc >> 3) & 3, 8 + (pc >> 3), pc & 7); }
            else if constexpr (t == 0) G1_LDA(G1_A0 + k / 6, k % 6);
            else G1_LDA(t + 3, k - g1_nb(t));
          }
          if constexpr (t >= 11) {
            constexpr int r = t - 9;
            if constexpr (pl < 8) rw_put<8 * r + pl>(ring + lane * 16, RW[8 * r + pl]);
            else {
              constexpr int j = (pl - 8) >> 1;
              Th[pl & 1] = G1_HALF(0, j >> 1, j & 1, pl & 1);
              if constexpr (pl & 1) BR[r][j] = cat(Th[0], Th[1]);
              SB0;
            }
          }
        }
        if constexpr (n >= 36) scan(IC<t + 1>{}, IC<n - 36>{});
        late(IC<48 * t + n>{});
      });
      check(IC<t + 1>{});
    });
    char* xo = smem + wid * G_WS + lane * 16;
    sfor<4>([&](auto PP) {
      constexpr int ph = PP, m = (ph + 1) & 3;
      sfor<36>([&](auto NN) {
        constexpr int n = NN, t = 13 + n / 12, c = (n / 6) & 1, f = n % 6;
        if constexpr (ph == 0 && n == 0) G1_WAIT(14, 6);
        if constexpr (ph == 0 && n == 12) { check(IC<14>{}); G1_WAIT(15, 0); }
        if constexpr (ph == 0 && n == 24) check(IC<15>{});
        if constexpr (t == 13) MFMA_VV(acc[m][c][f], cat(Bh[ph & 1][2 * c], Bh[ph & 1][2 * c + 1]), A[t & 3][f]);
        else MFMA_VV(acc[m][c][f], BR[t - 12][2 * m + c], A[t & 3][f]);
        if constexpr (n >= 1 && n < 5 && ph < 3) { Bh[(ph + 1) & 1][n - 1] = G1_HALF(3, (m + 1) & 3, (n - 1) >> 1, (n - 1) & 1); SB0; }
        if constexpr (ph == 0 && n < 24) scan(IC<14 + n / 12>{}, IC<n % 12>{});
        if constexpr (ph > 0 && n % 3 == 2) { constexpr int k = n / 3; pin_af(acc[ph][k / 6][k % 6]); *(f32x4*)(xo + ((ph - 1) * 12 + k) * 1024) = acc[ph][k / 6][k % 6]; SB0; }
        late(IC<624 + 36 * ph + n>{});
      });
    });
    asm volatile("s_nop 7" ::: "memory");
#pragma unroll
    for (int c = 0; c < 2; ++c)
#pragma unroll
      for (int t = 0; t < 6; ++t) pin_af(acc[0][c][t]);
    SB0;
    TS(14); TC(3);
    LDS_BARRIER();
    if (tid < 8) __builtin_amdgcn_raw_ptr_buffer_atomic_add_i32(1, rw, SY_CNT + ((8 + tid) * 16 + (tb & 15)) * 256, 0, SC1);
    f32x4 P[3][12];
#pragma unroll
    for (int k = 0; k < 12; ++k)
#pragma unroll
      for (int m = 1; m < 4; ++m) P[m - 1][(k & 1) * 6 + (k >> 1)] = *(const f32x4*)(smem + ((wid - m) & 3) * G_WS + ((m - 1) * 12 + (k & 1) * 6 + (k >> 1)) * 1024 + lane * 16);
    SB0;
    const f32x4 bv[2] = {f32x4{bflo(bw.x), bfhi(bw.x), bflo(bw.y), bfhi(bw.y)}, f32x4{bflo(bw.z), bfhi(bw.z), bflo(bw.w), bfhi(bw.w)}};
#pragma unroll
    for (int c = 0; c < 2; ++c)
#pragma unroll
      for (int t = 0; t < 6; ++t) acc[0][c][t] += bv[c];
    SB0;
    LDS_BARRIER();
    sfor<6>([&](auto TT) {
      constexpr int t = TT;
      sfor<4>([&](auto J) { G2_LDB(t, g2_qstep(t), J.value); });
      u32x4 o;
#pragma unroll
      for (int c = 0; c < 2; ++c) {
        const f32x4 u = (acc[0][c][t] + P[0][c * 6 + t]) + (P[1][c * 6 + t] + P[2][c * 6 + t]);
        const f32x4 z = u * __builtin_elementwise_fma(u * u, (f32x4)(-0.1029432397f), (f32x4)(-2.302208198f));
        const f32x4 den = f32x4{__builtin_amdgcn_exp2f(z.x), __builtin_amdgcn_exp2f(z.y), __builtin_amdgcn_exp2f(z.z), __builtin_amdgcn_exp2f(z.w)} + 1.0f;
        const f32x4 y = u * f32x4{__builtin_amdgcn_rcpf(den.x), __builtin_amdgcn_rcpf(den.y), __builtin_amdgcn_rcpf(den.z), __builtin_amdgcn_rcpf(den.w)};
        o[2 * c] = pkv(y.xy); o[2 * c + 1] = pkv(y.zw);
      }
      __builtin_amdgcn_raw_buffer_store_b128(o, rw, WS_GF + ((nt1 * 4 + wid) * 36 + mt * 6 + t) * 1024 + lane * 16, 0, SC1);
      SB0;
    });
    TS(15);
    sfor<12>([&](auto I) { G2_LDB(6 + I / 4, g2_qstep(6 + I / 4), I % 4); });
    arrive<12>(rw, 2, mt * 2 + (nt1 & 1));
    keep(pf3);
  } else {
    constexpr int n1 = L_ * W1L - PF1_E2 - PFN * 1280, n2 = (PF3_R0 - PF2_DON) * 4;
    const unsigned id = (idx - 27) * 256 + tid, n3 = n1 + PF2_DON * ((xc & 1) ? 5 : 3);
    warm<W1L>(sink, sb1, F_ * 2, w1c0, PF1_E2 + PFN * 1280, L_ * W1L, id, 1280);
    if (xc & 1) warm<5>(sink, sb2, L_ * 2, xc * 512, 0, PF2_DON * 5, (id + 12800 - n1) % 1280, 1280);
    else warm<3>(sink, sb2, L_ * 2, xc * 512 + 128, 0, PF2_DON * 3, (id + 12800 - n1) % 1280, 1280);
    warm<4>(sink, sb2, L_ * 2, xc * 512, PF2_DON * 4, PF3_R0 * 4, (id + 25600 - n3) % 1280, 1280);
    warm<4>(sink, sb2, L_ * 2, xc * 512, (PF3_R0 + 1728 + 432 * PF3_X) * 4, PF2_END * 4, (id + 38400 - n3 - n2) % 1280, 1280);
    arrive<PF_WIN + 1>(rw, 1, tb & 15);
    TS(15);
    sfor<36>([&](auto I) { if constexpr (I == 32) VMCNT(32); G2_LDB(I / 4, g2_qstep(I / 4), I % 4); });
  }

  {
    unsigned rb[4];
#pragma unroll
    for (int c = 0; c < 4; ++c) rb[c] = ring + 128 * (8 * g + q4) + 16 * ((2 * ((c + wid) & 3) + (p4 >> 1)) ^ (2 * ((q4 >> 1) | ((g & 1) << 1)))) + 8 * (p4 & 1);
#define G2_HALF(slot, c, h) trd(rb[c] + (slot) * 4096 + 512 * (h))
    u32x4 W[18][4];
    f32x4 acc[4][6];
    const int m0 = (72 * db) / 96, nsh = 2 * ((72 * db + 71) / 96 - m0 + 1);
    unsigned early = 0;
    sfor<6>([&](auto K) {
      if constexpr (K == 4) { if (wid == 0) VMCNT(25); else VMCNT(24); } else VMCNT(24);
      sfor<12>([&](auto I) { constexpr int q = 3 * K + I / 4; W[q][I % 4] = cat(G2_HALF(q % 9, I % 4, 0), G2_HALF(q % 9, I % 4, 1)); });
      asm volatile("s_waitcnt lgkmcnt(0)" ::: "memory");
      SB0;
      if constexpr (K == 3) { if (wid == 0) poll_issue(early, cnt_off(2, xc, 2 * m0, nsh, lane), sw); }
      sfor<12>([&](auto I) {
        constexpr int q = 3 * K + 9 + I / 4;
        G2_LDB(q % 9, g2_qstep(q), I % 4);
        if constexpr (3 * K + I / 4 >= 8) pin_a(W[3 * K + I / 4][I % 4]);
        if constexpr (K < 2) { acc[2 * K + I / 6][I % 6] = (f32x4)(K == 0 && I < 5 ? bflo(bias2) : 0.f); pin_af(acc[2 * K + I / 6][I % 6]); }
      });
    });
    asm volatile("" : "+v"(early));
    await(rw, 2, xc, 2 * m0, nsh, 18, wid == 0 && !__all(early == (lane < nsh ? 18u : 0u)), lane);
    TS(16);
    const int rt0 = 9 * (db >> 1) + 5 * odd, rth = 9 * (db >> 1) + 4;
    const unsigned va = rt0 * 1024 + lane * 16;
    const unsigned vh = rth * 1024 + ((lane & 48) + (lane & 7) + 8 * odd) * 16 + ((lane >> 3) & 1) * (4 * 36864);
    const unsigned a_base = WS_GF + wid * 36864;
    u32x4 A[36][4], Ah[18];
    auto lda = [&](auto J) {
      constexpr int p = J / 9, r = J % 9, t = 2 * p + (r >= 5), f = (r + 3) & 3;
      const unsigned so = a_base + (g2_kb(t) & ((ABL & 4) ? 1 : 63)) * (4 * 36864);
      if constexpr (r == 0) BLOAD(Ah[p], vh, sw, so, 0); else BLOAD(A[t][f], va, sw, so, f * 1024);
    };
    sfor<g2_aend(2)>(lda);
    const int s = nt2 * 64 + wid * 16 + r16;
    const int dfull = rt0 * 16 + 4 * g, dhalf = rth * 16 + 8 * odd + 4 * (g & 1);
    u32x2 hw[5];
    unsigned hpoll = 0;
    u32x2 Bh[8], Be[2][6];
    static_assert(G2_WP == 14 && G2_DMAX >= 4, "");
    wait5<g2_aend(2) - g2_aend(0)>(A[0], Ah[0]);
    TS(17); TC(4);
    sfor<33>([&](auto TT) {
      constexpr int t = TT;
      constexpr bool R = g2_isR(t);
      sfor<20>([&](auto NN) {
        constexpr int n = NN, c = n / 5, f = n % 5, op = G2S.op[t][n];
        if constexpr (n == G2_WP && t < 35) wait5<G2S.wait[t]>(A[t + 1], Ah[(t + 1) >> 1]);
        f32x4& ac = acc[c][f < 4 ? f : 4 + (t & 1)];
        const u32x4& av = f < 4 ? A[t][f & 3] : Ah[t >> 1];
        if constexpr (!R) MFMA_VV(ac, av, cat(Bh[2 * c], Bh[2 * c + 1]));
        else if constexpr (g2_ridx(t) >= 8) MFMA_VA(ac, av, W[g2_ridx(t)][c]);
        else MFMA_VV(ac, av, W[g2_ridx(t)][c]);
        if constexpr (!R && n < 4) { Bh[4 + n] = G2_HALF(g2_slot(t), 2 + (n >> 1), n & 1); SB0; }
        if constexpr (n >= G2_WP && t == 32) { Be[0][n - G2_WP] = G2_HALF(6 + ((n - G2_WP) >> 1), 1, n & 1); SB0; }
        if constexpr (n >= G2_WP && n < G2_WP + 4 && t < 32 && !g2_isR(t + 1)) { Bh[n - G2_WP] = G2_HALF(g2_slot(t + 1), (n - G2_WP) >> 1, (n - G2_WP) & 1); SB0; }
        if constexpr (op == 3000) {
          asm volatile("" : "+v"(hpoll));
          if (__builtin_expect(__builtin_amdgcn_ballot_w64(hpoll != (lane < 16 ? 16u : 0u)) != 0, 0)) spin_cnt(rw, cnt_off(1, xc, 0, 16, lane), lane < 16 ? 16u : 0u);
        }
        if constexpr (op == 4000) poll_issue(hpoll, cnt_off(1, xc, 0, 16, lane), sw);
        else if constexpr (op >= 3000) bload2(hw[op - 3000], WS_H + (s * D_ + (op < 3004 ? dfull + 16 * (op - 3000) : dhalf)) * 2, sw);
        else if constexpr (op >= 2000) G2_LDB((op - 2000) >> 2, 27 + ((op - 2000) >> 2), op & 3);
        else if constexpr (op >= 1000) lda(std::integral_constant<int, op - 1000>{});
      });
    });
    char* xo = smem + wid * G_WS + lane * 16;
    sfor<4>([&](auto PP) {
      constexpr int ph = PP, c = (ph + 1) & 3;
      sfor<15>([&](auto NN) {
        constexpr int n = NN, t = ph < 3 ? 33 + n / 5 : n < 3 ? 33 + n : 33 + (n - 3) / 4, f = ph < 3 ? n % 5 : n < 3 ? 4 : (n - 3) % 4;
        if constexpr (ph == 0 && (n == 5 || n == 10)) wait5<G2S.wait[t - 1]>(A[t], Ah[t >> 1]);
        MFMA_VV(acc[c][f < 4 ? f : 4 + (t & 1)], f < 4 ? A[t][f & 3] : Ah[t >> 1], cat(Be[ph & 1][2 * (t - 33)], Be[ph & 1][2 * (t - 33) + 1]));
        if constexpr (ph < 3 && !(n & 1) && n < 12) { Be[(ph + 1) & 1][n >> 1] = G2_HALF(6 + (n >> 2), (c + 1) & 3, (n >> 1) & 1); SB0; }
        if constexpr (ph > 0 && (n & 1) && n >= 3) { constexpr int k = (n + 5) / 2 % 6; pin_af(acc[ph][k]); *(f32x4*)(xo + (ph * 6 + k) * 1024) = acc[ph][k]; SB0; }
        if constexpr (ph == 3 && (n == 12 || n == 14)) { constexpr int k = n / 2 - 2; pin_af(acc[0][k]); *(f32x4*)(xo + k * 1024) = acc[0][k]; SB0; }
      });
    });
    TS(18); TC(7);
    LDS_BARRIER();
    f32x4 Q[3][4], Hh[8];
#pragma unroll
    for (int t = 0; t < 4; ++t)
#pragma unroll
      for (int c = 1; c < 4; ++c) Q[c - 1][t] = *(const f32x4*)(smem + ((wid - c) & 3) * G_WS + (c * 6 + t) * 1024 + lane * 16);
#pragma unroll
    for (int c = 0; c < 4; ++c) {
      const char* sp = smem + ((wid - c) & 3) * G_WS + (c * 6 + 4) * 1024 + (lane & 31) * 16;
      Hh[2 * c] = *(const f32x4*)(sp); Hh[2 * c + 1] = *(const f32x4*)(sp + 1536);
    }
    SB0;
    asm volatile("s_waitcnt vmcnt(0)" : "+v"(hw[0]), "+v"(hw[1]), "+v"(hw[2]), "+v"(hw[3]), "+v"(hw[4]));
#pragma unroll
    for (int t = 0; t < 4; ++t) { pin_af(acc[0][t]); acc[0][t] += f32x4{bflo(hw[t].x), bfhi(hw[t].x), bflo(hw[t].y), bfhi(hw[t].y)}; }
    f32x4 hh = {bflo(hw[4].x), bfhi(hw[4].x), bflo(hw[4].y), bfhi(hw[4].y)};
    pin_vf(hh);
    SB0;
    const rsrc_t ro = mk_rsrc(out, SZ);
    const unsigned oo = (s * D_ + rt0 * 16 + 16 * (g & 1) + 4 * (g & 2)) * 2;
#pragma unroll
    for (int p = 0; p < 2; ++p) {
      const f32x4 y0 = (acc[0][2 * p] + Q[0][2 * p]) + (Q[1][2 * p] + Q[2][2 * p]), y1 = (acc[0][2 * p + 1] + Q[0][2 * p + 1]) + (Q[1][2 * p + 1] + Q[2][2 * p + 1]);
      unsigned a0 = pkv(y0.xy), a1 = pkv(y0.zw), b0 = pkv(y1.xy), b1v = pkv(y1.zw);
      asm volatile("s_nop 1\n\tv_permlane16_swap_b32 %0, %2\n\tv_permlane16_swap_b32 %1, %3" : "+v"(a0), "+v"(a1), "+v"(b0), "+v"(b1v));
      __builtin_amdgcn_raw_buffer_store_b128(u32x4{a0, a1, b0, b1v}, ro, oo + 64 * p, 0, 0);
    }
    const f32x4 yh = (((Hh[0] + Hh[1]) + (Hh[2] + Hh[3])) + ((Hh[4] + Hh[5]) + (Hh[6] + Hh[7]))) + hh;
    __builtin_amdgcn_raw_buffer_store_b64(u32x2{pkv(yh.xy), pkv(yh.zw)}, ro, g < 2 ? (s * D_ + dhalf) * 2 : OOB, 0, 0);
    TS(19);
  }
  TSOUT();
  asm volatile(".Lk_all_end:");
}

extern "C" void kernel_launch(void* const* d_in, const int* in_sizes, int n_in,
                              void* d_out, int out_size, void* d_ws, size_t ws_size,
                              hipStream_t stream) {
  k_all<<<256, 256, 0, stream>>>((const u16*)d_in[0], (const u16*)d_in[1], (const u16*)d_in[2], (const u16*)d_in[3], (const u16*)d_in[4],
                                 (const u16*)d_in[5], (const u16*)d_in[6], (const u16*)d_in[8], (const u16*)d_in[9], (const u16*)d_in[10],
                                 (const u16*)d_in[11], (char*)d_ws, (u16*)d_out);
}
```

```cpp
#include <hip/hip_runtime.h>
#include <stdint.h>
#include <utility>

typedef unsigned short u16;
typedef unsigned long long u64;
typedef __attribute__((ext_vector_type(4))) short s16x4;
typedef __attribute__((ext_vector_type(2))) unsigned short u16x2;
typedef __attribute__((ext_vector_type(8))) __bf16 bf16x8;
typedef __attribute__((ext_vector_type(2))) __bf16 bf16x2;
typedef __attribute__((ext_vector_type(4))) float f32x4;
typedef __attribute__((ext_vector_type(2))) float f32x2;
typedef __attribute__((ext_vector_type(4))) unsigned u32x4;
typedef __attribute__((ext_vector_type(2))) unsigned u32x2;
typedef __attribute__((ext_vector_type(4))) int i32x4;
typedef __amdgpu_buffer_rsrc_t rsrc_t;
#define LAS __attribute__((address_space(3)))
#define DI __device__ __forceinline__

#define TSDECL
#define TS(i)
#define TC(i)
#define TSOUT()

#define G2_LEAD 4
#define PF_WIN 1
#define PF1_A 3
#define PF0 0
#define PF2_ORD 1
#define PFK 0
#define PFK_POL ""
#define G1_ROTM 5
#define G1_ROTS 0
#define SIG_C 0
#define NE 6
#define E_T9 705
#define PF1_B 2
#define PF1_C 3
#define PFN 3
#define PF3_R0 2304
#define PF3_X 0
#define PF2_END F_
#define PF3_T0 722
#define PF3_DT 11
#define PF2_DON 1280
#define W1L 9
#define SWAP1 1
#define SWAP2 0
#define ABL 0
#define SPARE_SLEEP 16
#define GATHER_AT 12
#define G1_DELAY 8
#define G1_A0 2
#define POLL_SLEEP 4

constexpr int L_ = 2048, D_ = 576, N_ = 128, F_ = 4608;
constexpr unsigned SZ = L_ * D_ * 2;
constexpr unsigned WS_UB = 0, WS_AT = SZ, WS_H = 2 * SZ, WS_HF = 3 * SZ, WS_GF = 4 * SZ, WS_SY = 7 * SZ, WS_BYTES = 7 * SZ + 131072;
constexpr unsigned SY_EPOCH = WS_SY, SY_FLAG = WS_SY + 4096, SY_CNT = WS_SY + 32768;
constexpr unsigned OOB = 0x7ff00000u;
constexpr int SC1 = 16;
constexpr unsigned ROWB = L_ * 2;
constexpr int NMARK = 9;

template <class Fn, int... I> DI void sfor_(Fn&& f, std::integer_sequence<int, I...>) { (f(std::integral_constant<int, I>{}), ...); }
template <int N, class Fn> DI void sfor(Fn&& f) { sfor_(f, std::make_integer_sequence<int, N>{}); }

DI float bflo(unsigned w) { return __uint_as_float(w << 16); }
DI float bfhi(unsigned w) { return __uint_as_float(w & 0xffff0000u); }
DI f32x2 unpk(unsigned w) { return f32x2{bflo(w), bfhi(w)}; }
DI float bf2f(u16 v) { return __uint_as_float(((unsigned)v) << 16); }
DI unsigned pkv(f32x2 v) { return __builtin_bit_cast(unsigned, __builtin_convertvector(v, bf16x2)); }
DI unsigned pk2(float lo, float hi) { return pkv(f32x2{lo, hi}); }
DI float rbf(float f) { return bflo(pk2(f, f)); }
DI unsigned perm_lo(unsigned a, unsigned b) { return __builtin_amdgcn_perm(b, a, 0x05040100u); }
DI unsigned perm_hi(unsigned a, unsigned b) { return __builtin_amdgcn_perm(b, a, 0x07060302u); }
DI f32x4 mfma16(u32x4 a, u32x4 b, f32x4 c) { return __builtin_amdgcn_mfma_f32_16x16x32_bf16(__builtin_bit_cast(bf16x8, a), __builtin_bit_cast(bf16x8, b), c, 0, 0, 0); }
DI rsrc_t mk_rsrc(const void* p, unsigned bytes) { return __builtin_amdgcn_make_buffer_rsrc((void*)p, 0, bytes, 0x00020000); }
DI i32x4 mk_srd(const void* p, unsigned bytes) {
  const u64 a = (u64)p;
  i32x4 r; r.x = (int)(unsigned)a; r.y = (int)((unsigned)(a >> 32) & 0xffffu); r.z = (int)bytes; r.w = 0x00020000; return r;
}
#define LDS_BARRIER() do { asm volatile("s_waitcnt lgkmcnt(0)" ::: "memory"); __builtin_amdgcn_s_barrier(); asm volatile("" ::: "memory"); } while (0)
template <int IMM> DI void bload_(u32x4& dst, unsigned voff, i32x4 srd, unsigned soff) {
  asm volatile("buffer_load_dwordx4 %0, %1, %2, %3 offen offset:%4 sc1" : "=v"(dst) : "v"(voff), "s"(srd), "s"(soff), "i"(IMM) : "memory");
}
template <int IMM> DI void bload_r(u32x4& dst, unsigned voff, i32x4 srd, unsigned soff) {
  asm volatile("buffer_load_dwordx4 %0, %1, %2, %3 offen offset:%4 sc1" : "+v"(dst) : "v"(voff), "s"(srd), "s"(soff), "i"(IMM) : "memory");
}
template <int M0OFF, int IMM> DI void bdma(unsigned ldsbase, unsigned voff, i32x4 srd, unsigned soff) {
  if constexpr (IMM == 0) asm volatile("s_add_u32 m0, %0, %1\n\ts_nop 0\n\tbuffer_load_dwordx4 %2, %3, %4 offen lds" :: "s"(ldsbase), "i"(M0OFF), "v"(voff), "s"(srd), "s"(soff) : "memory", "scc");
  else asm volatile("buffer_load_dwordx4 %0, %1, %2 offen offset:%3 lds" :: "v"(voff), "s"(srd), "s"(soff), "i"(IMM) : "memory");
}
template <int OFF> DI void put_row(unsigned v) { asm volatile("ds_write_addtid_b32 %0 offset:%1" :: "v"(v), "i"(OFF) : "memory"); }
DI void keep(unsigned v) { asm volatile("" :: "v"(v)); }
DI void bload2(u32x2& dst, unsigned voff, i32x4 srd) { asm volatile("buffer_load_dwordx2 %0, %1, %2, 0 offen sc1" : "=v"(dst) : "v"(voff), "s"(srd) : "memory"); }
DI void bload2r(u32x2& dst, unsigned voff, i32x4 srd) { asm volatile("buffer_load_dwordx2 %0, %1, %2, 0 offen sc1" : "+v"(dst) : "v"(voff), "s"(srd) : "memory"); }
DI void bstore(const u32x4& v, unsigned voff, i32x4 srd) { asm volatile("buffer_store_dwordx4 %0, %1, %2, 0 offen sc1\n\ts_nop 1" :: "v"(v), "v"(voff), "s"(srd) : "memory"); }
DI unsigned umax3(unsigned a, unsigned b, unsigned c) { return max(max(a, b), c); }
DI unsigned umax4(const u32x4& a, unsigned m) { return umax3(umax3(a.x, a.y, a.z), a.w, m); }
template <int Q, int IMM> DI void q_load(unsigned voff, i32x4 srd, unsigned soff) {
  asm volatile("buffer_load_dwordx4 a[%0:%1], %2, %3, %4 offen offset:%5" :: "i"(4 * Q), "i"(4 * Q + 3), "v"(voff), "s"(srd), "s"(soff), "i"(IMM));
}
template <int Q, int OFF> DI void q_put(unsigned addr) { asm volatile("ds_write_b128 %2, a[%0:%1] offset:%3" :: "i"(4 * Q), "i"(4 * Q + 3), "v"(addr), "i"(OFF) : "memory"); }
template <int Q, int OFF> DI void q_frag(unsigned addr) {
  asm volatile("ds_read_b64_tr_b16 a[%0:%1], %4 offset:%5\n\tds_read_b64_tr_b16 a[%2:%3], %4 offset:%6"
               :: "i"(4 * Q), "i"(4 * Q + 1), "i"(4 * Q + 2), "i"(4 * Q + 3), "v"(addr), "i"(OFF), "i"(OFF + 512) : "memory");
}
template <int Q> DI void q_mfma(const u32x4& a, const u32x4& b) { asm volatile("v_mfma_f32_16x16x32_bf16 a[%0:%1], %2, %3, a[%0:%1]" :: "i"(4 * Q), "i"(4 * Q + 3), "v"(a), "v"(b)); }
template <int Q> DI void q_mfma0(const u32x4& a, const u32x4& b) { asm volatile("v_mfma_f32_16x16x32_bf16 a[%0:%1], %2, %3, 0" :: "i"(4 * Q), "i"(4 * Q + 3), "v"(a), "v"(b)); }
template <int Q, int B> DI void q_mfma_b(const u32x4& a) {
  asm volatile("v_mfma_f32_16x16x32_bf16 a[%0:%1], %4, a[%2:%3], a[%0:%1]" :: "i"(4 * Q), "i"(4 * Q + 3), "i"(4 * B), "i"(4 * B + 3), "v"(a));
}
template <int Q> DI f32x4 q_get() {
  float x, y, z, w;
  asm volatile("v_accvgpr_read_b32 %0, a[%4]\n\tv_accvgpr_read_b32 %1, a[%4+1]\n\tv_accvgpr_read_b32 %2, a[%4+2]\n\tv_accvgpr_read_b32 %3, a[%4+3]" : "=v"(x), "=v"(y), "=v"(z), "=v"(w) : "i"(4 * Q));
  return f32x4{x, y, z, w};
}
template <int Q> DI void q_set(float v) {
  asm volatile("v_accvgpr_write_b32 a[%0], %1\n\tv_accvgpr_write_b32 a[%0+1], %1\n\tv_accvgpr_write_b32 a[%0+2], %1\n\tv_accvgpr_write_b32 a[%0+3], %1" :: "i"(4 * Q), "v"(v));
}
#define BLOAD(dst, voff, srd, soff, imm) bload_<(imm)>(dst, voff, srd, soff)
#define SB0 __builtin_amdgcn_sched_barrier(0)
DI void MFMA_V(f32x4& acc, const u32x4& a, const u32x4& b) { asm volatile("v_mfma_f32_16x16x32_bf16 %0, %1, %2, %0" : "+v"(acc) : "v"(a), "v"(b)); }
DI void MFMA_V0(f32x4& acc, const u32x4& a, const u32x4& b) { asm volatile("v_mfma_f32_16x16x32_bf16 %0, %1, %2, 0" : "=&v"(acc) : "v"(a), "v"(b)); }
DI void pin_vf(f32x4& v) { asm volatile("" : "+v"(v)); }
DI void fadd(float& a, float b) { asm volatile("v_add_f32 %0, %0, %1" : "+v"(a) : "v"(b)); }
template <int IMM> DI void gld32(unsigned& d, unsigned voff, const void* base) { asm volatile("global_load_dword %0, %1, %2 offset:%3" : "=v"(d) : "v"(voff), "s"(base), "i"(IMM) : "memory"); }
template <int IMM> DI void gld16(unsigned& d, unsigned voff, const void* base) { asm volatile("global_load_ushort %0, %1, %2 offset:%3" : "=v"(d) : "v"(voff), "s"(base), "i"(IMM) : "memory"); }
DI void gld128(u32x4& d, unsigned voff, const void* base) { asm volatile("global_load_dwordx4 %0, %1, %2" : "=v"(d) : "v"(voff), "s"(base) : "memory"); }
DI void set_m0(unsigned v) { asm volatile("s_mov_b32 m0, %0\n\ts_nop 0" :: "s"(v) : "memory"); }
template <int N> DI void wait5(u32x4* a, u32x4& h) { asm volatile("s_waitcnt vmcnt(%5)" : "+v"(a[0]), "+v"(a[1]), "+v"(a[2]), "+v"(a[3]), "+v"(h) : "i"(N) : "memory"); }
template <int N> DI void wait9(unsigned* a) { asm volatile("s_waitcnt vmcnt(%9)" : "+v"(a[0]), "+v"(a[1]), "+v"(a[2]), "+v"(a[3]), "+v"(a[4]), "+v"(a[5]), "+v"(a[6]), "+v"(a[7]), "+v"(a[8]) : "i"(N) : "memory"); }
DI void pin9(unsigned* a) { asm volatile("" : "+v"(a[0]), "+v"(a[1]), "+v"(a[2]), "+v"(a[3]), "+v"(a[4]), "+v"(a[5]), "+v"(a[6]), "+v"(a[7]), "+v"(a[8])); }
template <int N> DI void wait6(u32x4* a) { asm volatile("s_waitcnt vmcnt(%6)" : "+v"(a[0]), "+v"(a[1]), "+v"(a[2]), "+v"(a[3]), "+v"(a[4]), "+v"(a[5]) : "i"(N) : "memory"); }
#define VMCNT(n) asm volatile("s_waitcnt vmcnt(%0)" :: "i"(n) : "memory")
DI u32x2 trd(unsigned addr) { return __builtin_bit_cast(u32x2, __builtin_amdgcn_ds_read_tr16_b64_v4i16((LAS s16x4*)(size_t)addr)); }
DI u32x4 cat(u32x2 a, u32x2 b) { return u32x4{a.x, a.y, b.x, b.y}; }

template <int CTRL> DI float dpp_add(float v) { return v + __builtin_bit_cast(float, __builtin_amdgcn_update_dpp(0, __builtin_bit_cast(int, v), CTRL, 0xf, 0xf, true)); }
DI float half_sum(float v) {
  v = dpp_add<0xB1>(v); v = dpp_add<0x4E>(v); v = dpp_add<0x141>(v); v = dpp_add<0x140>(v);
  float w = v;
  asm volatile("s_nop 1\n\tv_permlane16_swap_b32 %0, %1" : "+v"(v), "+v"(w));
  return v + w;
}
template <int K> using IC = std::integral_constant<int, K>;
template <class Fn> DI void ln9(const f32x2* v, unsigned* o, Fn&& hook) {
  const f32x2 s = ((v[0] + v[1]) + (v[2] + v[3])) + ((v[4] + v[5]) + (v[6] + v[7])) + v[8];
  float a = s.x + s.y;
  hook(IC<0>{}, a);
  float mean = half_sum(a) * (1.0f / D_);
  hook(IC<1>{}, mean);
  f32x2 q0 = {0.f, 0.f}, q1 = {0.f, 0.f};
#pragma unroll
  for (int i = 0; i < 9; ++i) { const f32x2 t = v[i] - mean; if (i & 1) q1 = __builtin_elementwise_fma(t, t, q1); else q0 = __builtin_elementwise_fma(t, t, q0); }
  q0 += q1;
  a = q0.x + q0.y;
  hook(IC<2>{}, a);
  float var = half_sum(a) * (1.0f / D_);
  hook(IC<3>{}, var);
  const float mb = rbf(mean);
  const float sd = rbf(__builtin_amdgcn_sqrtf(rbf(rbf(var) + 1e-5f)));
  float inv = __builtin_amdgcn_rcpf(sd);
  f32x2 w[9];
#pragma unroll
  for (int i = 0; i < 9; ++i) w[i] = v[i] - mb;
  hook(IC<4>{}, inv);
#pragma unroll
  for (int i = 0; i < 9; ++i) o[i] = pkv(w[i]);
  SB0;
#pragma unroll
  for (int i = 0; i < 5; ++i) w[i] = unpk(o[i]);
  hook(IC<5>{}, inv);
#pragma unroll
  for (int i = 5; i < 9; ++i) w[i] = unpk(o[i]);
  SB0;
#pragma unroll
  for (int i = 0; i < 9; ++i) w[i] *= inv;
  hook(IC<6>{}, inv);
#pragma unroll
  for (int i = 0; i < 9; ++i) o[i] = pkv(w[i]);
  hook(IC<7>{}, inv);
}
DI f32x2 cmul(f32x2 a, f32x2 b) { const f32x2 bs = {-b.y, b.x}; return __builtin_elementwise_fma(a.yy, bs, a.xx * b); }
DI f32x2 cexp_p(float zl, float zv, float p) {
  const float m = __builtin_amdgcn_exp2f(zl * p);
  const float rev = __builtin_amdgcn_fractf(zv * p);
  return f32x2{__builtin_amdgcn_cosf(rev), __builtin_amdgcn_sinf(rev)} * m;
}
constexpr u64 G1_STEP = 0xDCBA98764310FE52ull;
DI unsigned g1_blk(unsigned q, unsigned strip) {
  const unsigned t = (unsigned)(G1_STEP >> (4 * q)) & 15u, k = t + (strip % G1_ROTM) * G1_ROTS;
  return t >= 12 ? t : k >= 12 ? k - 12 : k;
}
template <int LINES> DI unsigned slab_off(unsigned e, unsigned e1, unsigned pitch, unsigned c0) {
  unsigned r = e / LINES;
  const unsigned c = c0 + (e - r * LINES) * 128;
  if (LINES >= 9) r = g1_blk((r >> 7) & 15, c >> 8) * 128 + (r & 127);
  return e < e1 ? r * pitch + c : OOB;
}
DI void touch_nt(unsigned& sink, unsigned off, i32x4 srd) { asm volatile("buffer_load_dword %0, %1, %2, 0 offen" PFK_POL : "+v"(sink) : "v"(off), "s"(srd) : "memory"); }
DI void touch(unsigned& sink, unsigned off, i32x4 srd) { asm volatile("buffer_load_dword %0, %1, %2, 0 offen" : "=v"(sink) : "v"(off), "s"(srd) : "memory"); }
template <int LINES, int WIN = PF_WIN> DI void warm(unsigned lds, i32x4 srd, unsigned pitch, unsigned c0, unsigned e0, unsigned e1, unsigned idx, unsigned nthr) {
  set_m0(lds);
#pragma nounroll
  for (unsigned e = e0 + idx; e < e1 + idx; e += nthr) {
    const unsigned off = slab_off<LINES>(e, e1, pitch, c0);
    asm volatile("s_waitcnt vmcnt(%2)\n\tbuffer_load_dword %0, %1, 0 offen lds" :: "v"(off), "s"(srd), "i"(WIN) : "memory");
  }
}

template <int YOUNGER = 0> DI void arrive(rsrc_t rw, int ho, int shard) {
  VMCNT(YOUNGER);
  __builtin_amdgcn_s_barrier();
  if (threadIdx.x < 8) __builtin_amdgcn_raw_ptr_buffer_atomic_add_i32(1, rw, SY_CNT + ((ho * 8 + threadIdx.x) * 16 + shard) * 256, 0, SC1);
}
DI unsigned cnt_off(int ho, int rep, int s0, int ns, int lane) { return lane < ns ? SY_CNT + ((ho * 8 + rep) * 16 + s0 + lane) * 256 : OOB; }
DI void poll_issue(unsigned& f, unsigned off, i32x4 srd) { asm volatile("buffer_load_dword %0, %1, %2, 0 offen sc1" : "+v"(f) : "v"(off), "s"(srd) : "memory"); }
DI void add_one(unsigned one, unsigned off, i32x4 srd) { asm volatile("buffer_atomic_add %0, %1, %2, 0 offen sc1" :: "v"(one), "v"(off), "s"(srd) : "memory"); }
DI void spin_cnt(rsrc_t rw, unsigned off, unsigned w) {
#pragma nounroll
  for (int spin = 0; spin < (1 << 16); ++spin) {
    const unsigned f = __builtin_amdgcn_raw_buffer_load_b32(rw, off, 0, SC1);
    if (__all(f == w)) break;
    __builtin_amdgcn_s_sleep(POLL_SLEEP);
    asm volatile("" ::: "memory");
  }
}
DI void await(rsrc_t rw, int ho, int rep, int s0, int ns, unsigned want, bool poller, int lane) {
  if (poller) spin_cnt(rw, cnt_off(ho, rep, s0, ns, lane), lane < ns ? want : 0u);
  asm volatile("" ::: "memory");
  __builtin_amdgcn_s_barrier();
  asm volatile("" ::: "memory");
}

constexpr int KG_PITCH = 528;
constexpr int KG_B = 41 * KG_PITCH, KG_CH = 57 * KG_PITCH;
constexpr int KG_T = 3 * KG_CH, KG_TP = 2072 * 2;
constexpr int CV_KP = 259 * 16 + 16;
constexpr int CV_U = 4 * CV_KP;
constexpr int CV_UP = (29 + 256) * 16 + 64 + 16;
constexpr int CV_TB = CV_U + 2 * CV_UP;
constexpr int CV_P = 3 * CV_TB;
constexpr int G_WS = 36864;
constexpr int B0 = 16384;
constexpr int t2_off(int i) { return i < 7 ? 9216 + 1024 * i : B0 + 32768 + 1024 * (i - 7); }
constexpr int K_LDS = B0 + 4 * G_WS;
static_assert(CV_KP % 256 == 64 && CV_UP % 64 == 32 && CV_TB % 16 == 0 && KG_CH % 16 == 0 && CV_P + 65536 <= 4 * G_WS && KG_T >= CV_P && KG_T + 3 * KG_TP <= 4 * G_WS &&
              K_LDS <= 163840, "");

constexpr int kg_e(int r) { return r < 16 ? r & 7 : r < 24 ? 2 * (r & 3) : 4 * (r & 1); }
constexpr int kg_g(int r) { return r < 16 ? r >> 3 : r < 24 ? 2 + ((r - 16) >> 2) : 4 + ((r - 24) >> 1); }

constexpr int cv_ab(int j) {
  int n = 0;
  for (int m = 0; m < 8; ++m)
    for (int o = 0; o <= m && m + o < 8; ++o) { if (n++ == j) return 8 * m + o; if (o < m && n++ == j) return 8 * o + m; }
  return 0;
}
constexpr int cv_a(int j) { return cv_ab(j) >> 3; }
constexpr int cv_b(int j) { return cv_ab(j) & 7; }
constexpr bool cv_first(int j) { for (int i = 0; i < j; ++i) if (cv_a(i) + cv_b(i) == cv_a(j) + cv_b(j)) return false; return true; }
constexpr int cv_done(int j) { return j == 8 ? 0 : j == 10 ? 1 : j == 12 ? 2 : j == 15 ? 3 : j == 33 ? 4 : j == 34 ? 5 : -1; }

constexpr bool g2_isR(int t) { return t < 27 && t % 3 != 2; }
constexpr int g2_ridx(int t) { return t - t / 3; }
constexpr int g2_slot(int t) { return t < 27 ? t / 3 : t - 27; }
constexpr int g2_rstep(int r) { return r + r / 2; }
constexpr int G2_NR = 17 - NE;
constexpr int g2_qreg(int q) { return q < 9 - NE ? NE + q : NE + q + 1; }
constexpr int g2_qstep(int q) { return q < G2_NR ? g2_rstep(g2_qreg(q)) : 3 * (q - G2_NR) + 2; }
constexpr int g2_qslot(int q) { return (q + 18 - G2_NR) % 9; }
constexpr int g1_quad(int m, int k) { return (m == 0 ? 0 : m == 3 ? 12 : 12 + 12 * m) + k; }
constexpr int e_quad(int e, int c) { return (e < 4 ? 48 + 4 * e : 24 + 4 * (e - 4)) + c; }
constexpr int e_unit(int e) { return e == 0 || e == 9 ? -1 : e == 1 || e == 8 ? 8 : e < 4 ? e : e < 6 ? e - 4 : e - 2; }
constexpr int e_read(int G) {
  if (G >= 648 && G < 660) return G - 648;
  const int k = G >= 738 ? G - 738 + 60 : G - 666, j = k % 12;
  return G < 666 || (G >= 726 && G < 738) || k >= 12 * (NE - 3) || j == 2 || j > 4 ? -1 : 4 * (3 + k / 12) + j - (j > 2);
}
constexpr int e_issued(int G) {
  const int a = G <= 625 ? 0 : (G - 622) / 4, b = G <= E_T9 ? 0 : (G - E_T9 + 5) / 6;
  return (a > 4 * NE - 16 ? 4 * NE - 16 : a) + (b > 4 ? 4 : b);
}
constexpr int x_off(int j) { return ((0x876541032ull >> (4 * (j >> 2))) & 15) * 4096 + (j & 3) * 1024; }
#define G2_TEND 29
#define G2_TH 32
#define G2_TP 4
#define G2_LATE 0
#define G2_BURST 0
#define G2_B0 4
#define G1_BURST 0
#define G2_DMAX 4
constexpr int G2_WP = 14;
constexpr int g2_aend(int t) { return t > 35 ? 162 : 9 * (t / 2) + ((t & 1) ? 9 : 5); }
constexpr int G2_S0 = 12;
constexpr int g2_wp(int t) { return t + 1 < G2_S0 ? 10 : t < 33 ? G2_WP : 0; }
struct G2Sched { short op[36][20]; short wait[36], issued[36], tchk; };
constexpr G2Sched g2_make() {
  G2Sched s{};
  int apos[162] = {}, bpos[36] = {}, seq = g2_aend(2) + G2_LATE, na = g2_aend(2), ppos = 0, done = -1;
  for (int i = 0; i < na; ++i) apos[i] = i;
  for (int t = 0; t < 36; ++t) {
    int list[20] = {}, n = 0;
    if (t == G2_TP) list[n++] = 5000;
    if (t >= 3 && t < 30) { const int p = 4 * ((t - 3) / 3) + (t % 3 == 0 ? 0 : t % 3 + 1); list[n++] = 2000 + p; if (t % 3 == 0) list[n++] = 2000 + p + 1; }
    const int d = 3 + (t + 1) / 2 < G2_DMAX ? 3 + (t + 1) / 2 : t >= G2_TEND ? G2_DMAX + 1 : G2_DMAX;
    while (na < g2_aend(t + d) && n < 9) list[n++] = 1000 + na++;
    if (na > g2_aend(G2_S0 - 1) && !s.tchk) s.tchk = (short)(done > ppos ? t : -1);
    s.issued[t] = (short)na;
    if (t == G2_TH) for (int i = 0; i < 5; ++i) list[n++] = 3000 + i;
    if (t == 20) list[n++] = 4000;
    for (int m = 0, k = 0; m < 20; ++m) {
      if (m == g2_wp(t) && t < 35) {
        int need = apos[g2_aend(t + 1) - 1];
        const int tw = t >= 32 ? 35 : t + 1;
        if (tw >= 27 && bpos[4 * (tw - 27) + 3] > need) need = bpos[4 * (tw - 27) + 3];
        s.wait[t] = (short)(seq - 1 - need);
        done = need;
      }
      if (k < n && m == (G2_BURST ? (G2_B0 + G2_BURST * n <= 20 ? G2_B0 + G2_BURST * k : k) : (k * 20) / n)) {
        const int o = list[k++];
        s.op[t][m] = (short)o;
        if (o >= 2000 && o < 3000) bpos[o - 2000] = seq; else if (o < 2000) apos[o - 1000] = seq; else if (o == 5000) ppos = seq;
        ++seq;
      }
    }
  }
  return s;
}
constexpr G2Sched G2S = g2_make();
static_assert(G2S.tchk > 0, "");

constexpr bool g1_isR(int t) { return t == 2 || t == 5; }
constexpr int g1_lidx(int t) { return t < 2 ? t : t < 5 ? t - 1 : t - 2; }
constexpr int g1_kb(int t) { return t == 2 ? 0 : t == 5 ? 1 : t >= 14 ? t - 12 : 4 + g1_lidx(t); }
constexpr int g1_nb(int t) { return t < 1 ? 0 : t >= 7 ? 8 : ((t - 1) % 3 == 2 ? 4 : 6); }
constexpr int g1_b0(int t) { return t >= 7 ? 32 + 8 * (t - 7) : 16 * ((t - 1) / 3) + 6 * ((t - 1) % 3); }
constexpr int g1_ops(int t) { return t == 0 ? 6 * (4 - G1_A0) : g1_nb(t) + 6; }
constexpr int PF1_E1 = (PF1_A + PF1_B) * 2048, PF1_E2 = PF1_E1 + PF1_C * 1280;

__global__ __launch_bounds__(256, 1) void k_all(const u16* __restrict__ x, const u16* __restrict__ logA, const u16* __restrict__ Aim,
                                                const u16* __restrict__ Cre, const u16* __restrict__ Cim, const u16* __restrict__ logdt,
                                                const u16* __restrict__ Dsk, const u16* __restrict__ w1, const u16* __restrict__ b1,
                                                const u16* __restrict__ w2, const u16* __restrict__ b2, char* __restrict__ ws, u16* __restrict__ out) {
  __shared__ __attribute__((aligned(16))) char smem_[K_LDS];
  char* const smem = smem_ + B0;
  const int tid = threadIdx.x, lane = tid & 63, wid = __builtin_amdgcn_readfirstlane(tid >> 6);
  const int xc = blockIdx.x & 7, idx = blockIdx.x >> 3;
  TSDECL;
  TS(0);
  const rsrc_t rw = mk_rsrc(ws, WS_BYTES);
  const i32x4 sw = mk_srd(ws, WS_BYTES), sb1 = mk_srd(w1, L_ * F_ * 2), sb2 = mk_srd(w2, L_ * F_ * 2);
  const unsigned ldsT = (unsigned)(size_t)smem_, lds0 = ldsT + B0, ring = lds0 + wid * G_WS;
  const int g = lane >> 4, r16 = lane & 15, q4 = r16 >> 2, p4 = lane & 3;
  const int tb = xc * 24 + idx + (168 - 16 * xc) * ((idx + 8) >> 5);
  const int tok = wid * 2 + (lane >> 5), ll = lane & 31;
  const bool cv_on = idx < 24;
  const int d0 = xc * 72 + idx * 3;
  const unsigned w1c0 = W1L == 10 ? ((27 * xc) / 6) * 256 : xc * 1152;

  const int lin1 = xc * 27 + (SWAP1 && !(xc & 1) ? (SWAP1 == 1 ? max(26 - idx, 0) : (idx + 16) % 27) : idx), mt = lin1 % 6, nt1 = min(lin1 / 6, 35);
  const int db = idx & 7, lead = (idx >> 3) ^ (SWAP2 && idx >= 16 && db < 3), nt2 = xc * 4 + lead, odd = db & 1;
  unsigned xw[9], pp[18], bias2, pf0[3];
  u32x2 ep;
  u32x4 bw;
  sfor<9>([&](auto I) { gld32<128 * I>(xw[I], ((tb * 8 + tok) * (D_ / 2) + ll) * 4, x); });
  bload2(ep, SY_EPOCH, sw);
  gld128(bw, (nt1 * 128 + wid * 32 + 8 * g) * 2, b1);
  gld16<0>(bias2, (nt2 * 64 + wid * 16 + r16) * 2, b2);
  if (cv_on) {
    const unsigned o = (d0 * N_ + (wid & 1) * 64 + lane) * 2;
    sfor<3>([&](auto C) {
      gld16<256 * C>(pp[6 * C], o, logA); gld16<256 * C>(pp[6 * C + 1], o, Aim); gld16<256 * C>(pp[6 * C + 2], o, Cre); gld16<256 * C>(pp[6 * C + 3], o, Cim);
      gld16<2 * C>(pp[6 * C + 4], d0 * 2, logdt); gld16<2 * C>(pp[6 * C + 5], d0 * 2, Dsk);
    });
  } else {
    u64 pc; unsigned len;
    asm volatile("s_getpc_b64 %0\n.Lk_all_pc:\n\ts_mov_b32 %1, .Lk_all_end-.Lk_all_pc" : "=s"(pc), "=s"(len));
    const i32x4 sc = mk_srd((const void*)pc, idx == 31 ? len : 0u);
    touch(pf0[0], tid * 128, sc); touch(pf0[1], (256 + tid) * 128, sc);
    touch(pf0[2], slab_off<W1L>((idx - 24) * 256 + tid, PF0 * 2048, F_ * 2, w1c0), sb1);
  }
  unsigned kad[11], ugo[3], ulo[3], ufo[3], kto[4], zoff;
  {
#pragma unroll
    for (int k = 0; k < 6; ++k) {
      const int j0 = 2047 - (8 * tid + 2 * k);
      const unsigned pr = pk2((float)j0, (float)(j0 - 1));
      kad[2 * k] = lds0 + KG_T + 8 + 2 * (int)bflo(pr);
      if (k < 5) kad[2 * k + 1] = lds0 + KG_T + 8 + 2 * (int)bfhi(pr);
    }
#pragma unroll
    for (int k = 0; k < 3; ++k) {
      const unsigned i = tid + 256 * k, t3 = i / 3, c = i - 3 * t3;
      ugo[k] = WS_UB + t3 * (D_ * 16) + (d0 + c) * 16; ulo[k] = c * CV_TB + CV_U + (29 + t3) * 16; ufo[k] = SY_FLAG + xc * 2048 + t3 * 8;
    }
    int pb, o1, o2;
    if (wid == 0) { pb = 4 * (g & 1) + 128 * (g >> 1); o1 = 1; o2 = 2; }
    else if (wid == 1) { pb = g < 2 ? 256 + 128 * g : 256 * g; o1 = g < 2 ? 2 : 4; o2 = g < 2 ? 4 : 128; }
    else { pb = 1024 + 512 * g; o1 = 128; o2 = 256; }
    pb += 8 * r16;
    kto[0] = KG_T + 8 + 2 * min(pb, 2056); kto[1] = KG_T + 8 + 2 * min(pb + o1, 2056); kto[2] = KG_T + 8 + 2 * min(pb + o2, 2056); kto[3] = KG_T + 8 + 2 * min(pb + o1 + o2, 2056);
    const int zc = min(tid / 72, 2), zi = min(tid - 72 * zc, 71);
    zoff = zc * CV_TB + (zi < 12 ? (zi & 3) * CV_KP + (256 + (zi >> 2)) * 16 : CV_U + (zi & 1) * CV_UP + ((zi - 12) >> 1) * 16);
#pragma unroll
    for (int k = 0; k < 11; ++k) asm volatile("" : "+v"(kad[k]));
    asm volatile("" : "+v"(ugo[0]), "+v"(ugo[1]), "+v"(ugo[2]), "+v"(ulo[0]), "+v"(ulo[1]), "+v"(ulo[2]), "+v"(kto[0]), "+v"(kto[1]), "+v"(kto[2]), "+v"(kto[3]), "+v"(zoff), "+v"(ufo[0]), "+v"(ufo[1]), "+v"(ufo[2]));
  }
  const unsigned tro = q4 * 128 + g * 32 + p4 * 8;
  auto chunk = [&](unsigned base, int blk) { return cat(trd(base + tro + blk * 1024), trd(base + tro + blk * 1024 + 512)); };
  if (cv_on) VMCNT(21); else VMCNT(6);
  pin9(xw);
  if (blockIdx.x == 255) {
    __builtin_amdgcn_raw_buffer_store_b32(0u, rw, SY_CNT + tid * 256, 0, SC1);
    if (tid < 128) __builtin_amdgcn_raw_buffer_store_b32(0u, rw, SY_CNT + (256 + tid) * 256, 0, SC1);
  }
  {
    f32x2 v[9];
    unsigned o[9];
#pragma unroll
    for (int i = 0; i < 9; ++i) v[i] = unpk(xw[i]);
    ln9(v, o, [](auto, float&) { SB0; });
    set_m0(ldsT + wid * 256);
    sfor<9>([&](auto I) { put_row<1024 * I>(o[I]); });
    if (cv_on || blockIdx.x == 255) VMCNT(0); else VMCNT(3);
    pin9(pp); pin9(pp + 9);
    asm volatile("" : "+v"(bw), "+v"(bias2), "+v"(ep));
    LDS_BARRIER();
    const unsigned off = WS_UB + tb * (D_ * 16) + tid * 16;
    __builtin_amdgcn_raw_buffer_store_b128(chunk(ldsT, wid), rw, off, 0, SC1);
    __builtin_amdgcn_raw_buffer_store_b128(chunk(ldsT, wid + 4), rw, off + 4096, 0, SC1);
    if (wid == 0) __builtin_amdgcn_raw_buffer_store_b128(chunk(ldsT, 8), rw, off + 8192, 0, SC1);
  }
  const u64 tag64 = (((u64)ep.y << 32) | ep.x) * 0x9E3779B97F4A7C15ull + 0xD1B54A32D192ED03ull;
  const u32x2 tag = {(unsigned)tag64, (unsigned)(tag64 >> 32)};
  TS(1);
  const unsigned sink = ring + 35 * 1024;
  auto signal_u = [&]() {
    __builtin_amdgcn_s_barrier();
    if (tid < 8) __builtin_amdgcn_raw_buffer_store_b64(tag, rw, SY_FLAG + tid * 2048 + tb * 8, 0, SC1);
  };
  auto await_u = [&]() {
#pragma nounroll
    for (int spin = 0; spin < (1 << 16); ++spin) {
      const u32x4 f0 = __builtin_amdgcn_raw_buffer_load_b128(rw, SY_FLAG + xc * 2048 + lane * 16, 0, SC1);
      const u32x4 f1 = __builtin_amdgcn_raw_buffer_load_b128(rw, SY_FLAG + xc * 2048 + 1024 + lane * 16, 0, SC1);
      const unsigned d = ((f0.x ^ tag.x) | (f0.y ^ tag.y) | (f0.z ^ tag.x) | (f0.w ^ tag.y)) | ((f1.x ^ tag.x) | (f1.y ^ tag.y) | (f1.z ^ tag.x) | (f1.w ^ tag.y));
      if (__all(d == 0u)) break;
      __builtin_amdgcn_s_sleep(POLL_SLEEP);
      asm volatile("" ::: "memory");
    }
  };

  const bool g1_on = idx < 27;
  unsigned vb1[4], rb1[4];
#pragma unroll
  for (int j = 0; j < 4; ++j) vb1[j] = ((4 * j + g) * (F_ * 2) + ((r16 ^ (g << 2)) << 4) - j * 1024) | (OOB * ((idx + 5) >> 5));
#pragma unroll
  for (int m = 0; m < 4; ++m) rb1[m] = ring + 256 * (8 * g + q4) + 64 * (((m + wid) & 3) ^ q4) + 16 * p4;
  const unsigned b1_base = nt1 * 256 + wid * 32 * (F_ * 2);
#define G1_LDB(slot, kb, j) bdma<(slot) * 8192 + ((j) >> 2) * 4096, ((j) & 3) * 1024>(ring, vb1[(j) & 3], sb1, b1_base + (g1_blk(kb, nt1) * 128 + ((j) >> 2) * 16) * (F_ * 2))
#define G1_HALF(slot, m, c, h) trd(rb1[m] + (slot) * 8192 + 8 * (c) + 1024 * (h))
  const unsigned ao = WS_AT + 2 * tid * (L_ * 2) + tb * 16, ao1 = tid < 32 ? ao + 512 * (L_ * 2) : OOB;
  u32x4 ga[4];
#define G1_LDR(i) q_load<32 + (i), ((i) & 3) * 1024>( vb1[(i) & 3], sb1, b1_base + (g1_blk((i) >> 3, nt1) * 128 + (((i) >> 2) & 1) * 16) * (F_ * 2))
  auto gather = [&]() { bload_r<0>(ga[0], ao, sw, 0u); bload_r<0>(ga[1], ao, sw, ROWB); bload_r<0>(ga[2], ao1, sw, 0u); bload_r<0>(ga[3], ao1, sw, ROWB); };

  if (cv_on) {
    unsigned pfk = 0;
    {
      float zl[3], zv[3];
      const unsigned pfko = slab_off<W1L>(idx * 256 + tid, 6144, F_ * 2, w1c0);
      f32x2 G0[3];
#pragma unroll
      for (int c = 0; c < 3; ++c) {
        const float dt = rbf(__expf(bflo(pp[6 * c + 4])));
        const float zr = -rbf(__expf(bflo(pp[6 * c]))) * dt, zi = bflo(pp[6 * c + 1]) * dt;
        zl[c] = zr * 1.4426950408889634f; zv[c] = zi * 0.15915494309189535f;
        G0[c] = f32x2{2.0f * (bflo(pp[6 * c + 2]) * dt), -2.0f * (bflo(pp[6 * c + 3]) * dt)};
      }
#define PUT_A(row, v) put_row<B0 + (row) * KG_PITCH>(v)
#define PUT_B(row, v) put_row<B0 + (41 + (row)) * KG_PITCH>(v)
      if (wid < 2) {
        sfor<3>([&](auto C) {
          constexpr int c = C;
          f32x2 E[8], G[6];
          const f32x2 E128 = cexp_p(zl[c], -zv[c], 128.f);
          E[1] = cexp_p(zl[c], -zv[c], 1.f); E[2] = cexp_p(zl[c], -zv[c], 2.f); E[4] = cexp_p(zl[c], -zv[c], 4.f);
          G[0] = G0[c]; G[4] = cmul(cexp_p(zl[c], -zv[c], 512.f), G0[c]);
          G[1] = cmul(G[0], E128); E[3] = cmul(E[1], E[2]); G[5] = cmul(G[4], E128); E[5] = cmul(E[1], E[4]);
          G[2] = cmul(G[1], E128); E[6] = cmul(E[2], E[4]); E[7] = cmul(E[3], E[4]); G[3] = cmul(G[2], E128);
          set_m0(ldsT + c * KG_CH + wid * 256);
          f32x2 v[28], Gs[6];
          sfor<6>([&](auto t) { Gs[t] = f32x2{-G[t].y, G[t].x}; });
          sfor<28>([&](auto r) { if constexpr (kg_e(r) > 0) v[r] = E[kg_e(r)].xx * G[kg_g(r)]; });
          SB0;
          sfor<28>([&](auto r) { if constexpr (kg_e(r) > 0) v[r] = __builtin_elementwise_fma(E[kg_e(r)].yy, Gs[kg_g(r)], v[r]); else v[r] = G[kg_g(r)]; });
          SB0;
          unsigned p[28];
          sfor<29>([&](auto r) { if constexpr (r < 28) p[r] = pkv(v[r]); if constexpr (r > 0) PUT_A(r - 1, p[r - 1]); SB0; });
          if constexpr (c == SIG_C) { VMCNT(0); signal_u(); if (PFK) touch_nt(pfk, pfko, sb1); TS(2); }
        });
      } else {
        sfor<3>([&](auto C) {
          constexpr int c = C;
          const f32x2 E128 = cexp_p(zl[c], -zv[c], 128.f), E4 = cexp_p(zl[c], -zv[c], 4.f), E8 = cexp_p(zl[c], zv[c], 8.f);
          f32x2 b0 = E8, b1v = cexp_p(zl[c], zv[c], 64.f);
          f32x2 g0 = cmul(cexp_p(zl[c], -zv[c], 768.f), G0[c]), g1 = cmul(cexp_p(zl[c], -zv[c], 1536.f), G0[c]);
          const f32x2 E8s = {-E8.y, E8.x}, E128s = {-E128.y, E128.x}, E4s = {-E4.y, E4.x};
          set_m0(ldsT + c * KG_CH + (wid & 1) * 256);
          PUT_B(0, 0x00003f80u);
          sfor<8>([&](auto j) {
            f32x2 t0, t1, t2, t3, t4;
            if constexpr (j < 6) t0 = b0.xx * E8;
            if constexpr (j < 7) t1 = b1v.xx * E8;
            if constexpr (j < 5) t2 = g0.xx * E128;
            if constexpr (j < 4) t3 = g1.xx * E128;
            if constexpr (j < 2) t4 = g0.xx * E4;
            const unsigned q0 = pkv(b0), q1 = pkv(b1v), q2 = pkv(g0), q3 = pkv(g1);
            SB0;
            if constexpr (j < 2) t4 = __builtin_elementwise_fma(g0.yy, E4s, t4);
            if constexpr (j < 7) PUT_B(j + 1, q0);
            if constexpr (j < 6) b0 = __builtin_elementwise_fma(b0.yy, E8s, t0);
            SB0;
            PUT_B(8 + j, q1);
            if constexpr (j < 7) b1v = __builtin_elementwise_fma(b1v.yy, E8s, t1);
            SB0;
            if constexpr (j < 2) PUT_A(28 + 2 * j, q2); else if constexpr (j < 6) PUT_A(30 + j, q2);
            if constexpr (j < 5) g0 = __builtin_elementwise_fma(g0.yy, E128s, t2);
            SB0;
            if constexpr (j < 5) PUT_A(36 + j, q3);
            if constexpr (j < 4) g1 = __builtin_elementwise_fma(g1.yy, E128s, t3);
            if constexpr (j < 2) PUT_A(29 + 2 * j, pkv(t4));
            SB0;
          });
          if constexpr (c == SIG_C) { VMCNT(0); signal_u(); if (PFK) touch_nt(pfk, pfko, sb1); }
        });
      }
    }
    u32x2 fl[3];
#pragma unroll
    for (int k = 0; k < 3; ++k) bload2(fl[k], ufo[k], sw);
    LDS_BARRIER();
    TS(3);
    if (wid < 3) {
      const char* ap = smem + min(wid * 16 + r16, 40) * KG_PITCH + g * 16;
      const char* bp = smem + KG_B + r16 * KG_PITCH + g * 16;
      f32x4 a0[3] = {};
      u32x4 fa[2][3][4], fb[2][3][4];
#pragma unroll
      for (int h = 0; h < 2; ++h) {
#pragma unroll
        for (int s = 0; s < 4; ++s)
#pragma unroll
          for (int c = 0; c < 3; ++c) { fa[h][c][s] = *(const u32x4*)(ap + c * KG_CH + (4 * h + s) * 64); fb[h][c][s] = *(const u32x4*)(bp + c * KG_CH + (4 * h + s) * 64); }
        if (h == 1) {
#pragma unroll
          for (int s = 0; s < 4; ++s)
#pragma unroll
            for (int c = 0; c < 3; ++c) a0[c] = mfma16(fa[0][c][s], fb[0][c][s], a0[c]);
#pragma unroll
          for (int k = 0; k < 12; ++k) { __builtin_amdgcn_sched_group_barrier(0x100, 2, 0); __builtin_amdgcn_sched_group_barrier(0x8, 1, 0); }
        }
        SB0;
      }
#pragma unroll
      for (int s = 0; s < 4; ++s)
#pragma unroll
        for (int c = 0; c < 3; ++c) a0[c] = mfma16(fa[1][c][s], fb[1][c][s], a0[c]);
#pragma unroll
      for (int c = 0; c < 3; ++c) {
        a0[c].x += tid == 0 ? bflo(pp[6 * c + 5]) : 0.f;
        const unsigned k01 = pkv(a0[c].xy), k23 = pkv(a0[c].zw);
        *(u16*)(smem + kto[0] + c * KG_TP) = (u16)k01; *(u16*)(smem + kto[1] + c * KG_TP) = (u16)(k01 >> 16);
        *(u16*)(smem + kto[2] + c * KG_TP) = (u16)k23; *(u16*)(smem + kto[3] + c * KG_TP) = (u16)(k23 >> 16);
      }
    } else {
      if (lane < 3) *(u32x2*)(smem + KG_T + lane * KG_TP) = u32x2{0u, 0u};
    }
#pragma nounroll
    for (int spin = 0; spin < (1 << 16); ++spin) {
      asm volatile("s_waitcnt vmcnt(0)" : "+v"(fl[0]), "+v"(fl[1]), "+v"(fl[2]) :: "memory");
      const unsigned d = ((fl[0].x ^ tag.x) | (fl[0].y ^ tag.y)) | ((fl[1].x ^ tag.x) | (fl[1].y ^ tag.y)) | ((fl[2].x ^ tag.x) | (fl[2].y ^ tag.y));
      if (__all(d == 0u)) break;
      __builtin_amdgcn_s_sleep(POLL_SLEEP);
#pragma unroll
      for (int k = 0; k < 3; ++k) bload2r(fl[k], ufo[k], sw);
    }
    VMCNT(0);
    keep(pfk);
    u32x4 ug[3];
#pragma unroll
    for (int k = 0; k < 3; ++k) BLOAD(ug[k], ugo[k], sw, 0u, 0);
    const u32x4 none = {~0u, ~0u, ~0u, ~0u};
    const unsigned mk0 = WS_AT + d0 * (L_ * 2) + 16 * tid;
    auto side = [&](auto K0, auto N) {
      SB0;
      sfor<N>([&](auto J) {
        constexpr int k = K0 + J, i = k - NMARK;
        if constexpr (k < 3) bstore(none, mk0 + k * 4096, sw);
        else if constexpr (k < 6) bstore(none, mk0 + (WS_HF - WS_AT) + (k - 3) * 4096, sw);
        else if constexpr (k < 9) bstore(none, mk0 + (WS_GF - WS_AT) + (k - 6) * 4096, sw);
        else if constexpr (i < 32) G1_LDR(i);
      });
      SB0;
    };
#define SIDE(k0, n) side(IC<(k0)>{}, IC<(n)>{})
    SIDE(0, 3);
    LDS_BARRIER();
    TS(4);
    {
      if (blockIdx.x == 0 && tid == 0) __builtin_amdgcn_raw_buffer_store_b64(tag, rw, SY_EPOCH, 0, SC1);
      u16 kv[3][11];
#pragma unroll
      for (int c = 0; c < 3; ++c)
#pragma unroll
        for (int k = 0; k < 11; ++k) kv[c][k] = *(const LAS u16*)(size_t)(kad[k] + c * KG_TP);
      *(u32x4*)(smem + zoff) = u32x4{0u, 0u, 0u, 0u};
      SIDE(3, 2);
      sfor<3>([&](auto C) {
        constexpr int c = C;
        unsigned e[5], a[5];
#pragma unroll
        for (int k = 0; k < 5; ++k) { e[k] = kv[c][2 * k] | ((unsigned)kv[c][2 * k + 1] << 16); a[k] = kv[c][2 * k + 1] | ((unsigned)kv[c][2 * k + 2] << 16); }
        char* kp = smem + c * CV_TB + tid * 16;
        *(u32x4*)(kp + 3 * CV_KP) = u32x4{e[0], e[1], e[2], e[3]};
        *(u32x4*)(kp + 2 * CV_KP) = u32x4{a[0], a[1], a[2], a[3]};
        *(u32x4*)(kp + 1 * CV_KP) = u32x4{e[1], e[2], e[3], e[4]};
        *(u32x4*)(kp + 0 * CV_KP) = u32x4{a[1], a[2], a[3], a[4]};
        SIDE(5 + 2 * c, 2);
      });
      LDS_BARRIER();
      asm volatile("s_waitcnt vmcnt(11)" : "+v"(ug[0]), "+v"(ug[1]), "+v"(ug[2]) :: "memory");
#pragma unroll
      for (int k = 0; k < 3; ++k) {
        char* up = smem + ulo[k];
        *(u32x2*)(up + 8) = ug[k].xy; *(u32x2*)(up + 16) = ug[k].zw;
        *(u32x4*)(up + CV_UP) = ug[k];
      }
    }
    LDS_BARRIER();
    TS(5);
    {
      const char* ap0 = smem + (r16 & 3) * CV_KP + 2 * (2064 - 256 * 7 - 8 * (r16 >> 2) - 8 * g - 64 * wid - 32);
      const char* bp0 = smem + CV_U + (r16 & 1) * CV_UP + 2 * (232 + 32 * (r16 >> 1) - 64 * wid - 32 + 24 - 8 * g);
      char* pw = smem + CV_P + wid * 8192 + ((r16 & 1) + 2 * (g ^ ((r16 >> 1) & 3)) + 8 * (r16 >> 1)) * 16;
      unsigned pr = lds0 + CV_P + (tid >> 5) * 1024 + (2 * ((tid & 3) ^ ((tid >> 2) & 3)) + 8 * ((tid & 31) >> 2)) * 16;
      unsigned c0a = tid < 8 ? SY_CNT + (tid * 16 + ((xc * 24 + idx) & 15)) * 256 : OOB, c0p = cnt_off(0, xc, 0, 16, tid), one = 1;
      u32x4 FA[2][8], FB[2][8], ov;
      f32x4 acc[2][8], py[8];
      auto rd = [&](int u, int j) {
        const int o = (u >> 1) * CV_TB + 64 * (1 - (u & 1)), k = j >> 1;
        if (j & 1) FB[u & 1][k] = *(const u32x4*)(bp0 + o + 512 * k); else FA[u & 1][k] = *(const u32x4*)(ap0 + o + 512 * (7 - k));
      };
      auto wp = [&](int c, int k) { pin_vf(acc[c & 1][k]); *(f32x4*)(pw + (c & 1) * 32768 + k * 1024) = acc[c & 1][k]; };
      auto rp = [&](int c, int j) { py[j] = *(const LAS f32x4*)(size_t)(pr + (c & 1) * 32768 + (j >> 1) * 8192 + (j & 1) * 16); };
      auto fin = [&](int c, int j) {
        if (j < 12) {
          const int d = j < 8 ? (j & 1) + (j & 4) : j & 1, e = j & 2, sft = j < 8 ? 2 : 4;
          float x0 = py[d][e], x1 = py[d][e + 1];
          fadd(x0, py[d + sft][e]); fadd(x1, py[d + sft][e + 1]);
          py[d][e] = x0; py[d][e + 1] = x1;
        } else if (j < 14) { ov[j - 12] = pkv(j & 1 ? py[0].zw : py[0].xy); ov[j - 10] = pkv(j & 1 ? py[1].zw : py[1].xy); asm volatile("" : "+v"(ov)); }
        else if (j < 16) { ov[j - 14] = min(ov[j - 14], 0xfffffffeu); ov[j - 12] = min(ov[j - 12], 0xfffffffeu); asm volatile("" : "+v"(ov)); }
        else __builtin_amdgcn_raw_buffer_store_b128(ov, rw, mk0 + c * 4096, 0, SC1);
      };
      sfor<16>([&](auto J) { rd(0, J); });
      SB0;
      asm volatile("" : "+v"(pr), "+v"(c0a), "+v"(c0p), "+v"(one));
      SIDE(11, 1);
      unsigned pf = 0;
      sfor<216>([&](auto NN) {
        constexpr int n = NN, u = n / 36, j = n % 36, c1 = cv_a(j), c2 = cv_b(j);
        if constexpr (n == 108) {
          VMCNT(12 + 108 / 5 - NMARK);
          LDS_BARRIER();
          add_one(one, c0a, sw);
          TS(21);
        }
        if constexpr (n == 180) {
          LDS_BARRIER();
          TS(22);
        }
        if constexpr (!(u & 1) && cv_first(j)) MFMA_V0(acc[(u >> 1) & 1][c1 + c2], FA[u & 1][c1], FB[u & 1][c2]);
        else MFMA_V(acc[(u >> 1) & 1][c1 + c2], FA[u & 1][c1], FB[u & 1][c2]);
        if constexpr (u != 3 && u != 5) {
          if constexpr (!(j & 1)) { if constexpr (j < 32) rd(u + 1, j / 2); }
          else if constexpr (u >= 2 && j >= 3 && j < 19) wp(u / 2 - 1, (j - 3) / 2);
        } else {
          if constexpr (j < 8) rp(u / 2 - 1, j);
          if constexpr (u == 3 && j < 16) rd(4, j);
          if constexpr (j >= 16 && j < 33) fin(u / 2 - 1, j - 16);
          if constexpr (u == 5 && cv_done(j) >= 0) wp(2, cv_done(j));
        }
        if constexpr (n % 5 == 4) SIDE(12 + n / 5, 1);
        if constexpr (n == 191) poll_issue(pf, c0p, sw);
        SB0;
      });
      wp(2, 6);
      asm volatile("s_nop 7" ::: "memory");
      wp(2, 7);
      LDS_BARRIER();
      TS(23);
      sfor<8>([&](auto J) { rp(2, J); });
      SB0;
      py[0] = (py[0] + py[2]) + (py[4] + py[6]); py[1] = (py[1] + py[3]) + (py[5] + py[7]);
      sfor<5>([&](auto J) { fin(2, 12 + J); });
      TS(6);
      asm volatile("s_waitcnt vmcnt(2)" : "+v"(pf) :: "memory");
      await(rw, 0, xc, 0, 16, 12, wid == 0 && !__all(pf == (lane < 16 ? 12u : 0u)), lane);
    }
  } else {
    VMCNT(0);
    signal_u();
    keep(pf0[0]); keep(pf0[1]); keep(pf0[2]);
    warm<W1L>(sink, sb1, F_ * 2, w1c0, (PF0 + 3 * PFK) * 2048, PF1_A * 2048, (idx - 24) * 256 + tid, 2048);
    if (wid == 0) await_u();
    warm<W1L>(sink, sb1, F_ * 2, w1c0, PF1_A * 2048, PF1_E1, (idx - 24) * 256 + tid, 2048);
    if (g1_on) { sfor<32>([&](auto I) { G1_LDR(I); }); VMCNT(0); }
    else warm<W1L>(sink, sb1, F_ * 2, w1c0, PF1_E1, PF1_E2, (idx - 27) * 256 + tid, 1280);
  }
  TS(7);

  u32x4 BR[4][8];
  TS(8);
  {
    auto ride = [&](auto P) { SB0; G1_LDB(P / 8, 4 + P / 8, P % 8); SB0; };
    asm volatile("" : "=v"(ga[0]), "=v"(ga[1]), "=v"(ga[2]), "=v"(ga[3]));
    sfor<24>([&](auto I) {
      constexpr int k = I / 8 - 1, j = I % 8;
      if constexpr (I == GATHER_AT) { if (cv_on) gather(); }
      if constexpr (!(I & 1)) ride(IC<I / 2>{});
      if constexpr (I >= 8) { BR[k][j] = cat(G1_HALF(2 + k, j >> 1, j & 1, 0), G1_HALF(2 + k, j >> 1, j & 1, 1)); SB0; }
      if constexpr (I < 16) q_put<32 + I, 16384 + I * 1024>(ring + lane * 16);
    });
    sfor<4>([&](auto I) { ride(IC<12 + I>{}); });
    if (cv_on) VMCNT(16 - (GATHER_AT + 1) / 2);
    else {
      if (wid == 0) {
#pragma nounroll
        for (int spin = 0; spin < (1 << 16); ++spin) {
          const unsigned c = __builtin_amdgcn_raw_buffer_load_b32(rw, cnt_off(0, xc, 0, 16, lane), 0, SC1);
          const unsigned f = __builtin_amdgcn_raw_buffer_load_b32(rw, WS_AT + (9 * lane + 2) * (L_ * 2) + tb * 16, 0, SC1);
          if (__all(c == (lane < 16 ? 12u : 0u) && f != ~0u)) break;
          __builtin_amdgcn_s_sleep(SPARE_SLEEP);
          asm volatile("" ::: "memory");
        }
      }
      asm volatile("" ::: "memory");
      __builtin_amdgcn_s_barrier();
      gather();
      if (g1_on) VMCNT(0);
      else { warm<W1L, 63>(sink, sb1, F_ * 2, w1c0, PF1_E2, PF1_E2 + PFN * 1280, (idx - 27) * 256 + tid, 5 * 256); VMCNT(PFN); }
    }
    static_assert(PF1_E2 + PFN * 1280 <= L_ * W1L, "");
#pragma nounroll
    for (int spin = 0; spin < (1 << 16); ++spin) {
      asm volatile("" : "+v"(ga[0]), "+v"(ga[1]), "+v"(ga[2]), "+v"(ga[3]) :: "memory");
      if (__all(umax4(ga[0], umax4(ga[1], umax4(ga[2], umax4(ga[3], 0u)))) != ~0u)) break;
      __builtin_amdgcn_s_sleep(POLL_SLEEP);
      gather();
      VMCNT(0);
    }
    asm volatile("" : "+v"(ga[0]), "+v"(ga[1]), "+v"(ga[2]), "+v"(ga[3]) :: "memory");
    {
      unsigned* tp = (unsigned*)(smem_ + (tid >> 5) * 1024 + ll * 4);
#pragma unroll
      for (int k = 0; k < 4; ++k) { tp[64 * k] = perm_lo(ga[0][k], ga[1][k]); tp[64 * k + 32] = perm_hi(ga[0][k], ga[1][k]); }
      if (tid < 32) {
#pragma unroll
        for (int k = 0; k < 4; ++k) { tp[2048 + 64 * k] = perm_lo(ga[2][k], ga[3][k]); tp[2048 + 64 * k + 32] = perm_hi(ga[2][k], ga[3][k]); }
      }
    }
    LDS_BARRIER();
    TS(9);
    f32x2 v[9];
    unsigned o[9];
#pragma unroll
    for (int i = 0; i < 9; ++i) { o[i] = pkv(unpk(xw[i]) + unpk(*(const unsigned*)(smem_ + 1024 * i + tid * 4))); v[i] = unpk(o[i]); }
    set_m0(ldsT + wid * 256);
    sfor<9>([&](auto I) { put_row<1024 * I>(o[I]); });
    ln9(v, o, [&](auto K, float& x) { asm volatile("" : "+v"(x)); ride(IC<16 + K>{}); });
#pragma unroll
    for (int i = 0; i < 9; ++i) v[i] = unpk(o[i]);
    ln9(v, o, [&](auto K, float& x) { asm volatile("" : "+v"(x)); ride(IC<24 + K>{}); });
    set_m0(ldsT + wid * 256);
    sfor<9>([&](auto I) { put_row<t2_off(I)>(__builtin_bit_cast(unsigned, __builtin_elementwise_min(__builtin_bit_cast(u16x2, o[I]), u16x2{0xfffe, 0xfffe}))); });
    LDS_BARRIER();
    {
      const unsigned off = WS_HF + ((tb >> 2) * 36 + wid * 4 + g) * 1024 + (tb & 3) * 256 + r16 * 16;
      __builtin_amdgcn_raw_buffer_store_b128(chunk(ldsT + 9216, wid), rw, off, 0, SC1);
      __builtin_amdgcn_raw_buffer_store_b128(chunk(ldsT + (wid == 3 ? t2_off(7) - 7168 : 9216), wid + 4), rw, off + 16384, 0, SC1);
      if (wid == 0) __builtin_amdgcn_raw_buffer_store_b128(chunk(ldsT + t2_off(8), 0), rw, off + 32768, 0, SC1);
      const unsigned ho = WS_H + (tb * 8 + (lane >> 3)) * (D_ * 2) + (lane & 7) * 16;
      const char* hp = smem_ + lane * 16;
      __builtin_amdgcn_raw_buffer_store_b128(*(const u32x4*)(hp + wid * 1024), rw, ho + wid * 128, 0, SC1);
      __builtin_amdgcn_raw_buffer_store_b128(*(const u32x4*)(hp + (wid + 4) * 1024), rw, ho + (wid + 4) * 128, 0, SC1);
      if (wid == 3) __builtin_amdgcn_raw_buffer_store_b128(*(const u32x4*)(hp + 8192), rw, ho + 1024, 0, SC1);
    }
    TS(10);
  }

  unsigned vb2[4];
#pragma unroll
  for (int j = 0; j < 4; ++j) vb2[j] = (8 * j + (lane >> 3)) * (L_ * 2) + (((lane & 7) ^ (2 * ((g & 1) | ((j & 1) << 1)))) << 4) - j * 1024;
  const unsigned b2_base = nt2 * 128 + wid * 32 * (L_ * 2);
  auto g2_kb = [&](int t) {
    if (G2_LEAD == 0) return t;
    const int tau = t / G2_LEAD, last = 36 / G2_LEAD - 1;
    return t + (tau < last && (tau & 3) == lead ? G2_LEAD : tau > 0 && ((tau - 1) & 3) == lead ? -G2_LEAD : 0);
  };
#define G2_LDB(slot, i, j) bdma<(slot) * 4096, (j) * 1024>(ring, vb2[j], sb2, b2_base + g2_kb(i) * 128 * (L_ * 2))
  const unsigned front = ldsT + wid * 4096;
  unsigned rb[4];
#pragma unroll
  for (int c = 0; c < 4; ++c) rb[c] = ring + 128 * (8 * g + q4) + 16 * ((2 * ((c + wid) & 3) + (p4 >> 1)) ^ (2 * ((q4 >> 1) | ((g & 1) << 1)))) + 8 * (p4 & 1);
  auto e_dma = [&](auto E, auto U, auto J) { bdma<(U < 0 ? 0 : U * 4096), J * 1024>(U < 0 ? front : ring, vb2[J], sb2, b2_base + g2_kb(g2_rstep(E)) * 128 * (L_ * 2)); };
  auto e_get = [&](auto E, auto U, auto C) { q_frag<e_quad(E, C), (U < 0 ? 0 : U * 4096)>(U < 0 ? rb[C] + (front - ring) : rb[C]); };

  if (g1_on) {
    __builtin_amdgcn_s_sleep(G1_DELAY);
    TS(11);
    const unsigned va0 = lane * 16, va1 = lane * 16 + 4096;
    const unsigned a_base = WS_HF + wid * 36864 + mt * 6144;
    u32x4 A[4][6];
#define G1_AOFF(t) (a_base + g1_blk(g1_kb(t), nt1) * (4 * 36864))
#define G1_LDA(t, f) BLOAD(A[(t) & 3][f], (f) < 4 ? va0 : va1, sw, G1_AOFF(t), ((f) & 3) * 1024)
#define G1_WAIT(t, n) wait6<(n)>(A[(t) & 3])
    unsigned mx, my, pf3 = 0;
    auto late = [&](auto GG) {
      if constexpr (GG >= PF3_T0 && (GG - PF3_T0) % PF3_DT == 0 && (GG - PF3_T0) / PF3_DT < 4 + PF3_X) {
        constexpr int k = (GG - PF3_T0) / PF3_DT;
        if (wid == (k & 3)) touch_nt(pf3, slab_off<4>((PF3_R0 + 432 * k) * 4 + idx * 64 + lane, F_ * 4, L_ * 2, xc * 512), sb2);
      }
    };
    auto scan = [&](auto T, auto K) {
      const u32x4& a = A[T & 3][K >> 1];
      if constexpr (K & 1) { my = K == 1 ? max(a.z, a.w) : umax3(my, a.z, a.w); asm volatile("" : "+v"(my)); }
      else { mx = K == 0 ? max(a.x, a.y) : umax3(mx, a.x, a.y); asm volatile("" : "+v"(mx)); }
      SB0;
    };
    auto check = [&](auto T) {
      if (__builtin_expect(__builtin_amdgcn_ballot_w64(max(mx, my) == ~0u) != 0, 0)) {
#pragma nounroll
        for (int spin = 0; spin < (1 << 16); ++spin) {
          __builtin_amdgcn_s_sleep(POLL_SLEEP);
          sfor<18>([&](auto I) { constexpr int t = T + I / 6, f = I % 6; if constexpr (t < 16 && (T > 0 || t < G1_A0)) bload_r<(f & 3) * 1024>(A[t & 3][f], f < 4 ? va0 : va1, sw, G1_AOFF(t)); });
          G1_WAIT(T, 0);
          sfor<12>([&](auto K) { scan(T, K); });
          if (__builtin_amdgcn_ballot_w64(max(mx, my) == ~0u) == 0) break;
        }
      }
    };
    sfor<6 * G1_A0>([&](auto I) { G1_LDA(I / 6, I % 6); });
    __builtin_amdgcn_s_barrier();
    TS(12);
    u32x2 Bh[2][4], Th[2], Bl[12];
    G1_WAIT(0, 6 * G1_A0 - 6);
    sfor<12>([&](auto K) { scan(IC<0>{}, K); });
    check(IC<0>{});
    TS(13); TC(0);
    sfor<4>([&](auto I) { Bh[0][I] = G1_HALF(0, 0, I >> 1, I & 1); });
    SB0;
    sfor<13>([&](auto TT) {
      constexpr int t = TT;
      constexpr bool R = g1_isR(t);
      constexpr int slot = g1_lidx(t) & 3, nops = g1_ops(t);
      sfor<48>([&](auto NN) {
        constexpr int n = NN, m = n / 12, c = (n / 6) & 1, f = n % 6, i12 = n % 12;
        if constexpr (n == 36) G1_WAIT(t + 1, t == 0 ? 12 : t == 1 ? 6 + g1_ops(1) : g1_ops(t - 1) + g1_ops(t));
        if constexpr (t == 0) q_mfma0<g1_quad(m, i12)>(cat(Bh[m & 1][2 * c], Bh[m & 1][2 * c + 1]), A[t & 3][f]);
        else if constexpr (!R) q_mfma<g1_quad(m, i12)>(cat(Bh[m & 1][2 * c], Bh[m & 1][2 * c + 1]), A[t & 3][f]);
        else q_mfma<g1_quad(m, i12)>(BR[g1_kb(t)][2 * m + c], A[t & 3][f]);
        if constexpr (i12 < 4) {
          if constexpr (m < 3) { if constexpr (!R) { Bh[(m + 1) & 1][i12] = G1_HALF(slot, m + 1, i12 >> 1, i12 & 1); SB0; } }
          else if constexpr (!g1_isR(t + 1)) { Bh[0][i12] = G1_HALF(g1_lidx(t + 1) & 3, t == 12 ? 1 : 0, i12 >> 1, i12 & 1); SB0; }
        } else if constexpr (m < 3) {
          constexpr int pl = m * 8 + i12 - 4, per = (nops + 2) / 3, k = G1_BURST == 1 ? pl : G1_BURST == 2 ? m * per + i12 - 4 : (pl * nops + 23) / 24;
          if constexpr (k < nops && (G1_BURST == 1 || (G1_BURST == 2 && i12 - 4 < per) || (!G1_BURST && (k * 24) / nops == pl)) && !(ABL & 2)) {
            if constexpr (t >= 11 && k < 8) { constexpr int e = 2 * (t - 11) + k / 4; e_dma(IC<e>{}, IC<e_unit(e)>{}, IC<k % 4>{}); }
            else if constexpr (k < g1_nb(t)) { constexpr int pc = g1_b0(t) + k; G1_LDB((pc >> 3) & 3, 8 + (pc >> 3), pc & 7); }
            else if constexpr (t == 0) G1_LDA(G1_A0 + k / 6, k % 6);
            else G1_LDA(t + 3, k - g1_nb(t));
          }
          if constexpr (t >= 11) {
            constexpr int r = t - 9;
            if constexpr (pl < 8) q_put<32 + 8 * r + pl, pl * 1024>(ring + lane * 16);
            else {
              constexpr int j = (pl - 8) >> 1;
              Th[pl & 1] = G1_HALF(0, j >> 1, j & 1, pl & 1);
              if constexpr (pl & 1) BR[r][j] = cat(Th[0], Th[1]);
              SB0;
            }
          }
        }
        if constexpr (n >= 36) scan(IC<t + 1>{}, IC<n - 36>{});
        late(IC<48 * t + n>{});
      });
      check(IC<t + 1>{});
    });
    sfor<4>([&](auto PP) {
      constexpr int ph = PP, m = (ph + 1) & 3;
      sfor<36>([&](auto NN) {
        constexpr int n = NN, t = 13 + n / 12, c = (n / 6) & 1, f = n % 6;
        constexpr int G = 624 + 36 * ph + n, er = e_read(G);
        if constexpr (ph == 0 && n == 0) G1_WAIT(14, 14);
        if constexpr (ph == 0 && n == 12) { check(IC<14>{}); G1_WAIT(15, e_issued(G)); }
        if constexpr (ph == 0 && n == 24) check(IC<15>{});
        if constexpr (ph == 0 && n == 22) __builtin_amdgcn_s_waitcnt(0xc07f);
        if constexpr (t == 13 && ph == 0) q_mfma<g1_quad(m, n % 12)>(cat(Bh[0][2 * c], Bh[0][2 * c + 1]), A[t & 3][f]);
        else if constexpr (t == 13) q_mfma<g1_quad(m, n % 12)>(cat(Bl[4 * ph - 4 + 2 * c], Bl[4 * ph - 3 + 2 * c]), A[t & 3][f]);
        else q_mfma<g1_quad(m, n % 12)>(BR[t - 12][2 * m + c], A[t & 3][f]);
        if constexpr (ph == 0 && n >= 1 && n < 13) { Bl[n - 1] = G1_HALF(3, (2 + (n - 1) / 4) & 3, ((n - 1) >> 1) & 1, (n - 1) & 1); SB0; }
        if constexpr (ph == 0 && n < 24) scan(IC<14 + n / 12>{}, IC<n % 12>{});
        if constexpr (ph > 0 && n % 3 == 2 && !(ABL & 128)) q_put<g1_quad(ph, n / 3), x_off((ph - 1) * 12 + n / 3)>(ring + lane * 16);
        if constexpr (er >= 0) {
          if constexpr (er >= 16 && er % 4 == 0 && !(ABL & 32)) VMCNT(e_issued(G) - (er - 12));
          if constexpr (!(ABL & 16)) e_get(IC<er / 4>{}, IC<e_unit(er / 4)>{}, IC<er % 4>{});
        }
        if constexpr (e_issued(G + 1) > e_issued(G)) {
          constexpr int e = 4 + e_issued(G) / 4 < NE ? 4 + e_issued(G) / 4 : 9;
          if constexpr (e >= 8 && e_issued(G) % 4 == 0) asm volatile("s_waitcnt lgkmcnt(8)" ::: "memory");
          if constexpr (!(ABL & 64)) e_dma(IC<e>{}, IC<e_unit(e)>{}, IC<e_issued(G) % 4>{});
        }
        late(IC<624 + 36 * ph + n>{});
      });
    });
    TS(14); TC(3);
    LDS_BARRIER();
    if (tid < 8) __builtin_amdgcn_raw_ptr_buffer_atomic_add_i32(1, rw, SY_CNT + ((8 + tid) * 16 + (tb & 15)) * 256, 0, SC1);
    f32x4 P[3][12];
#pragma unroll
    for (int k = 0; k < 12; ++k)
#pragma unroll
      for (int m = 1; m < 4; ++m) P[m - 1][(k & 1) * 6 + (k >> 1)] = *(const f32x4*)(smem + ((wid - m) & 3) * G_WS + x_off((m - 1) * 12 + (k & 1) * 6 + (k >> 1)) + lane * 16);
    SB0;
    const f32x4 bv[2] = {f32x4{bflo(bw.x), bfhi(bw.x), bflo(bw.y), bfhi(bw.y)}, f32x4{bflo(bw.z), bfhi(bw.z), bflo(bw.w), bfhi(bw.w)}};
    f32x4 own[12];
    sfor<12>([&](auto K) { own[K] = q_get<K>() + bv[K / 6]; });
    SB0;
    LDS_BARRIER();
    sfor<6>([&](auto TT) {
      constexpr int t = TT;
      sfor<4>([&](auto J) { G2_LDB(g2_qslot(t), g2_qstep(t), J.value); });
      u32x4 o;
#pragma unroll
      for (int c = 0; c < 2; ++c) {
        const f32x4 u = (own[c * 6 + t] + P[0][c * 6 + t]) + (P[1][c * 6 + t] + P[2][c * 6 + t]);
        const f32x4 z = u * __builtin_elementwise_fma(u * u, (f32x4)(-0.1029432397f), (f32x4)(-2.302208198f));
        const f32x4 den = f32x4{__builtin_amdgcn_exp2f(z.x), __builtin_amdgcn_exp2f(z.y), __builtin_amdgcn_exp2f(z.z), __builtin_amdgcn_exp2f(z.w)} + 1.0f;
        const f32x4 y = u * f32x4{__builtin_amdgcn_rcpf(den.x), __builtin_amdgcn_rcpf(den.y), __builtin_amdgcn_rcpf(den.z), __builtin_amdgcn_rcpf(den.w)};
        o[2 * c] = min(pkv(y.xy), 0xfffffffeu); o[2 * c + 1] = min(pkv(y.zw), 0xfffffffeu);
      }
      __builtin_amdgcn_raw_buffer_store_b128(o, rw, WS_GF + ((nt1 * 4 + wid) * 36 + mt * 6 + t) * 1024 + lane * 16, 0, SC1);
      SB0;
    });
    TS(15);
    sfor<12>([&](auto I) { G2_LDB(g2_qslot(6 + I / 4), g2_qstep(6 + I / 4), I % 4); });
    VMCNT(42);
    keep(pf3);
  } else {
    constexpr int n1 = L_ * W1L - PF1_E2 - PFN * 1280, n2 = (PF3_R0 - PF2_DON) * 4;
    const unsigned id = (idx - 27) * 256 + tid, n3 = n1 + PF2_DON * ((xc & 1) ? 5 : 3);
    warm<W1L>(sink, sb1, F_ * 2, w1c0, PF1_E2 + PFN * 1280, L_ * W1L, id, 1280);
    if (xc & 1) warm<5>(sink, sb2, L_ * 2, xc * 512, 0, PF2_DON * 5, (id + 12800 - n1) % 1280, 1280);
    else warm<3>(sink, sb2, L_ * 2, xc * 512 + 128, 0, PF2_DON * 3, (id + 12800 - n1) % 1280, 1280);
    warm<4>(sink, sb2, L_ * 2, xc * 512, PF2_DON * 4, PF3_R0 * 4, (id + 25600 - n3) % 1280, 1280);
    warm<4>(sink, sb2, L_ * 2, xc * 512, (PF3_R0 + 1728 + 432 * PF3_X) * 4, PF2_END * 4, (id + 38400 - n3 - n2) % 1280, 1280);
    arrive<PF_WIN + 1>(rw, 1, tb & 15);
    TS(15);
    sfor<4 * NE + 4>([&](auto I) { if constexpr (I == 32) VMCNT(32); e_dma(IC<(I < 4 * NE ? I / 4 : 9)>{}, IC<(I < 4 * NE ? I / 4 : -1)>{}, IC<I % 4>{}); });
    VMCNT(4);
    sfor<4 * NE>([&](auto I) { e_get(IC<I / 4>{}, IC<I / 4>{}, IC<I % 4>{}); });
    asm volatile("s_waitcnt lgkmcnt(0)" ::: "memory");
    sfor<36>([&](auto I) { G2_LDB(g2_qslot(I / 4), g2_qstep(I / 4), I % 4); });
  }

  {
#define G2_HALF(slot, c, h) trd(rb[c] + (slot) * 4096 + 512 * (h))
    u32x4 W[8][4];
    const int m0 = (72 * db) / 96, nsh = 2 * ((72 * db + 71) / 96 - m0 + 1);
    const int rt0 = 9 * (db >> 1) + 5 * odd, rth = 9 * (db >> 1) + 4;
    const unsigned va = rt0 * 1024 + lane * 16;
    const unsigned vh = rth * 1024 + ((lane & 48) + (lane & 7) + 8 * odd) * 16 + ((lane >> 3) & 1) * (4 * 36864);
    const unsigned a_base = WS_GF + wid * 36864;
    u32x4 A[36][4], Ah[18];
    auto lda = [&](auto J) {
      constexpr int p = J / 9, r = J % 9, t = 2 * p + (r >= 5), f = (r + 3) & 3;
      const unsigned so = a_base + g2_kb(t) * (4 * 36864);
      if constexpr (r == 0) BLOAD(Ah[p], vh, sw, so, 0); else BLOAD(A[t][f], va, sw, so, f * 1024);
    };
    constexpr int NK = (G2_NR + 2) / 3;
    sfor<NK>([&](auto K) {
      constexpr int np = K < NK - 1 ? 12 : 4 * (G2_NR - 3 * K);
      VMCNT(24);
      if constexpr (K == 1) { if (g1_on) arrive<24>(rw, 2, mt * 2 + (nt1 & 1)); }
      sfor<np>([&](auto I) {
        constexpr int q = 3 * K + I / 4, r = g2_qreg(q);
        if constexpr (r < 10) e_get(IC<r>{}, IC<g2_qslot(q)>{}, IC<I % 4>{});
        else W[r - 10][I % 4] = cat(G2_HALF(g2_qslot(q), I % 4, 0), G2_HALF(g2_qslot(q), I % 4, 1));
      });
      if constexpr (K == 0) sfor<4>([&](auto C) { e_get(IC<9>{}, IC<-1>{}, C); });
      asm volatile("s_waitcnt lgkmcnt(0)" ::: "memory");
      SB0;
      sfor<np + 1>([&](auto I) {
        if constexpr (K == NK - 1 && I == np - G2_LATE) {
          if (!g1_on) await(rw, 2, xc, 2 * m0, nsh, 18, wid == 0, lane);
          TS(16);
          sfor<g2_aend(2)>(lda);
        }
        if constexpr (I < np) {
          constexpr int q = 3 * K + 9 + I / 4;
          G2_LDB(g2_qslot(q), g2_qstep(q), I % 4);
          if constexpr (K < 2) q_set<12 * K + I>(K == 0 && I < 5 ? bflo(bias2) : 0.f);
        }
      });
    });
    unsigned mx, my, gpoll = 0;
    auto scan = [&](auto T, auto K) {
      if constexpr (K < 8 || !(T & 1)) {
        const u32x4& a = K < 8 ? A[T][(K >> 1) & 3] : Ah[T >> 1];
        if constexpr (K & 1) { my = K == 1 ? max(a.z, a.w) : umax3(my, a.z, a.w); asm volatile("" : "+v"(my)); }
        else { mx = K == 0 ? max(a.x, a.y) : umax3(mx, a.x, a.y); asm volatile("" : "+v"(mx)); }
        SB0;
      }
    };
    auto check = [&](auto T) {
      if (__builtin_expect(__builtin_amdgcn_ballot_w64(max(mx, my) == ~0u) != 0, 0)) {
        constexpr int j0 = T == 0 ? 0 : g2_aend(T - 1), j1 = T == 0 ? g2_aend(2) : G2S.issued[T == 0 ? 0 : T - 1];
#pragma nounroll
        for (int spin = 0; spin < (1 << 16); ++spin) {
          __builtin_amdgcn_s_sleep(POLL_SLEEP);
          sfor<j1 - j0>([&](auto I) {
            constexpr int J = j0 + I, p = J / 9, r = J % 9, t = 2 * p + (r >= 5), f = (r + 3) & 3;
            const unsigned so = a_base + g2_kb(t) * (4 * 36864);
            if constexpr (r == 0) bload_r<0>(Ah[p], vh, sw, so); else bload_r<f * 1024>(A[t][f], va, sw, so);
          });
          wait5<0>(A[T], Ah[T >> 1]);
          sfor<10>([&](auto K) { scan(T, K); });
          if (__builtin_amdgcn_ballot_w64(max(mx, my) == ~0u) == 0) break;
        }
      }
    };
    const int s = nt2 * 64 + wid * 16 + r16;
    const int dfull = rt0 * 16 + 4 * g, dhalf = rth * 16 + 8 * odd + 4 * (g & 1);
    u32x2 hw[5];
    unsigned hpoll = 0;
    u32x2 Bh[8], Be[2][6];
    static_assert(G2_WP == 14 && G2_DMAX >= 4, "");
    wait5<g2_aend(2) - g2_aend(0) + G2_LATE>(A[0], Ah[0]);
    sfor<10>([&](auto K) { scan(IC<0>{}, K); });
    check(IC<0>{});
    TS(17); TC(4);
    sfor<33>([&](auto TT) {
      constexpr int t = TT;
      constexpr bool R = g2_isR(t);
      sfor<20>([&](auto NN) {
        constexpr int n = NN, c = n / 5, f = n % 5, op = G2S.op[t][n];
        if constexpr (n == g2_wp(t)) wait5<G2S.wait[t]>(A[t + 1], Ah[(t + 1) >> 1]);
        if constexpr (t == G2S.tchk && n == 0) {
          asm volatile("" : "+v"(gpoll));
          if (__builtin_expect(__builtin_amdgcn_ballot_w64(gpoll != (lane < nsh ? 18u : 0u)) != 0, 0)) {
            spin_cnt(rw, cnt_off(2, xc, 2 * m0, nsh, lane), lane < nsh ? 18u : 0u);
          }
        }
        constexpr int ac = 6 * c + (f < 4 ? f : 4 + (t & 1));
        const u32x4& av = f < 4 ? A[t][f & 3] : Ah[t >> 1];
        if constexpr (!R) q_mfma<ac>(av, cat(Bh[2 * c], Bh[2 * c + 1]));
        else if constexpr (g2_ridx(t) < 10) q_mfma_b<ac, e_quad(g2_ridx(t), c)>(av);
        else q_mfma<ac>(av, W[g2_ridx(t) - 10][c]);
        if constexpr (!R && n < 4) { Bh[4 + n] = G2_HALF(g2_slot(t), 2 + (n >> 1), n & 1); SB0; }
        if constexpr (n >= G2_WP && t == 32) { Be[0][n - G2_WP] = G2_HALF(6 + ((n - G2_WP) >> 1), 1, n & 1); SB0; }
        if constexpr (n >= G2_WP && n < G2_WP + 4 && t < 32 && !g2_isR(t + 1)) { Bh[n - G2_WP] = G2_HALF(g2_slot(t + 1), (n - G2_WP) >> 1, (n - G2_WP) & 1); SB0; }
        if constexpr (op == 3000) {
          asm volatile("" : "+v"(hpoll));
          if (__builtin_expect(__builtin_amdgcn_ballot_w64(hpoll != (lane < 16 ? 16u : 0u)) != 0, 0)) spin_cnt(rw, cnt_off(1, xc, 0, 16, lane), lane < 16 ? 16u : 0u);
        }
        if constexpr (op == 5000) poll_issue(gpoll, cnt_off(2, xc, 2 * m0, nsh, lane), sw);
        else if constexpr (op == 4000) poll_issue(hpoll, cnt_off(1, xc, 0, 16, lane), sw);
        else if constexpr (op >= 3000) bload2(hw[op - 3000], WS_H + (s * D_ + (op < 3004 ? dfull + 16 * (op - 3000) : dhalf)) * 2, sw);
        else if constexpr (op >= 2000) G2_LDB((op - 2000) >> 2, 27 + ((op - 2000) >> 2), op & 3);
        else if constexpr (op >= 1000) lda(std::integral_constant<int, op - 1000>{});
        if constexpr (t + 1 < G2_S0 && n >= 10) scan(IC<t + 1>{}, IC<n - 10>{});
      });
      if constexpr (t + 1 < G2_S0) check(IC<t + 1>{});
    });
    sfor<4>([&](auto PP) {
      constexpr int ph = PP, c = (ph + 1) & 3;
      sfor<15>([&](auto NN) {
        constexpr int n = NN, t = ph < 3 ? 33 + n / 5 : n < 3 ? 33 + n : 33 + (n - 3) / 4, f = ph < 3 ? n % 5 : n < 3 ? 4 : (n - 3) % 4;
        if constexpr (ph == 0 && (n == 5 || n == 10)) wait5<G2S.wait[t - 1]>(A[t], Ah[t >> 1]);
        q_mfma<6 * c + (f < 4 ? f : 4 + (t & 1))>(f < 4 ? A[t][f & 3] : Ah[t >> 1], cat(Be[ph & 1][2 * (t - 33)], Be[ph & 1][2 * (t - 33) + 1]));
        if constexpr (ph < 3 && !(n & 1) && n < 12) { Be[(ph + 1) & 1][n >> 1] = G2_HALF(6 + (n >> 2), (c + 1) & 3, (n >> 1) & 1); SB0; }
        if constexpr (ph > 0 && (n & 1) && n >= 3) { constexpr int k = 6 * ph + (n + 5) / 2 % 6; q_put<k, k * 1024>(ring + lane * 16); }
        if constexpr (ph == 3 && (n == 12 || n == 14)) q_put<n / 2 - 2, (n / 2 - 2) * 1024>(ring + lane * 16);
      });
    });
    TS(18); TC(7);
    LDS_BARRIER();
    f32x4 Q[3][4], Hh[8];
#pragma unroll
    for (int t = 0; t < 4; ++t)
#pragma unroll
      for (int c = 1; c < 4; ++c) Q[c - 1][t] = *(const f32x4*)(smem + ((wid - c) & 3) * G_WS + (c * 6 + t) * 1024 + lane * 16);
#pragma unroll
    for (int c = 0; c < 4; ++c) {
      const char* sp = smem + ((wid - c) & 3) * G_WS + (c * 6 + 4) * 1024 + (lane & 31) * 16;
      Hh[2 * c] = *(const f32x4*)(sp); Hh[2 * c + 1] = *(const f32x4*)(sp + 1536);
    }
    SB0;
    asm volatile("s_waitcnt vmcnt(0)" : "+v"(hw[0]), "+v"(hw[1]), "+v"(hw[2]), "+v"(hw[3]), "+v"(hw[4]));
    f32x4 own[4];
    sfor<4>([&](auto T) { own[T] = q_get<T>() + f32x4{bflo(hw[T].x), bfhi(hw[T].x), bflo(hw[T].y), bfhi(hw[T].y)}; });
    f32x4 hh = {bflo(hw[4].x), bfhi(hw[4].x), bflo(hw[4].y), bfhi(hw[4].y)};
    pin_vf(hh);
    SB0;
    const rsrc_t ro = mk_rsrc(out, SZ);
    const unsigned oo = (s * D_ + rt0 * 16 + 16 * (g & 1) + 4 * (g & 2)) * 2;
#pragma unroll
    for (int p = 0; p < 2; ++p) {
      const f32x4 y0 = (own[2 * p] + Q[0][2 * p]) + (Q[1][2 * p] + Q[2][2 * p]), y1 = (own[2 * p + 1] + Q[0][2 * p + 1]) + (Q[1][2 * p + 1] + Q[2][2 * p + 1]);
      unsigned a0 = pkv(y0.xy), a1 = pkv(y0.zw), b0 = pkv(y1.xy), b1v = pkv(y1.zw);
      asm volatile("s_nop 1\n\tv_permlane16_swap_b32 %0, %2\n\tv_permlane16_swap_b32 %1, %3" : "+v"(a0), "+v"(a1), "+v"(b0), "+v"(b1v));
      __builtin_amdgcn_raw_buffer_store_b128(u32x4{a0, a1, b0, b1v}, ro, oo + 64 * p, 0, 0);
    }
    const f32x4 yh = (((Hh[0] + Hh[1]) + (Hh[2] + Hh[3])) + ((Hh[4] + Hh[5]) + (Hh[6] + Hh[7]))) + hh;
    __builtin_amdgcn_raw_buffer_store_b64(u32x2{pkv(yh.xy), pkv(yh.zw)}, ro, g < 2 ? (s * D_ + dhalf) * 2 : OOB, 0, 0);
    TS(19);
  }
  TSOUT();
  asm volatile(".Lk_all_end:" ::: "a255");
}

extern "C" void kernel_launch(void* const* d_in, const int* in_sizes, int n_in,
                              void* d_out, int out_size, void* d_ws, size_t ws_size,
                              hipStream_t stream) {
  k_all<<<256, 256, 0, stream>>>((const u16*)d_in[0], (const u16*)d_in[1], (const u16*)d_in[2], (const u16*)d_in[3], (const u16*)d_in[4],
                                 (const u16*)d_in[5], (const u16*)d_in[6], (const u16*)d_in[8], (const u16*)d_in[9], (const u16*)d_in[10],
                                 (const u16*)d_in[11], (char*)d_ws, (u16*)d_out);
}
```

```cpp
#include <hip/hip_runtime.h>
#include <stdint.h>
#include <utility>

typedef unsigned short u16;
typedef unsigned long long u64;
typedef __attribute__((ext_vector_type(4))) short s16x4;
typedef __attribute__((ext_vector_type(2))) unsigned short u16x2;
typedef __attribute__((ext_vector_type(8))) __bf16 bf16x8;
typedef __attribute__((ext_vector_type(2))) __bf16 bf16x2;
typedef __attribute__((ext_vector_type(4))) float f32x4;
typedef __attribute__((ext_vector_type(2))) float f32x2;
typedef __attribute__((ext_vector_type(4))) unsigned u32x4;
typedef __attribute__((ext_vector_type(2))) unsigned u32x2;
typedef __attribute__((ext_vector_type(4))) int i32x4;
typedef __amdgpu_buffer_rsrc_t rsrc_t;
#define LAS __attribute__((address_space(3)))
#define DI __device__ __forceinline__

#define TSDECL
#define TS(i)
#define TC(i)
#define TSOUT()

#define G2_LEAD 4
#define PF_WIN 1
#define PF1_A 3
#define PF0 0
#define PF2_ORD 1
#define PFK 0
#define PFK_POL ""
#define G1_ROTM 5
#define G1_ROTS 0
#define SIG_C 0
#define NE 6
#define E_T9 705
#define G2_DELAY 0
#define X_DELAY 0
#define G1_BURST 0
#define PF1_B 2
#define PF1_C 3
#define PFN 3
#define PF3_R0 896
#define PF3_X 0
#define PF2_END F_
#define PF3_T0 637
#define PF3_DT 31
#define PF2_DON 1280
#define W1L 9
#define SWAP1 1
#define SWAP2 0
#define ABL 0
#define SPARE_SLEEP 16
#define GATHER_AT 12
#define G1_DELAY 8
#define G1_A0 2
#define POLL_SLEEP 4

constexpr int L_ = 2048, D_ = 576, N_ = 128, F_ = 4608;
constexpr unsigned SZ = L_ * D_ * 2;
constexpr unsigned WS_UB = 0, WS_AT = SZ, WS_H = 2 * SZ, WS_HF = 3 * SZ, WS_GF = 4 * SZ, WS_SY = 7 * SZ, WS_X = WS_SY + 131072, WS_BYTES = WS_X + 256 * 20480;
constexpr unsigned SY_EPOCH = WS_SY, SY_FLAG = WS_SY + 4096, SY_CNT = WS_SY + 32768;
constexpr unsigned OOB = 0x7ff00000u;
constexpr int SC1 = 16;
constexpr unsigned ROWB = L_ * 2;
constexpr int NMARK = 9;

template <class Fn, int... I> DI void sfor_(Fn&& f, std::integer_sequence<int, I...>) { (f(std::integral_constant<int, I>{}), ...); }
template <int N, class Fn> DI void sfor(Fn&& f) { sfor_(f, std::make_integer_sequence<int, N>{}); }

DI float bflo(unsigned w) { return __uint_as_float(w << 16); }
DI float bfhi(unsigned w) { return __uint_as_float(w & 0xffff0000u); }
DI f32x2 unpk(unsigned w) { return f32x2{bflo(w), bfhi(w)}; }
DI float bf2f(u16 v) { return __uint_as_float(((unsigned)v) << 16); }
DI unsigned pkv(f32x2 v) { return __builtin_bit_cast(unsigned, __builtin_convertvector(v, bf16x2)); }
DI unsigned pk2(float lo, float hi) { return pkv(f32x2{lo, hi}); }
DI float rbf(float f) { return bflo(pk2(f, f)); }
DI unsigned perm_lo(unsigned a, unsigned b) { return __builtin_amdgcn_perm(b, a, 0x05040100u); }
DI unsigned perm_hi(unsigned a, unsigned b) { return __builtin_amdgcn_perm(b, a, 0x07060302u); }
DI f32x4 mfma16(u32x4 a, u32x4 b, f32x4 c) { return __builtin_amdgcn_mfma_f32_16x16x32_bf16(__builtin_bit_cast(bf16x8, a), __builtin_bit_cast(bf16x8, b), c, 0, 0, 0); }
DI rsrc_t mk_rsrc(const void* p, unsigned bytes) { return __builtin_amdgcn_make_buffer_rsrc((void*)p, 0, bytes, 0x00020000); }
DI i32x4 mk_srd(const void* p, unsigned bytes) {
  const u64 a = (u64)p;
  i32x4 r; r.x = (int)(unsigned)a; r.y = (int)((unsigned)(a >> 32) & 0xffffu); r.z = (int)bytes; r.w = 0x00020000; return r;
}
#define LDS_BARRIER() do { asm volatile("s_waitcnt lgkmcnt(0)" ::: "memory"); __builtin_amdgcn_s_barrier(); asm volatile("" ::: "memory"); } while (0)
template <int IMM> DI void bload_(u32x4& dst, unsigned voff, i32x4 srd, unsigned soff) {
  asm volatile("buffer_load_dwordx4 %0, %1, %2, %3 offen offset:%4 sc1" : "=v"(dst) : "v"(voff), "s"(srd), "s"(soff), "i"(IMM) : "memory");
}
template <int IMM> DI void bload_r(u32x4& dst, unsigned voff, i32x4 srd, unsigned soff) {
  asm volatile("buffer_load_dwordx4 %0, %1, %2, %3 offen offset:%4 sc1" : "+v"(dst) : "v"(voff), "s"(srd), "s"(soff), "i"(IMM) : "memory");
}
template <int M0OFF, int IMM> DI void bdma(unsigned ldsbase, unsigned voff, i32x4 srd, unsigned soff) {
  if constexpr (IMM == 0) asm volatile("s_add_u32 m0, %0, %1\n\ts_nop 0\n\tbuffer_load_dwordx4 %2, %3, %4 offen lds" :: "s"(ldsbase), "i"(M0OFF), "v"(voff), "s"(srd), "s"(soff) : "memory", "scc");
  else asm volatile("buffer_load_dwordx4 %0, %1, %2 offen offset:%3 lds" :: "v"(voff), "s"(srd), "s"(soff), "i"(IMM) : "memory");
}
template <int OFF> DI void put_row(unsigned v) { asm volatile("ds_write_addtid_b32 %0 offset:%1" :: "v"(v), "i"(OFF) : "memory"); }
DI void keep(unsigned v) { asm volatile("" :: "v"(v)); }
DI void bload2(u32x2& dst, unsigned voff, i32x4 srd) { asm volatile("buffer_load_dwordx2 %0, %1, %2, 0 offen sc1" : "=v"(dst) : "v"(voff), "s"(srd) : "memory"); }
DI void bload2r(u32x2& dst, unsigned voff, i32x4 srd) { asm volatile("buffer_load_dwordx2 %0, %1, %2, 0 offen sc1" : "+v"(dst) : "v"(voff), "s"(srd) : "memory"); }
DI void bstore(const u32x4& v, unsigned voff, i32x4 srd) { asm volatile("buffer_store_dwordx4 %0, %1, %2, 0 offen sc1\n\ts_nop 1" :: "v"(v), "v"(voff), "s"(srd) : "memory"); }
DI unsigned umax3(unsigned a, unsigned b, unsigned c) { return max(max(a, b), c); }
DI unsigned umax4(const u32x4& a, unsigned m) { return umax3(umax3(a.x, a.y, a.z), a.w, m); }
template <int Q, int IMM> DI void q_load(unsigned voff, i32x4 srd, unsigned soff) {
  asm volatile("buffer_load_dwordx4 a[%0:%1], %2, %3, %4 offen offset:%5" :: "i"(4 * Q), "i"(4 * Q + 3), "v"(voff), "s"(srd), "s"(soff), "i"(IMM));
}
template <int Q, int OFF> DI void q_put(unsigned addr) { asm volatile("ds_write_b128 %2, a[%0:%1] offset:%3" :: "i"(4 * Q), "i"(4 * Q + 3), "v"(addr), "i"(OFF) : "memory"); }
template <int Q, int OFF> DI void q_frag(unsigned addr) {
  asm volatile("ds_read_b64_tr_b16 a[%0:%1], %4 offset:%5\n\tds_read_b64_tr_b16 a[%2:%3], %4 offset:%6"
               :: "i"(4 * Q), "i"(4 * Q + 1), "i"(4 * Q + 2), "i"(4 * Q + 3), "v"(addr), "i"(OFF), "i"(OFF + 512) : "memory");
}
template <int Q> DI void q_mfma(const u32x4& a, const u32x4& b) { asm volatile("v_mfma_f32_16x16x32_bf16 a[%0:%1], %2, %3, a[%0:%1]" :: "i"(4 * Q), "i"(4 * Q + 3), "v"(a), "v"(b)); }
template <int Q> DI void q_mfma0(const u32x4& a, const u32x4& b) { asm volatile("v_mfma_f32_16x16x32_bf16 a[%0:%1], %2, %3, 0" :: "i"(4 * Q), "i"(4 * Q + 3), "v"(a), "v"(b)); }
template <int Q, int B> DI void q_mfma_b(const u32x4& a) {
  asm volatile("v_mfma_f32_16x16x32_bf16 a[%0:%1], %4, a[%2:%3], a[%0:%1]" :: "i"(4 * Q), "i"(4 * Q + 3), "i"(4 * B), "i"(4 * B + 3), "v"(a));
}
template <int Q, int B> DI void q_mfma_b0(const u32x4& a) {
  asm volatile("v_mfma_f32_16x16x32_bf16 a[%0:%1], %4, a[%2:%3], 0" :: "i"(4 * Q), "i"(4 * Q + 3), "i"(4 * B), "i"(4 * B + 3), "v"(a));
}
template <int Q> DI f32x4 q_get() {
  float x, y, z, w;
  asm volatile("v_accvgpr_read_b32 %0, a[%4]\n\tv_accvgpr_read_b32 %1, a[%4+1]\n\tv_accvgpr_read_b32 %2, a[%4+2]\n\tv_accvgpr_read_b32 %3, a[%4+3]" : "=v"(x), "=v"(y), "=v"(z), "=v"(w) : "i"(4 * Q));
  return f32x4{x, y, z, w};
}
template <int R> DI void a_get2(float& x, float& y) { asm volatile("v_accvgpr_read_b32 %0, a[%2]\n\tv_accvgpr_read_b32 %1, a[%2+1]" : "=v"(x), "=v"(y) : "i"(R)); }
DI void umin_ip(float& a) { asm volatile("v_min_u32 %0, -2, %0" : "+v"(a)); }
template <int Q> DI void q_set(float v) {
  asm volatile("v_accvgpr_write_b32 a[%0], %1\n\tv_accvgpr_write_b32 a[%0+1], %1\n\tv_accvgpr_write_b32 a[%0+2], %1\n\tv_accvgpr_write_b32 a[%0+3], %1" :: "i"(4 * Q), "v"(v));
}
#define BLOAD(dst, voff, srd, soff, imm) bload_<(imm)>(dst, voff, srd, soff)
#define SB0 __builtin_amdgcn_sched_barrier(0)
DI void pin_vf(f32x4& v) { asm volatile("" : "+v"(v)); }
DI void fadd(float& a, float b) { asm volatile("v_add_f32 %0, %0, %1" : "+v"(a) : "v"(b)); }
template <int IMM> DI void gld32(unsigned& d, unsigned voff, const void* base) { asm volatile("global_load_dword %0, %1, %2 offset:%3" : "=v"(d) : "v"(voff), "s"(base), "i"(IMM) : "memory"); }
template <int IMM> DI void gld16(unsigned& d, unsigned voff, const void* base) { asm volatile("global_load_ushort %0, %1, %2 offset:%3" : "=v"(d) : "v"(voff), "s"(base), "i"(IMM) : "memory"); }
DI void gld128(u32x4& d, unsigned voff, const void* base) { asm volatile("global_load_dwordx4 %0, %1, %2" : "=v"(d) : "v"(voff), "s"(base) : "memory"); }
DI void set_m0(unsigned v) { asm volatile("s_mov_b32 m0, %0\n\ts_nop 0" :: "s"(v) : "memory"); }
template <int N, int F> DI void wait5(u32x4* a) {
  if constexpr (F == 5) asm volatile("s_waitcnt vmcnt(%5)" : "+v"(a[0]), "+v"(a[1]), "+v"(a[2]), "+v"(a[3]), "+v"(a[4]) : "i"(N) : "memory");
  else asm volatile("s_waitcnt vmcnt(%4)" : "+v"(a[0]), "+v"(a[1]), "+v"(a[2]), "+v"(a[3]) : "i"(N) : "memory");
}
template <int N> DI void wait9(unsigned* a) { asm volatile("s_waitcnt vmcnt(%9)" : "+v"(a[0]), "+v"(a[1]), "+v"(a[2]), "+v"(a[3]), "+v"(a[4]), "+v"(a[5]), "+v"(a[6]), "+v"(a[7]), "+v"(a[8]) : "i"(N) : "memory"); }
DI void pin9(unsigned* a) { asm volatile("" : "+v"(a[0]), "+v"(a[1]), "+v"(a[2]), "+v"(a[3]), "+v"(a[4]), "+v"(a[5]), "+v"(a[6]), "+v"(a[7]), "+v"(a[8])); }
template <int N> DI void wait6(u32x4* a) { asm volatile("s_waitcnt vmcnt(%6)" : "+v"(a[0]), "+v"(a[1]), "+v"(a[2]), "+v"(a[3]), "+v"(a[4]), "+v"(a[5]) : "i"(N) : "memory"); }
#define VMCNT(n) asm volatile("s_waitcnt vmcnt(%0)" :: "i"(n) : "memory")
DI u32x2 trd(unsigned addr) { return __builtin_bit_cast(u32x2, __builtin_amdgcn_ds_read_tr16_b64_v4i16((LAS s16x4*)(size_t)addr)); }
DI u32x4 cat(u32x2 a, u32x2 b) { return u32x4{a.x, a.y, b.x, b.y}; }

template <int CTRL> DI float dpp_add(float v) { return v + __builtin_bit_cast(float, __builtin_amdgcn_update_dpp(0, __builtin_bit_cast(int, v), CTRL, 0xf, 0xf, true)); }
DI float half_sum(float v) {
  v = dpp_add<0xB1>(v); v = dpp_add<0x4E>(v); v = dpp_add<0x141>(v); v = dpp_add<0x140>(v);
  float w = v;
  asm volatile("s_nop 1\n\tv_permlane16_swap_b32 %0, %1" : "+v"(v), "+v"(w));
  return v + w;
}
template <int K> using IC = std::integral_constant<int, K>;
template <class Fn> DI void ln9(const f32x2* v, unsigned* o, Fn&& hook) {
  const f32x2 s = ((v[0] + v[1]) + (v[2] + v[3])) + ((v[4] + v[5]) + (v[6] + v[7])) + v[8];
  float a = s.x + s.y;
  hook(IC<0>{}, a);
  float mean = half_sum(a) * (1.0f / D_);
  hook(IC<1>{}, mean);
  f32x2 q0 = {0.f, 0.f}, q1 = {0.f, 0.f};
#pragma unroll
  for (int i = 0; i < 9; ++i) { const f32x2 t = v[i] - mean; if (i & 1) q1 = __builtin_elementwise_fma(t, t, q1); else q0 = __builtin_elementwise_fma(t, t, q0); }
  q0 += q1;
  a = q0.x + q0.y;
  hook(IC<2>{}, a);
  float var = half_sum(a) * (1.0f / D_);
  hook(IC<3>{}, var);
  const float mb = rbf(mean);
  const float sd = rbf(__builtin_amdgcn_sqrtf(rbf(rbf(var) + 1e-5f)));
  float inv = __builtin_amdgcn_rcpf(sd);
  f32x2 w[9];
#pragma unroll
  for (int i = 0; i < 9; ++i) w[i] = v[i] - mb;
  hook(IC<4>{}, inv);
#pragma unroll
  for (int i = 0; i < 9; ++i) o[i] = pkv(w[i]);
  SB0;
#pragma unroll
  for (int i = 0; i < 5; ++i) w[i] = unpk(o[i]);
  hook(IC<5>{}, inv);
#pragma unroll
  for (int i = 5; i < 9; ++i) w[i] = unpk(o[i]);
  SB0;
#pragma unroll
  for (int i = 0; i < 9; ++i) w[i] *= inv;
  hook(IC<6>{}, inv);
#pragma unroll
  for (int i = 0; i < 9; ++i) o[i] = pkv(w[i]);
  hook(IC<7>{}, inv);
}
DI f32x2 cmul(f32x2 a, f32x2 b) { const f32x2 bs = {-b.y, b.x}; return __builtin_elementwise_fma(a.yy, bs, a.xx * b); }
DI f32x2 cexp_p(float zl, float zv, float p) {
  const float m = __builtin_amdgcn_exp2f(zl * p);
  const float rev = __builtin_amdgcn_fractf(zv * p);
  return f32x2{__builtin_amdgcn_cosf(rev), __builtin_amdgcn_sinf(rev)} * m;
}
constexpr u64 G1_STEP = 0xDCBA98764310FE52ull;
DI unsigned g1_blk(unsigned q, unsigned strip) {
  const unsigned t = (unsigned)(G1_STEP >> (4 * q)) & 15u, k = t + (strip % G1_ROTM) * G1_ROTS;
  return t >= 12 ? t : k >= 12 ? k - 12 : k;
}
template <int LINES> DI unsigned slab_off(unsigned e, unsigned e1, unsigned pitch, unsigned c0) {
  unsigned r = e / LINES;
  const unsigned c = c0 + (e - r * LINES) * 128;
  if (LINES >= 9) r = g1_blk((r >> 7) & 15, c >> 8) * 128 + (r & 127);
  return e < e1 ? r * pitch + c : OOB;
}
DI void touch_nt(unsigned& sink, unsigned off, i32x4 srd) { asm volatile("buffer_load_dword %0, %1, %2, 0 offen" PFK_POL : "+v"(sink) : "v"(off), "s"(srd) : "memory"); }
DI void touch(unsigned& sink, unsigned off, i32x4 srd) { asm volatile("buffer_load_dword %0, %1, %2, 0 offen" : "=v"(sink) : "v"(off), "s"(srd) : "memory"); }
template <int LINES, int WIN = PF_WIN> DI void warm(unsigned lds, i32x4 srd, unsigned pitch, unsigned c0, unsigned e0, unsigned e1, unsigned idx, unsigned nthr) {
  set_m0(lds);
#pragma nounroll
  for (unsigned e = e0 + idx; e < e1 + idx; e += nthr) {
    const unsigned off = slab_off<LINES>(e, e1, pitch, c0);
    asm volatile("s_waitcnt vmcnt(%2)\n\tbuffer_load_dword %0, %1, 0 offen lds" :: "v"(off), "s"(srd), "i"(WIN) : "memory");
  }
}

template <int YOUNGER = 0> DI void arrive(rsrc_t rw, int ho, int shard) {
  VMCNT(YOUNGER);
  __builtin_amdgcn_s_barrier();
  if (threadIdx.x < 8) __builtin_amdgcn_raw_ptr_buffer_atomic_add_i32(1, rw, SY_CNT + ((ho * 8 + threadIdx.x) * 16 + shard) * 256, 0, SC1);
}
DI unsigned cnt_off(int ho, int rep, int s0, int ns, int lane) { return lane < ns ? SY_CNT + ((ho * 8 + rep) * 16 + s0 + lane) * 256 : OOB; }
DI void poll_issue(unsigned& f, unsigned off, i32x4 srd) { asm volatile("buffer_load_dword %0, %1, %2, 0 offen sc1" : "+v"(f) : "v"(off), "s"(srd) : "memory"); }
DI void add_one(unsigned one, unsigned off, i32x4 srd) { asm volatile("buffer_atomic_add %0, %1, %2, 0 offen sc1" :: "v"(one), "v"(off), "s"(srd) : "memory"); }
DI void spin_cnt(rsrc_t rw, unsigned off, unsigned w) {
#pragma nounroll
  for (int spin = 0; spin < (1 << 16); ++spin) {
    const unsigned f = __builtin_amdgcn_raw_buffer_load_b32(rw, off, 0, SC1);
    if (__all(f == w)) break;
    __builtin_amdgcn_s_sleep(POLL_SLEEP);
    asm volatile("" ::: "memory");
  }
}
DI void await(rsrc_t rw, int ho, int rep, int s0, int ns, unsigned want, bool poller, int lane) {
  if (poller) spin_cnt(rw, cnt_off(ho, rep, s0, ns, lane), lane < ns ? want : 0u);
  asm volatile("" ::: "memory");
  __builtin_amdgcn_s_barrier();
  asm volatile("" ::: "memory");
}

constexpr int KG_PITCH = 528;
constexpr int KG_B = 41 * KG_PITCH, KG_CH = 57 * KG_PITCH;
constexpr int KG_T = 3 * KG_CH, KG_TP = 2072 * 2;
constexpr int CV_KP = 259 * 16 + 16;
constexpr int CV_U = 4 * CV_KP;
constexpr int CV_UP = (29 + 256) * 16 + 64 + 16;
constexpr int CV_TB = CV_U + 2 * CV_UP;
constexpr int CV_P = 3 * CV_TB;
constexpr int G_WS = 36864;
constexpr int B0 = 16384;
constexpr int t2_off(int i) { return i < 7 ? 9216 + 1024 * i : B0 + 32768 + 1024 * (i - 7); }
constexpr int K_LDS = B0 + 4 * G_WS;
static_assert(CV_KP % 256 == 64 && CV_UP % 64 == 32 && CV_TB % 16 == 0 && KG_CH % 16 == 0 && CV_P + 65536 <= 4 * G_WS && KG_T >= CV_P && KG_T + 3 * KG_TP <= 4 * G_WS &&
              K_LDS <= 163840, "");

constexpr int kg_e(int r) { return r < 16 ? r & 7 : r < 24 ? 2 * (r & 3) : 4 * (r & 1); }
constexpr int kg_g(int r) { return r < 16 ? r >> 3 : r < 24 ? 2 + ((r - 16) >> 2) : 4 + ((r - 24) >> 1); }

constexpr int cv_ab(int j) {
  int n = 0;
  for (int m = 0; m < 8; ++m)
    for (int o = 0; o <= m && m + o < 8; ++o) { if (n++ == j) return 8 * m + o; if (o < m && n++ == j) return 8 * o + m; }
  return 0;
}
constexpr int cv_a(int j) { return cv_ab(j) >> 3; }
constexpr int cv_b(int j) { return cv_ab(j) & 7; }
constexpr bool cv_first(int j) { for (int i = 0; i < j; ++i) if (cv_a(i) + cv_b(i) == cv_a(j) + cv_b(j)) return false; return true; }
constexpr int cv_done(int j) { return j == 8 ? 0 : j == 10 ? 1 : j == 12 ? 2 : j == 15 ? 3 : j == 33 ? 4 : j == 34 ? 5 : -1; }

constexpr int g1_quad(int m, int k) { return (m == 0 ? 0 : m == 3 ? 12 : 12 + 12 * m) + k; }
constexpr int g2_tile(int v) { return v < 18 ? v : v < 22 ? v - 9 : v < 28 ? v - 22 : v < 33 ? v - 15 : v - 27; }
constexpr int g2_nf(int v) { return v < 18 ? 5 : 4; }
constexpr int g2_aend(int v) { return v < 18 ? 5 * v + 5 : v > 35 ? 162 : 4 * v + 22; }
constexpr int g2_astep(int j) { return j < 90 ? j / 5 : (j - 18) / 4; }
constexpr int g2_af(int j) { return j < 90 ? j % 5 : (j - 18) % 4; }
#define G2_A0 2
#define G2_D 4
#define G2_S0 10
constexpr int g2_wp(int v) { return v + 1 < G2_S0 ? 10 : v < 18 ? 16 : v < 33 ? 12 : 0; }
struct G2Sched { short op[36][20], wait[36], issued[36], tchk, pwait; };
constexpr int g2_xs(int v, int m) { const int k = 16 * (v - 24) + m - 4; return v >= 24 && k >= 0 && k < 55 ? k : -1; }
constexpr G2Sched g2_make() {
  G2Sched s{};
  int apos[162] = {}, seq = g2_aend(G2_A0 - 1), na = seq, ppos = 0;
  for (int i = 0; i < na; ++i) apos[i] = i;
  for (int v = 0; v < 35; ++v) {
    int list[20] = {}, n = 0;
    const int nm = 4 * g2_nf(v);
    if (v == G2_S0 - G2_D - 2) list[n++] = 5000;
    if (v == 15) list[n++] = 4000;
    while (na < g2_aend(v + G2_D) && n < 8) list[n++] = 1000 + na++;
    if (na > 5 * G2_S0 && !s.tchk) { s.tchk = (short)v; s.pwait = (short)(seq - 1 - ppos); }
    s.issued[v] = (short)na;
    if (v == 32) for (int i = 0; i < 10; ++i) list[n++] = 3000 + i;
    for (int m = 0, k = 0; m < nm; ++m) {
      if (m == g2_wp(v)) s.wait[v] = (short)(seq - 1 - apos[g2_aend(v + 1) - 1]);
      if (k < n && m == (k * nm) / n) {
        const int o = list[k++];
        s.op[v][m] = (short)o;
        if (o < 2000) apos[o - 1000] = seq; else if (o == 5000) ppos = seq;
        ++seq;
      }
      if (g2_xs(v, m) % 11 == 10) ++seq;
    }
  }
  return s;
}
constexpr G2Sched G2S = g2_make();
static_assert(G2S.tchk == G2_S0 - G2_D && G2S.issued[31] == 162 && G2_S0 + 2 * !!G2_LEAD <= 14, "");

constexpr bool g1_isR(int t) { return t == 2 || t == 5; }
constexpr int g1_lidx(int t) { return t < 2 ? t : t < 5 ? t - 1 : t - 2; }
constexpr int g1_kb(int t) { return t == 2 ? 0 : t == 5 ? 1 : t >= 14 ? t - 12 : 4 + g1_lidx(t); }
constexpr int g1_nb(int t) { return (t < 1 || t > 10) ? 0 : t >= 7 ? 8 : ((t - 1) % 3 == 2 ? 4 : 6); }
constexpr int g1_b0(int t) { return t >= 7 ? 32 + 8 * (t - 7) : 16 * ((t - 1) / 3) + 6 * ((t - 1) % 3); }
constexpr int g1_ops(int t) { return t == 0 ? 6 * (4 - G1_A0) : g1_nb(t) + 6; }
constexpr int PF1_E1 = (PF1_A + PF1_B) * 2048, PF1_E2 = PF1_E1 + PF1_C * 1280;

__global__ __launch_bounds__(256, 1) void k_all(const u16* __restrict__ x, const u16* __restrict__ logA, const u16* __restrict__ Aim,
                                                const u16* __restrict__ Cre, const u16* __restrict__ Cim, const u16* __restrict__ logdt,
                                                const u16* __restrict__ Dsk, const u16* __restrict__ w1, const u16* __restrict__ b1,
                                                const u16* __restrict__ w2, const u16* __restrict__ b2, char* __restrict__ ws, u16* __restrict__ out) {
  __shared__ __attribute__((aligned(16))) char smem_[K_LDS];
  char* const smem = smem_ + B0;
  const int tid = threadIdx.x, lane = tid & 63, wid = __builtin_amdgcn_readfirstlane(tid >> 6);
  const int xc = blockIdx.x & 7, idx = blockIdx.x >> 3;
  TSDECL;
  TS(0);
  const rsrc_t rw = mk_rsrc(ws, WS_BYTES);
  const i32x4 sw = mk_srd(ws, WS_BYTES), sb1 = mk_srd(w1, L_ * F_ * 2), sb2 = mk_srd(w2, L_ * F_ * 2);
  const unsigned ldsT = (unsigned)(size_t)smem_, lds0 = ldsT + B0, ring = lds0 + wid * G_WS;
  const int g = lane >> 4, r16 = lane & 15, q4 = r16 >> 2, p4 = lane & 3;
  const int tb = xc * 24 + idx + (168 - 16 * xc) * ((idx + 8) >> 5);
  const int tok = wid * 2 + (lane >> 5), ll = lane & 31;
  const bool cv_on = idx < 24;
  const int d0 = xc * 72 + idx * 3;
  const unsigned w1c0 = W1L == 10 ? ((27 * xc) / 6) * 256 : xc * 1152;

  const int lin1 = xc * 27 + (SWAP1 && !(xc & 1) ? (SWAP1 == 1 ? max(26 - idx, 0) : (idx + 16) % 27) : idx), mt = lin1 % 6, nt1 = min(lin1 / 6, 35);
  const int kh = xc & 1, dq = idx & 3, lead = idx >> 2, nt2 = (xc >> 1) * 8 + (lead ^ 4);
  const unsigned w2c0 = kh * (F_ / 2) * (L_ * 2) + (xc >> 1) * 1024;
  unsigned xw[9], pp[18], bias2, pf0[3];
  u32x2 ep;
  u32x4 bw;
  sfor<9>([&](auto I) { gld32<128 * I>(xw[I], ((tb * 8 + tok) * (D_ / 2) + ll) * 4, x); });
  bload2(ep, SY_EPOCH, sw);
  gld128(bw, (nt1 * 128 + wid * 32 + 8 * g) * 2, b1);
  gld16<0>(bias2, (nt2 * 64 + wid * 16 + r16) * 2, b2);
  if (cv_on) {
    const unsigned o = (d0 * N_ + (wid & 1) * 64 + lane) * 2;
    sfor<3>([&](auto C) {
      gld16<256 * C>(pp[6 * C], o, logA); gld16<256 * C>(pp[6 * C + 1], o, Aim); gld16<256 * C>(pp[6 * C + 2], o, Cre); gld16<256 * C>(pp[6 * C + 3], o, Cim);
      gld16<2 * C>(pp[6 * C + 4], d0 * 2, logdt); gld16<2 * C>(pp[6 * C + 5], d0 * 2, Dsk);
    });
  } else {
    u64 pc; unsigned len;
    asm volatile("s_getpc_b64 %0\n.Lk_all_pc:\n\ts_mov_b32 %1, .Lk_all_end-.Lk_all_pc" : "=s"(pc), "=s"(len));
    const i32x4 sc = mk_srd((const void*)pc, idx == 31 ? len : 0u);
    touch(pf0[0], tid * 128, sc); touch(pf0[1], (256 + tid) * 128, sc);
    touch(pf0[2], slab_off<W1L>((idx - 24) * 256 + tid, PF0 * 2048, F_ * 2, w1c0), sb1);
  }
  unsigned kad[11], ugo[3], ulo[3], ufo[3], kto[4], zoff, gmk;
  {
#pragma unroll
    for (int k = 0; k < 6; ++k) {
      const int j0 = 2047 - (8 * tid + 2 * k);
      const unsigned pr = pk2((float)j0, (float)(j0 - 1));
      kad[2 * k] = lds0 + KG_T + 8 + 2 * (int)bflo(pr);
      if (k < 5) kad[2 * k + 1] = lds0 + KG_T + 8 + 2 * (int)bfhi(pr);
    }
#pragma unroll
    for (int k = 0; k < 3; ++k) {
      const unsigned i = tid + 256 * k, t3 = i / 3, c = i - 3 * t3;
      ugo[k] = WS_UB + t3 * (D_ * 16) + (d0 + c) * 16; ulo[k] = c * CV_TB + CV_U + (29 + t3) * 16; ufo[k] = SY_FLAG + xc * 2048 + t3 * 8;
    }
    int pb, o1, o2;
    if (wid == 0) { pb = 4 * (g & 1) + 128 * (g >> 1); o1 = 1; o2 = 2; }
    else if (wid == 1) { pb = g < 2 ? 256 + 128 * g : 256 * g; o1 = g < 2 ? 2 : 4; o2 = g < 2 ? 4 : 128; }
    else { pb = 1024 + 512 * g; o1 = 128; o2 = 256; }
    pb += 8 * r16;
    kto[0] = KG_T + 8 + 2 * min(pb, 2056); kto[1] = KG_T + 8 + 2 * min(pb + o1, 2056); kto[2] = KG_T + 8 + 2 * min(pb + o2, 2056); kto[3] = KG_T + 8 + 2 * min(pb + o1 + o2, 2056);
    {
      const unsigned j = ((xc >> 1) * 24 + idx) * 4 + wid, ks = j / 20, r = j - 20 * ks;
      gmk = (ks < 19 ? WS_GF + ((kh * 72 + ks) * 36 + r + 4 * (r / 5) + 4 - 4 * kh) * 1024 : OOB) + lane * 16;
    }
    const int zc = min(tid / 72, 2), zi = min(tid - 72 * zc, 71);
    zoff = zc * CV_TB + (zi < 12 ? (zi & 3) * CV_KP + (256 + (zi >> 2)) * 16 : CV_U + (zi & 1) * CV_UP + ((zi - 12) >> 1) * 16);
#pragma unroll
    for (int k = 0; k < 11; ++k) asm volatile("" : "+v"(kad[k]));
    asm volatile("" : "+v"(ugo[0]), "+v"(ugo[1]), "+v"(ugo[2]), "+v"(ulo[0]), "+v"(ulo[1]), "+v"(ulo[2]), "+v"(kto[0]), "+v"(kto[1]), "+v"(kto[2]), "+v"(kto[3]), "+v"(zoff), "+v"(ufo[0]), "+v"(ufo[1]), "+v"(ufo[2]), "+v"(gmk));
  }
  const unsigned tro = q4 * 128 + g * 32 + p4 * 8;
  auto chunk = [&](unsigned base, int blk) { return cat(trd(base + tro + blk * 1024), trd(base + tro + blk * 1024 + 512)); };
  if (cv_on) VMCNT(21); else VMCNT(6);
  pin9(xw);
  if (blockIdx.x == 255) {
    __builtin_amdgcn_raw_buffer_store_b32(0u, rw, SY_CNT + tid * 256, 0, SC1);
    if (tid < 128) __builtin_amdgcn_raw_buffer_store_b32(0u, rw, SY_CNT + (256 + tid) * 256, 0, SC1);
  }
  {
    f32x2 v[9];
    unsigned o[9];
#pragma unroll
    for (int i = 0; i < 9; ++i) v[i] = unpk(xw[i]);
    ln9(v, o, [](auto, float&) { SB0; });
    set_m0(ldsT + wid * 256);
    sfor<9>([&](auto I) { put_row<1024 * I>(o[I]); });
    if (cv_on || blockIdx.x == 255) VMCNT(0); else VMCNT(3);
    pin9(pp); pin9(pp + 9);
    asm volatile("" : "+v"(bw), "+v"(bias2), "+v"(ep));
    LDS_BARRIER();
    const unsigned off = WS_UB + tb * (D_ * 16) + tid * 16;
    __builtin_amdgcn_raw_buffer_store_b128(chunk(ldsT, wid), rw, off, 0, SC1);
    __builtin_amdgcn_raw_buffer_store_b128(chunk(ldsT, wid + 4), rw, off + 4096, 0, SC1);
    if (wid == 0) __builtin_amdgcn_raw_buffer_store_b128(chunk(ldsT, 8), rw, off + 8192, 0, SC1);
  }
  const u64 tag64 = (((u64)ep.y << 32) | ep.x) * 0x9E3779B97F4A7C15ull + 0xD1B54A32D192ED03ull;
  const u32x2 tag = {(unsigned)tag64, (unsigned)(tag64 >> 32)};
  TS(1);
  const unsigned sink = ring + 35 * 1024;
  auto signal_u = [&]() {
    __builtin_amdgcn_s_barrier();
    if (tid < 8) __builtin_amdgcn_raw_buffer_store_b64(tag, rw, SY_FLAG + tid * 2048 + tb * 8, 0, SC1);
  };
  auto await_u = [&]() {
#pragma nounroll
    for (int spin = 0; spin < (1 << 16); ++spin) {
      const u32x4 f0 = __builtin_amdgcn_raw_buffer_load_b128(rw, SY_FLAG + xc * 2048 + lane * 16, 0, SC1);
      const u32x4 f1 = __builtin_amdgcn_raw_buffer_load_b128(rw, SY_FLAG + xc * 2048 + 1024 + lane * 16, 0, SC1);
      const unsigned d = ((f0.x ^ tag.x) | (f0.y ^ tag.y) | (f0.z ^ tag.x) | (f0.w ^ tag.y)) | ((f1.x ^ tag.x) | (f1.y ^ tag.y) | (f1.z ^ tag.x) | (f1.w ^ tag.y));
      if (__all(d == 0u)) break;
      __builtin_amdgcn_s_sleep(POLL_SLEEP);
      asm volatile("" ::: "memory");
    }
  };

  const bool g1_on = idx < 27;
  unsigned vb1[4], rb1[4];
#pragma unroll
  for (int j = 0; j < 4; ++j) vb1[j] = ((4 * j + g) * (F_ * 2) + ((r16 ^ (g << 2)) << 4) - j * 1024) | (OOB * ((idx + 5) >> 5));
#pragma unroll
  for (int m = 0; m < 4; ++m) rb1[m] = ring + 256 * (8 * g + q4) + 64 * (((m + wid) & 3) ^ q4) + 16 * p4;
  const unsigned b1_base = nt1 * 256 + wid * 32 * (F_ * 2);
#define G1_LDB(slot, kb, j) bdma<(slot) * 8192 + ((j) >> 2) * 4096, ((j) & 3) * 1024>(ring, vb1[(j) & 3], sb1, b1_base + (g1_blk(kb, nt1) * 128 + ((j) >> 2) * 16) * (F_ * 2))
#define G1_HALF(slot, m, c, h) trd(rb1[m] + (slot) * 8192 + 8 * (c) + 1024 * (h))
  const unsigned ao = WS_AT + 2 * tid * (L_ * 2) + tb * 16, ao1 = tid < 32 ? ao + 512 * (L_ * 2) : OOB;
  u32x4 ga[4];
#define G1_LDR(i) q_load<32 + (i), ((i) & 3) * 1024>( vb1[(i) & 3], sb1, b1_base + (g1_blk((i) >> 3, nt1) * 128 + (((i) >> 2) & 1) * 16) * (F_ * 2))
  auto gather = [&]() { bload_r<0>(ga[0], ao, sw, 0u); bload_r<0>(ga[1], ao, sw, ROWB); bload_r<0>(ga[2], ao1, sw, 0u); bload_r<0>(ga[3], ao1, sw, ROWB); };

  if (cv_on) {
    unsigned pfk = 0;
    {
      float zl[3], zv[3];
      const unsigned pfko = slab_off<W1L>(idx * 256 + tid, 6144, F_ * 2, w1c0);
      f32x2 G0[3];
#pragma unroll
      for (int c = 0; c < 3; ++c) {
        const float dt = rbf(__expf(bflo(pp[6 * c + 4])));
        const float zr = -rbf(__expf(bflo(pp[6 * c]))) * dt, zi = bflo(pp[6 * c + 1]) * dt;
        zl[c] = zr * 1.4426950408889634f; zv[c] = zi * 0.15915494309189535f;
        G0[c] = f32x2{2.0f * (bflo(pp[6 * c + 2]) * dt), -2.0f * (bflo(pp[6 * c + 3]) * dt)};
      }
#define PUT_A(row, v) put_row<B0 + (row) * KG_PITCH>(v)
#define PUT_B(row, v) put_row<B0 + (41 + (row)) * KG_PITCH>(v)
      if (wid < 2) {
        sfor<3>([&](auto C) {
          constexpr int c = C;
          f32x2 E[8], G[6];
          const f32x2 E128 = cexp_p(zl[c], -zv[c], 128.f);
          E[1] = cexp_p(zl[c], -zv[c], 1.f); E[2] = cexp_p(zl[c], -zv[c], 2.f); E[4] = cexp_p(zl[c], -zv[c], 4.f);
          G[0] = G0[c]; G[4] = cmul(cexp_p(zl[c], -zv[c], 512.f), G0[c]);
          G[1] = cmul(G[0], E128); E[3] = cmul(E[1], E[2]); G[5] = cmul(G[4], E128); E[5] = cmul(E[1], E[4]);
          G[2] = cmul(G[1], E128); E[6] = cmul(E[2], E[4]); E[7] = cmul(E[3], E[4]); G[3] = cmul(G[2], E128);
          set_m0(ldsT + c * KG_CH + wid * 256);
          f32x2 v[28], Gs[6];
          sfor<6>([&](auto t) { Gs[t] = f32x2{-G[t].y, G[t].x}; });
          sfor<28>([&](auto r) { if constexpr (kg_e(r) > 0) v[r] = E[kg_e(r)].xx * G[kg_g(r)]; });
          SB0;
          sfor<28>([&](auto r) { if constexpr (kg_e(r) > 0) v[r] = __builtin_elementwise_fma(E[kg_e(r)].yy, Gs[kg_g(r)], v[r]); else v[r] = G[kg_g(r)]; });
          SB0;
          unsigned p[28];
          sfor<29>([&](auto r) { if constexpr (r < 28) p[r] = pkv(v[r]); if constexpr (r > 0) PUT_A(r - 1, p[r - 1]); SB0; });
          if constexpr (c == SIG_C) { VMCNT(0); signal_u(); if (PFK) touch_nt(pfk, pfko, sb1); TS(2); }
        });
      } else {
        sfor<3>([&](auto C) {
          constexpr int c = C;
          const f32x2 E128 = cexp_p(zl[c], -zv[c], 128.f), E4 = cexp_p(zl[c], -zv[c], 4.f), E8 = cexp_p(zl[c], zv[c], 8.f);
          f32x2 b0 = E8, b1v = cexp_p(zl[c], zv[c], 64.f);
          f32x2 g0 = cmul(cexp_p(zl[c], -zv[c], 768.f), G0[c]), g1 = cmul(cexp_p(zl[c], -zv[c], 1536.f), G0[c]);
          const f32x2 E8s = {-E8.y, E8.x}, E128s = {-E128.y, E128.x}, E4s = {-E4.y, E4.x};
          set_m0(ldsT + c * KG_CH + (wid & 1) * 256);
          PUT_B(0, 0x00003f80u);
          sfor<8>([&](auto j) {
            f32x2 t0, t1, t2, t3, t4;
            if constexpr (j < 6) t0 = b0.xx * E8;
            if constexpr (j < 7) t1 = b1v.xx * E8;
            if constexpr (j < 5) t2 = g0.xx * E128;
            if constexpr (j < 4) t3 = g1.xx * E128;
            if constexpr (j < 2) t4 = g0.xx * E4;
            const unsigned q0 = pkv(b0), q1 = pkv(b1v), q2 = pkv(g0), q3 = pkv(g1);
            SB0;
            if constexpr (j < 2) t4 = __builtin_elementwise_fma(g0.yy, E4s, t4);
            if constexpr (j < 7) PUT_B(j + 1, q0);
            if constexpr (j < 6) b0 = __builtin_elementwise_fma(b0.yy, E8s, t0);
            SB0;
            PUT_B(8 + j, q1);
            if constexpr (j < 7) b1v = __builtin_elementwise_fma(b1v.yy, E8s, t1);
            SB0;
            if constexpr (j < 2) PUT_A(28 + 2 * j, q2); else if constexpr (j < 6) PUT_A(30 + j, q2);
            if constexpr (j < 5) g0 = __builtin_elementwise_fma(g0.yy, E128s, t2);
            SB0;
            if constexpr (j < 5) PUT_A(36 + j, q3);
            if constexpr (j < 4) g1 = __builtin_elementwise_fma(g1.yy, E128s, t3);
            if constexpr (j < 2) PUT_A(29 + 2 * j, pkv(t4));
            SB0;
          });
          if constexpr (c == SIG_C) { VMCNT(0); signal_u(); if (PFK) touch_nt(pfk, pfko, sb1); }
        });
      }
    }
    u32x2 fl[3];
#pragma unroll
    for (int k = 0; k < 3; ++k) bload2(fl[k], ufo[k], sw);
    LDS_BARRIER();
    TS(3);
    if (wid < 3) {
      const char* ap = smem + min(wid * 16 + r16, 40) * KG_PITCH + g * 16;
      const char* bp = smem + KG_B + r16 * KG_PITCH + g * 16;
      f32x4 a0[3] = {};
      u32x4 fa[2][3][4], fb[2][3][4];
#pragma unroll
      for (int h = 0; h < 2; ++h) {
#pragma unroll
        for (int s = 0; s < 4; ++s)
#pragma unroll
          for (int c = 0; c < 3; ++c) { fa[h][c][s] = *(const u32x4*)(ap + c * KG_CH + (4 * h + s) * 64); fb[h][c][s] = *(const u32x4*)(bp + c * KG_CH + (4 * h + s) * 64); }
        if (h == 1) {
#pragma unroll
          for (int s = 0; s < 4; ++s)
#pragma unroll
            for (int c = 0; c < 3; ++c) a0[c] = mfma16(fa[0][c][s], fb[0][c][s], a0[c]);
#pragma unroll
          for (int k = 0; k < 12; ++k) { __builtin_amdgcn_sched_group_barrier(0x100, 2, 0); __builtin_amdgcn_sched_group_barrier(0x8, 1, 0); }
        }
        SB0;
      }
#pragma unroll
      for (int s = 0; s < 4; ++s)
#pragma unroll
        for (int c = 0; c < 3; ++c) a0[c] = mfma16(fa[1][c][s], fb[1][c][s], a0[c]);
#pragma unroll
      for (int c = 0; c < 3; ++c) {
        a0[c].x += tid == 0 ? bflo(pp[6 * c + 5]) : 0.f;
        const unsigned k01 = pkv(a0[c].xy), k23 = pkv(a0[c].zw);
        *(u16*)(smem + kto[0] + c * KG_TP) = (u16)k01; *(u16*)(smem + kto[1] + c * KG_TP) = (u16)(k01 >> 16);
        *(u16*)(smem + kto[2] + c * KG_TP) = (u16)k23; *(u16*)(smem + kto[3] + c * KG_TP) = (u16)(k23 >> 16);
      }
    } else {
      if (lane < 3) *(u32x2*)(smem + KG_T + lane * KG_TP) = u32x2{0u, 0u};
    }
    VMCNT(0);
#pragma nounroll
    for (int spin = 0; spin < (1 << 16); ++spin) {
      asm volatile("" : "+v"(fl[0]), "+v"(fl[1]), "+v"(fl[2]) :: "memory");
      const unsigned d = ((fl[0].x ^ tag.x) | (fl[0].y ^ tag.y)) | ((fl[1].x ^ tag.x) | (fl[1].y ^ tag.y)) | ((fl[2].x ^ tag.x) | (fl[2].y ^ tag.y));
      if (__all(d == 0u)) break;
      __builtin_amdgcn_s_sleep(POLL_SLEEP);
#pragma unroll
      for (int k = 0; k < 3; ++k) bload2r(fl[k], ufo[k], sw);
      VMCNT(0);
    }
    keep(pfk);
    u32x4 ug[3];
#pragma unroll
    for (int k = 0; k < 3; ++k) BLOAD(ug[k], ugo[k], sw, 0u, 0);
    const u32x4 none = {~0u, ~0u, ~0u, ~0u};
    const unsigned mk0 = WS_AT + d0 * (L_ * 2) + 16 * tid;
    auto side = [&](auto K0, auto N) {
      SB0;
      sfor<N>([&](auto J) {
        constexpr int k = K0 + J, i = k - NMARK;
        if constexpr (k < 3) bstore(none, mk0 + k * 4096, sw);
        else if constexpr (k < 6) bstore(none, mk0 + (WS_HF - WS_AT) + (k - 3) * 4096, sw);
        else if constexpr (k < 9) bstore(none, gmk + (k - 6) * (19 * 36864), sw);
        else if constexpr (i < 32) G1_LDR(i);
      });
      SB0;
    };
#define SIDE(k0, n) side(IC<(k0)>{}, IC<(n)>{})
    SIDE(0, 3);
    LDS_BARRIER();
    TS(4);
    {
      if (blockIdx.x == 0 && tid == 0) __builtin_amdgcn_raw_buffer_store_b64(tag, rw, SY_EPOCH, 0, SC1);
      u16 kv[3][11];
#pragma unroll
      for (int c = 0; c < 3; ++c)
#pragma unroll
        for (int k = 0; k < 11; ++k) kv[c][k] = *(const LAS u16*)(size_t)(kad[k] + c * KG_TP);
      *(u32x4*)(smem + zoff) = u32x4{0u, 0u, 0u, 0u};
      SIDE(3, 2);
      sfor<3>([&](auto C) {
        constexpr int c = C;
        unsigned e[5], a[5];
#pragma unroll
        for (int k = 0; k < 5; ++k) { e[k] = kv[c][2 * k] | ((unsigned)kv[c][2 * k + 1] << 16); a[k] = kv[c][2 * k + 1] | ((unsigned)kv[c][2 * k + 2] << 16); }
        char* kp = smem + c * CV_TB + tid * 16;
        *(u32x4*)(kp + 3 * CV_KP) = u32x4{e[0], e[1], e[2], e[3]};
        *(u32x4*)(kp + 2 * CV_KP) = u32x4{a[0], a[1], a[2], a[3]};
        *(u32x4*)(kp + 1 * CV_KP) = u32x4{e[1], e[2], e[3], e[4]};
        *(u32x4*)(kp + 0 * CV_KP) = u32x4{a[1], a[2], a[3], a[4]};
        SIDE(5 + 2 * c, 2);
      });
      LDS_BARRIER();
      asm volatile("s_waitcnt vmcnt(11)" : "+v"(ug[0]), "+v"(ug[1]), "+v"(ug[2]) :: "memory");
#pragma unroll
      for (int k = 0; k < 3; ++k) {
        char* up = smem + ulo[k];
        *(u32x2*)(up + 8) = ug[k].xy; *(u32x2*)(up + 16) = ug[k].zw;
        *(u32x4*)(up + CV_UP) = ug[k];
      }
    }
    LDS_BARRIER();
    TS(5);
    {
      const char* ap0 = smem + (r16 & 3) * CV_KP + 2 * (2064 - 256 * 7 - 8 * (r16 >> 2) - 8 * g - 64 * wid - 32);
      const char* bp0 = smem + CV_U + (r16 & 1) * CV_UP + 2 * (232 + 32 * (r16 >> 1) - 64 * wid - 32 + 24 - 8 * g);
      const unsigned pw = lds0 + CV_P + wid * 8192 + ((r16 & 1) + 2 * (g ^ ((r16 >> 1) & 3)) + 8 * (r16 >> 1)) * 16;
      unsigned pr = lds0 + CV_P + (tid >> 5) * 1024 + (2 * ((tid & 3) ^ ((tid >> 2) & 3)) + 8 * ((tid & 31) >> 2)) * 16;
      unsigned c0a = tid < 8 ? SY_CNT + (tid * 16 + ((xc * 24 + idx) & 15)) * 256 : OOB, c0p = cnt_off(0, xc, 0, 16, tid), one = 1;
      u32x4 FA[2][8], FB[2][8], ov;
      f32x4 py[8];
      auto rd = [&](int u, int j) {
        const int o = (u >> 1) * CV_TB + 64 * (1 - (u & 1)), k = j >> 1;
        if (j & 1) FB[u & 1][k] = *(const u32x4*)(bp0 + o + 512 * k); else FA[u & 1][k] = *(const u32x4*)(ap0 + o + 512 * (7 - k));
      };
#define CV_WP(c, k) q_put<8 * ((c) & 1) + (k), ((c) & 1) * 32768 + (k) * 1024>(pw)
      auto rp = [&](int c, int j) { py[j] = *(const LAS f32x4*)(size_t)(pr + (c & 1) * 32768 + (j >> 1) * 8192 + (j & 1) * 16); };
      auto fin = [&](int c, int j) {
        if (j < 12) {
          const int d = j < 8 ? (j & 1) + (j & 4) : j & 1, e = j & 2, sft = j < 8 ? 2 : 4;
          float x0 = py[d][e], x1 = py[d][e + 1];
          fadd(x0, py[d + sft][e]); fadd(x1, py[d + sft][e + 1]);
          py[d][e] = x0; py[d][e + 1] = x1;
        } else if (j < 14) { ov[j - 12] = pkv(j & 1 ? py[0].zw : py[0].xy); ov[j - 10] = pkv(j & 1 ? py[1].zw : py[1].xy); asm volatile("" : "+v"(ov)); }
        else if (j < 16) { ov[j - 14] = min(ov[j - 14], 0xfffffffeu); ov[j - 12] = min(ov[j - 12], 0xfffffffeu); asm volatile("" : "+v"(ov)); }
        else __builtin_amdgcn_raw_buffer_store_b128(ov, rw, mk0 + c * 4096, 0, SC1);
      };
      sfor<16>([&](auto J) { rd(0, J); });
      SB0;
      asm volatile("" : "+v"(pr), "+v"(c0a), "+v"(c0p), "+v"(one));
      SIDE(11, 1);
      unsigned pf = 0;
      sfor<216>([&](auto NN) {
        constexpr int n = NN, u = n / 36, j = n % 36, c1 = cv_a(j), c2 = cv_b(j);
        if constexpr (n == 108) {
          VMCNT(12 + 108 / 5 - NMARK);
          LDS_BARRIER();
          add_one(one, c0a, sw);
          TS(21);
        }
        if constexpr (n == 180) {
          LDS_BARRIER();
          TS(22);
        }
        if constexpr (!(u & 1) && cv_first(j)) q_mfma0<8 * ((u >> 1) & 1) + c1 + c2>(FA[u & 1][c1], FB[u & 1][c2]);
        else q_mfma<8 * ((u >> 1) & 1) + c1 + c2>(FA[u & 1][c1], FB[u & 1][c2]);
        if constexpr (u != 3 && u != 5) {
          if constexpr (!(j & 1)) { if constexpr (j < 32) rd(u + 1, j / 2); }
          else if constexpr (u >= 2 && j >= 3 && j < 19) CV_WP(u / 2 - 1, (j - 3) / 2);
        } else {
          if constexpr (j < 8) rp(u / 2 - 1, j);
          if constexpr (u == 3 && j < 16) rd(4, j);
          if constexpr (j >= 16 && j < 33) fin(u / 2 - 1, j - 16);
          if constexpr (u == 5 && cv_done(j) >= 0) CV_WP(2, cv_done(j));
        }
        if constexpr (n % 5 == 4) SIDE(12 + n / 5, 1);
        if constexpr (n == 191) poll_issue(pf, c0p, sw);
        SB0;
      });
      CV_WP(2, 6);
      asm volatile("s_nop 7" ::: "memory");
      CV_WP(2, 7);
      LDS_BARRIER();
      TS(23);
      sfor<8>([&](auto J) { rp(2, J); });
      SB0;
      py[0] = (py[0] + py[2]) + (py[4] + py[6]); py[1] = (py[1] + py[3]) + (py[5] + py[7]);
      sfor<5>([&](auto J) { fin(2, 12 + J); });
      TS(6);
      asm volatile("s_waitcnt vmcnt(2)" : "+v"(pf) :: "memory");
      await(rw, 0, xc, 0, 16, 12, wid == 0 && !__all(pf == (lane < 16 ? 12u : 0u)), lane);
    }
  } else {
    VMCNT(0);
    signal_u();
    keep(pf0[0]); keep(pf0[1]); keep(pf0[2]);
    warm<W1L>(sink, sb1, F_ * 2, w1c0, (PF0 + 3 * PFK) * 2048, PF1_A * 2048, (idx - 24) * 256 + tid, 2048);
    if (wid == 0) await_u();
    warm<W1L>(sink, sb1, F_ * 2, w1c0, PF1_A * 2048, PF1_E1, (idx - 24) * 256 + tid, 2048);
    if (g1_on) { sfor<32>([&](auto I) { G1_LDR(I); }); VMCNT(0); }
    else warm<W1L>(sink, sb1, F_ * 2, w1c0, PF1_E1, PF1_E2, (idx - 27) * 256 + tid, 1280);
  }
  TS(7);

  u32x4 BR[4][8];
  TS(8);
  {
    auto ride = [&](auto P) { SB0; G1_LDB(P / 8, 4 + P / 8, P % 8); SB0; };
    asm volatile("" : "=v"(ga[0]), "=v"(ga[1]), "=v"(ga[2]), "=v"(ga[3]));
    sfor<24>([&](auto I) {
      constexpr int k = I / 8 - 1, j = I % 8;
      if constexpr (I == GATHER_AT) { if (cv_on) gather(); }
      if constexpr (!(I & 1)) ride(IC<I / 2>{});
      if constexpr (I >= 8) { BR[k][j] = cat(G1_HALF(2 + k, j >> 1, j & 1, 0), G1_HALF(2 + k, j >> 1, j & 1, 1)); SB0; }
      if constexpr (I < 16) q_put<32 + I, 16384 + I * 1024>(ring + lane * 16);
    });
    sfor<4>([&](auto I) { ride(IC<12 + I>{}); });
    if (cv_on) VMCNT(16 - (GATHER_AT + 1) / 2);
    else {
      if (wid == 0) {
#pragma nounroll
        for (int spin = 0; spin < (1 << 16); ++spin) {
          const unsigned c = __builtin_amdgcn_raw_buffer_load_b32(rw, cnt_off(0, xc, 0, 16, lane), 0, SC1);
          const unsigned f = __builtin_amdgcn_raw_buffer_load_b32(rw, WS_AT + (9 * lane + 2) * (L_ * 2) + tb * 16, 0, SC1);
          if (__all(c == (lane < 16 ? 12u : 0u) && f != ~0u)) break;
          __builtin_amdgcn_s_sleep(SPARE_SLEEP);
          asm volatile("" ::: "memory");
        }
      }
      asm volatile("" ::: "memory");
      __builtin_amdgcn_s_barrier();
      gather();
      if (g1_on) VMCNT(0);
      else { warm<W1L, 63>(sink, sb1, F_ * 2, w1c0, PF1_E2, PF1_E2 + PFN * 1280, (idx - 27) * 256 + tid, 5 * 256); VMCNT(PFN); }
    }
    static_assert(PF1_E2 + PFN * 1280 <= L_ * W1L, "");
#pragma nounroll
    for (int spin = 0; spin < (1 << 16); ++spin) {
      asm volatile("" : "+v"(ga[0]), "+v"(ga[1]), "+v"(ga[2]), "+v"(ga[3]) :: "memory");
      if (__all(umax4(ga[0], umax4(ga[1], umax4(ga[2], umax4(ga[3], 0u)))) != ~0u)) break;
      __builtin_amdgcn_s_sleep(POLL_SLEEP);
      gather();
      VMCNT(0);
    }
    asm volatile("" : "+v"(ga[0]), "+v"(ga[1]), "+v"(ga[2]), "+v"(ga[3]) :: "memory");
    {
      unsigned* tp = (unsigned*)(smem_ + (tid >> 5) * 1024 + ll * 4);
#pragma unroll
      for (int k = 0; k < 4; ++k) { tp[64 * k] = perm_lo(ga[0][k], ga[1][k]); tp[64 * k + 32] = perm_hi(ga[0][k], ga[1][k]); }
      if (tid < 32) {
#pragma unroll
        for (int k = 0; k < 4; ++k) { tp[2048 + 64 * k] = perm_lo(ga[2][k], ga[3][k]); tp[2048 + 64 * k + 32] = perm_hi(ga[2][k], ga[3][k]); }
      }
    }
    LDS_BARRIER();
    TS(9);
    f32x2 v[9];
    unsigned o[9];
#pragma unroll
    for (int i = 0; i < 9; ++i) { o[i] = pkv(unpk(xw[i]) + unpk(*(const unsigned*)(smem_ + 1024 * i + tid * 4))); v[i] = unpk(o[i]); }
    set_m0(ldsT + wid * 256);
    sfor<9>([&](auto I) { put_row<1024 * I>(o[I]); });
    ln9(v, o, [&](auto K, float& x) { asm volatile("" : "+v"(x)); ride(IC<16 + K>{}); });
#pragma unroll
    for (int i = 0; i < 9; ++i) v[i] = unpk(o[i]);
    ln9(v, o, [&](auto K, float& x) { asm volatile("" : "+v"(x)); ride(IC<24 + K>{}); });
    set_m0(ldsT + wid * 256);
    sfor<9>([&](auto I) { put_row<t2_off(I)>(__builtin_bit_cast(unsigned, __builtin_elementwise_min(__builtin_bit_cast(u16x2, o[I]), u16x2{0xfffe, 0xfffe}))); });
    LDS_BARRIER();
    {
      const unsigned off = WS_HF + ((tb >> 2) * 36 + wid * 4 + g) * 1024 + (tb & 3) * 256 + r16 * 16;
      __builtin_amdgcn_raw_buffer_store_b128(chunk(ldsT + 9216, wid), rw, off, 0, SC1);
      __builtin_amdgcn_raw_buffer_store_b128(chunk(ldsT + (wid == 3 ? t2_off(7) - 7168 : 9216), wid + 4), rw, off + 16384, 0, SC1);
      if (wid == 0) __builtin_amdgcn_raw_buffer_store_b128(chunk(ldsT + t2_off(8), 0), rw, off + 32768, 0, SC1);
      const unsigned ho = WS_H + (tb * 8 + (lane >> 3)) * (D_ * 2) + (lane & 7) * 16;
      const char* hp = smem_ + lane * 16;
      __builtin_amdgcn_raw_buffer_store_b128(*(const u32x4*)(hp + wid * 1024), rw, ho + wid * 128, 0, SC1);
      __builtin_amdgcn_raw_buffer_store_b128(*(const u32x4*)(hp + (wid + 4) * 1024), rw, ho + (wid + 4) * 128, 0, SC1);
      if (wid == 3) __builtin_amdgcn_raw_buffer_store_b128(*(const u32x4*)(hp + 8192), rw, ho + 1024, 0, SC1);
    }
    TS(10);
  }

  const unsigned xin = WS_X + blockIdx.x * 20480 + wid * 5120 + lane * 16, xin4 = (lane >> 5) == kh ? xin + 4096 : OOB;
  auto xmark = [&](int i) { bstore(u32x4{~0u, ~0u, ~0u, ~0u}, i < 4 ? xin + i * 1024 : xin4, sw); };
  unsigned vb2[4];
#pragma unroll
  for (int j = 0; j < 4; ++j) vb2[j] = (8 * j + (lane >> 3)) * (L_ * 2) + (((lane & 7) ^ (2 * ((g & 1) | ((j & 1) << 1)))) << 4) - j * 1024;
  const unsigned b2_base = nt2 * 128 + (kh * (F_ / 2) + wid * 32) * (L_ * 2);
  auto g2_kb = [&](int t) { return G2_LEAD ? t + ((t >> 1) == lead ? 2 : (t >> 1) == lead + 1 ? -2 : 0) : t; };
#define G2_LDB(slot, i, j) bdma<(slot) * 4096, (j) * 1024>(ring, vb2[j], sb2, b2_base + g2_kb(i) * 128 * (L_ * 2))

  if (g1_on) {
    __builtin_amdgcn_s_sleep(G1_DELAY);
    TS(11);
    const unsigned va0 = lane * 16, va1 = lane * 16 + 4096;
    const unsigned a_base = WS_HF + wid * 36864 + mt * 6144;
    u32x4 A[4][6];
#define G1_AOFF(t) (a_base + g1_blk(g1_kb(t), nt1) * (4 * 36864))
#define G1_LDA(t, f) BLOAD(A[(t) & 3][f], (f) < 4 ? va0 : va1, sw, G1_AOFF(t), ((f) & 3) * 1024)
#define G1_WAIT(t, n) wait6<(n)>(A[(t) & 3])
    unsigned mx, my, pf3 = 0;
    auto late = [&](auto GG) {
      if constexpr (GG >= PF3_T0 && (GG - PF3_T0) % PF3_DT == 0 && (GG - PF3_T0) / PF3_DT < 4 + PF3_X) {
        constexpr int k = (GG - PF3_T0) / PF3_DT;
        if (wid == (k & 3)) touch_nt(pf3, slab_off<8>((PF3_R0 + 216 * k) * 8 + idx * 64 + lane, F_ * 4, L_ * 2, w2c0), sb2);
      }
    };
    auto scan = [&](auto T, auto K) {
      const u32x4& a = A[T & 3][K >> 1];
      if constexpr (K & 1) { my = K == 1 ? max(a.z, a.w) : umax3(my, a.z, a.w); asm volatile("" : "+v"(my)); }
      else { mx = K == 0 ? max(a.x, a.y) : umax3(mx, a.x, a.y); asm volatile("" : "+v"(mx)); }
      SB0;
    };
    auto check = [&](auto T) {
      if (__builtin_expect(__builtin_amdgcn_ballot_w64(max(mx, my) == ~0u) != 0, 0)) {
#pragma nounroll
        for (int spin = 0; spin < (1 << 16); ++spin) {
          __builtin_amdgcn_s_sleep(POLL_SLEEP);
          sfor<18>([&](auto I) { constexpr int t = T + I / 6, f = I % 6; if constexpr (t < 16 && (T > 0 || t < G1_A0)) bload_r<(f & 3) * 1024>(A[t & 3][f], f < 4 ? va0 : va1, sw, G1_AOFF(t)); });
          G1_WAIT(T, 0);
          sfor<12>([&](auto K) { scan(T, K); });
          if (__builtin_amdgcn_ballot_w64(max(mx, my) == ~0u) == 0) break;
        }
      }
    };
    sfor<6 * G1_A0>([&](auto I) { G1_LDA(I / 6, I % 6); });
    TS(12);
    u32x2 Bh[2][4], Th[2], Bl[12];
    G1_WAIT(0, 6 * G1_A0 - 6);
    sfor<12>([&](auto K) { scan(IC<0>{}, K); });
    check(IC<0>{});
    TS(13); TC(0);
    sfor<4>([&](auto I) { Bh[0][I] = G1_HALF(0, 0, I >> 1, I & 1); });
    SB0;
    sfor<13>([&](auto TT) {
      constexpr int t = TT;
      constexpr bool R = g1_isR(t);
      constexpr int slot = g1_lidx(t) & 3, nops = g1_ops(t);
      sfor<48>([&](auto NN) {
        constexpr int n = NN, m = n / 12, c = (n / 6) & 1, f = n % 6, i12 = n % 12;
        if constexpr (n == 36) G1_WAIT(t + 1, t == 0 ? 12 : t == 1 ? 6 + g1_ops(1) : g1_ops(t - 1) + g1_ops(t));
        if constexpr (t == 0) q_mfma0<g1_quad(m, i12)>(cat(Bh[m & 1][2 * c], Bh[m & 1][2 * c + 1]), A[t & 3][f]);
        else if constexpr (!R) q_mfma<g1_quad(m, i12)>(cat(Bh[m & 1][2 * c], Bh[m & 1][2 * c + 1]), A[t & 3][f]);
        else q_mfma<g1_quad(m, i12)>(BR[g1_kb(t)][2 * m + c], A[t & 3][f]);
        if constexpr (i12 < 4) {
          if constexpr (m < 3) { if constexpr (!R) { Bh[(m + 1) & 1][i12] = G1_HALF(slot, m + 1, i12 >> 1, i12 & 1); SB0; } }
          else if constexpr (!g1_isR(t + 1)) { Bh[0][i12] = G1_HALF(g1_lidx(t + 1) & 3, t == 12 ? 1 : 0, i12 >> 1, i12 & 1); SB0; }
        } else if constexpr (m < 3) {
          constexpr int pl = m * 8 + i12 - 4, per = (nops + 2) / 3, k = G1_BURST == 1 ? pl : G1_BURST == 2 ? m * per + i12 - 4 : (pl * nops + 23) / 24;
          if constexpr (k < nops && (G1_BURST == 1 || (G1_BURST == 2 && i12 - 4 < per) || (!G1_BURST && (k * 24) / nops == pl)) && !(ABL & 2)) {
            if constexpr (k < g1_nb(t)) { constexpr int pc = g1_b0(t) + k; G1_LDB((pc >> 3) & 3, 8 + (pc >> 3), pc & 7); }
            else if constexpr (t == 0) G1_LDA(G1_A0 + k / 6, k % 6);
            else G1_LDA(t + 3, k - g1_nb(t));
          }
          if constexpr (t >= 11) {
            constexpr int r = t - 9;
            if constexpr (pl < 8) q_put<32 + 8 * r + pl, pl * 1024>(ring + lane * 16);
            else {
              constexpr int j = (pl - 8) >> 1;
              Th[pl & 1] = G1_HALF(0, j >> 1, j & 1, pl & 1);
              if constexpr (pl & 1) BR[r][j] = cat(Th[0], Th[1]);
              SB0;
            }
          }
        }
        if constexpr (n >= 36) scan(IC<t + 1>{}, IC<n - 36>{});
        late(IC<48 * t + n>{});
      });
      check(IC<t + 1>{});
    });
    sfor<4>([&](auto PP) {
      constexpr int ph = PP, m = (ph + 1) & 3;
      sfor<36>([&](auto NN) {
        constexpr int n = NN, t = 13 + n / 12, c = (n / 6) & 1, f = n % 6;
        if constexpr (ph == 0 && n == 0) G1_WAIT(14, 6);
        if constexpr (ph == 0 && n == 12) { check(IC<14>{}); G1_WAIT(15, 0); }
        if constexpr (ph == 0 && n == 24) check(IC<15>{});
        if constexpr (ph == 0 && n == 22) __builtin_amdgcn_s_waitcnt(0xc07f);
        if constexpr (t == 13 && ph == 0) q_mfma<g1_quad(m, n % 12)>(cat(Bh[0][2 * c], Bh[0][2 * c + 1]), A[t & 3][f]);
        else if constexpr (t == 13) q_mfma<g1_quad(m, n % 12)>(cat(Bl[4 * ph - 4 + 2 * c], Bl[4 * ph - 3 + 2 * c]), A[t & 3][f]);
        else q_mfma<g1_quad(m, n % 12)>(BR[t - 12][2 * m + c], A[t & 3][f]);
        if constexpr (ph == 0 && n >= 1 && n < 13) { Bl[n - 1] = G1_HALF(3, (2 + (n - 1) / 4) & 3, ((n - 1) >> 1) & 1, (n - 1) & 1); SB0; }
        if constexpr (ph == 0 && n < 24) scan(IC<14 + n / 12>{}, IC<n % 12>{});
        if constexpr (ph > 0 && n % 3 == 2) q_put<g1_quad(ph, n / 3), ((ph - 1) * 12 + n / 3) * 1024>(ring + lane * 16);
        late(IC<624 + 36 * ph + n>{});
        if constexpr (ph == 0 && n >= 25 && n < 35 && (n & 1)) xmark((n - 25) / 2);
      });
    });
    TS(14); TC(3);
    LDS_BARRIER();
    f32x4 P[3][12];
#pragma unroll
    for (int k = 0; k < 12; ++k)
#pragma unroll
      for (int m = 1; m < 4; ++m) P[m - 1][(k & 1) * 6 + (k >> 1)] = *(const f32x4*)(smem + ((wid - m) & 3) * G_WS + ((m - 1) * 12 + (k & 1) * 6 + (k >> 1)) * 1024 + lane * 16);
    SB0;
    const f32x4 bv[2] = {f32x4{bflo(bw.x), bfhi(bw.x), bflo(bw.y), bfhi(bw.y)}, f32x4{bflo(bw.z), bfhi(bw.z), bflo(bw.w), bfhi(bw.w)}};
    f32x4 own[12];
    sfor<12>([&](auto K) { own[K] = q_get<K>() + bv[K / 6]; });
    SB0;
    LDS_BARRIER();
    sfor<6>([&](auto TT) {
      constexpr int t = TT;
      sfor<4>([&](auto J) { G2_LDB(t, t, J.value); });
      u32x4 o;
#pragma unroll
      for (int c = 0; c < 2; ++c) {
        const f32x4 u = (own[c * 6 + t] + P[0][c * 6 + t]) + (P[1][c * 6 + t] + P[2][c * 6 + t]);
        const f32x4 z = u * __builtin_elementwise_fma(u * u, (f32x4)(-0.1029432397f), (f32x4)(-2.302208198f));
        const f32x4 den = f32x4{__builtin_amdgcn_exp2f(z.x), __builtin_amdgcn_exp2f(z.y), __builtin_amdgcn_exp2f(z.z), __builtin_amdgcn_exp2f(z.w)} + 1.0f;
        const f32x4 y = u * f32x4{__builtin_amdgcn_rcpf(den.x), __builtin_amdgcn_rcpf(den.y), __builtin_amdgcn_rcpf(den.z), __builtin_amdgcn_rcpf(den.w)};
        o[2 * c] = min(pkv(y.xy), 0xfffffffeu); o[2 * c + 1] = min(pkv(y.zw), 0xfffffffeu);
      }
      __builtin_amdgcn_raw_buffer_store_b128(o, rw, WS_GF + ((nt1 * 4 + wid) * 36 + mt * 6 + t) * 1024 + lane * 16, 0, SC1);
      SB0;
    });
    TS(15);
    sfor<12>([&](auto I) { G2_LDB(6 + I / 4, 6 + I / 4, I % 4); });
    VMCNT(42);
    keep(pf3);
  } else {
#pragma unroll
    for (int i = 0; i < 5; ++i) xmark(i);
    constexpr int n1 = L_ * W1L - PF1_E2 - PFN * 1280;
    const unsigned id = (idx - 27) * 256 + tid;
    warm<W1L>(sink, sb1, F_ * 2, w1c0, PF1_E2 + PFN * 1280, L_ * W1L, id, 1280);
    warm<8>(sink, sb2, L_ * 2, w2c0, 0, PF3_R0 * 8, (id + 12800 - n1) % 1280, 1280);
    warm<8>(sink, sb2, L_ * 2, w2c0, (PF3_R0 + 864 + 216 * PF3_X) * 8, F_ * 4, (id + 38400 - n1 - PF3_R0 * 8) % 1280, 1280);
    arrive<PF_WIN + 1>(rw, 1, tb & 15);
    TS(15);
    sfor<36>([&](auto I) { if constexpr (I == 32) VMCNT(32); G2_LDB(I / 4, I / 4, I % 4); });
  }

  {
    unsigned rb[4];
#pragma unroll
    for (int c = 0; c < 4; ++c) rb[c] = ring + 128 * (8 * g + q4) + 16 * ((2 * ((c + wid) & 3) + (p4 >> 1)) ^ (2 * ((q4 >> 1) | ((g & 1) << 1)))) + 8 * (p4 & 1);
#define G2_HALF(slot, c, h) trd(rb[c] + (slot) * 4096 + 512 * (h))
    const int sh0 = kh * 6 + (3 * dq) / 2;
    const unsigned xo = xin + (kh ? -20480 : 20480), xo4 = (lane >> 5) == kh ? OOB : xo + 4096;
    const unsigned va[3] = {(unsigned)((9 * dq + 5 * kh) * 1024 + lane * 16), (unsigned)((9 * dq + 4) * 1024 + lane * 16), (unsigned)((9 * dq + 5 - 5 * kh) * 1024 + lane * 16)};
    const unsigned a_base = WS_GF + (kh * 72 + wid) * 36864;
    u32x4 A[36][5], W[2][4];
    unsigned aso[18];
#pragma unroll
    for (int i = 0; i < 18; ++i) { aso[i] = a_base + g2_kb(i) * (4 * 36864); asm volatile("" : "+s"(aso[i])); }
#define G2_AARG(J) A[g2_astep(J)][g2_af(J)], va[(J) >= 90 ? 0 : g2_af(J) == 4 ? 1 : 2], sw, aso[g2_tile(g2_astep(J))]
#define G2_AIMM(J) ((g2_af(J) & 3) * 1024)
    auto lda = [&](auto J) { bload_<G2_AIMM(J)>(G2_AARG(J)); };
    sfor<3>([&](auto K) {
      VMCNT(24);
      if constexpr (K == 1) {
        if (g1_on) {
          __builtin_amdgcn_s_barrier();
          if (tid < 16) __builtin_amdgcn_raw_ptr_buffer_atomic_add_i32(1, rw, SY_CNT + ((8 + tid) * 16 + (tid < 8 ? tb & 15 : (nt1 >= 18) * 6 + mt)) * 256, 0, SC1);
        }
      }
      sfor<12>([&](auto I) {
        constexpr int q = 3 * K + I / 4;
        if constexpr (q < 7) q_frag<36 + 4 * q + I % 4, q * 4096>(rb[I % 4]);
        else W[q - 7][I % 4] = cat(G2_HALF(q, I % 4, 0), G2_HALF(q, I % 4, 1));
      });
      if constexpr (K == 0) sfor<4>([&](auto I) { q_set<20 + I>(bflo(bias2)); });
      asm volatile("s_waitcnt lgkmcnt(0)" ::: "memory");
      SB0;
      sfor<12>([&](auto I) { constexpr int q = 3 * K + I / 4; G2_LDB(q, 9 + q, I % 4); });
    });
    if (!g1_on) await(rw, 2, xc, sh0, 2, 18, wid == 0, lane);
    TS(16);
    sfor<g2_aend(G2_A0 - 1)>(lda);
    unsigned mx, my, gpoll = 0, hpoll = 0;
    auto scan = [&](auto T, auto K) {
      const u32x4& a = A[T][K >> 1];
      if constexpr (K & 1) { my = K == 1 ? max(a.z, a.w) : umax3(my, a.z, a.w); asm volatile("" : "+v"(my)); }
      else { mx = K == 0 ? max(a.x, a.y) : umax3(mx, a.x, a.y); asm volatile("" : "+v"(mx)); }
      SB0;
    };
    auto check = [&](auto T) {
      if (__builtin_expect(__builtin_amdgcn_ballot_w64(max(mx, my) == ~0u) != 0, 0)) {
        constexpr int j1 = T == 0 ? g2_aend(G2_A0 - 1) : G2S.issued[T == 0 ? 0 : T - 1];
#pragma nounroll
        for (int spin = 0; spin < (1 << 16); ++spin) {
          __builtin_amdgcn_s_sleep(POLL_SLEEP);
          sfor<j1 - 5 * T>([&](auto I) { constexpr int J = 5 * T + I; bload_r<G2_AIMM(J)>(G2_AARG(J)); });
          wait5<0, 5>(A[T]);
          sfor<10>([&](auto K) { scan(T, K); });
          if (__builtin_amdgcn_ballot_w64(max(mx, my) == ~0u) == 0) break;
        }
      }
    };
    const int s = nt2 * 64 + wid * 16 + r16;
    const int dfull = 144 * dq + 80 * kh, dhalf = 144 * dq + 64 + 4 * g;
    u32x2 hw[5];
    u32x4 xr[5];
    u32x2 Bh[2][8];
    f32x4 Q[15];
    float xv[4];
    wait5<g2_aend(G2_A0 - 1) - 5, 5>(A[0]);
    sfor<10>([&](auto K) { scan(IC<0>{}, K); });
    check(IC<0>{});
    TS(17); TC(4);
    auto mm = [&](auto V, auto C, auto F) {
      constexpr int v = V, c = C, w = g2_tile(v), ac = v < 18 ? 5 * c + F : 20 + 4 * c + F;
      if constexpr (v == 0) q_mfma_b0<ac, 36 + c>(A[v][F]);
      else if constexpr (v == 18 && c > 0) q_mfma0<ac>(A[v][F], cat(Bh[v & 1][2 * c], Bh[v & 1][2 * c + 1]));
      else if constexpr (w < 7) q_mfma_b<ac, 36 + 4 * w + c>(A[v][F]);
      else if constexpr (w < 9) q_mfma<ac>(A[v][F], W[w - 7][c]);
      else q_mfma<ac>(A[v][F], cat(Bh[v & 1][2 * c], Bh[v & 1][2 * c + 1]));
    };
    sfor<33>([&](auto VV) {
      constexpr int v = VV, nf = g2_nf(v), nm = 4 * nf;
      sfor<nm>([&](auto NN) {
        constexpr int n = NN, op = G2S.op[v][n], xs = g2_xs(v, n);
        if constexpr (n == g2_wp(v)) wait5<G2S.wait[v], g2_nf(v + 1)>(A[v + 1]);
        if constexpr (v == G2S.tchk && n == 0) {
          asm volatile("s_waitcnt vmcnt(%1)" : "+v"(gpoll) : "i"(G2S.pwait));
          if (__builtin_expect(__builtin_amdgcn_ballot_w64(gpoll != (lane < 2 ? 18u : 0u)) != 0, 0)) {
            spin_cnt(rw, cnt_off(2, xc, sh0, 2, lane), lane < 2 ? 18u : 0u);
          }
        }
        if constexpr (v == 23 && n == 4) {
          asm volatile("" : "+v"(hpoll));
          if (__builtin_expect(__builtin_amdgcn_ballot_w64(hpoll != (lane < 16 ? 16u : 0u)) != 0, 0)) spin_cnt(rw, cnt_off(1, xc, 0, 16, lane), lane < 16 ? 16u : 0u);
          LDS_BARRIER();
        }
        mm(VV, IC<n / nf>{}, IC<n % nf>{});
        if constexpr (g2_tile(v + 1) >= 9) {
          if constexpr (n >= 1 && n < 9) { Bh[(v + 1) & 1][n - 1] = G2_HALF(g2_tile(v + 1) - 9, (n - 1) >> 1, (n - 1) & 1); SB0; }
          if constexpr (n == nm - 2) __builtin_amdgcn_s_waitcnt(0xc07f);
        }
        if constexpr ((v == 21 || v == 22) && (n & 1) && 8 * (v - 21) + n / 2 < 15) q_put<5 + 8 * (v - 21) + n / 2, (8 * (v - 21) + n / 2) * 1024>(ring + lane * 16);
        if constexpr (16 * v + n >= 16 * 23 + 4 && 16 * v + n < 16 * 23 + 19) {
          constexpr int k = 16 * (v - 23) + n - 4, c = 1 + k % 3, f = k / 3;
          Q[k] = *(const f32x4*)(smem + ((wid - c) & 3) * G_WS + (5 * c - 5 + f) * 1024 + lane * 16);
          SB0;
        }
        if constexpr (xs >= 0) {
          constexpr int f = xs / 11, j = xs % 11, e = 2 * (j & 1);
          if constexpr (j < 2) a_get2<4 * f + e>(xv[e], xv[e + 1]);
          else if constexpr (j < 8) { fadd(xv[e], Q[3 * f + j / 2 - 1][e]); fadd(xv[e + 1], Q[3 * f + j / 2 - 1][e + 1]); }
          else if constexpr (j < 10) { umin_ip(xv[e]); umin_ip(xv[e + 1]); }
          else bstore(__builtin_bit_cast(u32x4, f32x4{xv[0], xv[1], xv[2], xv[3]}), f < 4 ? xo + f * 1024 : xo4, sw);
        }
        if constexpr (op == 5000) poll_issue(gpoll, cnt_off(2, xc, sh0, 2, lane), sw);
        else if constexpr (op == 4000) poll_issue(hpoll, cnt_off(1, xc, 0, 16, lane), sw);
        else if constexpr (op >= 3005) bload_<((op - 3005) & 3) * 1024>(xr[op - 3005], op < 3009 ? xin : xin4, sw, 0u);
        else if constexpr (op >= 3000) bload2(hw[op - 3000], WS_H + (s * D_ + (op < 3004 ? dfull + 4 * g + 16 * (op - 3000) : dhalf)) * 2, sw);
        else if constexpr (op >= 1000) lda(IC<op - 1000>{});
        if constexpr (v + 1 < G2_S0 && n >= 10) scan(IC<v + 1>{}, IC<n - 10>{});
      });
      if constexpr (v + 1 < G2_S0) check(IC<v + 1>{});
    });
    sfor<4>([&](auto PP) {
      constexpr int ph = PP;
      sfor<12>([&](auto NN) {
        constexpr int n = NN, v = 33 + n / 4;
        if constexpr (ph == 0 && (n == 4 || n == 8)) wait5<G2S.wait[v - 1], 4>(A[v]);
        mm(IC<v>{}, IC<(ph + 1) & 3>{}, IC<n % 4>{});
        if constexpr (ph > 0 && n >= 4 && !(n & 1)) q_put<20 + 4 * ph + n / 2 - 2, (12 + 4 * ph + n / 2 - 2) * 1024>(ring + lane * 16);
      });
    });
    TS(18); TC(7);
    LDS_BARRIER();
#pragma unroll
    for (int k = 0; k < 12; ++k) Q[k] = *(const f32x4*)(smem + ((wid - 1 - k % 3) & 3) * G_WS + (16 + 4 * (k % 3) + k / 3) * 1024 + lane * 16);
    SB0;
    f32x4 own[5];
    asm volatile("s_waitcnt vmcnt(0)" : "+v"(hw[0]), "+v"(hw[1]), "+v"(hw[2]), "+v"(hw[3]), "+v"(hw[4]));
    sfor<5>([&](auto T) {
      const f32x4 h = {bflo(hw[T].x), bfhi(hw[T].x), bflo(hw[T].y), bfhi(hw[T].y)};
      if constexpr (T < 4) own[T] = q_get<20 + T>() + h;
      else own[T] = f32x4{xv[0], xv[1], xv[2], xv[3]} + (h + bflo(bias2));
    });
#pragma nounroll
    for (int spin = 0; spin < (1 << 16); ++spin) {
      wait5<63, 5>(xr);
      if (__all(umax4(xr[0], umax4(xr[1], umax4(xr[2], umax4(xr[3], umax4(xr[4], 0u))))) != ~0u)) break;
      __builtin_amdgcn_s_sleep(1);
      sfor<5>([&](auto F) { bload_r<(F & 3) * 1024>(xr[F], F < 4 ? xin : xin4, sw, 0u); });
      VMCNT(0);
    }
    wait5<63, 5>(xr);
#pragma unroll
    for (int f = 0; f < 4; ++f) own[f] += (Q[3 * f] + Q[3 * f + 1]) + Q[3 * f + 2];
    const rsrc_t ro = mk_rsrc(out, SZ);
    const unsigned oo = (s * D_ + dfull + 16 * (g & 1) + 4 * (g & 2)) * 2;
#pragma unroll
    for (int p = 0; p < 2; ++p) {
      const f32x4 y0 = own[2 * p] + __builtin_bit_cast(f32x4, xr[2 * p]), y1 = own[2 * p + 1] + __builtin_bit_cast(f32x4, xr[2 * p + 1]);
      unsigned a0 = pkv(y0.xy), a1 = pkv(y0.zw), b0 = pkv(y1.xy), b1v = pkv(y1.zw);
      asm volatile("s_nop 1\n\tv_permlane16_swap_b32 %0, %2\n\tv_permlane16_swap_b32 %1, %3" : "+v"(a0), "+v"(a1), "+v"(b0), "+v"(b1v));
      __builtin_amdgcn_raw_buffer_store_b128(u32x4{a0, a1, b0, b1v}, ro, oo + 64 * p, 0, 0);
    }
    const f32x4 yh = own[4] + __builtin_bit_cast(f32x4, xr[4]);
    __builtin_amdgcn_raw_buffer_store_b64(u32x2{pkv(yh.xy), pkv(yh.zw)}, ro, (lane >> 5) == kh ? (s * D_ + dhalf) * 2 : OOB, 0, 0);
    TS(19);
  }
  TSOUT();
  asm volatile(".Lk_all_end:" ::: "a255");
}

extern "C" void kernel_launch(void* const* d_in, const int* in_sizes, int n_in,
                              void* d_out, int out_size, void* d_ws, size_t ws_size,
                              hipStream_t stream) {
  k_all<<<256, 256, 0, stream>>>((const u16*)d_in[0], (const u16*)d_in[1], (const u16*)d_in[2], (const u16*)d_in[3], (const u16*)d_in[4],
                                 (const u16*)d_in[5], (const u16*)d_in[6], (const u16*)d_in[8], (const u16*)d_in[9], (const u16*)d_in[10],
                                 (const u16*)d_in[11], (char*)d_ws, (u16*)d_out);
}
```

```cpp
#include <hip/hip_runtime.h>
#include <stdint.h>
#include <utility>

typedef unsigned short u16;
typedef unsigned long long u64;
typedef __attribute__((ext_vector_type(4))) short s16x4;
typedef __attribute__((ext_vector_type(2))) unsigned short u16x2;
typedef __attribute__((ext_vector_type(8))) __bf16 bf16x8;
typedef __attribute__((ext_vector_type(2))) __bf16 bf16x2;
typedef __attribute__((ext_vector_type(4))) float f32x4;
typedef __attribute__((ext_vector_type(2))) float f32x2;
typedef __attribute__((ext_vector_type(4))) unsigned u32x4;
typedef __attribute__((ext_vector_type(2))) unsigned u32x2;
typedef __attribute__((ext_vector_type(4))) int i32x4;
typedef __amdgpu_buffer_rsrc_t rsrc_t;
#define LAS __attribute__((address_space(3)))
#define DI __device__ __forceinline__

#define TSDECL
#define TS(i)
#define TC(i)
#define TSOUT()

#define G2_LEAD 4
#define PF_WIN 1
#define PF1_A 3
#define PF0 0
#define PF2_ORD 1
#define PFK 0
#define PFK_POL ""
#define G1_ROTM 5
#define G1_ROTS 0
#define SIG_C 0
#define NE 6
#define E_T9 705
#define G2_DELAY 0
#define X_DELAY 0
#define G1_BURST 0
#define SGX 0
#define SP_N 10
#define RETRY_N 2
#define PF1_B 2
#define PF1_C 3
#define PFN 3
#define PF3_R0 896
#define PF3_X 0
#define PF2_END F_
#define PF3_T0 650
#define PF3_DT 30
#define PF2_DON 1280
#define W1L 9
#define SWAP1 1
#define SWAP2 0
#define ABL 0
#define SPARE_SLEEP 16
#define GATHER_AT 12
#define G1_DELAY 8
#define G1_A0 2
#define POLL_SLEEP 4

constexpr int L_ = 2048, D_ = 576, N_ = 128, F_ = 4608;
constexpr unsigned SZ = L_ * D_ * 2;
constexpr unsigned WS_UB = 0, WS_AT = SZ, WS_H = 2 * SZ, WS_HF = 3 * SZ, WS_GF = 4 * SZ, WS_SY = 7 * SZ, WS_X = WS_SY + 131072, WS_BYTES = WS_X + 256 * 20480;
constexpr unsigned SY_EPOCH = WS_SY, SY_FLAG = WS_SY + 4096, SY_CNT = WS_SY + 32768;
constexpr unsigned OOB = 0x7ff00000u;
constexpr int SC1 = 16;
constexpr unsigned ROWB = L_ * 2;
constexpr int NMARK = 9;

template <class Fn, int... I> DI void sfor_(Fn&& f, std::integer_sequence<int, I...>) { (f(std::integral_constant<int, I>{}), ...); }
template <int N, class Fn> DI void sfor(Fn&& f) { sfor_(f, std::make_integer_sequence<int, N>{}); }

DI float bflo(unsigned w) { return __uint_as_float(w << 16); }
DI float bfhi(unsigned w) { return __uint_as_float(w & 0xffff0000u); }
DI f32x2 unpk(unsigned w) { return f32x2{bflo(w), bfhi(w)}; }
DI float bf2f(u16 v) { return __uint_as_float(((unsigned)v) << 16); }
DI unsigned pkv(f32x2 v) { return __builtin_bit_cast(unsigned, __builtin_convertvector(v, bf16x2)); }
DI unsigned pk2(float lo, float hi) { return pkv(f32x2{lo, hi}); }
DI float rbf(float f) { return bflo(pk2(f, f)); }
DI unsigned perm_lo(unsigned a, unsigned b) { return __builtin_amdgcn_perm(b, a, 0x05040100u); }
DI unsigned perm_hi(unsigned a, unsigned b) { return __builtin_amdgcn_perm(b, a, 0x07060302u); }
DI f32x4 mfma16(u32x4 a, u32x4 b, f32x4 c) { return __builtin_amdgcn_mfma_f32_16x16x32_bf16(__builtin_bit_cast(bf16x8, a), __builtin_bit_cast(bf16x8, b), c, 0, 0, 0); }
DI rsrc_t mk_rsrc(const void* p, unsigned bytes) { return __builtin_amdgcn_make_buffer_rsrc((void*)p, 0, bytes, 0x00020000); }
DI i32x4 mk_srd(const void* p, unsigned bytes) {
  const u64 a = (u64)p;
  i32x4 r; r.x = (int)(unsigned)a; r.y = (int)((unsigned)(a >> 32) & 0xffffu); r.z = (int)bytes; r.w = 0x00020000; return r;
}
#define LDS_BARRIER() do { asm volatile("s_waitcnt lgkmcnt(0)" ::: "memory"); __builtin_amdgcn_s_barrier(); asm volatile("" ::: "memory"); } while (0)
template <int IMM> DI void bload_(u32x4& dst, unsigned voff, i32x4 srd, unsigned soff) {
  asm volatile("buffer_load_dwordx4 %0, %1, %2, %3 offen offset:%4 sc1" : "=v"(dst) : "v"(voff), "s"(srd), "s"(soff), "i"(IMM) : "memory");
}
template <int IMM> DI void bload_r(u32x4& dst, unsigned voff, i32x4 srd, unsigned soff) {
  asm volatile("buffer_load_dwordx4 %0, %1, %2, %3 offen offset:%4 sc1" : "+v"(dst) : "v"(voff), "s"(srd), "s"(soff), "i"(IMM) : "memory");
}
template <int M0OFF, int IMM> DI void bdma(unsigned ldsbase, unsigned voff, i32x4 srd, unsigned soff) {
  if constexpr (IMM == 0) asm volatile("s_add_u32 m0, %0, %1\n\ts_nop 0\n\tbuffer_load_dwordx4 %2, %3, %4 offen lds" :: "s"(ldsbase), "i"(M0OFF), "v"(voff), "s"(srd), "s"(soff) : "memory", "scc");
  else asm volatile("buffer_load_dwordx4 %0, %1, %2 offen offset:%3 lds" :: "v"(voff), "s"(srd), "s"(soff), "i"(IMM) : "memory");
}
template <int OFF> DI void put_row(unsigned v) { asm volatile("ds_write_addtid_b32 %0 offset:%1" :: "v"(v), "i"(OFF) : "memory"); }
DI void keep(unsigned v) { asm volatile("" :: "v"(v)); }
DI void bload2(u32x2& dst, unsigned voff, i32x4 srd) { asm volatile("buffer_load_dwordx2 %0, %1, %2, 0 offen sc1" : "=v"(dst) : "v"(voff), "s"(srd) : "memory"); }
DI void bload2r(u32x2& dst, unsigned voff, i32x4 srd) { asm volatile("buffer_load_dwordx2 %0, %1, %2, 0 offen sc1" : "+v"(dst) : "v"(voff), "s"(srd) : "memory"); }
DI void bstore(const u32x4& v, unsigned voff, i32x4 srd) { asm volatile("buffer_store_dwordx4 %0, %1, %2, 0 offen sc1\n\ts_nop 1" :: "v"(v), "v"(voff), "s"(srd) : "memory"); }
DI unsigned umax3(unsigned a, unsigned b, unsigned c) { return max(max(a, b), c); }
DI unsigned umax4(const u32x4& a, unsigned m) { return umax3(umax3(a.x, a.y, a.z), a.w, m); }
template <int Q, int IMM> DI void q_load(unsigned voff, i32x4 srd, unsigned soff) {
  asm volatile("buffer_load_dwordx4 a[%0:%1], %2, %3, %4 offen offset:%5" :: "i"(4 * Q), "i"(4 * Q + 3), "v"(voff), "s"(srd), "s"(soff), "i"(IMM));
}
template <int Q, int OFF> DI void q_put(unsigned addr) { asm volatile("ds_write_b128 %2, a[%0:%1] offset:%3" :: "i"(4 * Q), "i"(4 * Q + 3), "v"(addr), "i"(OFF) : "memory"); }
template <int Q, int OFF> DI void q_frag(unsigned addr) {
  asm volatile("ds_read_b64_tr_b16 a[%0:%1], %4 offset:%5\n\tds_read_b64_tr_b16 a[%2:%3], %4 offset:%6"
               :: "i"(4 * Q), "i"(4 * Q + 1), "i"(4 * Q + 2), "i"(4 * Q + 3), "v"(addr), "i"(OFF), "i"(OFF + 512) : "memory");
}
template <int Q> DI void q_mfma(const u32x4& a, const u32x4& b) { asm volatile("v_mfma_f32_16x16x32_bf16 a[%0:%1], %2, %3, a[%0:%1]" :: "i"(4 * Q), "i"(4 * Q + 3), "v"(a), "v"(b)); }
template <int Q> DI void q_mfma0(const u32x4& a, const u32x4& b) { asm volatile("v_mfma_f32_16x16x32_bf16 a[%0:%1], %2, %3, 0" :: "i"(4 * Q), "i"(4 * Q + 3), "v"(a), "v"(b)); }
template <int Q, int B> DI void q_mfma_b(const u32x4& a) {
  asm volatile("v_mfma_f32_16x16x32_bf16 a[%0:%1], %4, a[%2:%3], a[%0:%1]" :: "i"(4 * Q), "i"(4 * Q + 3), "i"(4 * B), "i"(4 * B + 3), "v"(a));
}
template <int Q, int B> DI void q_mfma_b0(const u32x4& a) {
  asm volatile("v_mfma_f32_16x16x32_bf16 a[%0:%1], %4, a[%2:%3], 0" :: "i"(4 * Q), "i"(4 * Q + 3), "i"(4 * B), "i"(4 * B + 3), "v"(a));
}
template <int Q> DI f32x4 q_get() {
  float x, y, z, w;
  asm volatile("v_accvgpr_read_b32 %0, a[%4]\n\tv_accvgpr_read_b32 %1, a[%4+1]\n\tv_accvgpr_read_b32 %2, a[%4+2]\n\tv_accvgpr_read_b32 %3, a[%4+3]" : "=v"(x), "=v"(y), "=v"(z), "=v"(w) : "i"(4 * Q));
  return f32x4{x, y, z, w};
}
template <int R> DI void a_get2(float& x, float& y) { asm volatile("v_accvgpr_read_b32 %0, a[%2]\n\tv_accvgpr_read_b32 %1, a[%2+1]" : "=v"(x), "=v"(y) : "i"(R)); }
DI void umin_ip(float& a) { asm volatile("v_min_u32 %0, -2, %0" : "+v"(a)); }
template <int Q> DI void q_set(float v) {
  asm volatile("v_accvgpr_write_b32 a[%0], %1\n\tv_accvgpr_write_b32 a[%0+1], %1\n\tv_accvgpr_write_b32 a[%0+2], %1\n\tv_accvgpr_write_b32 a[%0+3], %1" :: "i"(4 * Q), "v"(v));
}
#define BLOAD(dst, voff, srd, soff, imm) bload_<(imm)>(dst, voff, srd, soff)
#define SB0 __builtin_amdgcn_sched_barrier(0)
DI void pin_vf(f32x4& v) { asm volatile("" : "+v"(v)); }
DI void fadd(float& a, float b) { asm volatile("v_add_f32 %0, %0, %1" : "+v"(a) : "v"(b)); }
template <int IMM> DI void gld32(unsigned& d, unsigned voff, const void* base) { asm volatile("global_load_dword %0, %1, %2 offset:%3" : "=v"(d) : "v"(voff), "s"(base), "i"(IMM) : "memory"); }
template <int IMM> DI void gld16(unsigned& d, unsigned voff, const void* base) { asm volatile("global_load_ushort %0, %1, %2 offset:%3" : "=v"(d) : "v"(voff), "s"(base), "i"(IMM) : "memory"); }
DI void gld128(u32x4& d, unsigned voff, const void* base) { asm volatile("global_load_dwordx4 %0, %1, %2" : "=v"(d) : "v"(voff), "s"(base) : "memory"); }
DI void set_m0(unsigned v) { asm volatile("s_mov_b32 m0, %0\n\ts_nop 0" :: "s"(v) : "memory"); }
template <int N, int F> DI void wait5(u32x4* a) {
  if constexpr (F == 5) asm volatile("s_waitcnt vmcnt(%5)" : "+v"(a[0]), "+v"(a[1]), "+v"(a[2]), "+v"(a[3]), "+v"(a[4]) : "i"(N) : "memory");
  else asm volatile("s_waitcnt vmcnt(%4)" : "+v"(a[0]), "+v"(a[1]), "+v"(a[2]), "+v"(a[3]) : "i"(N) : "memory");
}
template <int N> DI void wait9(unsigned* a) { asm volatile("s_waitcnt vmcnt(%9)" : "+v"(a[0]), "+v"(a[1]), "+v"(a[2]), "+v"(a[3]), "+v"(a[4]), "+v"(a[5]), "+v"(a[6]), "+v"(a[7]), "+v"(a[8]) : "i"(N) : "memory"); }
DI void pin9(unsigned* a) { asm volatile("" : "+v"(a[0]), "+v"(a[1]), "+v"(a[2]), "+v"(a[3]), "+v"(a[4]), "+v"(a[5]), "+v"(a[6]), "+v"(a[7]), "+v"(a[8])); }
template <int N> DI void wait6(u32x4* a) { asm volatile("s_waitcnt vmcnt(%6)" : "+v"(a[0]), "+v"(a[1]), "+v"(a[2]), "+v"(a[3]), "+v"(a[4]), "+v"(a[5]) : "i"(N) : "memory"); }
#define VMCNT(n) asm volatile("s_waitcnt vmcnt(%0)" :: "i"(n) : "memory")
DI u32x2 trd(unsigned addr) { return __builtin_bit_cast(u32x2, __builtin_amdgcn_ds_read_tr16_b64_v4i16((LAS s16x4*)(size_t)addr)); }
DI u32x4 cat(u32x2 a, u32x2 b) { return u32x4{a.x, a.y, b.x, b.y}; }

template <int CTRL> DI float dpp_add(float v) { return v + __builtin_bit_cast(float, __builtin_amdgcn_update_dpp(0, __builtin_bit_cast(int, v), CTRL, 0xf, 0xf, true)); }
DI float half_sum(float v) {
  v = dpp_add<0xB1>(v); v = dpp_add<0x4E>(v); v = dpp_add<0x141>(v); v = dpp_add<0x140>(v);
  float w = v;
  asm volatile("s_nop 1\n\tv_permlane16_swap_b32 %0, %1" : "+v"(v), "+v"(w));
  return v + w;
}
template <int K> using IC = std::integral_constant<int, K>;
template <class Fn> DI void ln9(const f32x2* v, unsigned* o, Fn&& hook) {
  const f32x2 s = ((v[0] + v[1]) + (v[2] + v[3])) + ((v[4] + v[5]) + (v[6] + v[7])) + v[8];
  float a = s.x + s.y;
  hook(IC<0>{}, a);
  float mean = half_sum(a) * (1.0f / D_);
  hook(IC<1>{}, mean);
  f32x2 q0 = {0.f, 0.f}, q1 = {0.f, 0.f};
#pragma unroll
  for (int i = 0; i < 9; ++i) { const f32x2 t = v[i] - mean; if (i & 1) q1 = __builtin_elementwise_fma(t, t, q1); else q0 = __builtin_elementwise_fma(t, t, q0); }
  q0 += q1;
  a = q0.x + q0.y;
  hook(IC<2>{}, a);
  float var = half_sum(a) * (1.0f / D_);
  hook(IC<3>{}, var);
  const float mb = rbf(mean);
  const float sd = rbf(__builtin_amdgcn_sqrtf(rbf(rbf(var) + 1e-5f)));
  float inv = __builtin_amdgcn_rcpf(sd);
  f32x2 w[9];
#pragma unroll
  for (int i = 0; i < 9; ++i) w[i] = v[i] - mb;
  hook(IC<4>{}, inv);
#pragma unroll
  for (int i = 0; i < 9; ++i) o[i] = pkv(w[i]);
  SB0;
#pragma unroll
  for (int i = 0; i < 5; ++i) w[i] = unpk(o[i]);
  hook(IC<5>{}, inv);
#pragma unroll
  for (int i = 5; i < 9; ++i) w[i] = unpk(o[i]);
  SB0;
#pragma unroll
  for (int i = 0; i < 9; ++i) w[i] *= inv;
  hook(IC<6>{}, inv);
#pragma unroll
  for (int i = 0; i < 9; ++i) o[i] = pkv(w[i]);
  hook(IC<7>{}, inv);
}
DI f32x2 cmul(f32x2 a, f32x2 b) { const f32x2 bs = {-b.y, b.x}; return __builtin_elementwise_fma(a.yy, bs, a.xx * b); }
DI f32x2 cexp_p(float zl, float zv, float p) {
  const float m = __builtin_amdgcn_exp2f(zl * p);
  const float rev = __builtin_amdgcn_fractf(zv * p);
  return f32x2{__builtin_amdgcn_cosf(rev), __builtin_amdgcn_sinf(rev)} * m;
}
constexpr u64 G1_STEP = 0xDCBA98764310FE52ull;
DI unsigned g1_blk(unsigned q, unsigned strip) {
  const unsigned t = (unsigned)(G1_STEP >> (4 * q)) & 15u, k = t + (strip % G1_ROTM) * G1_ROTS;
  return t >= 12 ? t : k >= 12 ? k - 12 : k;
}
template <int LINES> DI unsigned slab_off(unsigned e, unsigned e1, unsigned pitch, unsigned c0) {
  unsigned r = e / LINES;
  const unsigned c = c0 + (e - r * LINES) * 128;
  if (LINES >= 9) r = g1_blk((r >> 7) & 15, c >> 8) * 128 + (r & 127);
  return e < e1 ? r * pitch + c : OOB;
}
DI void touch_nt(unsigned& sink, unsigned off, i32x4 srd) { asm volatile("buffer_load_dword %0, %1, %2, 0 offen" PFK_POL : "+v"(sink) : "v"(off), "s"(srd) : "memory"); }
DI void touch(unsigned& sink, unsigned off, i32x4 srd) { asm volatile("buffer_load_dword %0, %1, %2, 0 offen" : "=v"(sink) : "v"(off), "s"(srd) : "memory"); }
template <int LINES, int WIN = PF_WIN> DI void warm(unsigned lds, i32x4 srd, unsigned pitch, unsigned c0, unsigned e0, unsigned e1, unsigned idx, unsigned nthr) {
  set_m0(lds);
#pragma nounroll
  for (unsigned e = e0 + idx; e < e1 + idx; e += nthr) {
    const unsigned off = slab_off<LINES>(e, e1, pitch, c0);
    asm volatile("s_waitcnt vmcnt(%2)\n\tbuffer_load_dword %0, %1, 0 offen lds" :: "v"(off), "s"(srd), "i"(WIN) : "memory");
  }
}

template <int YOUNGER = 0> DI void arrive(rsrc_t rw, int ho, int shard) {
  VMCNT(YOUNGER);
  __builtin_amdgcn_s_barrier();
  if (threadIdx.x < 8) __builtin_amdgcn_raw_ptr_buffer_atomic_add_i32(1, rw, SY_CNT + ((ho * 8 + threadIdx.x) * 16 + shard) * 256, 0, SC1);
}
DI unsigned cnt_off(int ho, int rep, int s0, int ns, int lane) { return lane < ns ? SY_CNT + ((ho * 8 + rep) * 16 + s0 + lane) * 256 : OOB; }
DI void poll_issue(unsigned& f, unsigned off, i32x4 srd) { asm volatile("buffer_load_dword %0, %1, %2, 0 offen sc1" : "+v"(f) : "v"(off), "s"(srd) : "memory"); }
DI void add_one(unsigned one, unsigned off, i32x4 srd) { asm volatile("buffer_atomic_add %0, %1, %2, 0 offen sc1" :: "v"(one), "v"(off), "s"(srd) : "memory"); }
DI void spin_cnt(rsrc_t rw, unsigned off, unsigned w) {
#pragma nounroll
  for (int spin = 0; spin < (1 << 16); ++spin) {
    const unsigned f = __builtin_amdgcn_raw_buffer_load_b32(rw, off, 0, SC1);
    if (__all(f == w)) break;
    __builtin_amdgcn_s_sleep(POLL_SLEEP);
    asm volatile("" ::: "memory");
  }
}
DI void await(rsrc_t rw, int ho, int rep, int s0, int ns, unsigned want, bool poller, int lane) {
  if (poller) spin_cnt(rw, cnt_off(ho, rep, s0, ns, lane), lane < ns ? want : 0u);
  asm volatile("" ::: "memory");
  __builtin_amdgcn_s_barrier();
  asm volatile("" ::: "memory");
}

constexpr int KG_PITCH = 528;
constexpr int KG_B = 41 * KG_PITCH, KG_CH = 57 * KG_PITCH;
constexpr int KG_T = 3 * KG_CH, KG_TP = 2072 * 2;
constexpr int CV_KP = 259 * 16 + 16;
constexpr int CV_U = 4 * CV_KP;
constexpr int CV_UP = (29 + 256) * 16 + 64 + 16;
constexpr int CV_TB = CV_U + 2 * CV_UP;
constexpr int CV_P = 3 * CV_TB;
constexpr int G_WS = 36864;
constexpr int B0 = 16384;
constexpr int t2_off(int i) { return i < 7 ? 9216 + 1024 * i : B0 + 32768 + 1024 * (i - 7); }
constexpr int K_LDS = B0 + 4 * G_WS;
static_assert(CV_KP % 256 == 64 && CV_UP % 64 == 32 && CV_TB % 16 == 0 && KG_CH % 16 == 0 && CV_P + 65536 <= 4 * G_WS && KG_T >= CV_P && KG_T + 3 * KG_TP <= 4 * G_WS &&
              K_LDS <= 163840, "");

constexpr int kg_e(int r) { return r < 16 ? r & 7 : r < 24 ? 2 * (r & 3) : 4 * (r & 1); }
constexpr int kg_g(int r) { return r < 16 ? r >> 3 : r < 24 ? 2 + ((r - 16) >> 2) : 4 + ((r - 24) >> 1); }

constexpr int cv_ab(int j) {
  int n = 0;
  for (int m = 0; m < 8; ++m)
    for (int o = 0; o <= m && m + o < 8; ++o) { if (n++ == j) return 8 * m + o; if (o < m && n++ == j) return 8 * o + m; }
  return 0;
}
constexpr int cv_a(int j) { return cv_ab(j) >> 3; }
constexpr int cv_b(int j) { return cv_ab(j) & 7; }
constexpr bool cv_first(int j) { for (int i = 0; i < j; ++i) if (cv_a(i) + cv_b(i) == cv_a(j) + cv_b(j)) return false; return true; }
constexpr int cv_done(int j) { return j == 8 ? 0 : j == 10 ? 1 : j == 12 ? 2 : j == 15 ? 3 : j == 33 ? 4 : j == 34 ? 5 : -1; }

constexpr int g1_quad(int m, int k) { return (m == 0 ? 0 : m == 3 ? 12 : 12 + 12 * m) + k; }
constexpr int g2_tile(int v) { return v < 18 ? v : v < 22 ? v - 9 : v < 28 ? v - 22 : v < 33 ? v - 15 : v - 27; }
constexpr int g2_nf(int v) { return v < 18 ? 5 : 4; }
constexpr int g2_aend(int v) { return v < 18 ? 5 * v + 5 : v > 35 ? 162 : 4 * v + 22; }
constexpr int g2_astep(int j) { return j < 90 ? j / 5 : (j - 18) / 4; }
constexpr int g2_af(int j) { return j < 90 ? j % 5 : (j - 18) % 4; }
#define G2_A0 2
#define G2_D 4
#define G2_S0 10
constexpr int g2_wp(int v) { return v + 1 < G2_S0 ? 10 : v < 18 ? 16 : v < 33 ? 12 : 0; }
struct G2Sched { short op[36][20], wait[36], issued[36], tchk, pwait; };
constexpr int g2_xs(int v, int m) { const int k = 16 * (v - 24) + m - 4; return v >= 24 && k >= 0 && k < 55 ? k : -1; }
constexpr G2Sched g2_make() {
  G2Sched s{};
  int apos[162] = {}, seq = g2_aend(G2_A0 - 1), na = seq, ppos = 0;
  for (int i = 0; i < na; ++i) apos[i] = i;
  for (int v = 0; v < 35; ++v) {
    int list[20] = {}, n = 0;
    const int nm = 4 * g2_nf(v);
    if (v == G2_S0 - G2_D - 2) list[n++] = 5000;
    if (v == 15) list[n++] = 4000;
    while (na < g2_aend(v + G2_D) && n < 8) list[n++] = 1000 + na++;
    if (na > 5 * G2_S0 && !s.tchk) { s.tchk = (short)v; s.pwait = (short)(seq - 1 - ppos); }
    s.issued[v] = (short)na;
    if (v == 32) for (int i = 0; i < 10; ++i) list[n++] = 3000 + i;
    for (int m = 0, k = 0; m < nm; ++m) {
      if (m == g2_wp(v)) s.wait[v] = (short)(seq - 1 - apos[g2_aend(v + 1) - 1]);
      if (k < n && m == (k * nm) / n) {
        const int o = list[k++];
        s.op[v][m] = (short)o;
        if (o < 2000) apos[o - 1000] = seq; else if (o == 5000) ppos = seq;
        ++seq;
      }
      if (g2_xs(v, m) % 11 == 10) ++seq;
    }
  }
  return s;
}
constexpr G2Sched G2S = g2_make();
static_assert(G2S.tchk == G2_S0 - G2_D && G2S.issued[31] == 162 && G2_S0 + 2 * !!G2_LEAD <= 14, "");

constexpr bool g1_isR(int t) { return t == 2 || t == 5; }
constexpr int g1_lidx(int t) { return t < 2 ? t : t < 5 ? t - 1 : t - 2; }
constexpr int g1_kb(int t) { return t == 2 ? 0 : t == 5 ? 1 : t >= 14 ? t - 12 : 4 + g1_lidx(t); }
constexpr int g1_nb(int t) { return (t < 1 || t > 11) ? 0 : t == 11 ? 5 : t >= 7 ? 8 : ((t - 1) % 3 == 2 ? 4 : 6); }
constexpr int g1_b0(int t) { return t >= 7 ? 32 + 8 * (t - 7) : 16 * ((t - 1) / 3) + 6 * ((t - 1) % 3); }
constexpr int g1_ops(int t) { return t == 0 ? 6 * (4 - G1_A0) : g1_nb(t) + 6; }
constexpr int PF1_E1 = (PF1_A + PF1_B) * 2048, PF1_E2 = PF1_E1 + PF1_C * 1280;

__global__ __launch_bounds__(256, 1) void k_all(const u16* __restrict__ x, const u16* __restrict__ logA, const u16* __restrict__ Aim,
                                                const u16* __restrict__ Cre, const u16* __restrict__ Cim, const u16* __restrict__ logdt,
                                                const u16* __restrict__ Dsk, const u16* __restrict__ w1, const u16* __restrict__ b1,
                                                const u16* __restrict__ w2, const u16* __restrict__ b2, char* __restrict__ ws, u16* __restrict__ out) {
  __shared__ __attribute__((aligned(16))) char smem_[K_LDS];
  char* const smem = smem_ + B0;
  const int tid = threadIdx.x, lane = tid & 63, wid = __builtin_amdgcn_readfirstlane(tid >> 6);
  const int xc = blockIdx.x & 7, idx = blockIdx.x >> 3;
  TSDECL;
  TS(0);
  const rsrc_t rw = mk_rsrc(ws, WS_BYTES);
  const i32x4 sw = mk_srd(ws, WS_BYTES), sb1 = mk_srd(w1, L_ * F_ * 2), sb2 = mk_srd(w2, L_ * F_ * 2);
  const unsigned ldsT = (unsigned)(size_t)smem_, lds0 = ldsT + B0, ring = lds0 + wid * G_WS;
  const int g = lane >> 4, r16 = lane & 15, q4 = r16 >> 2, p4 = lane & 3;
  const int tb = xc * 24 + idx + (168 - 16 * xc) * ((idx + 8) >> 5);
  const int tok = wid * 2 + (lane >> 5), ll = lane & 31;
  const bool cv_on = idx < 24;
  const int d0 = xc * 72 + idx * 3;
  const unsigned w1c0 = W1L == 10 ? ((27 * xc) / 6) * 256 : xc * 1152;

  const int lin1 = xc * 27 + (SWAP1 && !(xc & 1) ? (SWAP1 == 1 ? max(26 - idx, 0) : (idx + 16) % 27) : idx), mt = lin1 % 6, nt1 = min(lin1 / 6, 35);
  const int kh = xc & 1, dq = idx & 3, lead = idx >> 2, sg = (xc >> 1) ^ SGX, nt2 = sg * 8 + (lead ^ 4);
  const unsigned w2c0 = kh * (F_ / 2) * (L_ * 2) + sg * 1024;
  unsigned xw[9], pp[18], bias2, pf0[3];
  u32x2 ep;
  u32x4 bw;
  sfor<9>([&](auto I) { gld32<128 * I>(xw[I], ((tb * 8 + tok) * (D_ / 2) + ll) * 4, x); });
  bload2(ep, SY_EPOCH, sw);
  gld128(bw, (nt1 * 128 + wid * 32 + 8 * g) * 2, b1);
  gld16<0>(bias2, (nt2 * 64 + wid * 16 + r16) * 2, b2);
  if (cv_on) {
    const unsigned o = (d0 * N_ + (wid & 1) * 64 + lane) * 2;
    sfor<3>([&](auto C) {
      gld16<256 * C>(pp[6 * C], o, logA); gld16<256 * C>(pp[6 * C + 1], o, Aim); gld16<256 * C>(pp[6 * C + 2], o, Cre); gld16<256 * C>(pp[6 * C + 3], o, Cim);
      gld16<2 * C>(pp[6 * C + 4], d0 * 2, logdt); gld16<2 * C>(pp[6 * C + 5], d0 * 2, Dsk);
    });
  } else {
    u64 pc; unsigned len;
    asm volatile("s_getpc_b64 %0\n.Lk_all_pc:\n\ts_mov_b32 %1, .Lk_all_end-.Lk_all_pc" : "=s"(pc), "=s"(len));
    const i32x4 sc = mk_srd((const void*)pc, idx == 31 ? len : 0u);
    touch(pf0[0], tid * 128, sc); touch(pf0[1], (256 + tid) * 128, sc);
    touch(pf0[2], slab_off<W1L>((idx - 24) * 256 + tid, PF0 * 2048, F_ * 2, w1c0), sb1);
  }
  unsigned kad[11], ugo[3], ulo[3], ufo[3], kto[4], zoff, gmk;
  {
#pragma unroll
    for (int k = 0; k < 6; ++k) {
      const int j0 = 2047 - (8 * tid + 2 * k);
      const unsigned pr = pk2((float)j0, (float)(j0 - 1));
      kad[2 * k] = lds0 + KG_T + 8 + 2 * (int)bflo(pr);
      if (k < 5) kad[2 * k + 1] = lds0 + KG_T + 8 + 2 * (int)bfhi(pr);
    }
#pragma unroll
    for (int k = 0; k < 3; ++k) {
      const unsigned i = tid + 256 * k, t3 = i / 3, c = i - 3 * t3;
      ugo[k] = WS_UB + t3 * (D_ * 16) + (d0 + c) * 16; ulo[k] = c * CV_TB + CV_U + (29 + t3) * 16; ufo[k] = SY_FLAG + xc * 2048 + t3 * 8;
    }
    int pb, o1, o2;
    if (wid == 0) { pb = 4 * (g & 1) + 128 * (g >> 1); o1 = 1; o2 = 2; }
    else if (wid == 1) { pb = g < 2 ? 256 + 128 * g : 256 * g; o1 = g < 2 ? 2 : 4; o2 = g < 2 ? 4 : 128; }
    else { pb = 1024 + 512 * g; o1 = 128; o2 = 256; }
    pb += 8 * r16;
    kto[0] = KG_T + 8 + 2 * min(pb, 2056); kto[1] = KG_T + 8 + 2 * min(pb + o1, 2056); kto[2] = KG_T + 8 + 2 * min(pb + o2, 2056); kto[3] = KG_T + 8 + 2 * min(pb + o1 + o2, 2056);
    {
      const unsigned j = ((xc >> 1) * 24 + idx) * 4 + wid, ks = j / 20, r = j - 20 * ks;
      gmk = (ks < 19 ? WS_GF + ((kh * 72 + ks) * 36 + r + 4 * (r / 5) + 4 - 4 * kh) * 1024 : OOB) + lane * 16;
    }
    const int zc = min(tid / 72, 2), zi = min(tid - 72 * zc, 71);
    zoff = zc * CV_TB + (zi < 12 ? (zi & 3) * CV_KP + (256 + (zi >> 2)) * 16 : CV_U + (zi & 1) * CV_UP + ((zi - 12) >> 1) * 16);
#pragma unroll
    for (int k = 0; k < 11; ++k) asm volatile("" : "+v"(kad[k]));
    asm volatile("" : "+v"(ugo[0]), "+v"(ugo[1]), "+v"(ugo[2]), "+v"(ulo[0]), "+v"(ulo[1]), "+v"(ulo[2]), "+v"(kto[0]), "+v"(kto[1]), "+v"(kto[2]), "+v"(kto[3]), "+v"(zoff), "+v"(ufo[0]), "+v"(ufo[1]), "+v"(ufo[2]), "+v"(gmk));
  }
  const unsigned tro = q4 * 128 + g * 32 + p4 * 8;
  auto chunk = [&](unsigned base, int blk) { return cat(trd(base + tro + blk * 1024), trd(base + tro + blk * 1024 + 512)); };
  if (cv_on) VMCNT(21); else VMCNT(6);
  pin9(xw);
  if (blockIdx.x == 255) {
    __builtin_amdgcn_raw_buffer_store_b32(0u, rw, SY_CNT + tid * 256, 0, SC1);
    if (tid < 128) __builtin_amdgcn_raw_buffer_store_b32(0u, rw, SY_CNT + (256 + tid) * 256, 0, SC1);
  }
  {
    f32x2 v[9];
    unsigned o[9];
#pragma unroll
    for (int i = 0; i < 9; ++i) v[i] = unpk(xw[i]);
    ln9(v, o, [](auto, float&) { SB0; });
    set_m0(ldsT + wid * 256);
    sfor<9>([&](auto I) { put_row<1024 * I>(o[I]); });
    if (cv_on || blockIdx.x == 255) VMCNT(0); else VMCNT(3);
    pin9(pp); pin9(pp + 9);
    asm volatile("" : "+v"(bw), "+v"(bias2), "+v"(ep));
    LDS_BARRIER();
    const unsigned off = WS_UB + tb * (D_ * 16) + tid * 16;
    __builtin_amdgcn_raw_buffer_store_b128(chunk(ldsT, wid), rw, off, 0, SC1);
    __builtin_amdgcn_raw_buffer_store_b128(chunk(ldsT, wid + 4), rw, off + 4096, 0, SC1);
    if (wid == 0) __builtin_amdgcn_raw_buffer_store_b128(chunk(ldsT, 8), rw, off + 8192, 0, SC1);
  }
  const u64 tag64 = (((u64)ep.y << 32) | ep.x) * 0x9E3779B97F4A7C15ull + 0xD1B54A32D192ED03ull;
  const u32x2 tag = {(unsigned)tag64, (unsigned)(tag64 >> 32)};
  TS(1);
  const unsigned sink = ring + 35 * 1024;
  auto signal_u = [&]() {
    __builtin_amdgcn_s_barrier();
    if (tid < 8) __builtin_amdgcn_raw_buffer_store_b64(tag, rw, SY_FLAG + tid * 2048 + tb * 8, 0, SC1);
  };
  auto await_u = [&]() {
#pragma nounroll
    for (int spin = 0; spin < (1 << 16); ++spin) {
      const u32x4 f0 = __builtin_amdgcn_raw_buffer_load_b128(rw, SY_FLAG + xc * 2048 + lane * 16, 0, SC1);
      const u32x4 f1 = __builtin_amdgcn_raw_buffer_load_b128(rw, SY_FLAG + xc * 2048 + 1024 + lane * 16, 0, SC1);
      const unsigned d = ((f0.x ^ tag.x) | (f0.y ^ tag.y) | (f0.z ^ tag.x) | (f0.w ^ tag.y)) | ((f1.x ^ tag.x) | (f1.y ^ tag.y) | (f1.z ^ tag.x) | (f1.w ^ tag.y));
      if (__all(d == 0u)) break;
      __builtin_amdgcn_s_sleep(POLL_SLEEP);
      asm volatile("" ::: "memory");
    }
  };

  const bool g1_on = idx < 27;
  unsigned vb1[4], rb1[4];
#pragma unroll
  for (int j = 0; j < 4; ++j) vb1[j] = ((4 * j + g) * (F_ * 2) + ((r16 ^ (g << 2)) << 4) - j * 1024) | (OOB * ((idx + 5) >> 5));
#pragma unroll
  for (int m = 0; m < 4; ++m) rb1[m] = ring + 256 * (8 * g + q4) + 64 * (((m + wid) & 3) ^ q4) + 16 * p4;
  const unsigned b1_base = nt1 * 256 + wid * 32 * (F_ * 2);
#define G1_LDB(slot, kb, j) bdma<(slot) * 8192 + ((j) >> 2) * 4096, ((j) & 3) * 1024>(ring, vb1[(j) & 3], sb1, b1_base + (g1_blk(kb, nt1) * 128 + ((j) >> 2) * 16) * (F_ * 2))
#define G1_HALF(slot, m, c, h) trd(rb1[m] + (slot) * 8192 + 8 * (c) + 1024 * (h))
  const unsigned ao = WS_AT + 2 * tid * (L_ * 2) + tb * 16, ao1 = tid < 32 ? ao + 512 * (L_ * 2) : OOB;
  u32x4 ga[4];
#define G1_LDR(i) q_load<32 + (i), ((i) & 3) * 1024>( vb1[(i) & 3], sb1, b1_base + (g1_blk((i) >> 3, nt1) * 128 + (((i) >> 2) & 1) * 16) * (F_ * 2))
  auto gather = [&]() { bload_r<0>(ga[0], ao, sw, 0u); bload_r<0>(ga[1], ao, sw, ROWB); bload_r<0>(ga[2], ao1, sw, 0u); bload_r<0>(ga[3], ao1, sw, ROWB); };

  if (cv_on) {
    unsigned pfk = 0;
    {
      float zl[3], zv[3];
      const unsigned pfko = slab_off<W1L>(idx * 256 + tid, 6144, F_ * 2, w1c0);
      f32x2 G0[3];
#pragma unroll
      for (int c = 0; c < 3; ++c) {
        const float dt = rbf(__expf(bflo(pp[6 * c + 4])));
        const float zr = -rbf(__expf(bflo(pp[6 * c]))) * dt, zi = bflo(pp[6 * c + 1]) * dt;
        zl[c] = zr * 1.4426950408889634f; zv[c] = zi * 0.15915494309189535f;
        G0[c] = f32x2{2.0f * (bflo(pp[6 * c + 2]) * dt), -2.0f * (bflo(pp[6 * c + 3]) * dt)};
      }
#define PUT_A(row, v) put_row<B0 + (row) * KG_PITCH>(v)
#define PUT_B(row, v) put_row<B0 + (41 + (row)) * KG_PITCH>(v)
      if (wid < 2) {
        sfor<3>([&](auto C) {
          constexpr int c = C;
          f32x2 E[8], G[6];
          const f32x2 E128 = cexp_p(zl[c], -zv[c], 128.f);
          E[1] = cexp_p(zl[c], -zv[c], 1.f); E[2] = cexp_p(zl[c], -zv[c], 2.f); E[4] = cexp_p(zl[c], -zv[c], 4.f);
          G[0] = G0[c]; G[4] = cmul(cexp_p(zl[c], -zv[c], 512.f), G0[c]);
          G[1] = cmul(G[0], E128); E[3] = cmul(E[1], E[2]); G[5] = cmul(G[4], E128); E[5] = cmul(E[1], E[4]);
          G[2] = cmul(G[1], E128); E[6] = cmul(E[2], E[4]); E[7] = cmul(E[3], E[4]); G[3] = cmul(G[2], E128);
          set_m0(ldsT + c * KG_CH + wid * 256);
          f32x2 v[28], Gs[6];
          sfor<6>([&](auto t) { Gs[t] = f32x2{-G[t].y, G[t].x}; });
          sfor<28>([&](auto r) { if constexpr (kg_e(r) > 0) v[r] = E[kg_e(r)].xx * G[kg_g(r)]; });
          SB0;
          sfor<28>([&](auto r) { if constexpr (kg_e(r) > 0) v[r] = __builtin_elementwise_fma(E[kg_e(r)].yy, Gs[kg_g(r)], v[r]); else v[r] = G[kg_g(r)]; });
          SB0;
          unsigned p[28];
          sfor<29>([&](auto r) { if constexpr (r < 28) p[r] = pkv(v[r]); if constexpr (r > 0) PUT_A(r - 1, p[r - 1]); SB0; });
          if constexpr (c == SIG_C) { VMCNT(0); signal_u(); if (PFK) touch_nt(pfk, pfko, sb1); TS(2); }
        });
      } else {
        sfor<3>([&](auto C) {
          constexpr int c = C;
          const f32x2 E128 = cexp_p(zl[c], -zv[c], 128.f), E4 = cexp_p(zl[c], -zv[c], 4.f), E8 = cexp_p(zl[c], zv[c], 8.f);
          f32x2 b0 = E8, b1v = cexp_p(zl[c], zv[c], 64.f);
          f32x2 g0 = cmul(cexp_p(zl[c], -zv[c], 768.f), G0[c]), g1 = cmul(cexp_p(zl[c], -zv[c], 1536.f), G0[c]);
          const f32x2 E8s = {-E8.y, E8.x}, E128s = {-E128.y, E128.x}, E4s = {-E4.y, E4.x};
          set_m0(ldsT + c * KG_CH + (wid & 1) * 256);
          PUT_B(0, 0x00003f80u);
          sfor<8>([&](auto j) {
            f32x2 t0, t1, t2, t3, t4;
            if constexpr (j < 6) t0 = b0.xx * E8;
            if constexpr (j < 7) t1 = b1v.xx * E8;
            if constexpr (j < 5) t2 = g0.xx * E128;
            if constexpr (j < 4) t3 = g1.xx * E128;
            if constexpr (j < 2) t4 = g0.xx * E4;
            const unsigned q0 = pkv(b0), q1 = pkv(b1v), q2 = pkv(g0), q3 = pkv(g1);
            SB0;
            if constexpr (j < 2) t4 = __builtin_elementwise_fma(g0.yy, E4s, t4);
            if constexpr (j < 7) PUT_B(j + 1, q0);
            if constexpr (j < 6) b0 = __builtin_elementwise_fma(b0.yy, E8s, t0);
            SB0;
            PUT_B(8 + j, q1);
            if constexpr (j < 7) b1v = __builtin_elementwise_fma(b1v.yy, E8s, t1);
            SB0;
            if constexpr (j < 2) PUT_A(28 + 2 * j, q2); else if constexpr (j < 6) PUT_A(30 + j, q2);
            if constexpr (j < 5) g0 = __builtin_elementwise_fma(g0.yy, E128s, t2);
            SB0;
            if constexpr (j < 5) PUT_A(36 + j, q3);
            if constexpr (j < 4) g1 = __builtin_elementwise_fma(g1.yy, E128s, t3);
            if constexpr (j < 2) PUT_A(29 + 2 * j, pkv(t4));
            SB0;
          });
          if constexpr (c == SIG_C) { VMCNT(0); signal_u(); if (PFK) touch_nt(pfk, pfko, sb1); }
        });
      }
    }
    u32x2 fl[3];
#pragma unroll
    for (int k = 0; k < 3; ++k) bload2(fl[k], ufo[k], sw);
    LDS_BARRIER();
    TS(3);
    if (wid < 3) {
      const char* ap = smem + min(wid * 16 + r16, 40) * KG_PITCH + g * 16;
      const char* bp = smem + KG_B + r16 * KG_PITCH + g * 16;
      f32x4 a0[3] = {};
      u32x4 fa[2][3][4], fb[2][3][4];
#pragma unroll
      for (int h = 0; h < 2; ++h) {
#pragma unroll
        for (int s = 0; s < 4; ++s)
#pragma unroll
          for (int c = 0; c < 3; ++c) { fa[h][c][s] = *(const u32x4*)(ap + c * KG_CH + (4 * h + s) * 64); fb[h][c][s] = *(const u32x4*)(bp + c * KG_CH + (4 * h + s) * 64); }
        if (h == 1) {
#pragma unroll
          for (int s = 0; s < 4; ++s)
#pragma unroll
            for (int c = 0; c < 3; ++c) a0[c] = mfma16(fa[0][c][s], fb[0][c][s], a0[c]);
#pragma unroll
          for (int k = 0; k < 12; ++k) { __builtin_amdgcn_sched_group_barrier(0x100, 2, 0); __builtin_amdgcn_sched_group_barrier(0x8, 1, 0); }
        }
        SB0;
      }
#pragma unroll
      for (int s = 0; s < 4; ++s)
#pragma unroll
        for (int c = 0; c < 3; ++c) a0[c] = mfma16(fa[1][c][s], fb[1][c][s], a0[c]);
#pragma unroll
      for (int c = 0; c < 3; ++c) {
        a0[c].x += tid == 0 ? bflo(pp[6 * c + 5]) : 0.f;
        const unsigned k01 = pkv(a0[c].xy), k23 = pkv(a0[c].zw);
        *(u16*)(smem + kto[0] + c * KG_TP) = (u16)k01; *(u16*)(smem + kto[1] + c * KG_TP) = (u16)(k01 >> 16);
        *(u16*)(smem + kto[2] + c * KG_TP) = (u16)k23; *(u16*)(smem + kto[3] + c * KG_TP) = (u16)(k23 >> 16);
      }
    } else {
      if (lane < 3) *(u32x2*)(smem + KG_T + lane * KG_TP) = u32x2{0u, 0u};
    }
    VMCNT(0);
#pragma nounroll
    for (int spin = 0; spin < (1 << 16); ++spin) {
      asm volatile("" : "+v"(fl[0]), "+v"(fl[1]), "+v"(fl[2]) :: "memory");
      const unsigned d = ((fl[0].x ^ tag.x) | (fl[0].y ^ tag.y)) | ((fl[1].x ^ tag.x) | (fl[1].y ^ tag.y)) | ((fl[2].x ^ tag.x) | (fl[2].y ^ tag.y));
      if (__all(d == 0u)) break;
      __builtin_amdgcn_s_sleep(POLL_SLEEP);
#pragma unroll
      for (int k = 0; k < 3; ++k) bload2r(fl[k], ufo[k], sw);
      VMCNT(0);
    }
    keep(pfk);
    u32x4 ug[3];
#pragma unroll
    for (int k = 0; k < 3; ++k) BLOAD(ug[k], ugo[k], sw, 0u, 0);
    const u32x4 none = {~0u, ~0u, ~0u, ~0u};
    const unsigned mk0 = WS_AT + d0 * (L_ * 2) + 16 * tid;
    auto side = [&](auto K0, auto N) {
      SB0;
      sfor<N>([&](auto J) {
        constexpr int k = K0 + J, i = k - NMARK;
        if constexpr (k < 3) bstore(none, mk0 + k * 4096, sw);
        else if constexpr (k < 6) bstore(none, mk0 + (WS_HF - WS_AT) + (k - 3) * 4096, sw);
        else if constexpr (k < 9) bstore(none, gmk + (k - 6) * (19 * 36864), sw);
        else if constexpr (i < 32) G1_LDR(i);
      });
      SB0;
    };
#define SIDE(k0, n) side(IC<(k0)>{}, IC<(n)>{})
    SIDE(0, 3);
    LDS_BARRIER();
    TS(4);
    {
      if (blockIdx.x == 0 && tid == 0) __builtin_amdgcn_raw_buffer_store_b64(tag, rw, SY_EPOCH, 0, SC1);
      u16 kv[3][11];
#pragma unroll
      for (int c = 0; c < 3; ++c)
#pragma unroll
        for (int k = 0; k < 11; ++k) kv[c][k] = *(const LAS u16*)(size_t)(kad[k] + c * KG_TP);
      *(u32x4*)(smem + zoff) = u32x4{0u, 0u, 0u, 0u};
      SIDE(3, 2);
      sfor<3>([&](auto C) {
        constexpr int c = C;
        unsigned e[5], a[5];
#pragma unroll
        for (int k = 0; k < 5; ++k) { e[k] = kv[c][2 * k] | ((unsigned)kv[c][2 * k + 1] << 16); a[k] = kv[c][2 * k + 1] | ((unsigned)kv[c][2 * k + 2] << 16); }
        char* kp = smem + c * CV_TB + tid * 16;
        *(u32x4*)(kp + 3 * CV_KP) = u32x4{e[0], e[1], e[2], e[3]};
        *(u32x4*)(kp + 2 * CV_KP) = u32x4{a[0], a[1], a[2], a[3]};
        *(u32x4*)(kp + 1 * CV_KP) = u32x4{e[1], e[2], e[3], e[4]};
        *(u32x4*)(kp + 0 * CV_KP) = u32x4{a[1], a[2], a[3], a[4]};
        SIDE(5 + 2 * c, 2);
      });
      LDS_BARRIER();
      asm volatile("s_waitcnt vmcnt(11)" : "+v"(ug[0]), "+v"(ug[1]), "+v"(ug[2]) :: "memory");
#pragma unroll
      for (int k = 0; k < 3; ++k) {
        char* up = smem + ulo[k];
        *(u32x2*)(up + 8) = ug[k].xy; *(u32x2*)(up + 16) = ug[k].zw;
        *(u32x4*)(up + CV_UP) = ug[k];
      }
    }
    LDS_BARRIER();
    TS(5);
    {
      const char* ap0 = smem + (r16 & 3) * CV_KP + 2 * (2064 - 256 * 7 - 8 * (r16 >> 2) - 8 * g - 64 * wid - 32);
      const char* bp0 = smem + CV_U + (r16 & 1) * CV_UP + 2 * (232 + 32 * (r16 >> 1) - 64 * wid - 32 + 24 - 8 * g);
      const unsigned pw = lds0 + CV_P + wid * 8192 + ((r16 & 1) + 2 * (g ^ ((r16 >> 1) & 3)) + 8 * (r16 >> 1)) * 16;
      unsigned pr = lds0 + CV_P + (tid >> 5) * 1024 + (2 * ((tid & 3) ^ ((tid >> 2) & 3)) + 8 * ((tid & 31) >> 2)) * 16;
      unsigned c0a = tid < 8 ? SY_CNT + (tid * 16 + ((xc * 24 + idx) & 15)) * 256 : OOB, c0p = cnt_off(0, xc, 0, 16, tid), one = 1;
      u32x4 FA[2][8], FB[2][8], ov;
      f32x4 py[8];
      auto rd = [&](int u, int j) {
        const int o = (u >> 1) * CV_TB + 64 * (1 - (u & 1)), k = j >> 1;
        if (j & 1) FB[u & 1][k] = *(const u32x4*)(bp0 + o + 512 * k); else FA[u & 1][k] = *(const u32x4*)(ap0 + o + 512 * (7 - k));
      };
#define CV_WP(c, k) q_put<8 * ((c) & 1) + (k), ((c) & 1) * 32768 + (k) * 1024>(pw)
      auto rp = [&](int c, int j) { py[j] = *(const LAS f32x4*)(size_t)(pr + (c & 1) * 32768 + (j >> 1) * 8192 + (j & 1) * 16); };
      auto fin = [&](int c, int j) {
        if (j < 12) {
          const int d = j < 8 ? (j & 1) + (j & 4) : j & 1, e = j & 2, sft = j < 8 ? 2 : 4;
          float x0 = py[d][e], x1 = py[d][e + 1];
          fadd(x0, py[d + sft][e]); fadd(x1, py[d + sft][e + 1]);
          py[d][e] = x0; py[d][e + 1] = x1;
        } else if (j < 14) { ov[j - 12] = pkv(j & 1 ? py[0].zw : py[0].xy); ov[j - 10] = pkv(j & 1 ? py[1].zw : py[1].xy); asm volatile("" : "+v"(ov)); }
        else if (j < 16) { ov[j - 14] = min(ov[j - 14], 0xfffffffeu); ov[j - 12] = min(ov[j - 12], 0xfffffffeu); asm volatile("" : "+v"(ov)); }
        else __builtin_amdgcn_raw_buffer_store_b128(ov, rw, mk0 + c * 4096, 0, SC1);
      };
      sfor<16>([&](auto J) { rd(0, J); });
      SB0;
      asm volatile("" : "+v"(pr), "+v"(c0a), "+v"(c0p), "+v"(one));
      SIDE(11, 1);
      unsigned pf = 0;
      sfor<216>([&](auto NN) {
        constexpr int n = NN, u = n / 36, j = n % 36, c1 = cv_a(j), c2 = cv_b(j);
        if constexpr (n == 108) {
          VMCNT(12 + 108 / 5 - NMARK);
          LDS_BARRIER();
          add_one(one, c0a, sw);
          TS(21);
        }
        if constexpr (n == 180) {
          LDS_BARRIER();
          TS(22);
        }
        if constexpr (!(u & 1) && cv_first(j)) q_mfma0<8 * ((u >> 1) & 1) + c1 + c2>(FA[u & 1][c1], FB[u & 1][c2]);
        else q_mfma<8 * ((u >> 1) & 1) + c1 + c2>(FA[u & 1][c1], FB[u & 1][c2]);
        if constexpr (u != 3 && u != 5) {
          if constexpr (!(j & 1)) { if constexpr (j < 32) rd(u + 1, j / 2); }
          else if constexpr (u >= 2 && j >= 3 && j < 19) CV_WP(u / 2 - 1, (j - 3) / 2);
        } else {
          if constexpr (j < 8) rp(u / 2 - 1, j);
          if constexpr (u == 3 && j < 16) rd(4, j);
          if constexpr (j >= 16 && j < 33) fin(u / 2 - 1, j - 16);
          if constexpr (u == 5 && cv_done(j) >= 0) CV_WP(2, cv_done(j));
        }
        if constexpr (n % 5 == 4) SIDE(12 + n / 5, 1);
        if constexpr (n == 191) poll_issue(pf, c0p, sw);
        SB0;
      });
      CV_WP(2, 6);
      asm volatile("s_nop 7" ::: "memory");
      CV_WP(2, 7);
      LDS_BARRIER();
      TS(23);
      sfor<8>([&](auto J) { rp(2, J); });
      SB0;
      py[0] = (py[0] + py[2]) + (py[4] + py[6]); py[1] = (py[1] + py[3]) + (py[5] + py[7]);
      sfor<5>([&](auto J) { fin(2, 12 + J); });
      TS(6);
      asm volatile("s_waitcnt vmcnt(2)" : "+v"(pf) :: "memory");
      await(rw, 0, xc, 0, 16, 12, wid == 0 && !__all(pf == (lane < 16 ? 12u : 0u)), lane);
    }
  } else {
    VMCNT(0);
    signal_u();
    keep(pf0[0]); keep(pf0[1]); keep(pf0[2]);
    warm<W1L>(sink, sb1, F_ * 2, w1c0, (PF0 + 3 * PFK) * 2048, PF1_A * 2048, (idx - 24) * 256 + tid, 2048);
    if (wid == 0) await_u();
    warm<W1L>(sink, sb1, F_ * 2, w1c0, PF1_A * 2048, PF1_E1, (idx - 24) * 256 + tid, 2048);
    if (g1_on) { sfor<32>([&](auto I) { G1_LDR(I); }); VMCNT(0); }
    else warm<W1L>(sink, sb1, F_ * 2, w1c0, PF1_E1, PF1_E2, (idx - 27) * 256 + tid, 1280);
  }
  TS(7);

  u32x4 BR[4][8];
  TS(8);
  {
    auto ride = [&](auto P) { SB0; G1_LDB(P / 8, 4 + P / 8, P % 8); SB0; };
    asm volatile("" : "=v"(ga[0]), "=v"(ga[1]), "=v"(ga[2]), "=v"(ga[3]));
    sfor<24>([&](auto I) {
      constexpr int k = I / 8 - 1, j = I % 8;
      if constexpr (I == GATHER_AT) { if (cv_on) gather(); }
      if constexpr (!(I & 1)) ride(IC<I / 2>{});
      if constexpr (I >= 8) { BR[k][j] = cat(G1_HALF(2 + k, j >> 1, j & 1, 0), G1_HALF(2 + k, j >> 1, j & 1, 1)); SB0; }
      if constexpr (I < 16) q_put<32 + I, 16384 + I * 1024>(ring + lane * 16);
    });
    sfor<4>([&](auto I) { ride(IC<12 + I>{}); });
    if (cv_on) VMCNT(16 - (GATHER_AT + 1) / 2);
    else {
      if (wid == 0) {
#pragma nounroll
        for (int spin = 0; spin < (1 << 16); ++spin) {
          const unsigned c = __builtin_amdgcn_raw_buffer_load_b32(rw, cnt_off(0, xc, 0, 16, lane), 0, SC1);
          const unsigned f = __builtin_amdgcn_raw_buffer_load_b32(rw, WS_AT + (9 * lane + 2) * (L_ * 2) + tb * 16, 0, SC1);
          if (__all(c == (lane < 16 ? 12u : 0u) && f != ~0u)) break;
          __builtin_amdgcn_s_sleep(SPARE_SLEEP);
          asm volatile("" ::: "memory");
        }
      }
      asm volatile("" ::: "memory");
      __builtin_amdgcn_s_barrier();
      gather();
      if (g1_on) VMCNT(0);
      else { warm<W1L, 63>(sink, sb1, F_ * 2, w1c0, PF1_E2, PF1_E2 + PFN * 1280, (idx - 27) * 256 + tid, 5 * 256); VMCNT(PFN); }
    }
    static_assert(PF1_E2 + PFN * 1280 <= L_ * W1L, "");
#pragma nounroll
    for (int spin = 0; spin < (1 << 16); ++spin) {
      asm volatile("" : "+v"(ga[0]), "+v"(ga[1]), "+v"(ga[2]), "+v"(ga[3]) :: "memory");
      if (__all(umax4(ga[0], umax4(ga[1], umax4(ga[2], umax4(ga[3], 0u)))) != ~0u)) break;
      __builtin_amdgcn_s_sleep(POLL_SLEEP);
      gather();
      VMCNT(0);
    }
    asm volatile("" : "+v"(ga[0]), "+v"(ga[1]), "+v"(ga[2]), "+v"(ga[3]) :: "memory");
    {
      unsigned* tp = (unsigned*)(smem_ + (tid >> 5) * 1024 + ll * 4);
#pragma unroll
      for (int k = 0; k < 4; ++k) { tp[64 * k] = perm_lo(ga[0][k], ga[1][k]); tp[64 * k + 32] = perm_hi(ga[0][k], ga[1][k]); }
      if (tid < 32) {
#pragma unroll
        for (int k = 0; k < 4; ++k) { tp[2048 + 64 * k] = perm_lo(ga[2][k], ga[3][k]); tp[2048 + 64 * k + 32] = perm_hi(ga[2][k], ga[3][k]); }
      }
    }
    LDS_BARRIER();
    TS(9);
    f32x2 v[9];
    unsigned o[9];
#pragma unroll
    for (int i = 0; i < 9; ++i) { o[i] = pkv(unpk(xw[i]) + unpk(*(const unsigned*)(smem_ + 1024 * i + tid * 4))); v[i] = unpk(o[i]); }
    set_m0(ldsT + wid * 256);
    sfor<9>([&](auto I) { put_row<1024 * I>(o[I]); });
    ln9(v, o, [&](auto K, float& x) { asm volatile("" : "+v"(x)); ride(IC<16 + K>{}); });
#pragma unroll
    for (int i = 0; i < 9; ++i) v[i] = unpk(o[i]);
    ln9(v, o, [&](auto K, float& x) { asm volatile("" : "+v"(x)); ride(IC<24 + K>{}); });
    set_m0(ldsT + wid * 256);
    sfor<9>([&](auto I) { put_row<t2_off(I)>(__builtin_bit_cast(unsigned, __builtin_elementwise_min(__builtin_bit_cast(u16x2, o[I]), u16x2{0xfffe, 0xfffe}))); });
    LDS_BARRIER();
    {
      const unsigned off = WS_HF + ((tb >> 2) * 36 + wid * 4 + g) * 1024 + (tb & 3) * 256 + r16 * 16;
      __builtin_amdgcn_raw_buffer_store_b128(chunk(ldsT + 9216, wid), rw, off, 0, SC1);
      __builtin_amdgcn_raw_buffer_store_b128(chunk(ldsT + (wid == 3 ? t2_off(7) - 7168 : 9216), wid + 4), rw, off + 16384, 0, SC1);
      if (wid == 0) __builtin_amdgcn_raw_buffer_store_b128(chunk(ldsT + t2_off(8), 0), rw, off + 32768, 0, SC1);
      const unsigned ho = WS_H + (tb * 8 + (lane >> 3)) * (D_ * 2) + (lane & 7) * 16;
      const char* hp = smem_ + lane * 16;
      __builtin_amdgcn_raw_buffer_store_b128(*(const u32x4*)(hp + wid * 1024), rw, ho + wid * 128, 0, SC1);
      __builtin_amdgcn_raw_buffer_store_b128(*(const u32x4*)(hp + (wid + 4) * 1024), rw, ho + (wid + 4) * 128, 0, SC1);
      if (wid == 3) __builtin_amdgcn_raw_buffer_store_b128(*(const u32x4*)(hp + 8192), rw, ho + 1024, 0, SC1);
    }
    TS(10);
  }

  const unsigned xin = WS_X + blockIdx.x * 20480 + wid * 5120 + lane * 16, xin4 = (lane >> 5) == kh ? xin + 4096 : OOB;
  auto xmark = [&](int i) { bstore(u32x4{~0u, ~0u, ~0u, ~0u}, i < 4 ? xin + i * 1024 : xin4, sw); };
  unsigned vb2[4];
#pragma unroll
  for (int j = 0; j < 4; ++j) vb2[j] = (8 * j + (lane >> 3)) * (L_ * 2) + (((lane & 7) ^ (2 * ((g & 1) | ((j & 1) << 1)))) << 4) - j * 1024;
  const unsigned b2_base = nt2 * 128 + (kh * (F_ / 2) + wid * 32) * (L_ * 2);
  auto g2_kb = [&](int t) { return G2_LEAD ? t + ((t >> 1) == lead ? 2 : (t >> 1) == lead + 1 ? -2 : 0) : t; };
#define G2_LDB(slot, i, j) bdma<(slot) * 4096, (j) * 1024>(ring, vb2[j], sb2, b2_base + g2_kb(i) * 128 * (L_ * 2))

  if (g1_on) {
    __builtin_amdgcn_s_sleep(G1_DELAY);
    TS(11);
    const unsigned va0 = lane * 16, va1 = lane * 16 + 4096;
    const unsigned a_base = WS_HF + wid * 36864 + mt * 6144;
    u32x4 A[4][6];
#define G1_AOFF(t) (a_base + g1_blk(g1_kb(t), nt1) * (4 * 36864))
#define G1_LDA(t, f) BLOAD(A[(t) & 3][f], (f) < 4 ? va0 : va1, sw, G1_AOFF(t), ((f) & 3) * 1024)
#define G1_WAIT(t, n) wait6<(n)>(A[(t) & 3])
    unsigned mx, my, pf3 = 0;
    auto late = [&](auto GG) {
      if constexpr (GG >= PF3_T0 && (GG - PF3_T0) % PF3_DT == 0 && (GG - PF3_T0) / PF3_DT < 4 + PF3_X) {
        constexpr int k = (GG - PF3_T0) / PF3_DT;
        if (wid == (k & 3)) touch_nt(pf3, slab_off<8>((PF3_R0 + 216 * k) * 8 + idx * 64 + lane, F_ * 4, L_ * 2, w2c0), sb2);
      }
    };
    auto scan = [&](auto T, auto K) {
      const u32x4& a = A[T & 3][K >> 1];
      if constexpr (K & 1) { my = K == 1 ? max(a.z, a.w) : umax3(my, a.z, a.w); asm volatile("" : "+v"(my)); }
      else { mx = K == 0 ? max(a.x, a.y) : umax3(mx, a.x, a.y); asm volatile("" : "+v"(mx)); }
      SB0;
    };
    auto check = [&](auto T) {
      if (__builtin_expect(__builtin_amdgcn_ballot_w64(max(mx, my) == ~0u) != 0, 0)) {
#pragma nounroll
        for (int spin = 0; spin < (1 << 16); ++spin) {
          sfor<6 * RETRY_N>([&](auto I) { constexpr int t = T + I / 6, f = I % 6; if constexpr (t < 16 && (T > 0 || t < G1_A0)) bload_r<(f & 3) * 1024>(A[t & 3][f], f < 4 ? va0 : va1, sw, G1_AOFF(t)); });
          G1_WAIT(T, 0);
          sfor<12>([&](auto K) { scan(T, K); });
          if (__builtin_amdgcn_ballot_w64(max(mx, my) == ~0u) == 0) break;
          __builtin_amdgcn_s_sleep(POLL_SLEEP);
        }
      }
    };
    sfor<6 * G1_A0>([&](auto I) { G1_LDA(I / 6, I % 6); });
    TS(12);
    u32x2 Bh[2][4], Th[2], Bl[12];
    G1_WAIT(0, 6 * G1_A0 - 6);
    sfor<12>([&](auto K) { scan(IC<0>{}, K); });
    check(IC<0>{});
    TS(13); TC(0);
    sfor<4>([&](auto I) { Bh[0][I] = G1_HALF(0, 0, I >> 1, I & 1); });
    SB0;
    sfor<13>([&](auto TT) {
      constexpr int t = TT;
      constexpr bool R = g1_isR(t);
      constexpr int slot = g1_lidx(t) & 3, nops = g1_ops(t);
      sfor<48>([&](auto NN) {
        constexpr int n = NN, m = n / 12, c = (n / 6) & 1, f = n % 6, i12 = n % 12;
        if constexpr (n == 36) G1_WAIT(t + 1, t == 0 ? 12 : t == 1 ? 6 + g1_ops(1) : g1_ops(t - 1) + g1_ops(t));
        if constexpr (t == 0) q_mfma0<g1_quad(m, i12)>(cat(Bh[m & 1][2 * c], Bh[m & 1][2 * c + 1]), A[t & 3][f]);
        else if constexpr (!R) q_mfma<g1_quad(m, i12)>(cat(Bh[m & 1][2 * c], Bh[m & 1][2 * c + 1]), A[t & 3][f]);
        else q_mfma<g1_quad(m, i12)>(BR[g1_kb(t)][2 * m + c], A[t & 3][f]);
        if constexpr (i12 < 4) {
          if constexpr (m < 3) { if constexpr (!R) { Bh[(m + 1) & 1][i12] = G1_HALF(slot, m + 1, i12 >> 1, i12 & 1); SB0; } }
          else if constexpr (!g1_isR(t + 1)) { Bh[0][i12] = G1_HALF(g1_lidx(t + 1) & 3, t == 12 ? 1 : 0, i12 >> 1, i12 & 1); SB0; }
        } else if constexpr (m < 3) {
          constexpr int pl = m * 8 + i12 - 4, per = (nops + 2) / 3, k = G1_BURST == 1 ? pl : G1_BURST == 2 ? m * per + i12 - 4 : (pl * nops + 23) / 24;
          if constexpr (k < nops && (G1_BURST == 1 || (G1_BURST == 2 && i12 - 4 < per) || (!G1_BURST && (k * 24) / nops == pl)) && !(ABL & 2)) {
            if constexpr (t == 11 && k < 5) xmark(k);
            else if constexpr (k < g1_nb(t)) { constexpr int pc = g1_b0(t) + k; G1_LDB((pc >> 3) & 3, 8 + (pc >> 3), pc & 7); }
            else if constexpr (t == 0) G1_LDA(G1_A0 + k / 6, k % 6);
            else G1_LDA(t + 3, k - g1_nb(t));
          }
          if constexpr (t >= 11) {
            constexpr int r = t - 9;
            if constexpr (pl < 8) q_put<32 + 8 * r + pl, pl * 1024>(ring + lane * 16);
            else {
              constexpr int j = (pl - 8) >> 1;
              Th[pl & 1] = G1_HALF(0, j >> 1, j & 1, pl & 1);
              if constexpr (pl & 1) BR[r][j] = cat(Th[0], Th[1]);
              SB0;
            }
          }
        }
        if constexpr (n >= 36) scan(IC<t + 1>{}, IC<n - 36>{});
        late(IC<48 * t + n>{});
      });
      check(IC<t + 1>{});
    });
    sfor<4>([&](auto PP) {
      constexpr int ph = PP, m = (ph + 1) & 3;
      sfor<36>([&](auto NN) {
        constexpr int n = NN, t = 13 + n / 12, c = (n / 6) & 1, f = n % 6;
        if constexpr (ph == 0 && n == 0) G1_WAIT(14, 6);
        if constexpr (ph == 0 && n == 12) { check(IC<14>{}); G1_WAIT(15, 0); }
        if constexpr (ph == 0 && n == 24) check(IC<15>{});
        if constexpr (ph == 0 && n == 22) __builtin_amdgcn_s_waitcnt(0xc07f);
        if constexpr (t == 13 && ph == 0) q_mfma<g1_quad(m, n % 12)>(cat(Bh[0][2 * c], Bh[0][2 * c + 1]), A[t & 3][f]);
        else if constexpr (t == 13) q_mfma<g1_quad(m, n % 12)>(cat(Bl[4 * ph - 4 + 2 * c], Bl[4 * ph - 3 + 2 * c]), A[t & 3][f]);
        else q_mfma<g1_quad(m, n % 12)>(BR[t - 12][2 * m + c], A[t & 3][f]);
        if constexpr (ph == 0 && n >= 1 && n < 13) { Bl[n - 1] = G1_HALF(3, (2 + (n - 1) / 4) & 3, ((n - 1) >> 1) & 1, (n - 1) & 1); SB0; }
        if constexpr (ph == 0 && n < 24) scan(IC<14 + n / 12>{}, IC<n % 12>{});
        if constexpr (ph > 0 && n % 3 == 2) q_put<g1_quad(ph, n / 3), ((ph - 1) * 12 + n / 3) * 1024>(ring + lane * 16);
        late(IC<624 + 36 * ph + n>{});
      });
    });
    TS(14); TC(3);
    LDS_BARRIER();
    f32x4 P[3][12];
#pragma unroll
    for (int k = 0; k < 12; ++k)
#pragma unroll
      for (int m = 1; m < 4; ++m) P[m - 1][(k & 1) * 6 + (k >> 1)] = *(const f32x4*)(smem + ((wid - m) & 3) * G_WS + ((m - 1) * 12 + (k & 1) * 6 + (k >> 1)) * 1024 + lane * 16);
    SB0;
    const f32x4 bv[2] = {f32x4{bflo(bw.x), bfhi(bw.x), bflo(bw.y), bfhi(bw.y)}, f32x4{bflo(bw.z), bfhi(bw.z), bflo(bw.w), bfhi(bw.w)}};
    f32x4 own[12];
    sfor<12>([&](auto K) { own[K] = q_get<K>() + bv[K / 6]; });
    SB0;
    LDS_BARRIER();
    sfor<6>([&](auto TT) {
      constexpr int t = TT;
      sfor<4>([&](auto J) { G2_LDB(t, t, J.value); });
      u32x4 o;
#pragma unroll
      for (int c = 0; c < 2; ++c) {
        const f32x4 u = (own[c * 6 + t] + P[0][c * 6 + t]) + (P[1][c * 6 + t] + P[2][c * 6 + t]);
        const f32x4 z = u * __builtin_elementwise_fma(u * u, (f32x4)(-0.1029432397f), (f32x4)(-2.302208198f));
        const f32x4 den = f32x4{__builtin_amdgcn_exp2f(z.x), __builtin_amdgcn_exp2f(z.y), __builtin_amdgcn_exp2f(z.z), __builtin_amdgcn_exp2f(z.w)} + 1.0f;
        const f32x4 y = u * f32x4{__builtin_amdgcn_rcpf(den.x), __builtin_amdgcn_rcpf(den.y), __builtin_amdgcn_rcpf(den.z), __builtin_amdgcn_rcpf(den.w)};
        o[2 * c] = min(pkv(y.xy), 0xfffffffeu); o[2 * c + 1] = min(pkv(y.zw), 0xfffffffeu);
      }
      __builtin_amdgcn_raw_buffer_store_b128(o, rw, WS_GF + ((nt1 * 4 + wid) * 36 + mt * 6 + t) * 1024 + lane * 16, 0, SC1);
      SB0;
    });
    TS(15);
    sfor<12>([&](auto I) { G2_LDB(6 + I / 4, 6 + I / 4, I % 4); });
    VMCNT(42);
    keep(pf3);
  } else {
#pragma unroll
    for (int i = 0; i < 5; ++i) xmark(i);
    constexpr int n1 = L_ * W1L - PF1_E2 - PFN * 1280;
    const unsigned id = (idx - 27) * 256 + tid;
    warm<W1L>(sink, sb1, F_ * 2, w1c0, PF1_E2 + PFN * 1280, L_ * W1L, id, 1280);
    warm<8>(sink, sb2, L_ * 2, w2c0, 0, PF3_R0 * 8, (id + 12800 - n1) % 1280, 1280);
    warm<8>(sink, sb2, L_ * 2, w2c0, (PF3_R0 + 864 + 216 * PF3_X) * 8, F_ * 4, (id + 38400 - n1 - PF3_R0 * 8) % 1280, 1280);
    arrive<PF_WIN + 1>(rw, 1, tb & 15);
    TS(15);
    sfor<36>([&](auto I) { if constexpr (I == 32) VMCNT(32); G2_LDB(I / 4, I / 4, I % 4); });
  }

  {
    unsigned rb[4];
#pragma unroll
    for (int c = 0; c < 4; ++c) rb[c] = ring + 128 * (8 * g + q4) + 16 * ((2 * ((c + wid) & 3) + (p4 >> 1)) ^ (2 * ((q4 >> 1) | ((g & 1) << 1)))) + 8 * (p4 & 1);
#define G2_HALF(slot, c, h) trd(rb[c] + (slot) * 4096 + 512 * (h))
    const int sh0 = kh * 6 + (3 * dq) / 2;
    const unsigned xo = xin + (kh ? -20480 : 20480), xo4 = (lane >> 5) == kh ? OOB : xo + 4096;
    const unsigned va[3] = {(unsigned)((9 * dq + 5 * kh) * 1024 + lane * 16), (unsigned)((9 * dq + 4) * 1024 + lane * 16), (unsigned)((9 * dq + 5 - 5 * kh) * 1024 + lane * 16)};
    const unsigned a_base = WS_GF + (kh * 72 + wid) * 36864;
    u32x4 A[36][5], W[2][4];
    unsigned aso[18];
#pragma unroll
    for (int i = 0; i < 18; ++i) { aso[i] = a_base + g2_kb(i) * (4 * 36864); asm volatile("" : "+s"(aso[i])); }
#define G2_AARG(J) A[g2_astep(J)][g2_af(J)], va[(J) >= 90 ? 0 : g2_af(J) == 4 ? 1 : 2], sw, aso[g2_tile(g2_astep(J))]
#define G2_AIMM(J) ((g2_af(J) & 3) * 1024)
    auto lda = [&](auto J) { bload_<G2_AIMM(J)>(G2_AARG(J)); };
    sfor<3>([&](auto K) {
      VMCNT(24);
      if constexpr (K == 1) {
        if (g1_on) {
          __builtin_amdgcn_s_barrier();
          if (tid < 16) __builtin_amdgcn_raw_ptr_buffer_atomic_add_i32(1, rw, SY_CNT + ((8 + tid) * 16 + (tid < 8 ? tb & 15 : (nt1 >= 18) * 6 + mt)) * 256, 0, SC1);
        }
      }
      sfor<12>([&](auto I) {
        constexpr int q = 3 * K + I / 4;
        if constexpr (q < 7) q_frag<36 + 4 * q + I % 4, q * 4096>(rb[I % 4]);
        else W[q - 7][I % 4] = cat(G2_HALF(q, I % 4, 0), G2_HALF(q, I % 4, 1));
      });
      if constexpr (K == 0) sfor<4>([&](auto I) { q_set<20 + I>(bflo(bias2)); });
      asm volatile("s_waitcnt lgkmcnt(0)" ::: "memory");
      SB0;
      sfor<12>([&](auto I) { constexpr int q = 3 * K + I / 4; G2_LDB(q, 9 + q, I % 4); });
    });
    if (!g1_on) {
#pragma nounroll
      for (int spin = 0; spin < (1 << 16); ++spin) {
        const unsigned f = __builtin_amdgcn_raw_buffer_load_b32(rw, lane < SP_N ? a_base + lane * (4 * 36864) + (9 * dq + 4) * 1024 : OOB, 0, SC1);
        if (__all(f != ~0u)) break;
        __builtin_amdgcn_s_sleep(SPARE_SLEEP);
        asm volatile("" ::: "memory");
      }
    }
    TS(16);
    sfor<g2_aend(G2_A0 - 1)>(lda);
    unsigned mx, my, gpoll = 0, hpoll = 0;
    auto scan = [&](auto T, auto K) {
      const u32x4& a = A[T][K >> 1];
      if constexpr (K & 1) { my = K == 1 ? max(a.z, a.w) : umax3(my, a.z, a.w); asm volatile("" : "+v"(my)); }
      else { mx = K == 0 ? max(a.x, a.y) : umax3(mx, a.x, a.y); asm volatile("" : "+v"(mx)); }
      SB0;
    };
    auto check = [&](auto T) {
      if (__builtin_expect(__builtin_amdgcn_ballot_w64(max(mx, my) == ~0u) != 0, 0)) {
        constexpr int j1 = T == 0 ? g2_aend(G2_A0 - 1) : G2S.issued[T == 0 ? 0 : T - 1];
#pragma nounroll
        for (int spin = 0; spin < (1 << 16); ++spin) {
          __builtin_amdgcn_s_sleep(POLL_SLEEP);
          sfor<j1 - 5 * T>([&](auto I) { constexpr int J = 5 * T + I; bload_r<G2_AIMM(J)>(G2_AARG(J)); });
          wait5<0, 5>(A[T]);
          sfor<10>([&](auto K) { scan(T, K); });
          if (__builtin_amdgcn_ballot_w64(max(mx, my) == ~0u) == 0) break;
        }
      }
    };
    const int s = nt2 * 64 + wid * 16 + r16;
    const int dfull = 144 * dq + 80 * kh, dhalf = 144 * dq + 64 + 4 * g;
    u32x2 hw[5];
    u32x4 xr[5];
    u32x2 Bh[2][8];
    f32x4 Q[15];
    float xv[4];
    wait5<g2_aend(G2_A0 - 1) - 5, 5>(A[0]);
    sfor<10>([&](auto K) { scan(IC<0>{}, K); });
    check(IC<0>{});
    TS(17); TC(4);
    auto mm = [&](auto V, auto C, auto F) {
      constexpr int v = V, c = C, w = g2_tile(v), ac = v < 18 ? 5 * c + F : 20 + 4 * c + F;
      if constexpr (v == 0) q_mfma_b0<ac, 36 + c>(A[v][F]);
      else if constexpr (v == 18 && c > 0) q_mfma0<ac>(A[v][F], cat(Bh[v & 1][2 * c], Bh[v & 1][2 * c + 1]));
      else if constexpr (w < 7) q_mfma_b<ac, 36 + 4 * w + c>(A[v][F]);
      else if constexpr (w < 9) q_mfma<ac>(A[v][F], W[w - 7][c]);
      else q_mfma<ac>(A[v][F], cat(Bh[v & 1][2 * c], Bh[v & 1][2 * c + 1]));
    };
    sfor<33>([&](auto VV) {
      constexpr int v = VV, nf = g2_nf(v), nm = 4 * nf;
      sfor<nm>([&](auto NN) {
        constexpr int n = NN, op = G2S.op[v][n], xs = g2_xs(v, n);
        if constexpr (n == g2_wp(v)) wait5<G2S.wait[v], g2_nf(v + 1)>(A[v + 1]);
        if constexpr (v == G2S.tchk && n == 0) {
          asm volatile("s_waitcnt vmcnt(%1)" : "+v"(gpoll) : "i"(G2S.pwait));
          if (__builtin_expect(__builtin_amdgcn_ballot_w64(gpoll != (lane < 2 ? 18u : 0u)) != 0, 0)) {
            spin_cnt(rw, cnt_off(2, xc, sh0, 2, lane), lane < 2 ? 18u : 0u);
          }
        }
        if constexpr (v == 23 && n == 4) {
          asm volatile("" : "+v"(hpoll));
          if (__builtin_expect(__builtin_amdgcn_ballot_w64(hpoll != (lane < 16 ? 16u : 0u)) != 0, 0)) spin_cnt(rw, cnt_off(1, xc, 0, 16, lane), lane < 16 ? 16u : 0u);
          LDS_BARRIER();
        }
        mm(VV, IC<n / nf>{}, IC<n % nf>{});
        if constexpr (g2_tile(v + 1) >= 9) {
          if constexpr (n >= 1 && n < 9) { Bh[(v + 1) & 1][n - 1] = G2_HALF(g2_tile(v + 1) - 9, (n - 1) >> 1, (n - 1) & 1); SB0; }
          if constexpr (n == nm - 2) __builtin_amdgcn_s_waitcnt(0xc07f);
        }
        if constexpr ((v == 21 || v == 22) && (n & 1) && 8 * (v - 21) + n / 2 < 15) q_put<5 + 8 * (v - 21) + n / 2, (8 * (v - 21) + n / 2) * 1024>(ring + lane * 16);
        if constexpr (16 * v + n >= 16 * 23 + 4 && 16 * v + n < 16 * 23 + 19) {
          constexpr int k = 16 * (v - 23) + n - 4, c = 1 + k % 3, f = k / 3;
          Q[k] = *(const f32x4*)(smem + ((wid - c) & 3) * G_WS + (5 * c - 5 + f) * 1024 + lane * 16);
          SB0;
        }
        if constexpr (xs >= 0) {
          constexpr int f = xs / 11, j = xs % 11, e = 2 * (j & 1);
          if constexpr (j < 2) a_get2<4 * f + e>(xv[e], xv[e + 1]);
          else if constexpr (j < 8) { fadd(xv[e], Q[3 * f + j / 2 - 1][e]); fadd(xv[e + 1], Q[3 * f + j / 2 - 1][e + 1]); }
          else if constexpr (j < 10) { umin_ip(xv[e]); umin_ip(xv[e + 1]); }
          else bstore(__builtin_bit_cast(u32x4, f32x4{xv[0], xv[1], xv[2], xv[3]}), f < 4 ? xo + f * 1024 : xo4, sw);
        }
        if constexpr (op == 5000) poll_issue(gpoll, cnt_off(2, xc, sh0, 2, lane), sw);
        else if constexpr (op == 4000) poll_issue(hpoll, cnt_off(1, xc, 0, 16, lane), sw);
        else if constexpr (op >= 3005) bload_<((op - 3005) & 3) * 1024>(xr[op - 3005], op < 3009 ? xin : xin4, sw, 0u);
        else if constexpr (op >= 3000) bload2(hw[op - 3000], WS_H + (s * D_ + (op < 3004 ? dfull + 4 * g + 16 * (op - 3000) : dhalf)) * 2, sw);
        else if constexpr (op >= 1000) lda(IC<op - 1000>{});
        if constexpr (v + 1 < G2_S0 && n >= 10) scan(IC<v + 1>{}, IC<n - 10>{});
      });
      if constexpr (v + 1 < G2_S0) check(IC<v + 1>{});
    });
    sfor<4>([&](auto PP) {
      constexpr int ph = PP;
      sfor<12>([&](auto NN) {
        constexpr int n = NN, v = 33 + n / 4;
        if constexpr (ph == 0 && (n == 4 || n == 8)) wait5<G2S.wait[v - 1], 4>(A[v]);
        mm(IC<v>{}, IC<(ph + 1) & 3>{}, IC<n % 4>{});
        if constexpr (ph > 0 && n >= 4 && !(n & 1)) q_put<20 + 4 * ph + n / 2 - 2, (12 + 4 * ph + n / 2 - 2) * 1024>(ring + lane * 16);
      });
    });
    TS(18); TC(7);
    LDS_BARRIER();
#pragma unroll
    for (int k = 0; k < 12; ++k) Q[k] = *(const f32x4*)(smem + ((wid - 1 - k % 3) & 3) * G_WS + (16 + 4 * (k % 3) + k / 3) * 1024 + lane * 16);
    SB0;
    f32x4 own[5];
    asm volatile("s_waitcnt vmcnt(0)" : "+v"(hw[0]), "+v"(hw[1]), "+v"(hw[2]), "+v"(hw[3]), "+v"(hw[4]));
    sfor<5>([&](auto T) {
      const f32x4 h = {bflo(hw[T].x), bfhi(hw[T].x), bflo(hw[T].y), bfhi(hw[T].y)};
      if constexpr (T < 4) own[T] = q_get<20 + T>() + h;
      else own[T] = f32x4{xv[0], xv[1], xv[2], xv[3]} + (h + bflo(bias2));
    });
#pragma nounroll
    for (int spin = 0; spin < (1 << 16); ++spin) {
      wait5<63, 5>(xr);
      if (__all(umax4(xr[0], umax4(xr[1], umax4(xr[2], umax4(xr[3], umax4(xr[4], 0u))))) != ~0u)) break;
      __builtin_amdgcn_s_sleep(1);
      sfor<5>([&](auto F) { bload_r<(F & 3) * 1024>(xr[F], F < 4 ? xin : xin4, sw, 0u); });
      VMCNT(0);
    }
    wait5<63, 5>(xr);
#pragma unroll
    for (int f = 0; f < 4; ++f) own[f] += (Q[3 * f] + Q[3 * f + 1]) + Q[3 * f + 2];
    const rsrc_t ro = mk_rsrc(out, SZ);
    const unsigned oo = (s * D_ + dfull + 16 * (g & 1) + 4 * (g & 2)) * 2;
#pragma unroll
    for (int p = 0; p < 2; ++p) {
      const f32x4 y0 = own[2 * p] + __builtin_bit_cast(f32x4, xr[2 * p]), y1 = own[2 * p + 1] + __builtin_bit_cast(f32x4, xr[2 * p + 1]);
      unsigned a0 = pkv(y0.xy), a1 = pkv(y0.zw), b0 = pkv(y1.xy), b1v = pkv(y1.zw);
      asm volatile("s_nop 1\n\tv_permlane16_swap_b32 %0, %2\n\tv_permlane16_swap_b32 %1, %3" : "+v"(a0), "+v"(a1), "+v"(b0), "+v"(b1v));
      __builtin_amdgcn_raw_buffer_store_b128(u32x4{a0, a1, b0, b1v}, ro, oo + 64 * p, 0, 0);
    }
    const f32x4 yh = own[4] + __builtin_bit_cast(f32x4, xr[4]);
    __builtin_amdgcn_raw_buffer_store_b64(u32x2{pkv(yh.xy), pkv(yh.zw)}, ro, (lane >> 5) == kh ? (s * D_ + dhalf) * 2 : OOB, 0, 0);
    TS(19);
  }
  TSOUT();
  asm volatile(".Lk_all_end:" ::: "a255");
}

extern "C" void kernel_launch(void* const* d_in, const int* in_sizes, int n_in,
                              void* d_out, int out_size, void* d_ws, size_t ws_size,
                              hipStream_t stream) {
  k_all<<<256, 256, 0, stream>>>((const u16*)d_in[0], (const u16*)d_in[1], (const u16*)d_in[2], (const u16*)d_in[3], (const u16*)d_in[4],
                                 (const u16*)d_in[5], (const u16*)d_in[6], (const u16*)d_in[8], (const u16*)d_in[9], (const u16*)d_in[10],
                                 (const u16*)d_in[11], (char*)d_ws, (u16*)d_out);
}
```

```cpp
#include <hip/hip_runtime.h>
#include <stdint.h>
#include <utility>

typedef unsigned short u16;
typedef unsigned long long u64;
typedef __attribute__((ext_vector_type(4))) short s16x4;
typedef __attribute__((ext_vector_type(2))) unsigned short u16x2;
typedef __attribute__((ext_vector_type(8))) __bf16 bf16x8;
typedef __attribute__((ext_vector_type(2))) __bf16 bf16x2;
typedef __attribute__((ext_vector_type(4))) float f32x4;
typedef __attribute__((ext_vector_type(2))) float f32x2;
typedef __attribute__((ext_vector_type(4))) unsigned u32x4;
typedef __attribute__((ext_vector_type(2))) unsigned u32x2;
typedef __attribute__((ext_vector_type(3))) unsigned u32x3;
typedef __attribute__((ext_vector_type(4))) int i32x4;
typedef __attribute__((ext_vector_type(16))) unsigned u32x16;
typedef __attribute__((ext_vector_type(8))) unsigned u32x8;
typedef __attribute__((ext_vector_type(16))) float f32x16;
typedef __amdgpu_buffer_rsrc_t rsrc_t;
#define LAS __attribute__((address_space(3)))
#define DI __device__ __forceinline__

#define PFL 32
#define PFL3 24
#define PF1_G 1
#define PFM 2
#define PC0 27
#define MC 4
#define MGA 26
#define NA 20
#define GATHER_AT 9
#define POLL_SLEEP 2

constexpr int L_ = 2048, D_ = 576, N_ = 128, F_ = 4608;
constexpr unsigned SZ = L_ * D_ * 2, UGP_ = 12288, AGP = 8192, AGH = 192 * AGP + 16 * UGP_;
constexpr unsigned WS_AG = 0, WS_H = 2 * SZ, WS_GF = 4 * SZ, WS_Y = WS_GF + 5184 * 1024, WS_SY = WS_Y + 256 * 24576, WS_X = WS_SY + 131072, WS_XF = WS_X + 256 * 36864,
                   WS_UG = WS_XF + 1024 * 128, WS_HF = WS_UG, WS_BYTES = WS_UG + 192 * UGP_;
constexpr unsigned NMARK = 5184 + 1728 - 384;
constexpr unsigned u_mv(unsigned q) { return (q < 14 ? WS_GF + NMARK * 1024 + (q - 12) * 16 * UGP_ : WS_AG + (q - 14) * AGH + 192 * AGP) - (WS_UG + q * 16 * UGP_); }
constexpr unsigned SY_EPOCH = WS_SY, SY_CNT = WS_SY + 32768;
constexpr unsigned OOB = 0x7ff00000u;
constexpr int SC1 = 16;
constexpr unsigned ROWB = L_ * 2, W1B = F_ * 2;
static_assert(2 * AGH <= WS_H, "");

template <class Fn, int... I> DI void sfor_(Fn&& f, std::integer_sequence<int, I...>) { (f(std::integral_constant<int, I>{}), ...); }
template <int N, class Fn> DI void sfor(Fn&& f) { sfor_(f, std::make_integer_sequence<int, N>{}); }
template <int K> using IC = std::integral_constant<int, K>;

DI float bflo(unsigned w) { return __uint_as_float(w << 16); }
DI float bfhi(unsigned w) { return __uint_as_float(w & 0xffff0000u); }
DI f32x2 unpk(unsigned w) { return f32x2{bflo(w), bfhi(w)}; }
DI unsigned pkv(f32x2 v) { return __builtin_bit_cast(unsigned, __builtin_convertvector(v, bf16x2)); }
DI unsigned pk2(float lo, float hi) { return pkv(f32x2{lo, hi}); }
DI unsigned pk2a(float lo, float hi) { unsigned d; asm("v_cvt_pk_bf16_f32 %0, %1, %2" : "=v"(d) : "v"(lo), "v"(hi)); return d; }
DI unsigned pk2s(float lo, unsigned hi) { unsigned d; asm("v_cvt_pk_bf16_f32 %0, %1, %2" : "=v"(d) : "v"(lo), "s"(hi)); return d; }
DI float rbf(float f) { return bflo(pk2(f, f)); }
DI unsigned perm_lo(unsigned a, unsigned b) { return __builtin_amdgcn_perm(b, a, 0x05040100u); }
DI unsigned perm_hi(unsigned a, unsigned b) { return __builtin_amdgcn_perm(b, a, 0x07060302u); }
DI rsrc_t mk_rsrc(const void* p, unsigned bytes) { return __builtin_amdgcn_make_buffer_rsrc((void*)p, 0, bytes, 0x00020000); }
DI i32x4 mk_srd(const void* p, unsigned bytes) {
  const u64 a = (u64)p;
  i32x4 r; r.x = (int)(unsigned)a; r.y = (int)((unsigned)(a >> 32) & 0xffffu); r.z = (int)bytes; r.w = 0x00020000; return r;
}
#define LDS_BARRIER() do { asm volatile("s_waitcnt lgkmcnt(0)" ::: "memory"); __builtin_amdgcn_s_barrier(); asm volatile("" ::: "memory"); } while (0)
#define SB0 __builtin_amdgcn_sched_barrier(0)
#define VMCNT(n) asm volatile("s_waitcnt vmcnt(%0)" :: "i"(n) : "memory")
template <int M0OFF, int IMM> DI void bdma(unsigned ldsbase, unsigned voff, i32x4 srd, unsigned soff) {
  if constexpr (IMM == 0) asm volatile("s_add_u32 m0, %0, %1\n\ts_nop 0\n\tbuffer_load_dwordx4 %2, %3, %4 offen lds" :: "s"(ldsbase), "i"(M0OFF), "v"(voff), "s"(srd), "s"(soff) : "memory", "scc");
  else asm volatile("buffer_load_dwordx4 %0, %1, %2 offen offset:%3 lds" :: "v"(voff), "s"(srd), "s"(soff), "i"(IMM) : "memory");
}
template <int OFF> DI void put_row(unsigned v) { asm volatile("ds_write_addtid_b32 %0 offset:%1" :: "v"(v), "i"(OFF) : "memory"); }
DI void pin1(unsigned& a) { asm volatile("" : "+v"(a)); }
DI void pin2(f32x2& a) { asm volatile("" : "+v"(a)); }
DI void pin3(u32x3& a) { asm volatile("" : "+v"(a)); }
DI void undef1(unsigned& a) { asm volatile("" : "=v"(a)); }
DI void pins(unsigned& a) { asm volatile("" : "+s"(a)); }
DI void pin4(unsigned* a) { asm volatile("" : "+v"(a[0]), "+v"(a[1]), "+v"(a[2]), "+v"(a[3])); }
DI void pin9(float* a) { asm volatile("" : "+v"(a[0]), "+v"(a[1]), "+v"(a[2]), "+v"(a[3]), "+v"(a[4]), "+v"(a[5]), "+v"(a[6]), "+v"(a[7]), "+v"(a[8])); }
DI void keep(unsigned v) { asm volatile("" :: "v"(v)); }
DI void bstore_s(const u32x4& v, unsigned voff, i32x4 srd, unsigned soff) { asm volatile("buffer_store_dwordx4 %0, %1, %2, %3 offen sc1" :: "v"(v), "v"(voff), "s"(srd), "s"(soff) : "memory"); }
template <int IMM> DI void bstore3(const u32x3& v, unsigned voff, i32x4 srd, unsigned soff) { asm volatile("buffer_store_dwordx3 %0, %1, %2, %3 offen offset:%4 sc1" :: "v"(v), "v"(voff), "s"(srd), "s"(soff), "i"(IMM) : "memory"); }
template <int W> DI void put3(unsigned a, const unsigned* o) { asm volatile("ds_write2_b32 %0, %1, %2 offset0:%4 offset1:%5\n\tds_write_b32 %0, %3 offset:%6" :: "v"(a), "v"(o[0]), "v"(o[1]), "v"(o[2]), "i"(W), "i"(W + 1), "i"(4 * W + 8) : "memory"); }
template <int Q, int OFF> DI void q_frag(unsigned addr) {
  asm volatile("ds_read_b64_tr_b16 a[%0:%1], %4 offset:%5\n\tds_read_b64_tr_b16 a[%2:%3], %4 offset:%6"
               :: "i"(4 * Q), "i"(4 * Q + 1), "i"(4 * Q + 2), "i"(4 * Q + 3), "v"(addr), "i"(OFF), "i"(OFF + 512) : "memory");
}
template <int IMM> DI void gld16(unsigned& d, unsigned voff, const void* base) { asm volatile("global_load_ushort %0, %1, %2 offset:%3" : "=v"(d) : "v"(voff), "s"(base), "i"(IMM) : "memory"); }
template <int IMM> DI void gldh(float& d, unsigned voff, const void* base) { d = 0.f; asm volatile("global_load_short_d16_hi %0, %1, %2 offset:%3" : "+v"(d) : "v"(voff), "s"(base), "i"(IMM) : "memory"); }
template <int IMM> DI void gld64(u32x2& d, unsigned voff, const void* base) { asm volatile("global_load_dwordx2 %0, %1, %2 offset:%3" : "=v"(d) : "v"(voff), "s"(base), "i"(IMM) : "memory"); }
template <int IMM> DI void gld96(u32x3& d, unsigned voff, const void* base) { asm volatile("global_load_dwordx3 %0, %1, %2 offset:%3" : "=v"(d) : "v"(voff), "s"(base), "i"(IMM) : "memory"); }
DI void set_m0(unsigned v) { asm volatile("s_mov_b32 m0, %0\n\ts_nop 0" :: "s"(v) : "memory"); }
DI u32x2 trd(unsigned addr) { return __builtin_bit_cast(u32x2, __builtin_amdgcn_ds_read_tr16_b64_v4i16((LAS s16x4*)(size_t)addr)); }
DI u32x4 cat(u32x2 a, u32x2 b) { return u32x4{a.x, a.y, b.x, b.y}; }
template <class T> DI T ldsr(unsigned a) { return *(const LAS T*)(size_t)a; }
template <class T> DI void ldsw(unsigned a, T v) { *(LAS T*)(size_t)a = v; }

#define TLD(r, s) "buffer_load_dwordx4 v[" r "], %1, %2, " s " offen sc1\n\t"
#define TLD3 TLD("240:243", "%3") TLD("244:247", "%4") TLD("248:251", "%5")
#define TCK(d, a, b) "v_bitop3_b32 " d ", " a ", " b ", %7 bitop3:0x7e\n\t"
#define TCK3 TCK("v236", "v241", "v243") TCK("v237", "v245", "v247") TCK("v238", "v249", "v251") "v_or3_b32 v236, v236, v237, v238\n\t"
#define STR_(x) #x
#define STR(x) STR_(x)
#define TLOOP(ck, ld) "s_waitcnt vmcnt(%8)\n\ts_mov_b32 s98, 0x10000\n1:\n\t" ck "v_cmp_lt_u32 vcc, 0xffff, v236\n\ts_cbranch_vccz 2f\n\t" "s_sleep " STR(POLL_SLEEP) "\n\t" ld \
                      "s_waitcnt vmcnt(0)\n\ts_sub_u32 s98, s98, 1\n\ts_cbranch_scc0 1b\n2:"
template <int LATE> DI void t_ask(u32x16& d, unsigned vo, i32x4 srd, const unsigned* so, unsigned idx) {
  asm volatile("s_cmp_lt_u32 %6, 24\n\ts_cbranch_scc%7 1f\n\t" TLD3 "\n1:" : "+{v[240:255]}"(d) : "v"(vo), "s"(srd), "s"(so[0]), "s"(so[1]), "s"(so[2]), "s"(idx), "i"(LATE) : "memory", "scc");
}
template <int N, int YOUNGER> DI void t_take(u32x16& d, unsigned vo, i32x4 srd, const unsigned* so, unsigned tag) {
  if constexpr (N == 4) asm volatile(TLOOP(TCK3 TCK("v237", "v253", "v255") "v_or_b32 v236, v236, v237\n\t", TLD3 TLD("252:255", "%6")) : "+{v[240:255]}"(d)
                                     : "v"(vo), "s"(srd), "s"(so[0]), "s"(so[1]), "s"(so[2]), "s"(so[3]), "s"(tag), "i"(YOUNGER) : "memory", "vcc", "scc", "s98", "v236", "v237", "v238", "v239");
  else asm volatile(TLOOP(TCK3, TLD3) : "+{v[240:255]}"(d)
                    : "v"(vo), "s"(srd), "s"(so[0]), "s"(so[1]), "s"(so[2]), "i"(0), "s"(tag), "i"(YOUNGER) : "memory", "vcc", "scc", "s98", "v236", "v237", "v238", "v239");
}

template <int CTRL> DI float dpp_add(float v) { return v + __builtin_bit_cast(float, __builtin_amdgcn_update_dpp(0, __builtin_bit_cast(int, v), CTRL, 0xf, 0xf, true)); }
DI void ln_stats(const f32x2* v, float& mean, float& var) {
  f32x2 q[3];
#pragma unroll
  for (int i = 0; i < 9; ++i) q[i % 3] = i < 3 ? v[i] * v[i] : __builtin_elementwise_fma(v[i], v[i], q[i % 3]);
  const f32x2 s = ((v[0] + v[1]) + (v[2] + v[3])) + ((v[4] + v[5]) + (v[6] + v[7])) + v[8];
  q[0] = (q[0] + q[1]) + q[2];
  float a = s.x + s.y, b = q[0].x + q[0].y;
  a = dpp_add<0xB1>(a); b = dpp_add<0xB1>(b); a = dpp_add<0x4E>(a); b = dpp_add<0x4E>(b);
  a = dpp_add<0x141>(a); b = dpp_add<0x141>(b); a = dpp_add<0x140>(a); b = dpp_add<0x140>(b);
  float c = a, d = b;
  asm volatile("s_nop 1\n\tv_permlane16_swap_b32 %0, %1\n\tv_permlane16_swap_b32 %2, %3" : "+v"(a), "+v"(c), "+v"(b), "+v"(d));
  mean = (a + c) * (1.0f / D_);
  var = fmaxf(__builtin_fmaf(-mean, mean, (b + d) * (1.0f / D_)), 0.f);
}
DI float ln_inv(float var) { return __builtin_amdgcn_rcpf(rbf(__builtin_amdgcn_sqrtf(rbf(rbf(var) + 1e-5f)))); }
template <int HI> DI unsigned mad16(unsigned a, unsigned c) { unsigned d; asm("v_mad_u32_u16 %0, %1, 8, %2 op_sel:[%3,0,0,0]" : "=v"(d) : "v"(a), "s"(c), "i"(HI)); return d; }
DI unsigned slab_off(unsigned e, unsigned e1, unsigned c0) {
  const unsigned r = e / 18, c = c0 + (e - r * 18) * 128;
  return e < e1 ? r * W1B + c : OOB;
}
DI void touch_l(unsigned off, i32x4 srd, unsigned soff) { asm volatile("buffer_load_dword %0, %1, %2 offen lds" :: "v"(off), "s"(srd), "s"(soff) : "memory"); }
DI void touch_a(unsigned off, i32x4 srd, unsigned soff) { asm volatile("buffer_load_dword a0, %0, %1, %2 offen" :: "v"(off), "s"(srd), "s"(soff) : "memory"); }
DI void warm(unsigned lds, i32x4 srd, unsigned c0, unsigned e0, unsigned e1, unsigned idx, unsigned nthr) {
  set_m0(lds);
#pragma nounroll
  for (unsigned e = e0 + idx; e < e1 + idx; e += nthr) {
    const unsigned off = slab_off(e, e1, c0);
    asm volatile("s_waitcnt vmcnt(1)\n\tbuffer_load_dword %0, %1, 0 offen lds" :: "v"(off), "s"(srd) : "memory");
  }
}

constexpr int CB = 64;
constexpr int KG_PITCH = 528;
constexpr int KG_B = 41 * KG_PITCH, KG_CH = 57 * KG_PITCH;
constexpr int CV_KP = 259 * 16 + 16;
constexpr int CW = CB + 12 * CV_KP + 256, WA = 4288;
constexpr int CV_U = CW + 20 * WA;
constexpr int CV_UP = (29 + 256) * 16 + 64 + 16, CV_UC = 2 * CV_UP;
constexpr int KG_T = CB + 3 * KG_CH - 16, KT0 = 0x3f7e;
constexpr int U_ST = CV_U;
constexpr int G_WS = 40960, N_A = 32768, N_H = 36096, G_SINK = 40704;
constexpr int K_LDS = 163840;
static_assert(CV_KP % 256 == 64 && CV_UP % 64 == 32 && CV_U + 3 * CV_UC <= K_LDS && KG_T + (0x4509 - KT0) * 8 <= CW + 12 * WA, "");

#define C10(p, n) p #n "0", p #n "1", p #n "2", p #n "3", p #n "4", p #n "5", p #n "6", p #n "7", p #n "8", p #n "9"
#define C70UP(p) C10(p, 7), C10(p, 8), C10(p, 9), C10(p, 10), C10(p, 11), C10(p, 12), C10(p, 13), C10(p, 14), C10(p, 15), C10(p, 16), C10(p, 17), \
                 C10(p, 18), C10(p, 19), C10(p, 20), C10(p, 21), C10(p, 22), C10(p, 23), C10(p, 24), p "250", p "251", p "252", p "253", p "254", p "255"
#define C50UP(p) C10(p, 5), C10(p, 6), C70UP(p)
template <class T> DI T* mkp(unsigned lo, unsigned hi) { return (T*)(((u64)hi << 32) | lo); }
__global__ __launch_bounds__(256, 1) void k_all(const u16*, const u16*, const u16*, const u16*, const u16*, const u16*, const u16*, const u16*, const u16*, const u16*, const u16*, char*, u16*) {
  __shared__ __attribute__((aligned(16))) char smem_[K_LDS];
  u32x16 ka; u32x8 kb; u32x2 kc;
  asm volatile("s_load_dwordx16 %0, %3, 0\n\ts_load_dwordx8 %1, %3, 64\n\ts_load_dwordx2 %2, %3, 96" : "=&s"(ka), "=&s"(kb), "=&s"(kc) : "s"(__builtin_amdgcn_kernarg_segment_ptr()));
  const int tid = threadIdx.x, lane = tid & 63, wid = __builtin_amdgcn_readfirstlane(tid >> 6);
  const int xc = blockIdx.x & 7, idx = blockIdx.x >> 3;
  unsigned pfc;
  undef1(pfc);
  u64 pc; unsigned plen, psp;
  asm volatile("s_getpc_b64 %0\n.Lk_pc:\n\ts_mov_b32 %1, .Lgs_end-.Lk_pc\n\ts_mov_b32 %2, .Lk_sp-.Lk_pc-244*128" : "=s"(pc), "=s"(plen), "=s"(psp));
  if (idx == PC0) asm volatile("global_load_dword %0, %2, %1" : "+v"(pfc) : "s"(pc), "v"(tid * 128 + (tid < 244 ? 0u : psp)) : "memory");
  const int g = lane >> 4, r16 = lane & 15, q4 = r16 >> 2, p4 = lane & 3;
  const int cg = xc * 24 + idx, tb = idx < 24 ? cg + 64 : xc * 8 + idx - 24;
  const int tok = wid * 2 + (lane >> 5), ll = lane & 31;
  const int d0 = cg * 3;
  const int dq1 = idx % 3, mt = 2 * dq1 + (xc & 1), nt1 = min((xc >> 1) * 9 + idx / 3, 35);
  const int kh = xc & 1, kq = xc >> 1, mq = idx & 3, nt2 = kh * 8 + ((idx >> 2) ^ 2);
  const int sw2 = (idx >> 3) == 1, nt2a = nt2 ^ (sw2 & (wid >> 1)), nt2b = nt2a ^ sw2;
  unsigned x3o = (tb * 8 + tok) * (D_ * 2) + ll * 12, b1o = (nt1 * 128 + (wid ^ 2) * 32 + r16) * 2, b2o = (nt2b * 128 + (wid ^ 2) * 32 + 4 * g) * 2;
  unsigned po = (d0 * N_ + (tid & 127)) * 2, pd = d0 * 2;
  asm volatile("" : "+v"(x3o), "+v"(b1o), "+v"(b2o), "+v"(po), "+v"(pd));
  const unsigned ldsT = (unsigned)(size_t)smem_, lds0 = ldsT + CB, wbase = ldsT + wid * G_WS;
  auto pfoff = [&](unsigned e) { return __umul24(__umul24(e, 58255) >> 20, W1B - 18 * 128) + e * 128; };
  unsigned pfo0, pfo1, pfo2;
  float pp[18];
  unsigned kad[12], kto[4], zoff, kap, kbp, ap0, bp0, pr0, rb[4];
  const unsigned kt = ldsT + KG_T - KT0 * 8;
  auto index = [&](auto K) {
    if constexpr (K == 0) {
      const unsigned e1 = 9216 + 6912 + (idx * 4 + wid) * PFL + lane;
      pfo1 = pfoff(min(e1, e1 - 18432)) | (lane < PFL && idx < 27 ? 0u : OOB); pfo2 = pfoff(9216 - 96 * PFL3 + (idx * 4 + wid) * PFL3 + lane) | (lane < PFL3 ? 0u : OOB);
      pin1(pfo1); pin1(pfo2);
    } else if constexpr (K == 1) {
    const int R = min(wid * 16 + r16, 40), sh = min(max((R >> 3) - 1, 0), 3), t = (R - 8 * sh) << sh;
    const float p0 = (float)((t & 7) + ((t & ~7) << 4) + 32 * g + (wid >> 1));
    const unsigned k01 = pk2(fmaxf(p0, 0.99609375f), p0 + 8.f), k23 = pk2(p0 + 16.f, p0 + 24.f);
    kto[0] = mad16<0>(k01, kt); kto[1] = mad16<1>(k01, kt); kto[2] = mad16<0>(k23, kt); kto[3] = mad16<1>(k23, kt);
    const unsigned kp = lds0 + (g & 1) * 256 + (g >> 1) * 128;
    kap = R * KG_PITCH + kp; kbp = r16 * KG_PITCH + kp + KG_B;
    const unsigned zn = min(tid >> 4, 11);
    zoff = lds0 + zn * CV_KP + 4092 + 2 * (zn & 2) + 4 * r16;
    pin4(kto); pin1(zoff); pin1(kap); pin1(kbp);
    } else if constexpr (K == 2) {
    const float f0 = (float)(2047 - 512 * wid - 2 * lane);
    sfor<4>([&](auto Q) {
      const float f1 = f0 - (128 * Q + 1);
      const unsigned p = pk2(f0 - 128 * Q, Q == 3 ? fmaxf(f1, 0.99609375f) : f1);
      kad[3 * Q] = mad16<0>(p, kt); kad[3 * Q + 1] = mad16<1>(p, kt);
      if constexpr (Q & 1) {
        const float f2 = f0 - (128 * Q + 2);
        const unsigned r = pk2(f0 - (128 * Q - 126), Q == 3 ? fmaxf(f2, 0.9921875f) : f2);
        kad[3 * Q - 1] = mad16<0>(r, kt); kad[3 * Q + 2] = mad16<1>(r, kt);
      }
    });
#pragma unroll
    for (int k = 0; k < 12; ++k) pin1(kad[k]);
    } else {
    const int c1 = (wid + 1) >> 1, s1 = wid == 2 ? 448 : 192;
    ap0 = lds0 + c1 * 4 * CV_KP - s1 + p4 * CV_KP + 2 * (272 - 8 * q4 - 8 * g);
    bp0 = ldsT + CV_U + c1 * CV_UC - s1 + (r16 & 1) * CV_UP + 2 * (256 + 32 * (r16 >> 1) - 8 * g);
    pr0 = ldsT + CW + wid * 128 + ((lane >> 1) & 1) * 2112 + (g & 1) * (4 * WA + 128) + (lane >> 5) * 64 + q4 * 16 + (lane & 1) * 8;
#pragma unroll
    for (int m = 0; m < 4; ++m) rb[m] = ldsT + CW + wid * WA + 1024 * g + 128 * q4 + 32 * (m ^ (q4 >> 1) ^ (((g ^ wid) & 1) << 1)) + 8 * p4;
    pin4(rb); pin1(pr0);
    pin1(ap0); pin1(bp0);
    }
    SB0;
  };
  asm volatile("s_waitcnt lgkmcnt(0)" : "+s"(ka), "+s"(kb), "+s"(kc));
  const u16 *x = mkp<u16>(ka[0], ka[1]), *logA = mkp<u16>(ka[2], ka[3]), *Aim = mkp<u16>(ka[4], ka[5]), *Cre = mkp<u16>(ka[6], ka[7]), *Cim = mkp<u16>(ka[8], ka[9]),
            *logdt = mkp<u16>(ka[10], ka[11]), *Dsk = mkp<u16>(ka[12], ka[13]), *w1 = mkp<u16>(ka[14], ka[15]), *b1 = mkp<u16>(kb[0], kb[1]), *w2 = mkp<u16>(kb[2], kb[3]),
            *b2 = mkp<u16>(kb[4], kb[5]), *out = mkp<u16>(kc[0], kc[1]);
  char* const ws = mkp<char>(kb[6], kb[7]);
  unsigned bias1[2];
  u32x3 x3[3];
  sfor<3>([&](auto I) { gld96<384 * I>(x3[I], x3o, x); });
  u64 ep;
  asm volatile("s_load_dwordx2 %0, %1, %2" : "=&s"(ep) : "s"(ws), "s"(SY_EPOCH) : "memory");
  const rsrc_t rw = mk_rsrc(ws, WS_BYTES);
  const i32x4 sw = mk_srd(ws, WS_BYTES), sb1 = mk_srd(w1, L_ * F_ * 2), sb2 = mk_srd(w2, L_ * F_ * 2);
  const bool cv_on = idx < 24;
  const unsigned w1c0 = (xc & 1) * (L_ / 2) * (F_ * 2) + (xc >> 1) * 2304;

  u32x2 bias2[2];
  if (cv_on) {
    sfor<3>([&](auto C) {
      gldh<256 * C>(pp[6 * C], po, logA); gldh<256 * C>(pp[6 * C + 1], po, Aim); gldh<256 * C>(pp[6 * C + 2], po, Cre); gldh<256 * C>(pp[6 * C + 3], po, Cim);
      gldh<2 * C>(pp[6 * C + 4], pd, logdt); gldh<2 * C>(pp[6 * C + 5], pd, Dsk);
    });
  } else {
    asm volatile("buffer_store_dword %0, %1, %2, %3 offen sc1" :: "v"(0u), "v"(blockIdx.x == 255 ? tid * 256 : OOB), "s"(sw), "s"(SY_CNT) : "memory");
  }
  sfor<2>([&](auto C) { gld16<32 * C>(bias1[C], b1o, b1); gld64<32 * C>(bias2[C], b2o, b2); });
  const bool g1_on = idx < 27;
  pfo0 = pfoff(9216 + idx * 256 + tid) | (OOB * ((idx + 5) >> 5));
  touch_a(pfo0, sb1, w1c0);
  sfor<4>(index);
  auto early = [&](unsigned w2o, int i) {
    const unsigned o = w2o + (i ? (i - 1) * 160 : 992) * ROWB;
    touch_l(o | (((o >> 11) & 128) ^ (i ? 0 : 128)), sb2, 0);
  };
  unsigned vb1[4], l16 = lane * 16, uw = ldsT + U_ST + wid * 3136 + ll * 16 + (lane >> 5) * 8, ur = ldsT + U_ST + p4 * 3136 + (tid >> 2) * 16, ugo = (lane >> 2) * UGP_ + p4 * 16;
#pragma unroll
  for (int j = 0; j < 4; ++j) vb1[j] = ((8 * j + (lane >> 3)) * W1B + (((lane & 7) ^ (2 * ((g & 1) | ((j & 1) << 1)))) << 4) - j * 1024) | (OOB * ((idx + 5) >> 5));
  pin4(vb1); pin1(l16); pin1(uw); pin1(ur); pin1(ugo);
  unsigned yout = WS_AG + kh * AGH + ((blockIdx.x >> 1) * 4 + wid) * 4096, gout = WS_GF + ((nt1 * 4 + wid) * 36 + mt * 6) * 1024, uout = WS_UG + tb * UGP_ + wid * 1024, uin[4];
  uout += ((tb + 64) >> 8) * u_mv(12) + ((tb + 32) >> 8) * (u_mv(14) - u_mv(12)) + ((tb + 16) >> 8) * (u_mv(15) - u_mv(14));
#pragma unroll
  for (int j = 0; j < 4; ++j) { uin[j] = WS_UG + (wid * 64 + j * 16) * UGP_ + ((wid + 1) >> 2) * u_mv(12 + j) + ((cg & 192) + (cg & 1) * 32 + ((cg & 63) >> 1)) * 64; pins(uin[j]); }
  unsigned b1_base = nt1 * 256 + (wid & 2) * 64 + ((xc & 1) * (L_ / 2) + wid * 32) * W1B;
  pins(uout); pins(b1_base);
  if (cv_on) VMCNT(23); else VMCNT(6);
  asm volatile("" : "+v"(x3[0]), "+v"(x3[1]), "+v"(x3[2]));
  keep(pfc);
  unsigned tagx, tagy, tagu;
  f32x16 kp;
  {
    f32x2 v[9];
#pragma unroll
    for (int i = 0; i < 9; ++i) v[i] = unpk(x3[i / 3][i % 3]);
    float mean, var;
    ln_stats(v, mean, var);
    asm volatile("s_waitcnt lgkmcnt(0)" : "+s"(ep));
    const u64 tag64 = ep * 0x9E3779B97F4A7C15ull + 0xD1B54A32D192ED03ull;
    tagx = (unsigned)tag64; tagy = (unsigned)(tag64 >> 32);
    tagu = (tagx | 0xffc0u) << 16;
    const float inv = ln_inv(var), c = -rbf(mean) * inv;
#pragma unroll
    for (int i = 0; i < 9; ++i) v[i] = __builtin_elementwise_fma(v[i], f32x2{inv, inv}, f32x2{c, c});
    SB0;
    u32x2 o[6];
    sfor<7>([&](auto I) {
      constexpr int j = 3 * (I / 2);
      if constexpr (I > 0) *(LAS u32x2*)(size_t)(uw + (I - 1) * 512) = o[I - 1];
      if constexpr (I < 6) o[I] = I & 1 ? u32x2{pk2a(v[j + 1].y, v[j + 2].x), pk2s(v[j + 2].y, tagu)} : u32x2{pkv(v[j]), pk2s(v[j + 1].x, tagu)};
      SB0;
    });
    if (!cv_on) VMCNT(5);
    asm volatile("s_mov_b64 exec, 0\n\ts_cmp_lt_u32 %0, 1\n\ts_cbranch_scc1 .Lkg_a\n\ts_cmp_lt_u32 %0, 2\n\ts_cbranch_scc1 .Lwk_b1\n\ts_cmp_lt_u32 %0, 3\n\ts_cbranch_scc1 .Lkg_b\n\ts_branch .Lwk_cv0\n"
                 ".Lwk_back:\n\ts_mov_b64 exec, -1" :: "s"(wid) : "scc", "memory");
    LDS_BARRIER();
    u32x4 d[3];
#pragma unroll
    for (int k = 0; k < 3; ++k) d[k] = *(const LAS u32x4*)(size_t)(ur + k * 1024);
    SB0;
    VMCNT(5);
    pin9(pp); pin9(pp + 9);
#pragma unroll
    for (int c = 0; c < 3; ++c) {
      const float dt = rbf(__expf(pp[6 * c + 4]));
      kp[6 + c] = rbf(__expf(pp[6 * c])) * (dt * -1.4426950408889634f); kp[9 + c] = pp[6 * c + 1] * (dt * -0.15915494309189535f); kp[12 + c] = tid == 0 ? pp[6 * c + 5] : 0.f;
      kp[2 * c] = pp[6 * c + 2] * (dt + dt); kp[2 * c + 1] = -pp[6 * c + 3] * (dt + dt);
      if (c == 1) {
        SB0;
#pragma unroll
        for (int k = 0; k < 3; ++k) bstore_s(d[k], l16, sw, uout + k * 4096);
        SB0;
      }
    }
    asm volatile("" : "+{v[44:59]}"(kp));
  }
  const unsigned sink = wbase + G_SINK;
  const unsigned rot1 = (idx / 3) & 3;
  auto ride = [&](auto P, unsigned base, auto OFF) {
    constexpr int t = P < 32 ? 4 + P / 8 : 7 - P / 8;
    SB0; bdma<OFF, (P & 3) * 1024>(base, vb1[P & 3], sb1, (b1_base + (t ^ rot1) * 128 * W1B) ^ (P & 4 ? 128 : 0)); SB0;
  };
  auto ride2 = [&](auto P) { ride(IC<32 + P>{}, wbase, IC<P / 4 * 4096>{}); };
  auto marks = [&](unsigned e0, unsigned e1, unsigned st) {
    u32x4 none = {~0u, ~0u, ~0u, ~0u};
    asm volatile("" : "+v"(none));
#pragma nounroll
    for (unsigned e = e0; e < e1; e += st) bstore_s(none, l16, sw, WS_GF + e * 1024);
  };
  constexpr unsigned ME0 = 768 * MC, ME1 = ME0 + 96 * MGA;
  u32x16 tg;
  if (__builtin_expect(cv_on, 1)) {
    asm volatile(
"s_cmp_gt_u32 %14, 1\n"
"s_cbranch_scc1 .Lkg_b\n"
"s_branch .Lkg_a\n"
".p2align 12\n"
".Lkg_a:\n"
"s_cbranch_execz .Lwk_back\n"
"s_add_u32 m0, %13, 0\n"
"buffer_load_dword a0, %9, %11, %12 offen\n"
"v_cvt_pk_bf16_f32 v120, v44, v45\n"
"v_mul_f32 v121, 0x43000000, v53\n"
"v_mul_f32 v123, 0x43000000, v50\n"
"ds_write_addtid_b32 v120 offset:64\n"
"v_fract_f32 v124, v121\n"
"v_exp_f32 v122, v123\n"
"v_fract_f32 v120, v53\n"
"v_cos_f32 v126, v124\n"
"v_sin_f32 v127, v124\n"
"v_exp_f32 v128, v50\n"
"v_cos_f32 v130, v120\n"
"v_pk_mul_f32 v[124:125], v[122:123], v[126:127] op_sel_hi:[0,1]\n"
"v_sin_f32 v131, v120\n"
"v_mov_b32 v121, 0x3f80\n"
"v_pk_mul_f32 v[122:123], v[44:45], v[124:125] op_sel_hi:[0,1]\n"
"v_pk_mul_f32 v[126:127], v[128:129], v[130:131] op_sel_hi:[0,1]\n"
"ds_write_addtid_b32 v121 offset:21712\n"
"v_pk_fma_f32 v[132:133], v[44:45], v[124:125], v[122:123] op_sel:[1,1,0] op_sel_hi:[1,0,1] neg_lo:[0,1,0]\n"
"v_pk_mul_f32 v[128:129], v[44:45], v[126:127] op_sel_hi:[0,1]\n"
"v_pk_mul_f32 v[120:121], v[126:127], v[126:127] op_sel_hi:[0,1]\n"
"v_cvt_pk_bf16_f32 v122, v132, v133\n"
"v_pk_mul_f32 v[130:131], v[132:133], v[126:127] op_sel_hi:[0,1]\n"
"v_pk_fma_f32 v[134:135], v[44:45], v[126:127], v[128:129] op_sel:[1,1,0] op_sel_hi:[1,0,1] neg_lo:[0,1,0]\n"
"ds_write_addtid_b32 v122 offset:4288\n"
"v_pk_fma_f32 v[136:137], v[132:133], v[126:127], v[130:131] op_sel:[1,1,0] op_sel_hi:[1,0,1] neg_lo:[0,1,0]\n"
"v_cvt_pk_bf16_f32 v123, v134, v135\n"
"v_pk_mul_f32 v[128:129], v[134:135], v[126:127] op_sel_hi:[0,1]\n"
"v_cvt_pk_bf16_f32 v122, v136, v137\n"
"ds_write_addtid_b32 v123 offset:592\n"
"v_pk_mul_f32 v[130:131], v[136:137], v[126:127] op_sel_hi:[0,1]\n"
"ds_write_addtid_b32 v122 offset:4816\n"
"v_pk_fma_f32 v[138:139], v[134:135], v[126:127], v[128:129] op_sel:[1,1,0] op_sel_hi:[1,0,1] neg_lo:[0,1,0]\n"
"v_pk_fma_f32 v[140:141], v[136:137], v[126:127], v[130:131] op_sel:[1,1,0] op_sel_hi:[1,0,1] neg_lo:[0,1,0]\n"
"v_pk_mul_f32 v[122:123], v[132:133], v[124:125] op_sel_hi:[0,1]\n"
"v_cvt_pk_bf16_f32 v128, v138, v139\n"
"v_cvt_pk_bf16_f32 v129, v140, v141\n"
"v_pk_mul_f32 v[130:131], v[138:139], v[126:127] op_sel_hi:[0,1]\n"
"ds_write_addtid_b32 v128 offset:1120\n"
"v_pk_mul_f32 v[134:135], v[140:141], v[126:127] op_sel_hi:[0,1]\n"
"ds_write_addtid_b32 v129 offset:5344\n"
"v_pk_fma_f32 v[136:137], v[132:133], v[124:125], v[122:123] op_sel:[1,1,0] op_sel_hi:[1,0,1] neg_lo:[0,1,0]\n"
"v_pk_fma_f32 v[142:143], v[138:139], v[126:127], v[130:131] op_sel:[1,1,0] op_sel_hi:[1,0,1] neg_lo:[0,1,0]\n"
"v_pk_fma_f32 v[128:129], v[140:141], v[126:127], v[134:135] op_sel:[1,1,0] op_sel_hi:[1,0,1] neg_lo:[0,1,0]\n"
"v_cvt_pk_bf16_f32 v122, v136, v137\n"
"v_cvt_pk_bf16_f32 v123, v142, v143\n"
"v_cvt_pk_bf16_f32 v130, v128, v129\n"
"ds_write_addtid_b32 v122 offset:8512\n"
"v_pk_mul_f32 v[132:133], v[136:137], v[124:125] op_sel_hi:[0,1]\n"
"ds_write_addtid_b32 v123 offset:1648\n"
"v_pk_mul_f32 v[134:135], v[142:143], v[126:127] op_sel_hi:[0,1]\n"
"ds_write_addtid_b32 v130 offset:5872\n"
"v_pk_mul_f32 v[122:123], v[128:129], v[126:127] op_sel_hi:[0,1]\n"
"v_pk_fma_f32 v[138:139], v[126:127], v[126:127], v[120:121] op_sel:[1,1,0] op_sel_hi:[1,0,1] neg_lo:[0,1,0]\n"
"v_pk_fma_f32 v[130:131], v[136:137], v[124:125], v[132:133] op_sel:[1,1,0] op_sel_hi:[1,0,1] neg_lo:[0,1,0]\n"
"v_pk_fma_f32 v[140:141], v[142:143], v[126:127], v[134:135] op_sel:[1,1,0] op_sel_hi:[1,0,1] neg_lo:[0,1,0]\n"
"v_pk_fma_f32 v[120:121], v[128:129], v[126:127], v[122:123] op_sel:[1,1,0] op_sel_hi:[1,0,1] neg_lo:[0,1,0]\n"
"v_cvt_pk_bf16_f32 v132, v130, v131\n"
"v_cvt_pk_bf16_f32 v133, v140, v141\n"
"v_cvt_pk_bf16_f32 v122, v120, v121\n"
"ds_write_addtid_b32 v132 offset:10624\n"
"v_pk_mul_f32 v[128:129], v[130:131], v[124:125] op_sel_hi:[0,1]\n"
"ds_write_addtid_b32 v133 offset:2176\n"
"v_pk_mul_f32 v[134:135], v[140:141], v[126:127] op_sel_hi:[0,1]\n"
"ds_write_addtid_b32 v122 offset:6400\n"
"v_pk_mul_f32 v[132:133], v[120:121], v[126:127] op_sel_hi:[0,1]\n"
"v_pk_mul_f32 v[142:143], v[136:137], v[138:139] op_sel_hi:[0,1]\n"
"v_pk_mul_f32 v[122:123], v[130:131], v[138:139] op_sel_hi:[0,1]\n"
"v_pk_fma_f32 v[144:145], v[130:131], v[124:125], v[128:129] op_sel:[1,1,0] op_sel_hi:[1,0,1] neg_lo:[0,1,0]\n"
"v_pk_fma_f32 v[146:147], v[140:141], v[126:127], v[134:135] op_sel:[1,1,0] op_sel_hi:[1,0,1] neg_lo:[0,1,0]\n"
"v_pk_fma_f32 v[148:149], v[120:121], v[126:127], v[132:133] op_sel:[1,1,0] op_sel_hi:[1,0,1] neg_lo:[0,1,0]\n"
"v_cvt_pk_bf16_f32 v128, v144, v145\n"
"v_cvt_pk_bf16_f32 v129, v146, v147\n"
"v_cvt_pk_bf16_f32 v120, v148, v149\n"
"ds_write_addtid_b32 v128 offset:12736\n"
"v_pk_fma_f32 v[132:133], v[136:137], v[138:139], v[142:143] op_sel:[1,1,0] op_sel_hi:[1,0,1] neg_lo:[0,1,0]\n"
"ds_write_addtid_b32 v129 offset:2704\n"
"v_pk_fma_f32 v[134:135], v[130:131], v[138:139], v[122:123] op_sel:[1,1,0] op_sel_hi:[1,0,1] neg_lo:[0,1,0]\n"
"ds_write_addtid_b32 v120 offset:6928\n"
"v_cvt_pk_bf16_f32 v121, v132, v133\n"
"v_cvt_pk_bf16_f32 v122, v134, v135\n"
"v_pk_mul_f32 v[128:129], v[138:139], v[138:139] op_sel_hi:[0,1]\n"
"ds_write_addtid_b32 v121 offset:9040\n"
"v_pk_mul_f32 v[130:131], v[144:145], v[124:125] op_sel_hi:[0,1]\n"
"ds_write_addtid_b32 v122 offset:11152\n"
"v_pk_mul_f32 v[120:121], v[146:147], v[126:127] op_sel_hi:[0,1]\n"
"v_pk_mul_f32 v[136:137], v[148:149], v[126:127] op_sel_hi:[0,1]\n"
"v_pk_mul_f32 v[122:123], v[132:133], v[138:139] op_sel_hi:[0,1]\n"
"v_pk_mul_f32 v[140:141], v[134:135], v[138:139] op_sel_hi:[0,1]\n"
"v_pk_fma_f32 v[142:143], v[138:139], v[138:139], v[128:129] op_sel:[1,1,0] op_sel_hi:[1,0,1] neg_lo:[0,1,0]\n"
"v_pk_fma_f32 v[150:151], v[144:145], v[124:125], v[130:131] op_sel:[1,1,0] op_sel_hi:[1,0,1] neg_lo:[0,1,0]\n"
"v_pk_fma_f32 v[152:153], v[146:147], v[126:127], v[120:121] op_sel:[1,1,0] op_sel_hi:[1,0,1] neg_lo:[0,1,0]\n"
"v_pk_fma_f32 v[128:129], v[148:149], v[126:127], v[136:137] op_sel:[1,1,0] op_sel_hi:[1,0,1] neg_lo:[0,1,0]\n"
"v_cvt_pk_bf16_f32 v124, v150, v151\n"
"v_cvt_pk_bf16_f32 v120, v152, v153\n"
"v_cvt_pk_bf16_f32 v121, v128, v129\n"
"ds_write_addtid_b32 v124 offset:13792\n"
"v_pk_fma_f32 v[130:131], v[132:133], v[138:139], v[122:123] op_sel:[1,1,0] op_sel_hi:[1,0,1] neg_lo:[0,1,0]\n"
"ds_write_addtid_b32 v120 offset:3232\n"
"v_pk_fma_f32 v[124:125], v[134:135], v[138:139], v[140:141] op_sel:[1,1,0] op_sel_hi:[1,0,1] neg_lo:[0,1,0]\n"
"ds_write_addtid_b32 v121 offset:7456\n"
"v_cvt_pk_bf16_f32 v120, v130, v131\n"
"v_cvt_pk_bf16_f32 v122, v124, v125\n"
"v_pk_mul_f32 v[132:133], v[152:153], v[126:127] op_sel_hi:[0,1]\n"
"ds_write_addtid_b32 v120 offset:9568\n"
"v_pk_mul_f32 v[134:135], v[128:129], v[126:127] op_sel_hi:[0,1]\n"
"ds_write_addtid_b32 v122 offset:11680\n"
"v_pk_mul_f32 v[120:121], v[130:131], v[138:139] op_sel_hi:[0,1]\n"
"v_pk_mul_f32 v[136:137], v[124:125], v[138:139] op_sel_hi:[0,1]\n"
"v_pk_mul_f32 v[122:123], v[144:145], v[142:143] op_sel_hi:[0,1]\n"
"v_pk_mul_f32 v[140:141], v[150:151], v[142:143] op_sel_hi:[0,1]\n"
"v_pk_fma_f32 v[146:147], v[152:153], v[126:127], v[132:133] op_sel:[1,1,0] op_sel_hi:[1,0,1] neg_lo:[0,1,0]\n"
"v_pk_fma_f32 v[148:149], v[128:129], v[126:127], v[134:135] op_sel:[1,1,0] op_sel_hi:[1,0,1] neg_lo:[0,1,0]\n"
"v_pk_fma_f32 v[154:155], v[130:131], v[138:139], v[120:121] op_sel:[1,1,0] op_sel_hi:[1,0,1] neg_lo:[0,1,0]\n"
"v_cvt_pk_bf16_f32 v132, v146, v147\n"
"v_cvt_pk_bf16_f32 v126, v148, v149\n"
"v_cvt_pk_bf16_f32 v120, v154, v155\n"
"ds_write_addtid_b32 v132 offset:3760\n"
"v_pk_fma_f32 v[128:129], v[124:125], v[138:139], v[136:137] op_sel:[1,1,0] op_sel_hi:[1,0,1] neg_lo:[0,1,0]\n"
"ds_write_addtid_b32 v126 offset:7984\n"
"v_pk_fma_f32 v[130:131], v[144:145], v[142:143], v[122:123] op_sel:[1,1,0] op_sel_hi:[1,0,1] neg_lo:[0,1,0]\n"
"ds_write_addtid_b32 v120 offset:10096\n"
"v_cvt_pk_bf16_f32 v121, v128, v129\n"
"v_cvt_pk_bf16_f32 v122, v130, v131\n"
"v_pk_fma_f32 v[124:125], v[150:151], v[142:143], v[140:141] op_sel:[1,1,0] op_sel_hi:[1,0,1] neg_lo:[0,1,0]\n"
"ds_write_addtid_b32 v121 offset:12208\n"
"ds_write_addtid_b32 v122 offset:13264\n"
"v_cvt_pk_bf16_f32 v120, v124, v125\n"
"v_mul_f32 v121, 0x43000000, v54\n"
"v_mul_f32 v123, 0x43000000, v51\n"
"ds_write_addtid_b32 v120 offset:14320\n"
"v_fract_f32 v124, v121\n"
"v_exp_f32 v122, v123\n"
"v_fract_f32 v120, v54\n"
"v_cos_f32 v126, v124\n"
"v_sin_f32 v127, v124\n"
"v_exp_f32 v128, v51\n"
"v_cos_f32 v130, v120\n"
"v_pk_mul_f32 v[124:125], v[122:123], v[126:127] op_sel_hi:[0,1]\n"
"v_sin_f32 v131, v120\n"
"v_mov_b32 v121, 0x3f80\n"
"v_pk_mul_f32 v[122:123], v[46:47], v[124:125] op_sel_hi:[0,1]\n"
"s_add_u32 m0, %13, 30096\n"
"v_pk_mul_f32 v[126:127], v[128:129], v[130:131] op_sel_hi:[0,1]\n"
"s_waitcnt lgkmcnt(0)\n"
"s_barrier\n"
"ds_read_b128 v[220:223], %2 offset:0\n"
"ds_write_addtid_b32 v121 offset:21712\n"
"v_cvt_pk_bf16_f32 v120, v46, v47\n"
"ds_read_b128 v[216:219], %3 offset:0\n"
"v_pk_fma_f32 v[128:129], v[46:47], v[124:125], v[122:123] op_sel:[1,1,0] op_sel_hi:[1,0,1] neg_lo:[0,1,0]\n"
"v_pk_mul_f32 v[130:131], v[46:47], v[126:127] op_sel_hi:[0,1]\n"
"ds_read_b128 v[212:215], %2 offset:16\n"
"ds_write_addtid_b32 v120 offset:64\n"
"v_cvt_pk_bf16_f32 v121, v128, v129\n"
"ds_read_b128 v[208:211], %3 offset:16\n"
"v_pk_mul_f32 v[122:123], v[128:129], v[126:127] op_sel_hi:[0,1]\n"
"v_pk_fma_f32 v[132:133], v[46:47], v[126:127], v[130:131] op_sel:[1,1,0] op_sel_hi:[1,0,1] neg_lo:[0,1,0]\n"
"ds_read_b128 v[204:207], %2 offset:32\n"
"ds_write_addtid_b32 v121 offset:4288\n"
"v_pk_fma_f32 v[130:131], v[128:129], v[126:127], v[122:123] op_sel:[1,1,0] op_sel_hi:[1,0,1] neg_lo:[0,1,0]\n"
"ds_read_b128 v[200:203], %3 offset:32\n"
"v_cvt_pk_bf16_f32 v120, v132, v133\n"
"v_pk_mul_f32 v[122:123], v[132:133], v[126:127] op_sel_hi:[0,1]\n"
"ds_read_b128 v[196:199], %2 offset:48\n"
"v_cvt_pk_bf16_f32 v121, v130, v131\n"
"ds_write_addtid_b32 v120 offset:592\n"
"ds_read_b128 v[192:195], %3 offset:48\n"
"v_pk_mul_f32 v[134:135], v[130:131], v[126:127] op_sel_hi:[0,1]\n"
"ds_write_addtid_b32 v121 offset:4816\n"
"ds_read_b128 v[188:191], %2 offset:64\n"
"v_pk_fma_f32 v[136:137], v[132:133], v[126:127], v[122:123] op_sel:[1,1,0] op_sel_hi:[1,0,1] neg_lo:[0,1,0]\n"
"v_pk_fma_f32 v[120:121], v[130:131], v[126:127], v[134:135] op_sel:[1,1,0] op_sel_hi:[1,0,1] neg_lo:[0,1,0]\n"
"ds_read_b128 v[184:187], %3 offset:64\n"
"v_pk_mul_f32 v[122:123], v[128:129], v[124:125] op_sel_hi:[0,1]\n"
"v_cvt_pk_bf16_f32 v130, v136, v137\n"
"ds_read_b128 v[180:183], %2 offset:80\n"
"v_cvt_pk_bf16_f32 v131, v120, v121\n"
"v_pk_mul_f32 v[132:133], v[136:137], v[126:127] op_sel_hi:[0,1]\n"
"ds_read_b128 v[176:179], %3 offset:80\n"
"ds_write_addtid_b32 v130 offset:1120\n"
"v_pk_mul_f32 v[134:135], v[120:121], v[126:127] op_sel_hi:[0,1]\n"
"ds_read_b128 v[172:175], %2 offset:96\n"
"ds_write_addtid_b32 v131 offset:5344\n"
"v_pk_fma_f32 v[138:139], v[128:129], v[124:125], v[122:123] op_sel:[1,1,0] op_sel_hi:[1,0,1] neg_lo:[0,1,0]\n"
"ds_read_b128 v[168:171], %3 offset:96\n"
"v_pk_fma_f32 v[130:131], v[136:137], v[126:127], v[132:133] op_sel:[1,1,0] op_sel_hi:[1,0,1] neg_lo:[0,1,0]\n"
"v_pk_fma_f32 v[122:123], v[120:121], v[126:127], v[134:135] op_sel:[1,1,0] op_sel_hi:[1,0,1] neg_lo:[0,1,0]\n"
"ds_read_b128 v[164:167], %2 offset:112\n"
"v_cvt_pk_bf16_f32 v128, v138, v139\n"
"v_cvt_pk_bf16_f32 v120, v130, v131\n"
"ds_read_b128 v[160:163], %3 offset:112\n"
"v_cvt_pk_bf16_f32 v121, v122, v123\n"
"ds_write_addtid_b32 v128 offset:8512\n"
"v_pk_mul_f32 v[132:133], v[126:127], v[126:127] op_sel_hi:[0,1]\n"
"ds_write_addtid_b32 v120 offset:1648\n"
"v_pk_mul_f32 v[128:129], v[138:139], v[124:125] op_sel_hi:[0,1]\n"
"ds_write_addtid_b32 v121 offset:5872\n"
"s_waitcnt lgkmcnt(15)\n"
"v_mfma_f32_16x16x32_bf16 v[224:227], v[216:219], v[220:223], 0\n"
"v_pk_mul_f32 v[120:121], v[130:131], v[126:127] op_sel_hi:[0,1]\n"
"v_pk_mul_f32 v[134:135], v[122:123], v[126:127] op_sel_hi:[0,1]\n"
"v_pk_fma_f32 v[136:137], v[126:127], v[126:127], v[132:133] op_sel:[1,1,0] op_sel_hi:[1,0,1] neg_lo:[0,1,0]\n"
"v_pk_fma_f32 v[140:141], v[138:139], v[124:125], v[128:129] op_sel:[1,1,0] op_sel_hi:[1,0,1] neg_lo:[0,1,0]\n"
"v_pk_fma_f32 v[142:143], v[130:131], v[126:127], v[120:121] op_sel:[1,1,0] op_sel_hi:[1,0,1] neg_lo:[0,1,0]\n"
"v_pk_fma_f32 v[132:133], v[122:123], v[126:127], v[134:135] op_sel:[1,1,0] op_sel_hi:[1,0,1] neg_lo:[0,1,0]\n"
"v_cvt_pk_bf16_f32 v128, v140, v141\n"
"v_cvt_pk_bf16_f32 v120, v142, v143\n"
"v_cvt_pk_bf16_f32 v121, v132, v133\n"
"v_mfma_f32_16x16x32_bf16 v[224:227], v[208:211], v[212:215], v[224:227]\n"
"ds_write_addtid_b32 v128 offset:10624\n"
"v_pk_mul_f32 v[122:123], v[140:141], v[124:125] op_sel_hi:[0,1]\n"
"ds_write_addtid_b32 v120 offset:2176\n"
"v_pk_mul_f32 v[128:129], v[142:143], v[126:127] op_sel_hi:[0,1]\n"
"ds_write_addtid_b32 v121 offset:6400\n"
"v_pk_mul_f32 v[130:131], v[132:133], v[126:127] op_sel_hi:[0,1]\n"
"v_pk_mul_f32 v[134:135], v[138:139], v[136:137] op_sel_hi:[0,1]\n"
"v_pk_mul_f32 v[120:121], v[140:141], v[136:137] op_sel_hi:[0,1]\n"
"v_pk_fma_f32 v[144:145], v[140:141], v[124:125], v[122:123] op_sel:[1,1,0] op_sel_hi:[1,0,1] neg_lo:[0,1,0]\n"
"v_mfma_f32_16x16x32_bf16 v[224:227], v[200:203], v[204:207], v[224:227]\n"
"v_pk_fma_f32 v[146:147], v[142:143], v[126:127], v[128:129] op_sel:[1,1,0] op_sel_hi:[1,0,1] neg_lo:[0,1,0]\n"
"v_pk_fma_f32 v[122:123], v[132:133], v[126:127], v[130:131] op_sel:[1,1,0] op_sel_hi:[1,0,1] neg_lo:[0,1,0]\n"
"v_cvt_pk_bf16_f32 v148, v144, v145\n"
"v_cvt_pk_bf16_f32 v128, v146, v147\n"
"v_cvt_pk_bf16_f32 v129, v122, v123\n"
"ds_write_addtid_b32 v148 offset:12736\n"
"v_pk_fma_f32 v[130:131], v[138:139], v[136:137], v[134:135] op_sel:[1,1,0] op_sel_hi:[1,0,1] neg_lo:[0,1,0]\n"
"ds_write_addtid_b32 v128 offset:2704\n"
"v_pk_fma_f32 v[132:133], v[140:141], v[136:137], v[120:121] op_sel:[1,1,0] op_sel_hi:[1,0,1] neg_lo:[0,1,0]\n"
"s_waitcnt lgkmcnt(15)\n"
"v_mfma_f32_16x16x32_bf16 v[224:227], v[192:195], v[196:199], v[224:227]\n"
"ds_write_addtid_b32 v129 offset:6928\n"
"v_cvt_pk_bf16_f32 v120, v130, v131\n"
"v_cvt_pk_bf16_f32 v121, v132, v133\n"
"v_pk_mul_f32 v[128:129], v[136:137], v[136:137] op_sel_hi:[0,1]\n"
"ds_write_addtid_b32 v120 offset:9040\n"
"v_pk_mul_f32 v[134:135], v[144:145], v[124:125] op_sel_hi:[0,1]\n"
"ds_write_addtid_b32 v121 offset:11152\n"
"v_pk_mul_f32 v[138:139], v[146:147], v[126:127] op_sel_hi:[0,1]\n"
"v_pk_mul_f32 v[140:141], v[122:123], v[126:127] op_sel_hi:[0,1]\n"
"v_mfma_f32_16x16x32_bf16 v[224:227], v[184:187], v[188:191], v[224:227]\n"
"v_pk_mul_f32 v[120:121], v[130:131], v[136:137] op_sel_hi:[0,1]\n"
"v_pk_mul_f32 v[142:143], v[132:133], v[136:137] op_sel_hi:[0,1]\n"
"v_pk_fma_f32 v[148:149], v[136:137], v[136:137], v[128:129] op_sel:[1,1,0] op_sel_hi:[1,0,1] neg_lo:[0,1,0]\n"
"v_pk_fma_f32 v[150:151], v[144:145], v[124:125], v[134:135] op_sel:[1,1,0] op_sel_hi:[1,0,1] neg_lo:[0,1,0]\n"
"v_pk_fma_f32 v[152:153], v[146:147], v[126:127], v[138:139] op_sel:[1,1,0] op_sel_hi:[1,0,1] neg_lo:[0,1,0]\n"
"v_pk_fma_f32 v[128:129], v[122:123], v[126:127], v[140:141] op_sel:[1,1,0] op_sel_hi:[1,0,1] neg_lo:[0,1,0]\n"
"v_cvt_pk_bf16_f32 v124, v150, v151\n"
"v_cvt_pk_bf16_f32 v125, v152, v153\n"
"v_cvt_pk_bf16_f32 v122, v128, v129\n"
"s_waitcnt lgkmcnt(15)\n"
"v_mfma_f32_16x16x32_bf16 v[224:227], v[176:179], v[180:183], v[224:227]\n"
"ds_write_addtid_b32 v124 offset:13792\n"
"v_pk_fma_f32 v[134:135], v[130:131], v[136:137], v[120:121] op_sel:[1,1,0] op_sel_hi:[1,0,1] neg_lo:[0,1,0]\n"
"ds_write_addtid_b32 v125 offset:3232\n"
"v_pk_fma_f32 v[138:139], v[132:133], v[136:137], v[142:143] op_sel:[1,1,0] op_sel_hi:[1,0,1] neg_lo:[0,1,0]\n"
"ds_write_addtid_b32 v122 offset:7456\n"
"v_cvt_pk_bf16_f32 v120, v134, v135\n"
"v_cvt_pk_bf16_f32 v121, v138, v139\n"
"v_pk_mul_f32 v[122:123], v[152:153], v[126:127] op_sel_hi:[0,1]\n"
"ds_write_addtid_b32 v120 offset:9568\n"
"s_waitcnt lgkmcnt(15)\n"
"v_mfma_f32_16x16x32_bf16 v[224:227], v[168:171], v[172:175], v[224:227]\n"
"v_pk_mul_f32 v[124:125], v[128:129], v[126:127] op_sel_hi:[0,1]\n"
"ds_write_addtid_b32 v121 offset:11680\n"
"v_pk_mul_f32 v[130:131], v[134:135], v[136:137] op_sel_hi:[0,1]\n"
"v_pk_mul_f32 v[132:133], v[138:139], v[136:137] op_sel_hi:[0,1]\n"
"v_pk_mul_f32 v[120:121], v[144:145], v[148:149] op_sel_hi:[0,1]\n"
"v_pk_mul_f32 v[140:141], v[150:151], v[148:149] op_sel_hi:[0,1]\n"
"v_pk_fma_f32 v[142:143], v[152:153], v[126:127], v[122:123] op_sel:[1,1,0] op_sel_hi:[1,0,1] neg_lo:[0,1,0]\n"
"v_pk_fma_f32 v[146:147], v[128:129], v[126:127], v[124:125] op_sel:[1,1,0] op_sel_hi:[1,0,1] neg_lo:[0,1,0]\n"
"v_pk_fma_f32 v[154:155], v[134:135], v[136:137], v[130:131] op_sel:[1,1,0] op_sel_hi:[1,0,1] neg_lo:[0,1,0]\n"
"v_mfma_f32_16x16x32_bf16 v[224:227], v[160:163], v[164:167], v[224:227]\n"
"v_cvt_pk_bf16_f32 v122, v142, v143\n"
"v_cvt_pk_bf16_f32 v123, v146, v147\n"
"v_cvt_pk_bf16_f32 v124, v154, v155\n"
"ds_write_addtid_b32 v122 offset:3760\n"
"v_pk_fma_f32 v[126:127], v[138:139], v[136:137], v[132:133] op_sel:[1,1,0] op_sel_hi:[1,0,1] neg_lo:[0,1,0]\n"
"ds_write_addtid_b32 v123 offset:7984\n"
"v_pk_fma_f32 v[128:129], v[144:145], v[148:149], v[120:121] op_sel:[1,1,0] op_sel_hi:[1,0,1] neg_lo:[0,1,0]\n"
"ds_write_addtid_b32 v124 offset:10096\n"
"v_cvt_pk_bf16_f32 v122, v126, v127\n"
"v_cvt_pk_bf16_f32 v120, v128, v129\n"
"v_pk_fma_f32 v[124:125], v[150:151], v[148:149], v[140:141] op_sel:[1,1,0] op_sel_hi:[1,0,1] neg_lo:[0,1,0]\n"
"ds_write_addtid_b32 v122 offset:12208\n"
"ds_write_addtid_b32 v120 offset:13264\n"
"v_cvt_pk_bf16_f32 v121, v124, v125\n"
"v_mul_f32 v122, 0x43000000, v55\n"
"v_mul_f32 v127, 0x43000000, v52\n"
"ds_write_addtid_b32 v121 offset:14320\n"
"v_fract_f32 v120, v122\n"
"v_exp_f32 v126, v127\n"
"v_fract_f32 v121, v55\n"
"v_cos_f32 v122, v120\n"
"v_sin_f32 v123, v120\n"
"v_exp_f32 v124, v52\n"
"v_cos_f32 v128, v121\n"
"v_pk_mul_f32 v[130:131], v[126:127], v[122:123] op_sel_hi:[0,1]\n"
"v_sin_f32 v129, v121\n"
"v_mov_b32 v120, 0x3f80\n"
"v_pk_mul_f32 v[122:123], v[48:49], v[130:131] op_sel_hi:[0,1]\n"
"s_add_u32 m0, %13, 60192\n"
"v_pk_mul_f32 v[126:127], v[124:125], v[128:129] op_sel_hi:[0,1]\n"
"s_waitcnt lgkmcnt(0)\n"
"s_barrier\n"
"ds_read_b128 v[220:223], %2 offset:30096\n"
"ds_write_addtid_b32 v120 offset:21712\n"
"v_cvt_pk_bf16_f32 v121, v48, v49\n"
"ds_read_b128 v[216:219], %3 offset:30096\n"
"v_pk_fma_f32 v[124:125], v[48:49], v[130:131], v[122:123] op_sel:[1,1,0] op_sel_hi:[1,0,1] neg_lo:[0,1,0]\n"
"v_pk_mul_f32 v[128:129], v[48:49], v[126:127] op_sel_hi:[0,1]\n"
"ds_read_b128 v[212:215], %2 offset:30112\n"
"ds_write_addtid_b32 v121 offset:64\n"
"v_cvt_pk_bf16_f32 v120, v124, v125\n"
"ds_read_b128 v[208:211], %3 offset:30112\n"
"v_pk_mul_f32 v[122:123], v[124:125], v[126:127] op_sel_hi:[0,1]\n"
"v_pk_fma_f32 v[132:133], v[48:49], v[126:127], v[128:129] op_sel:[1,1,0] op_sel_hi:[1,0,1] neg_lo:[0,1,0]\n"
"ds_read_b128 v[204:207], %2 offset:30128\n"
"ds_write_addtid_b32 v120 offset:4288\n"
"v_pk_fma_f32 v[128:129], v[124:125], v[126:127], v[122:123] op_sel:[1,1,0] op_sel_hi:[1,0,1] neg_lo:[0,1,0]\n"
"ds_read_b128 v[200:203], %3 offset:30128\n"
"v_cvt_pk_bf16_f32 v120, v132, v133\n"
"v_pk_mul_f32 v[122:123], v[132:133], v[126:127] op_sel_hi:[0,1]\n"
"ds_read_b128 v[196:199], %2 offset:30144\n"
"v_cvt_pk_bf16_f32 v121, v128, v129\n"
"ds_write_addtid_b32 v120 offset:592\n"
"ds_read_b128 v[192:195], %3 offset:30144\n"
"v_pk_mul_f32 v[134:135], v[128:129], v[126:127] op_sel_hi:[0,1]\n"
"ds_write_addtid_b32 v121 offset:4816\n"
"ds_read_b128 v[188:191], %2 offset:30160\n"
"v_pk_fma_f32 v[136:137], v[132:133], v[126:127], v[122:123] op_sel:[1,1,0] op_sel_hi:[1,0,1] neg_lo:[0,1,0]\n"
"v_pk_fma_f32 v[120:121], v[128:129], v[126:127], v[134:135] op_sel:[1,1,0] op_sel_hi:[1,0,1] neg_lo:[0,1,0]\n"
"ds_read_b128 v[184:187], %3 offset:30160\n"
"v_pk_mul_f32 v[122:123], v[124:125], v[130:131] op_sel_hi:[0,1]\n"
"v_cvt_pk_bf16_f32 v128, v136, v137\n"
"ds_read_b128 v[180:183], %2 offset:30176\n"
"v_cvt_pk_bf16_f32 v129, v120, v121\n"
"v_pk_mul_f32 v[132:133], v[136:137], v[126:127] op_sel_hi:[0,1]\n"
"ds_read_b128 v[176:179], %3 offset:30176\n"
"ds_write_addtid_b32 v128 offset:1120\n"
"v_pk_mul_f32 v[134:135], v[120:121], v[126:127] op_sel_hi:[0,1]\n"
"ds_read_b128 v[172:175], %2 offset:30192\n"
"ds_write_addtid_b32 v129 offset:5344\n"
"v_pk_fma_f32 v[138:139], v[124:125], v[130:131], v[122:123] op_sel:[1,1,0] op_sel_hi:[1,0,1] neg_lo:[0,1,0]\n"
"ds_read_b128 v[168:171], %3 offset:30192\n"
"v_pk_fma_f32 v[128:129], v[136:137], v[126:127], v[132:133] op_sel:[1,1,0] op_sel_hi:[1,0,1] neg_lo:[0,1,0]\n"
"v_pk_fma_f32 v[122:123], v[120:121], v[126:127], v[134:135] op_sel:[1,1,0] op_sel_hi:[1,0,1] neg_lo:[0,1,0]\n"
"ds_read_b128 v[164:167], %2 offset:30208\n"
"v_cvt_pk_bf16_f32 v124, v138, v139\n"
"v_cvt_pk_bf16_f32 v120, v128, v129\n"
"ds_read_b128 v[160:163], %3 offset:30208\n"
"v_cvt_pk_bf16_f32 v121, v122, v123\n"
"ds_write_addtid_b32 v124 offset:8512\n"
"v_pk_mul_f32 v[132:133], v[126:127], v[126:127] op_sel_hi:[0,1]\n"
"ds_write_addtid_b32 v120 offset:1648\n"
"v_pk_mul_f32 v[124:125], v[138:139], v[130:131] op_sel_hi:[0,1]\n"
"ds_write_addtid_b32 v121 offset:5872\n"
"s_waitcnt lgkmcnt(15)\n"
"v_mfma_f32_16x16x32_bf16 v[228:231], v[216:219], v[220:223], 0\n"
"v_pk_mul_f32 v[120:121], v[128:129], v[126:127] op_sel_hi:[0,1]\n"
"v_pk_mul_f32 v[134:135], v[122:123], v[126:127] op_sel_hi:[0,1]\n"
"v_pk_fma_f32 v[136:137], v[126:127], v[126:127], v[132:133] op_sel:[1,1,0] op_sel_hi:[1,0,1] neg_lo:[0,1,0]\n"
"v_pk_fma_f32 v[140:141], v[138:139], v[130:131], v[124:125] op_sel:[1,1,0] op_sel_hi:[1,0,1] neg_lo:[0,1,0]\n"
"v_pk_fma_f32 v[142:143], v[128:129], v[126:127], v[120:121] op_sel:[1,1,0] op_sel_hi:[1,0,1] neg_lo:[0,1,0]\n"
"v_pk_fma_f32 v[132:133], v[122:123], v[126:127], v[134:135] op_sel:[1,1,0] op_sel_hi:[1,0,1] neg_lo:[0,1,0]\n"
"v_cvt_pk_bf16_f32 v124, v140, v141\n"
"v_cvt_pk_bf16_f32 v120, v142, v143\n"
"buffer_load_dwordx4 v[240:243], %8, %10, %15 offen sc1\n"
"buffer_load_dwordx4 v[244:247], %8, %10, %16 offen sc1\n"
"buffer_load_dwordx4 v[248:251], %8, %10, %17 offen sc1\n"
"buffer_load_dwordx4 v[252:255], %8, %10, %18 offen sc1\n"
"v_cvt_pk_bf16_f32 v121, v132, v133\n"
"v_mfma_f32_16x16x32_bf16 v[228:231], v[208:211], v[212:215], v[228:231]\n"
"ds_write_addtid_b32 v124 offset:10624\n"
"v_pk_mul_f32 v[122:123], v[140:141], v[130:131] op_sel_hi:[0,1]\n"
"ds_write_addtid_b32 v120 offset:2176\n"
"v_pk_mul_f32 v[124:125], v[142:143], v[126:127] op_sel_hi:[0,1]\n"
"ds_write_addtid_b32 v121 offset:6400\n"
"v_pk_mul_f32 v[128:129], v[132:133], v[126:127] op_sel_hi:[0,1]\n"
"v_pk_mul_f32 v[134:135], v[138:139], v[136:137] op_sel_hi:[0,1]\n"
"v_pk_mul_f32 v[120:121], v[140:141], v[136:137] op_sel_hi:[0,1]\n"
"v_pk_fma_f32 v[144:145], v[140:141], v[130:131], v[122:123] op_sel:[1,1,0] op_sel_hi:[1,0,1] neg_lo:[0,1,0]\n"
"v_mfma_f32_16x16x32_bf16 v[228:231], v[200:203], v[204:207], v[228:231]\n"
"v_pk_fma_f32 v[146:147], v[142:143], v[126:127], v[124:125] op_sel:[1,1,0] op_sel_hi:[1,0,1] neg_lo:[0,1,0]\n"
"v_pk_fma_f32 v[122:123], v[132:133], v[126:127], v[128:129] op_sel:[1,1,0] op_sel_hi:[1,0,1] neg_lo:[0,1,0]\n"
"v_cvt_pk_bf16_f32 v148, v144, v145\n"
"v_cvt_pk_bf16_f32 v124, v146, v147\n"
"v_cvt_pk_bf16_f32 v125, v122, v123\n"
"ds_write_addtid_b32 v148 offset:12736\n"
"v_pk_fma_f32 v[128:129], v[138:139], v[136:137], v[134:135] op_sel:[1,1,0] op_sel_hi:[1,0,1] neg_lo:[0,1,0]\n"
"ds_write_addtid_b32 v124 offset:2704\n"
"v_pk_fma_f32 v[132:133], v[140:141], v[136:137], v[120:121] op_sel:[1,1,0] op_sel_hi:[1,0,1] neg_lo:[0,1,0]\n"
"s_waitcnt lgkmcnt(15)\n"
"v_mfma_f32_16x16x32_bf16 v[228:231], v[192:195], v[196:199], v[228:231]\n"
"ds_write_addtid_b32 v125 offset:6928\n"
"v_cvt_pk_bf16_f32 v120, v128, v129\n"
"v_cvt_pk_bf16_f32 v121, v132, v133\n"
"v_pk_mul_f32 v[124:125], v[136:137], v[136:137] op_sel_hi:[0,1]\n"
"ds_write_addtid_b32 v120 offset:9040\n"
"v_pk_mul_f32 v[134:135], v[144:145], v[130:131] op_sel_hi:[0,1]\n"
"ds_write_addtid_b32 v121 offset:11152\n"
"v_pk_mul_f32 v[138:139], v[146:147], v[126:127] op_sel_hi:[0,1]\n"
"v_pk_mul_f32 v[140:141], v[122:123], v[126:127] op_sel_hi:[0,1]\n"
"v_mfma_f32_16x16x32_bf16 v[228:231], v[184:187], v[188:191], v[228:231]\n"
"v_pk_mul_f32 v[120:121], v[128:129], v[136:137] op_sel_hi:[0,1]\n"
"v_pk_mul_f32 v[142:143], v[132:133], v[136:137] op_sel_hi:[0,1]\n"
"v_pk_fma_f32 v[148:149], v[136:137], v[136:137], v[124:125] op_sel:[1,1,0] op_sel_hi:[1,0,1] neg_lo:[0,1,0]\n"
"v_pk_fma_f32 v[150:151], v[144:145], v[130:131], v[134:135] op_sel:[1,1,0] op_sel_hi:[1,0,1] neg_lo:[0,1,0]\n"
"v_pk_fma_f32 v[152:153], v[146:147], v[126:127], v[138:139] op_sel:[1,1,0] op_sel_hi:[1,0,1] neg_lo:[0,1,0]\n"
"v_pk_fma_f32 v[124:125], v[122:123], v[126:127], v[140:141] op_sel:[1,1,0] op_sel_hi:[1,0,1] neg_lo:[0,1,0]\n"
"v_cvt_pk_bf16_f32 v130, v150, v151\n"
"v_cvt_pk_bf16_f32 v131, v152, v153\n"
"v_cvt_pk_bf16_f32 v122, v124, v125\n"
"s_waitcnt lgkmcnt(15)\n"
"v_mfma_f32_16x16x32_bf16 v[228:231], v[176:179], v[180:183], v[228:231]\n"
"ds_write_addtid_b32 v130 offset:13792\n"
"v_pk_fma_f32 v[134:135], v[128:129], v[136:137], v[120:121] op_sel:[1,1,0] op_sel_hi:[1,0,1] neg_lo:[0,1,0]\n"
"ds_write_addtid_b32 v131 offset:3232\n"
"v_pk_fma_f32 v[138:139], v[132:133], v[136:137], v[142:143] op_sel:[1,1,0] op_sel_hi:[1,0,1] neg_lo:[0,1,0]\n"
"ds_write_addtid_b32 v122 offset:7456\n"
"v_cvt_pk_bf16_f32 v120, v134, v135\n"
"v_cvt_pk_bf16_f32 v121, v138, v139\n"
"v_pk_mul_f32 v[122:123], v[152:153], v[126:127] op_sel_hi:[0,1]\n"
"ds_write_addtid_b32 v120 offset:9568\n"
"s_waitcnt lgkmcnt(15)\n"
"v_mfma_f32_16x16x32_bf16 v[228:231], v[168:171], v[172:175], v[228:231]\n"
"v_pk_mul_f32 v[128:129], v[124:125], v[126:127] op_sel_hi:[0,1]\n"
"ds_write_addtid_b32 v121 offset:11680\n"
"v_pk_mul_f32 v[130:131], v[134:135], v[136:137] op_sel_hi:[0,1]\n"
"v_pk_mul_f32 v[132:133], v[138:139], v[136:137] op_sel_hi:[0,1]\n"
"v_pk_mul_f32 v[120:121], v[144:145], v[148:149] op_sel_hi:[0,1]\n"
"v_pk_mul_f32 v[140:141], v[150:151], v[148:149] op_sel_hi:[0,1]\n"
"v_pk_fma_f32 v[142:143], v[152:153], v[126:127], v[122:123] op_sel:[1,1,0] op_sel_hi:[1,0,1] neg_lo:[0,1,0]\n"
"v_pk_fma_f32 v[146:147], v[124:125], v[126:127], v[128:129] op_sel:[1,1,0] op_sel_hi:[1,0,1] neg_lo:[0,1,0]\n"
"v_pk_fma_f32 v[154:155], v[134:135], v[136:137], v[130:131] op_sel:[1,1,0] op_sel_hi:[1,0,1] neg_lo:[0,1,0]\n"
"v_mfma_f32_16x16x32_bf16 v[228:231], v[160:163], v[164:167], v[228:231]\n"
"v_cvt_pk_bf16_f32 v122, v142, v143\n"
"v_cvt_pk_bf16_f32 v123, v146, v147\n"
"v_cvt_pk_bf16_f32 v124, v154, v155\n"
"ds_write_addtid_b32 v122 offset:3760\n"
"v_pk_fma_f32 v[126:127], v[138:139], v[136:137], v[132:133] op_sel:[1,1,0] op_sel_hi:[1,0,1] neg_lo:[0,1,0]\n"
"ds_write_addtid_b32 v123 offset:7984\n"
"v_pk_fma_f32 v[128:129], v[144:145], v[148:149], v[120:121] op_sel:[1,1,0] op_sel_hi:[1,0,1] neg_lo:[0,1,0]\n"
"ds_write_addtid_b32 v124 offset:10096\n"
"v_cvt_pk_bf16_f32 v122, v126, v127\n"
"v_cvt_pk_bf16_f32 v120, v128, v129\n"
"v_pk_fma_f32 v[124:125], v[150:151], v[148:149], v[140:141] op_sel:[1,1,0] op_sel_hi:[1,0,1] neg_lo:[0,1,0]\n"
"ds_write_addtid_b32 v122 offset:12208\n"
"ds_write_addtid_b32 v120 offset:13264\n"
"v_cvt_pk_bf16_f32 v121, v124, v125\n"
"ds_write_addtid_b32 v121 offset:14320\n"
"s_branch .Lkg_fin\n"
".Lkg_zero:\n"
"v_mov_b32 v120, 0\n"
"v_mov_b32 v121, 0\n"
"v_mov_b32 v122, %13\n"
"s_add_u32 m0, %13, 135696\n"
"v_add_u32 v122, 90080, v122\n"
"ds_write_b64 v122, v[120:121]\n"
"ds_write_addtid_b32 v120 offset:8\n"
"ds_write_addtid_b32 v120 offset:264\n"
"ds_write_addtid_b32 v120 offset:4640\n"
"ds_write_addtid_b32 v120 offset:4896\n"
"ds_write_addtid_b32 v120 offset:9288\n"
"ds_write_addtid_b32 v120 offset:9544\n"
"ds_write_addtid_b32 v120 offset:13920\n"
"ds_write_addtid_b32 v120 offset:14176\n"
"ds_write_addtid_b32 v120 offset:18568\n"
"ds_write_addtid_b32 v120 offset:18824\n"
"ds_write_addtid_b32 v120 offset:23200\n"
"ds_write_addtid_b32 v120 offset:23456\n"
"s_mov_b64 exec, 0\n"
"s_branch .Lwk_cv1\n"
".p2align 12\n"
".skip 176\n"
".Lkg_b:\n"
"s_cbranch_execz .Lwk_back\n"
"s_add_u32 m0, %13, 0\n"
"buffer_load_dword a0, %9, %11, %12 offen\n"
"v_mul_f32 v120, 0x40800000, v53\n"
"v_mul_f32 v123, 0x40800000, v50\n"
"v_mul_f32 v121, 0x43000000, v53\n"
"v_fract_f32 v124, v120\n"
"v_exp_f32 v122, v123\n"
"v_mul_f32 v125, 0x44400000, v53\n"
"v_cos_f32 v126, v124\n"
"v_sin_f32 v127, v124\n"
"v_mul_f32 v129, 0x43000000, v50\n"
"v_fract_f32 v120, v121\n"
"v_pk_mul_f32 v[130:131], v[122:123], v[126:127] op_sel_hi:[0,1]\n"
"v_mul_f32 v133, 0x44400000, v50\n"
"v_fract_f32 v121, v125\n"
"v_pk_mul_f32 v[122:123], v[130:131], v[130:131] op_sel_hi:[0,1]\n"
"v_exp_f32 v128, v129\n"
"v_cos_f32 v124, v120\n"
"v_pk_fma_f32 v[126:127], v[130:131], v[130:131], v[122:123] op_sel:[1,1,0] op_sel_hi:[1,0,1] neg_lo:[0,1,0] neg_hi:[0,1,1]\n"
"v_sin_f32 v125, v120\n"
"v_exp_f32 v132, v133\n"
"v_cvt_pk_bf16_f32 v122, v126, v127\n"
"v_pk_mul_f32 v[134:135], v[126:127], v[126:127] op_sel_hi:[0,1]\n"
"v_cos_f32 v136, v121\n"
"ds_write_addtid_b32 v122 offset:22240\n"
"v_pk_fma_f32 v[138:139], v[126:127], v[126:127], v[134:135] op_sel:[1,1,0] op_sel_hi:[1,0,1] neg_lo:[0,1,0]\n"
"v_sin_f32 v137, v121\n"
"v_pk_mul_f32 v[122:123], v[128:129], v[124:125] op_sel_hi:[0,1]\n"
"v_cvt_pk_bf16_f32 v120, v138, v139\n"
"v_pk_mul_f32 v[134:135], v[126:127], v[138:139] op_sel_hi:[0,1]\n"
"v_pk_mul_f32 v[124:125], v[132:133], v[136:137] op_sel_hi:[0,1]\n"
"ds_write_addtid_b32 v120 offset:22768\n"
"v_pk_fma_f32 v[128:129], v[126:127], v[138:139], v[134:135] op_sel:[1,1,0] op_sel_hi:[1,0,1] neg_lo:[0,1,0]\n"
"v_pk_mul_f32 v[132:133], v[124:125], v[44:45] op_sel_hi:[0,1]\n"
"v_pk_mul_f32 v[120:121], v[122:123], v[122:123] op_sel_hi:[0,1]\n"
"v_cvt_pk_bf16_f32 v126, v128, v129\n"
"v_pk_mul_f32 v[134:135], v[128:129], v[138:139] op_sel_hi:[0,1]\n"
"v_pk_mul_f32 v[136:137], v[138:139], v[138:139] op_sel_hi:[0,1]\n"
"ds_write_addtid_b32 v126 offset:23296\n"
"v_pk_fma_f32 v[140:141], v[124:125], v[44:45], v[132:133] op_sel:[1,1,0] op_sel_hi:[1,0,1] neg_lo:[0,1,0]\n"
"v_pk_fma_f32 v[142:143], v[122:123], v[122:123], v[120:121] op_sel:[1,1,0] op_sel_hi:[1,0,1] neg_lo:[0,1,0]\n"
"v_pk_fma_f32 v[126:127], v[128:129], v[138:139], v[134:135] op_sel:[1,1,0] op_sel_hi:[1,0,1] neg_lo:[0,1,0]\n"
"v_cvt_pk_bf16_f32 v124, v140, v141\n"
"v_pk_fma_f32 v[120:121], v[138:139], v[138:139], v[136:137] op_sel:[1,1,0] op_sel_hi:[1,0,1] neg_lo:[0,1,0]\n"
"v_cvt_pk_bf16_f32 v125, v126, v127\n"
"ds_write_addtid_b32 v124 offset:14848\n"
"v_cvt_pk_bf16_f32 v128, v120, v121\n"
"ds_write_addtid_b32 v125 offset:24352\n"
"v_pk_mul_f32 v[132:133], v[140:141], v[122:123] op_sel_hi:[0,1]\n"
"ds_write_addtid_b32 v128 offset:23824\n"
"v_pk_mul_f32 v[124:125], v[126:127], v[138:139] op_sel_hi:[0,1]\n"
"v_pk_mul_f32 v[134:135], v[120:121], v[138:139] op_sel_hi:[0,1]\n"
"v_pk_mul_f32 v[128:129], v[140:141], v[142:143] op_sel_hi:[0,1]\n"
"v_pk_fma_f32 v[136:137], v[140:141], v[122:123], v[132:133] op_sel:[1,1,0] op_sel_hi:[1,0,1] neg_lo:[0,1,0]\n"
"v_pk_fma_f32 v[144:145], v[126:127], v[138:139], v[124:125] op_sel:[1,1,0] op_sel_hi:[1,0,1] neg_lo:[0,1,0]\n"
"v_pk_fma_f32 v[146:147], v[120:121], v[138:139], v[134:135] op_sel:[1,1,0] op_sel_hi:[1,0,1] neg_lo:[0,1,0]\n"
"v_cvt_pk_bf16_f32 v122, v136, v137\n"
"v_cvt_pk_bf16_f32 v123, v144, v145\n"
"v_cvt_pk_bf16_f32 v120, v146, v147\n"
"ds_write_addtid_b32 v122 offset:15904\n"
"v_pk_fma_f32 v[124:125], v[140:141], v[142:143], v[128:129] op_sel:[1,1,0] op_sel_hi:[1,0,1] neg_lo:[0,1,0]\n"
"ds_write_addtid_b32 v123 offset:25408\n"
"v_pk_mul_f32 v[126:127], v[144:145], v[138:139] op_sel_hi:[0,1]\n"
"ds_write_addtid_b32 v120 offset:24880\n"
"v_cvt_pk_bf16_f32 v121, v124, v125\n"
"v_pk_mul_f32 v[122:123], v[146:147], v[138:139] op_sel_hi:[0,1]\n"
"v_pk_mul_f32 v[128:129], v[124:125], v[142:143] op_sel_hi:[0,1]\n"
"ds_write_addtid_b32 v121 offset:16960\n"
"v_pk_mul_f32 v[132:133], v[136:137], v[142:143] op_sel_hi:[0,1]\n"
"v_pk_fma_f32 v[134:135], v[144:145], v[138:139], v[126:127] op_sel:[1,1,0] op_sel_hi:[1,0,1] neg_lo:[0,1,0]\n"
"v_pk_fma_f32 v[120:121], v[146:147], v[138:139], v[122:123] op_sel:[1,1,0] op_sel_hi:[1,0,1] neg_lo:[0,1,0]\n"
"v_pk_fma_f32 v[148:149], v[124:125], v[142:143], v[128:129] op_sel:[1,1,0] op_sel_hi:[1,0,1] neg_lo:[0,1,0]\n"
"v_cvt_pk_bf16_f32 v126, v134, v135\n"
"v_cvt_pk_bf16_f32 v122, v120, v121\n"
"v_cvt_pk_bf16_f32 v123, v148, v149\n"
"ds_write_addtid_b32 v126 offset:26464\n"
"v_pk_fma_f32 v[124:125], v[136:137], v[142:143], v[132:133] op_sel:[1,1,0] op_sel_hi:[1,0,1] neg_lo:[0,1,0]\n"
"ds_write_addtid_b32 v122 offset:25936\n"
"v_pk_mul_f32 v[126:127], v[134:135], v[138:139] op_sel_hi:[0,1]\n"
"ds_write_addtid_b32 v123 offset:18016\n"
"v_cvt_pk_bf16_f32 v122, v124, v125\n"
"v_pk_mul_f32 v[128:129], v[120:121], v[138:139] op_sel_hi:[0,1]\n"
"v_pk_mul_f32 v[132:133], v[148:149], v[142:143] op_sel_hi:[0,1]\n"
"ds_write_addtid_b32 v122 offset:17488\n"
"v_pk_mul_f32 v[144:145], v[124:125], v[142:143] op_sel_hi:[0,1]\n"
"v_pk_fma_f32 v[146:147], v[134:135], v[138:139], v[126:127] op_sel:[1,1,0] op_sel_hi:[1,0,1] neg_lo:[0,1,0]\n"
"v_pk_fma_f32 v[122:123], v[120:121], v[138:139], v[128:129] op_sel:[1,1,0] op_sel_hi:[1,0,1] neg_lo:[0,1,0]\n"
"v_pk_fma_f32 v[150:151], v[148:149], v[142:143], v[132:133] op_sel:[1,1,0] op_sel_hi:[1,0,1] neg_lo:[0,1,0]\n"
"v_cvt_pk_bf16_f32 v126, v146, v147\n"
"v_cvt_pk_bf16_f32 v120, v122, v123\n"
"v_cvt_pk_bf16_f32 v121, v150, v151\n"
"ds_write_addtid_b32 v126 offset:27520\n"
"v_pk_fma_f32 v[128:129], v[124:125], v[142:143], v[144:145] op_sel:[1,1,0] op_sel_hi:[1,0,1] neg_lo:[0,1,0]\n"
"ds_write_addtid_b32 v120 offset:26992\n"
"v_pk_mul_f32 v[126:127], v[146:147], v[138:139] op_sel_hi:[0,1]\n"
"ds_write_addtid_b32 v121 offset:19072\n"
"v_cvt_pk_bf16_f32 v120, v128, v129\n"
"v_pk_mul_f32 v[124:125], v[122:123], v[138:139] op_sel_hi:[0,1]\n"
"v_pk_mul_f32 v[132:133], v[150:151], v[142:143] op_sel_hi:[0,1]\n"
"ds_write_addtid_b32 v120 offset:18544\n"
"v_pk_mul_f32 v[134:135], v[128:129], v[142:143] op_sel_hi:[0,1]\n"
"v_pk_fma_f32 v[144:145], v[146:147], v[138:139], v[126:127] op_sel:[1,1,0] op_sel_hi:[1,0,1] neg_lo:[0,1,0]\n"
"v_pk_fma_f32 v[120:121], v[122:123], v[138:139], v[124:125] op_sel:[1,1,0] op_sel_hi:[1,0,1] neg_lo:[0,1,0]\n"
"v_pk_fma_f32 v[148:149], v[150:151], v[142:143], v[132:133] op_sel:[1,1,0] op_sel_hi:[1,0,1] neg_lo:[0,1,0]\n"
"v_cvt_pk_bf16_f32 v126, v144, v145\n"
"v_cvt_pk_bf16_f32 v122, v120, v121\n"
"v_cvt_pk_bf16_f32 v123, v148, v149\n"
"ds_write_addtid_b32 v126 offset:28576\n"
"v_pk_fma_f32 v[124:125], v[128:129], v[142:143], v[134:135] op_sel:[1,1,0] op_sel_hi:[1,0,1] neg_lo:[0,1,0]\n"
"ds_write_addtid_b32 v122 offset:28048\n"
"v_pk_mul_f32 v[126:127], v[140:141], v[130:131] op_sel_hi:[0,1]\n"
"ds_write_addtid_b32 v123 offset:20128\n"
"v_cvt_pk_bf16_f32 v122, v124, v125\n"
"v_pk_mul_f32 v[128:129], v[136:137], v[130:131] op_sel_hi:[0,1]\n"
"v_pk_mul_f32 v[132:133], v[144:145], v[138:139] op_sel_hi:[0,1]\n"
"ds_write_addtid_b32 v122 offset:19600\n"
"v_pk_mul_f32 v[134:135], v[120:121], v[138:139] op_sel_hi:[0,1]\n"
"v_pk_mul_f32 v[146:147], v[148:149], v[142:143] op_sel_hi:[0,1]\n"
"v_pk_mul_f32 v[122:123], v[124:125], v[142:143] op_sel_hi:[0,1]\n"
"v_pk_fma_f32 v[150:151], v[140:141], v[130:131], v[126:127] op_sel:[1,1,0] op_sel_hi:[1,0,1] neg_lo:[0,1,0]\n"
"v_pk_fma_f32 v[152:153], v[136:137], v[130:131], v[128:129] op_sel:[1,1,0] op_sel_hi:[1,0,1] neg_lo:[0,1,0]\n"
"v_pk_fma_f32 v[154:155], v[144:145], v[138:139], v[132:133] op_sel:[1,1,0] op_sel_hi:[1,0,1] neg_lo:[0,1,0]\n"
"v_cvt_pk_bf16_f32 v126, v150, v151\n"
"v_cvt_pk_bf16_f32 v127, v152, v153\n"
"v_cvt_pk_bf16_f32 v128, v154, v155\n"
"ds_write_addtid_b32 v126 offset:15376\n"
"v_pk_fma_f32 v[130:131], v[120:121], v[138:139], v[134:135] op_sel:[1,1,0] op_sel_hi:[1,0,1] neg_lo:[0,1,0]\n"
"ds_write_addtid_b32 v127 offset:16432\n"
"v_pk_fma_f32 v[132:133], v[148:149], v[142:143], v[146:147] op_sel:[1,1,0] op_sel_hi:[1,0,1] neg_lo:[0,1,0]\n"
"ds_write_addtid_b32 v128 offset:29632\n"
"v_cvt_pk_bf16_f32 v120, v130, v131\n"
"v_cvt_pk_bf16_f32 v121, v132, v133\n"
"v_pk_fma_f32 v[126:127], v[124:125], v[142:143], v[122:123] op_sel:[1,1,0] op_sel_hi:[1,0,1] neg_lo:[0,1,0]\n"
"ds_write_addtid_b32 v120 offset:29104\n"
"ds_write_addtid_b32 v121 offset:21184\n"
"v_cvt_pk_bf16_f32 v122, v126, v127\n"
"v_mul_f32 v120, 0x40800000, v54\n"
"v_mul_f32 v125, 0x40800000, v51\n"
"ds_write_addtid_b32 v122 offset:20656\n"
"v_fract_f32 v121, v120\n"
"v_exp_f32 v124, v125\n"
"v_mul_f32 v122, 0x43000000, v54\n"
"v_cos_f32 v126, v121\n"
"v_sin_f32 v127, v121\n"
"v_mul_f32 v120, 0x44400000, v54\n"
"v_mul_f32 v129, 0x43000000, v51\n"
"v_pk_mul_f32 v[130:131], v[124:125], v[126:127] op_sel_hi:[0,1]\n"
"v_fract_f32 v121, v122\n"
"v_mul_f32 v133, 0x44400000, v51\n"
"v_pk_mul_f32 v[124:125], v[130:131], v[130:131] op_sel_hi:[0,1]\n"
"v_fract_f32 v122, v120\n"
"v_exp_f32 v128, v129\n"
"v_pk_fma_f32 v[126:127], v[130:131], v[130:131], v[124:125] op_sel:[1,1,0] op_sel_hi:[1,0,1] neg_lo:[0,1,0] neg_hi:[0,1,1]\n"
"s_waitcnt lgkmcnt(0)\n"
"s_barrier\n"
"ds_read_b128 v[220:223], %2 offset:0\n"
"v_cos_f32 v124, v121\n"
"v_sin_f32 v125, v121\n"
"ds_read_b128 v[216:219], %3 offset:0\n"
"v_cvt_pk_bf16_f32 v120, v126, v127\n"
"v_pk_mul_f32 v[134:135], v[126:127], v[126:127] op_sel_hi:[0,1]\n"
"s_add_u32 m0, %13, 30096\n"
"ds_read_b128 v[212:215], %2 offset:16\n"
"v_exp_f32 v132, v133\n"
"ds_write_addtid_b32 v120 offset:22240\n"
"ds_read_b128 v[208:211], %3 offset:16\n"
"v_pk_fma_f32 v[136:137], v[126:127], v[126:127], v[134:135] op_sel:[1,1,0] op_sel_hi:[1,0,1] neg_lo:[0,1,0]\n"
"v_cos_f32 v120, v122\n"
"ds_read_b128 v[204:207], %2 offset:32\n"
"v_sin_f32 v121, v122\n"
"v_cvt_pk_bf16_f32 v123, v136, v137\n"
"ds_read_b128 v[200:203], %3 offset:32\n"
"v_pk_mul_f32 v[134:135], v[126:127], v[136:137] op_sel_hi:[0,1]\n"
"v_pk_mul_f32 v[138:139], v[128:129], v[124:125] op_sel_hi:[0,1]\n"
"ds_read_b128 v[196:199], %2 offset:48\n"
"ds_write_addtid_b32 v123 offset:22768\n"
"v_pk_mul_f32 v[124:125], v[132:133], v[120:121] op_sel_hi:[0,1]\n"
"ds_read_b128 v[192:195], %3 offset:48\n"
"v_pk_fma_f32 v[122:123], v[126:127], v[136:137], v[134:135] op_sel:[1,1,0] op_sel_hi:[1,0,1] neg_lo:[0,1,0]\n"
"v_pk_mul_f32 v[120:121], v[138:139], v[138:139] op_sel_hi:[0,1]\n"
"ds_read_b128 v[188:191], %2 offset:64\n"
"v_pk_mul_f32 v[126:127], v[124:125], v[46:47] op_sel_hi:[0,1]\n"
"v_cvt_pk_bf16_f32 v128, v122, v123\n"
"ds_read_b128 v[184:187], %3 offset:64\n"
"v_pk_mul_f32 v[132:133], v[122:123], v[136:137] op_sel_hi:[0,1]\n"
"v_pk_mul_f32 v[134:135], v[136:137], v[136:137] op_sel_hi:[0,1]\n"
"ds_read_b128 v[180:183], %2 offset:80\n"
"ds_write_addtid_b32 v128 offset:23296\n"
"v_pk_fma_f32 v[140:141], v[124:125], v[46:47], v[126:127] op_sel:[1,1,0] op_sel_hi:[1,0,1] neg_lo:[0,1,0]\n"
"ds_read_b128 v[176:179], %3 offset:80\n"
"v_pk_fma_f32 v[128:129], v[138:139], v[138:139], v[120:121] op_sel:[1,1,0] op_sel_hi:[1,0,1] neg_lo:[0,1,0]\n"
"v_pk_fma_f32 v[124:125], v[122:123], v[136:137], v[132:133] op_sel:[1,1,0] op_sel_hi:[1,0,1] neg_lo:[0,1,0]\n"
"ds_read_b128 v[172:175], %2 offset:96\n"
"v_cvt_pk_bf16_f32 v120, v140, v141\n"
"v_pk_fma_f32 v[122:123], v[136:137], v[136:137], v[134:135] op_sel:[1,1,0] op_sel_hi:[1,0,1] neg_lo:[0,1,0]\n"
"ds_read_b128 v[168:171], %3 offset:96\n"
"v_cvt_pk_bf16_f32 v121, v124, v125\n"
"ds_write_addtid_b32 v120 offset:14848\n"
"ds_read_b128 v[164:167], %2 offset:112\n"
"v_cvt_pk_bf16_f32 v126, v122, v123\n"
"ds_write_addtid_b32 v121 offset:24352\n"
"ds_read_b128 v[160:163], %3 offset:112\n"
"v_pk_mul_f32 v[132:133], v[140:141], v[138:139] op_sel_hi:[0,1]\n"
"ds_write_addtid_b32 v126 offset:23824\n"
"v_pk_mul_f32 v[120:121], v[124:125], v[136:137] op_sel_hi:[0,1]\n"
"v_pk_mul_f32 v[134:135], v[122:123], v[136:137] op_sel_hi:[0,1]\n"
"v_pk_mul_f32 v[126:127], v[140:141], v[128:129] op_sel_hi:[0,1]\n"
"v_pk_fma_f32 v[142:143], v[140:141], v[138:139], v[132:133] op_sel:[1,1,0] op_sel_hi:[1,0,1] neg_lo:[0,1,0]\n"
"s_waitcnt lgkmcnt(15)\n"
"v_mfma_f32_16x16x32_bf16 v[224:227], v[216:219], v[220:223], 0\n"
"v_pk_fma_f32 v[132:133], v[124:125], v[136:137], v[120:121] op_sel:[1,1,0] op_sel_hi:[1,0,1] neg_lo:[0,1,0]\n"
"v_pk_fma_f32 v[138:139], v[122:123], v[136:137], v[134:135] op_sel:[1,1,0] op_sel_hi:[1,0,1] neg_lo:[0,1,0]\n"
"v_cvt_pk_bf16_f32 v144, v142, v143\n"
"v_cvt_pk_bf16_f32 v120, v132, v133\n"
"v_cvt_pk_bf16_f32 v121, v138, v139\n"
"ds_write_addtid_b32 v144 offset:15904\n"
"v_pk_fma_f32 v[122:123], v[140:141], v[128:129], v[126:127] op_sel:[1,1,0] op_sel_hi:[1,0,1] neg_lo:[0,1,0]\n"
"ds_write_addtid_b32 v120 offset:25408\n"
"v_pk_mul_f32 v[124:125], v[132:133], v[136:137] op_sel_hi:[0,1]\n"
"v_mfma_f32_16x16x32_bf16 v[224:227], v[208:211], v[212:215], v[224:227]\n"
"ds_write_addtid_b32 v121 offset:24880\n"
"v_cvt_pk_bf16_f32 v120, v122, v123\n"
"v_pk_mul_f32 v[126:127], v[138:139], v[136:137] op_sel_hi:[0,1]\n"
"v_pk_mul_f32 v[134:135], v[122:123], v[128:129] op_sel_hi:[0,1]\n"
"ds_write_addtid_b32 v120 offset:16960\n"
"v_pk_mul_f32 v[144:145], v[142:143], v[128:129] op_sel_hi:[0,1]\n"
"v_pk_fma_f32 v[146:147], v[132:133], v[136:137], v[124:125] op_sel:[1,1,0] op_sel_hi:[1,0,1] neg_lo:[0,1,0]\n"
"v_pk_fma_f32 v[120:121], v[138:139], v[136:137], v[126:127] op_sel:[1,1,0] op_sel_hi:[1,0,1] neg_lo:[0,1,0]\n"
"v_pk_fma_f32 v[148:149], v[122:123], v[128:129], v[134:135] op_sel:[1,1,0] op_sel_hi:[1,0,1] neg_lo:[0,1,0]\n"
"v_mfma_f32_16x16x32_bf16 v[224:227], v[200:203], v[204:207], v[224:227]\n"
"v_cvt_pk_bf16_f32 v124, v146, v147\n"
"v_cvt_pk_bf16_f32 v122, v120, v121\n"
"v_cvt_pk_bf16_f32 v123, v148, v149\n"
"ds_write_addtid_b32 v124 offset:26464\n"
"v_pk_fma_f32 v[126:127], v[142:143], v[128:129], v[144:145] op_sel:[1,1,0] op_sel_hi:[1,0,1] neg_lo:[0,1,0]\n"
"ds_write_addtid_b32 v122 offset:25936\n"
"v_pk_mul_f32 v[124:125], v[146:147], v[136:137] op_sel_hi:[0,1]\n"
"ds_write_addtid_b32 v123 offset:18016\n"
"v_cvt_pk_bf16_f32 v122, v126, v127\n"
"s_waitcnt lgkmcnt(15)\n"
"v_mfma_f32_16x16x32_bf16 v[224:227], v[192:195], v[196:199], v[224:227]\n"
"v_pk_mul_f32 v[132:133], v[120:121], v[136:137] op_sel_hi:[0,1]\n"
"v_pk_mul_f32 v[134:135], v[148:149], v[128:129] op_sel_hi:[0,1]\n"
"ds_write_addtid_b32 v122 offset:17488\n"
"v_pk_mul_f32 v[138:139], v[126:127], v[128:129] op_sel_hi:[0,1]\n"
"v_pk_fma_f32 v[144:145], v[146:147], v[136:137], v[124:125] op_sel:[1,1,0] op_sel_hi:[1,0,1] neg_lo:[0,1,0]\n"
"v_pk_fma_f32 v[122:123], v[120:121], v[136:137], v[132:133] op_sel:[1,1,0] op_sel_hi:[1,0,1] neg_lo:[0,1,0]\n"
"v_pk_fma_f32 v[150:151], v[148:149], v[128:129], v[134:135] op_sel:[1,1,0] op_sel_hi:[1,0,1] neg_lo:[0,1,0]\n"
"v_cvt_pk_bf16_f32 v124, v144, v145\n"
"v_cvt_pk_bf16_f32 v120, v122, v123\n"
"v_mfma_f32_16x16x32_bf16 v[224:227], v[184:187], v[188:191], v[224:227]\n"
"v_cvt_pk_bf16_f32 v121, v150, v151\n"
"ds_write_addtid_b32 v124 offset:27520\n"
"v_pk_fma_f32 v[132:133], v[126:127], v[128:129], v[138:139] op_sel:[1,1,0] op_sel_hi:[1,0,1] neg_lo:[0,1,0]\n"
"ds_write_addtid_b32 v120 offset:26992\n"
"v_pk_mul_f32 v[124:125], v[144:145], v[136:137] op_sel_hi:[0,1]\n"
"ds_write_addtid_b32 v121 offset:19072\n"
"v_cvt_pk_bf16_f32 v120, v132, v133\n"
"v_pk_mul_f32 v[126:127], v[122:123], v[136:137] op_sel_hi:[0,1]\n"
"v_pk_mul_f32 v[134:135], v[150:151], v[128:129] op_sel_hi:[0,1]\n"
"s_waitcnt lgkmcnt(15)\n"
"v_mfma_f32_16x16x32_bf16 v[224:227], v[176:179], v[180:183], v[224:227]\n"
"ds_write_addtid_b32 v120 offset:18544\n"
"v_pk_mul_f32 v[138:139], v[132:133], v[128:129] op_sel_hi:[0,1]\n"
"v_pk_fma_f32 v[146:147], v[144:145], v[136:137], v[124:125] op_sel:[1,1,0] op_sel_hi:[1,0,1] neg_lo:[0,1,0]\n"
"v_pk_fma_f32 v[120:121], v[122:123], v[136:137], v[126:127] op_sel:[1,1,0] op_sel_hi:[1,0,1] neg_lo:[0,1,0]\n"
"v_pk_fma_f32 v[148:149], v[150:151], v[128:129], v[134:135] op_sel:[1,1,0] op_sel_hi:[1,0,1] neg_lo:[0,1,0]\n"
"v_cvt_pk_bf16_f32 v124, v146, v147\n"
"v_cvt_pk_bf16_f32 v122, v120, v121\n"
"v_cvt_pk_bf16_f32 v123, v148, v149\n"
"ds_write_addtid_b32 v124 offset:28576\n"
"v_mfma_f32_16x16x32_bf16 v[224:227], v[168:171], v[172:175], v[224:227]\n"
"v_pk_fma_f32 v[126:127], v[132:133], v[128:129], v[138:139] op_sel:[1,1,0] op_sel_hi:[1,0,1] neg_lo:[0,1,0]\n"
"ds_write_addtid_b32 v122 offset:28048\n"
"v_pk_mul_f32 v[124:125], v[140:141], v[130:131] op_sel_hi:[0,1]\n"
"ds_write_addtid_b32 v123 offset:20128\n"
"v_cvt_pk_bf16_f32 v122, v126, v127\n"
"v_pk_mul_f32 v[132:133], v[142:143], v[130:131] op_sel_hi:[0,1]\n"
"v_pk_mul_f32 v[134:135], v[146:147], v[136:137] op_sel_hi:[0,1]\n"
"ds_write_addtid_b32 v122 offset:19600\n"
"v_pk_mul_f32 v[138:139], v[120:121], v[136:137] op_sel_hi:[0,1]\n"
"s_waitcnt lgkmcnt(15)\n"
"v_mfma_f32_16x16x32_bf16 v[224:227], v[160:163], v[164:167], v[224:227]\n"
"v_pk_mul_f32 v[122:123], v[148:149], v[128:129] op_sel_hi:[0,1]\n"
"v_pk_mul_f32 v[144:145], v[126:127], v[128:129] op_sel_hi:[0,1]\n"
"v_pk_fma_f32 v[150:151], v[140:141], v[130:131], v[124:125] op_sel:[1,1,0] op_sel_hi:[1,0,1] neg_lo:[0,1,0]\n"
"v_pk_fma_f32 v[152:153], v[142:143], v[130:131], v[132:133] op_sel:[1,1,0] op_sel_hi:[1,0,1] neg_lo:[0,1,0]\n"
"v_pk_fma_f32 v[154:155], v[146:147], v[136:137], v[134:135] op_sel:[1,1,0] op_sel_hi:[1,0,1] neg_lo:[0,1,0]\n"
"v_cvt_pk_bf16_f32 v124, v150, v151\n"
"v_cvt_pk_bf16_f32 v125, v152, v153\n"
"v_cvt_pk_bf16_f32 v130, v154, v155\n"
"ds_write_addtid_b32 v124 offset:15376\n"
"v_pk_fma_f32 v[132:133], v[120:121], v[136:137], v[138:139] op_sel:[1,1,0] op_sel_hi:[1,0,1] neg_lo:[0,1,0]\n"
"ds_write_addtid_b32 v125 offset:16432\n"
"v_pk_fma_f32 v[134:135], v[148:149], v[128:129], v[122:123] op_sel:[1,1,0] op_sel_hi:[1,0,1] neg_lo:[0,1,0]\n"
"ds_write_addtid_b32 v130 offset:29632\n"
"v_cvt_pk_bf16_f32 v120, v132, v133\n"
"v_cvt_pk_bf16_f32 v121, v134, v135\n"
"v_pk_fma_f32 v[122:123], v[126:127], v[128:129], v[144:145] op_sel:[1,1,0] op_sel_hi:[1,0,1] neg_lo:[0,1,0]\n"
"ds_write_addtid_b32 v120 offset:29104\n"
"ds_write_addtid_b32 v121 offset:21184\n"
"v_cvt_pk_bf16_f32 v124, v122, v123\n"
"v_mul_f32 v120, 0x40800000, v55\n"
"v_mul_f32 v127, 0x40800000, v52\n"
"ds_write_addtid_b32 v124 offset:20656\n"
"v_fract_f32 v121, v120\n"
"v_exp_f32 v126, v127\n"
"v_mul_f32 v122, 0x43000000, v55\n"
"v_cos_f32 v124, v121\n"
"v_sin_f32 v125, v121\n"
"v_mul_f32 v120, 0x44400000, v55\n"
"v_mul_f32 v129, 0x43000000, v52\n"
"v_pk_mul_f32 v[130:131], v[126:127], v[124:125] op_sel_hi:[0,1]\n"
"v_fract_f32 v121, v122\n"
"v_mul_f32 v133, 0x44400000, v52\n"
"v_pk_mul_f32 v[124:125], v[130:131], v[130:131] op_sel_hi:[0,1]\n"
"v_fract_f32 v122, v120\n"
"v_exp_f32 v128, v129\n"
"v_pk_fma_f32 v[126:127], v[130:131], v[130:131], v[124:125] op_sel:[1,1,0] op_sel_hi:[1,0,1] neg_lo:[0,1,0] neg_hi:[0,1,1]\n"
"s_waitcnt lgkmcnt(0)\n"
"s_barrier\n"
"ds_read_b128 v[220:223], %2 offset:30096\n"
"v_cos_f32 v124, v121\n"
"v_sin_f32 v125, v121\n"
"ds_read_b128 v[216:219], %3 offset:30096\n"
"v_cvt_pk_bf16_f32 v120, v126, v127\n"
"v_pk_mul_f32 v[134:135], v[126:127], v[126:127] op_sel_hi:[0,1]\n"
"s_add_u32 m0, %13, 60192\n"
"ds_read_b128 v[212:215], %2 offset:30112\n"
"v_exp_f32 v132, v133\n"
"ds_write_addtid_b32 v120 offset:22240\n"
"ds_read_b128 v[208:211], %3 offset:30112\n"
"v_pk_fma_f32 v[136:137], v[126:127], v[126:127], v[134:135] op_sel:[1,1,0] op_sel_hi:[1,0,1] neg_lo:[0,1,0]\n"
"v_cos_f32 v120, v122\n"
"ds_read_b128 v[204:207], %2 offset:30128\n"
"v_sin_f32 v121, v122\n"
"v_cvt_pk_bf16_f32 v123, v136, v137\n"
"ds_read_b128 v[200:203], %3 offset:30128\n"
"v_pk_mul_f32 v[134:135], v[126:127], v[136:137] op_sel_hi:[0,1]\n"
"v_pk_mul_f32 v[138:139], v[128:129], v[124:125] op_sel_hi:[0,1]\n"
"ds_read_b128 v[196:199], %2 offset:30144\n"
"ds_write_addtid_b32 v123 offset:22768\n"
"v_pk_mul_f32 v[124:125], v[132:133], v[120:121] op_sel_hi:[0,1]\n"
"ds_read_b128 v[192:195], %3 offset:30144\n"
"v_pk_fma_f32 v[122:123], v[126:127], v[136:137], v[134:135] op_sel:[1,1,0] op_sel_hi:[1,0,1] neg_lo:[0,1,0]\n"
"v_pk_mul_f32 v[120:121], v[138:139], v[138:139] op_sel_hi:[0,1]\n"
"ds_read_b128 v[188:191], %2 offset:30160\n"
"v_pk_mul_f32 v[126:127], v[124:125], v[48:49] op_sel_hi:[0,1]\n"
"v_cvt_pk_bf16_f32 v128, v122, v123\n"
"ds_read_b128 v[184:187], %3 offset:30160\n"
"v_pk_mul_f32 v[132:133], v[122:123], v[136:137] op_sel_hi:[0,1]\n"
"v_pk_mul_f32 v[134:135], v[136:137], v[136:137] op_sel_hi:[0,1]\n"
"ds_read_b128 v[180:183], %2 offset:30176\n"
"ds_write_addtid_b32 v128 offset:23296\n"
"v_pk_fma_f32 v[140:141], v[124:125], v[48:49], v[126:127] op_sel:[1,1,0] op_sel_hi:[1,0,1] neg_lo:[0,1,0]\n"
"ds_read_b128 v[176:179], %3 offset:30176\n"
"v_pk_fma_f32 v[128:129], v[138:139], v[138:139], v[120:121] op_sel:[1,1,0] op_sel_hi:[1,0,1] neg_lo:[0,1,0]\n"
"v_pk_fma_f32 v[124:125], v[122:123], v[136:137], v[132:133] op_sel:[1,1,0] op_sel_hi:[1,0,1] neg_lo:[0,1,0]\n"
"ds_read_b128 v[172:175], %2 offset:30192\n"
"v_cvt_pk_bf16_f32 v120, v140, v141\n"
"v_pk_fma_f32 v[122:123], v[136:137], v[136:137], v[134:135] op_sel:[1,1,0] op_sel_hi:[1,0,1] neg_lo:[0,1,0]\n"
"ds_read_b128 v[168:171], %3 offset:30192\n"
"v_cvt_pk_bf16_f32 v121, v124, v125\n"
"ds_write_addtid_b32 v120 offset:14848\n"
"ds_read_b128 v[164:167], %2 offset:30208\n"
"v_cvt_pk_bf16_f32 v126, v122, v123\n"
"ds_write_addtid_b32 v121 offset:24352\n"
"ds_read_b128 v[160:163], %3 offset:30208\n"
"v_pk_mul_f32 v[132:133], v[140:141], v[138:139] op_sel_hi:[0,1]\n"
"ds_write_addtid_b32 v126 offset:23824\n"
"v_pk_mul_f32 v[120:121], v[124:125], v[136:137] op_sel_hi:[0,1]\n"
"v_pk_mul_f32 v[134:135], v[122:123], v[136:137] op_sel_hi:[0,1]\n"
"v_pk_mul_f32 v[126:127], v[140:141], v[128:129] op_sel_hi:[0,1]\n"
"v_pk_fma_f32 v[142:143], v[140:141], v[138:139], v[132:133] op_sel:[1,1,0] op_sel_hi:[1,0,1] neg_lo:[0,1,0]\n"
"s_waitcnt lgkmcnt(15)\n"
"v_mfma_f32_16x16x32_bf16 v[228:231], v[216:219], v[220:223], 0\n"
"v_pk_fma_f32 v[132:133], v[124:125], v[136:137], v[120:121] op_sel:[1,1,0] op_sel_hi:[1,0,1] neg_lo:[0,1,0]\n"
"v_pk_fma_f32 v[138:139], v[122:123], v[136:137], v[134:135] op_sel:[1,1,0] op_sel_hi:[1,0,1] neg_lo:[0,1,0]\n"
"v_cvt_pk_bf16_f32 v144, v142, v143\n"
"v_cvt_pk_bf16_f32 v120, v132, v133\n"
"v_cvt_pk_bf16_f32 v121, v138, v139\n"
"ds_write_addtid_b32 v144 offset:15904\n"
"v_pk_fma_f32 v[122:123], v[140:141], v[128:129], v[126:127] op_sel:[1,1,0] op_sel_hi:[1,0,1] neg_lo:[0,1,0]\n"
"ds_write_addtid_b32 v120 offset:25408\n"
"v_pk_mul_f32 v[124:125], v[132:133], v[136:137] op_sel_hi:[0,1]\n"
"v_mfma_f32_16x16x32_bf16 v[228:231], v[208:211], v[212:215], v[228:231]\n"
"ds_write_addtid_b32 v121 offset:24880\n"
"v_cvt_pk_bf16_f32 v120, v122, v123\n"
"v_pk_mul_f32 v[126:127], v[138:139], v[136:137] op_sel_hi:[0,1]\n"
"v_pk_mul_f32 v[134:135], v[122:123], v[128:129] op_sel_hi:[0,1]\n"
"buffer_load_dwordx4 v[240:243], %8, %10, %15 offen sc1\n"
"buffer_load_dwordx4 v[244:247], %8, %10, %16 offen sc1\n"
"buffer_load_dwordx4 v[248:251], %8, %10, %17 offen sc1\n"
"buffer_load_dwordx4 v[252:255], %8, %10, %18 offen sc1\n"
"ds_write_addtid_b32 v120 offset:16960\n"
"v_pk_mul_f32 v[144:145], v[142:143], v[128:129] op_sel_hi:[0,1]\n"
"v_pk_fma_f32 v[146:147], v[132:133], v[136:137], v[124:125] op_sel:[1,1,0] op_sel_hi:[1,0,1] neg_lo:[0,1,0]\n"
"v_pk_fma_f32 v[120:121], v[138:139], v[136:137], v[126:127] op_sel:[1,1,0] op_sel_hi:[1,0,1] neg_lo:[0,1,0]\n"
"v_pk_fma_f32 v[148:149], v[122:123], v[128:129], v[134:135] op_sel:[1,1,0] op_sel_hi:[1,0,1] neg_lo:[0,1,0]\n"
"v_mfma_f32_16x16x32_bf16 v[228:231], v[200:203], v[204:207], v[228:231]\n"
"v_cvt_pk_bf16_f32 v124, v146, v147\n"
"v_cvt_pk_bf16_f32 v122, v120, v121\n"
"v_cvt_pk_bf16_f32 v123, v148, v149\n"
"ds_write_addtid_b32 v124 offset:26464\n"
"v_pk_fma_f32 v[126:127], v[142:143], v[128:129], v[144:145] op_sel:[1,1,0] op_sel_hi:[1,0,1] neg_lo:[0,1,0]\n"
"ds_write_addtid_b32 v122 offset:25936\n"
"v_pk_mul_f32 v[124:125], v[146:147], v[136:137] op_sel_hi:[0,1]\n"
"ds_write_addtid_b32 v123 offset:18016\n"
"v_cvt_pk_bf16_f32 v122, v126, v127\n"
"s_waitcnt lgkmcnt(15)\n"
"v_mfma_f32_16x16x32_bf16 v[228:231], v[192:195], v[196:199], v[228:231]\n"
"v_pk_mul_f32 v[132:133], v[120:121], v[136:137] op_sel_hi:[0,1]\n"
"v_pk_mul_f32 v[134:135], v[148:149], v[128:129] op_sel_hi:[0,1]\n"
"ds_write_addtid_b32 v122 offset:17488\n"
"v_pk_mul_f32 v[138:139], v[126:127], v[128:129] op_sel_hi:[0,1]\n"
"v_pk_fma_f32 v[144:145], v[146:147], v[136:137], v[124:125] op_sel:[1,1,0] op_sel_hi:[1,0,1] neg_lo:[0,1,0]\n"
"v_pk_fma_f32 v[122:123], v[120:121], v[136:137], v[132:133] op_sel:[1,1,0] op_sel_hi:[1,0,1] neg_lo:[0,1,0]\n"
"v_pk_fma_f32 v[150:151], v[148:149], v[128:129], v[134:135] op_sel:[1,1,0] op_sel_hi:[1,0,1] neg_lo:[0,1,0]\n"
"v_cvt_pk_bf16_f32 v124, v144, v145\n"
"v_cvt_pk_bf16_f32 v120, v122, v123\n"
"v_mfma_f32_16x16x32_bf16 v[228:231], v[184:187], v[188:191], v[228:231]\n"
"v_cvt_pk_bf16_f32 v121, v150, v151\n"
"ds_write_addtid_b32 v124 offset:27520\n"
"v_pk_fma_f32 v[132:133], v[126:127], v[128:129], v[138:139] op_sel:[1,1,0] op_sel_hi:[1,0,1] neg_lo:[0,1,0]\n"
"ds_write_addtid_b32 v120 offset:26992\n"
"v_pk_mul_f32 v[124:125], v[144:145], v[136:137] op_sel_hi:[0,1]\n"
"ds_write_addtid_b32 v121 offset:19072\n"
"v_cvt_pk_bf16_f32 v120, v132, v133\n"
"v_pk_mul_f32 v[126:127], v[122:123], v[136:137] op_sel_hi:[0,1]\n"
"v_pk_mul_f32 v[134:135], v[150:151], v[128:129] op_sel_hi:[0,1]\n"
"s_waitcnt lgkmcnt(15)\n"
"v_mfma_f32_16x16x32_bf16 v[228:231], v[176:179], v[180:183], v[228:231]\n"
"ds_write_addtid_b32 v120 offset:18544\n"
"v_pk_mul_f32 v[138:139], v[132:133], v[128:129] op_sel_hi:[0,1]\n"
"v_pk_fma_f32 v[146:147], v[144:145], v[136:137], v[124:125] op_sel:[1,1,0] op_sel_hi:[1,0,1] neg_lo:[0,1,0]\n"
"v_pk_fma_f32 v[120:121], v[122:123], v[136:137], v[126:127] op_sel:[1,1,0] op_sel_hi:[1,0,1] neg_lo:[0,1,0]\n"
"v_pk_fma_f32 v[148:149], v[150:151], v[128:129], v[134:135] op_sel:[1,1,0] op_sel_hi:[1,0,1] neg_lo:[0,1,0]\n"
"v_cvt_pk_bf16_f32 v124, v146, v147\n"
"v_cvt_pk_bf16_f32 v122, v120, v121\n"
"v_cvt_pk_bf16_f32 v123, v148, v149\n"
"ds_write_addtid_b32 v124 offset:28576\n"
"v_mfma_f32_16x16x32_bf16 v[228:231], v[168:171], v[172:175], v[228:231]\n"
"v_pk_fma_f32 v[126:127], v[132:133], v[128:129], v[138:139] op_sel:[1,1,0] op_sel_hi:[1,0,1] neg_lo:[0,1,0]\n"
"ds_write_addtid_b32 v122 offset:28048\n"
"v_pk_mul_f32 v[124:125], v[140:141], v[130:131] op_sel_hi:[0,1]\n"
"ds_write_addtid_b32 v123 offset:20128\n"
"v_cvt_pk_bf16_f32 v122, v126, v127\n"
"v_pk_mul_f32 v[132:133], v[142:143], v[130:131] op_sel_hi:[0,1]\n"
"v_pk_mul_f32 v[134:135], v[146:147], v[136:137] op_sel_hi:[0,1]\n"
"ds_write_addtid_b32 v122 offset:19600\n"
"v_pk_mul_f32 v[138:139], v[120:121], v[136:137] op_sel_hi:[0,1]\n"
"s_waitcnt lgkmcnt(15)\n"
"v_mfma_f32_16x16x32_bf16 v[228:231], v[160:163], v[164:167], v[228:231]\n"
"v_pk_mul_f32 v[122:123], v[148:149], v[128:129] op_sel_hi:[0,1]\n"
"v_pk_mul_f32 v[144:145], v[126:127], v[128:129] op_sel_hi:[0,1]\n"
"v_pk_fma_f32 v[150:151], v[140:141], v[130:131], v[124:125] op_sel:[1,1,0] op_sel_hi:[1,0,1] neg_lo:[0,1,0]\n"
"v_pk_fma_f32 v[152:153], v[142:143], v[130:131], v[132:133] op_sel:[1,1,0] op_sel_hi:[1,0,1] neg_lo:[0,1,0]\n"
"v_pk_fma_f32 v[154:155], v[146:147], v[136:137], v[134:135] op_sel:[1,1,0] op_sel_hi:[1,0,1] neg_lo:[0,1,0]\n"
"v_cvt_pk_bf16_f32 v124, v150, v151\n"
"v_cvt_pk_bf16_f32 v125, v152, v153\n"
"v_cvt_pk_bf16_f32 v130, v154, v155\n"
"ds_write_addtid_b32 v124 offset:15376\n"
"v_pk_fma_f32 v[132:133], v[120:121], v[136:137], v[138:139] op_sel:[1,1,0] op_sel_hi:[1,0,1] neg_lo:[0,1,0]\n"
"ds_write_addtid_b32 v125 offset:16432\n"
"v_pk_fma_f32 v[134:135], v[148:149], v[128:129], v[122:123] op_sel:[1,1,0] op_sel_hi:[1,0,1] neg_lo:[0,1,0]\n"
"ds_write_addtid_b32 v130 offset:29632\n"
"v_cvt_pk_bf16_f32 v120, v132, v133\n"
"v_cvt_pk_bf16_f32 v121, v134, v135\n"
"v_pk_fma_f32 v[122:123], v[126:127], v[128:129], v[144:145] op_sel:[1,1,0] op_sel_hi:[1,0,1] neg_lo:[0,1,0]\n"
"ds_write_addtid_b32 v120 offset:29104\n"
"ds_write_addtid_b32 v121 offset:21184\n"
"v_cvt_pk_bf16_f32 v124, v122, v123\n"
"ds_write_addtid_b32 v124 offset:20656\n"
".Lkg_fin:\n"
"s_waitcnt lgkmcnt(0)\n"
"s_barrier\n"
"s_cmp_eq_u32 %14, 3\n"
"s_cbranch_scc1 .Lkg_zero\n"
"ds_read_b128 v[220:223], %2 offset:60192\n"
"ds_read_b128 v[216:219], %3 offset:60192\n"
"ds_read_b128 v[212:215], %2 offset:60208\n"
"ds_read_b128 v[208:211], %3 offset:60208\n"
"ds_read_b128 v[204:207], %2 offset:60224\n"
"ds_read_b128 v[200:203], %3 offset:60224\n"
"ds_read_b128 v[196:199], %2 offset:60240\n"
"ds_read_b128 v[192:195], %3 offset:60240\n"
"ds_read_b128 v[188:191], %2 offset:60256\n"
"ds_read_b128 v[184:187], %3 offset:60256\n"
"ds_read_b128 v[180:183], %2 offset:60272\n"
"ds_read_b128 v[176:179], %3 offset:60272\n"
"ds_read_b128 v[172:175], %2 offset:60288\n"
"ds_read_b128 v[168:171], %3 offset:60288\n"
"ds_read_b128 v[164:167], %2 offset:60304\n"
"ds_read_b128 v[160:163], %3 offset:60304\n"
"v_add_f32 v224, v56, v224\n"
"v_add_f32 v228, v57, v228\n"
"v_cvt_pk_bf16_f32 v120, v225, v229\n"
"v_cvt_pk_bf16_f32 v122, v226, v230\n"
"v_cvt_pk_bf16_f32 v124, v227, v231\n"
"v_cvt_pk_bf16_f32 v126, v224, v228\n"
"s_waitcnt lgkmcnt(14)\n"
"v_mfma_f32_16x16x32_bf16 v[232:235], v[216:219], v[220:223], 0\n"
"s_waitcnt lgkmcnt(12)\n"
"v_mfma_f32_16x16x32_bf16 v[232:235], v[208:211], v[212:215], v[232:235]\n"
"s_waitcnt lgkmcnt(10)\n"
"v_mfma_f32_16x16x32_bf16 v[232:235], v[200:203], v[204:207], v[232:235]\n"
"s_waitcnt lgkmcnt(8)\n"
"v_mfma_f32_16x16x32_bf16 v[232:235], v[192:195], v[196:199], v[232:235]\n"
"s_waitcnt lgkmcnt(6)\n"
"v_mfma_f32_16x16x32_bf16 v[232:235], v[184:187], v[188:191], v[232:235]\n"
"s_waitcnt lgkmcnt(4)\n"
"v_mfma_f32_16x16x32_bf16 v[232:235], v[176:179], v[180:183], v[232:235]\n"
"s_waitcnt lgkmcnt(2)\n"
"v_mfma_f32_16x16x32_bf16 v[232:235], v[168:171], v[172:175], v[232:235]\n"
"s_waitcnt lgkmcnt(0)\n"
"v_mfma_f32_16x16x32_bf16 v[232:235], v[160:163], v[164:167], v[232:235]\n"
"s_nop 7\n"
"v_add_f32 v232, v58, v232\n"
"v_cvt_pk_bf16_f32 v121, v233, v233\n"
"v_cvt_pk_bf16_f32 v123, v234, v234\n"
"v_cvt_pk_bf16_f32 v125, v235, v235\n"
"v_cvt_pk_bf16_f32 v127, v232, v232\n"
"ds_write_b64 %5, v[120:121]\n"
"ds_write_b64 %6, v[122:123]\n"
"ds_write_b64 %7, v[124:125]\n"
"ds_write_b64 %4, v[126:127]\n"
".Lwk_b1:\n"
"s_cbranch_execz .Lwk_back\n"
".Lkg_end:\n"
"s_mov_b64 exec, -1\n"
      : "=&{v[240:255]}"(tg) : "{v[44:59]}"(kp), "v"(kap), "v"(kbp), "v"(kto[0]), "v"(kto[1]), "v"(kto[2]), "v"(kto[3]), "v"(ugo), "v"(pfo1),
        "s"(sw), "s"(sb1), "s"(w1c0), "s"(ldsT + (wid & 1) * 256), "s"(wid), "s"(uin[0]), "s"(uin[1]), "s"(uin[2]), "s"(uin[3])
      : "memory", "scc", "s98", "s92", "s93", C10("v", 12), C10("v", 13), C10("v", 14), C10("v", 15),
        C10("v", 16), C10("v", 17), C10("v", 18), C10("v", 19), C10("v", 20), C10("v", 21), C10("v", 22), C10("v", 23));
    touch_a(pfo2, sb1, w1c0);
    LDS_BARRIER();
    unsigned sda, sdb, swa, sao, smk;
    {
      auto utake = [&]() {
        t_take<4, 1>(tg, ugo, sw, uin, tagu);
        set_m0(ldsT + CV_U + wid * 1024);
        unsigned w[12];
        sfor<14>([&](auto I) {
          constexpr int o = (I - 2) % 3 * CV_UC + (I - 2) / 3 * 256 + 29 * 16;
          if constexpr (I > 1) { put_row<o + 8>(w[I - 2]); put_row<o + CV_UP>(w[I - 2]); }
          if constexpr (I < 12) w[I] = I % 3 == 0 ? perm_lo(tg[I / 3 * 4], tg[I / 3 * 4 + 2]) : I % 3 == 1 ? perm_hi(tg[I / 3 * 4], tg[I / 3 * 4 + 2]) : perm_lo(tg[I / 3 * 4 + 1], tg[I / 3 * 4 + 3]);
          SB0;
        });
      };
      u32x2 kv[12];
#pragma unroll
      for (int k = 0; k < 12; ++k) kv[k] = *(const LAS u32x2*)(size_t)kad[k];
      ldsw<unsigned>(zoff, 0u);
      SB0;
      sda = wid == 1 ? -4 * CV_KP - 256 : wid == 2 ? 4 * CV_KP : -128, sdb = wid == 1 ? -CV_UC - 256 : wid == 2 ? CV_UC : -128;
      swa = ldsT + CW + wid * WA, sao = WS_AG + cg * AGP + wid * 1024, smk = WS_GF + (cg * 4 + wid) * MC * 1024;
      pins(sda); pins(sdb); pins(swa); pins(sao); pins(smk);
      SB0;
      set_m0(lds0 + wid * 1024 - 4);
      asm volatile("" : "+v"(kv[0]), "+v"(kv[1]), "+v"(kv[2]), "+v"(kv[3]), "+v"(kv[4]), "+v"(kv[5]), "+v"(kv[6]), "+v"(kv[7]), "+v"(kv[8]), "+v"(kv[9]), "+v"(kv[10]), "+v"(kv[11]));
      unsigned e[24];
      sfor<26>([&](auto I) {
        constexpr int J = I - 2, o = (J % 6 / 2 * 4 + 1 - (J & 1)) * CV_KP + J / 6 * 256, c = I % 6 / 2, m = I / 6 * 3 + (I & 1);
        if constexpr (I > 1) { put_row<o + 2 * CV_KP + 4>(e[J]); put_row<o>(e[J]); }
        if constexpr (I < 24) e[I] = c == 0 ? perm_lo(kv[m].x, kv[m + 1].x) : c == 1 ? perm_hi(kv[m].x, kv[m + 1].x) : perm_lo(kv[m].y, kv[m + 1].y);
        SB0;
      });
      utake();
    }
    {
      LDS_BARRIER();
      asm volatile(
"ds_read_b128 v[252:255], %0 offset:3776\n"
"ds_read_b128 v[248:251], %1 offset:192\n"
"ds_read_b128 v[244:247], %0 offset:3264\n"
"ds_read_b128 v[240:243], %1 offset:704\n"
"ds_read_b128 v[236:239], %0 offset:2752\n"
"ds_read_b128 v[232:235], %1 offset:1216\n"
"ds_read_b128 v[228:231], %0 offset:2240\n"
"ds_read_b128 v[224:227], %1 offset:1728\n"
"ds_read_b128 v[220:223], %0 offset:1728\n"
"ds_read_b128 v[216:219], %1 offset:2240\n"
"ds_read_b128 v[212:215], %0 offset:1216\n"
"ds_read_b128 v[208:211], %1 offset:2752\n"
"ds_read_b128 v[204:207], %0 offset:704\n"
"ds_read_b128 v[200:203], %1 offset:3264\n"
"ds_read_b128 v[196:199], %0 offset:192\n"
"ds_read_b128 v[192:195], %1 offset:3776\n"
"s_branch .Lwk_cv0\n"
".p2align 11\n"
".Lwk_cv0:\n"
"s_cbranch_execz .Lwk_back\n"
"s_waitcnt lgkmcnt(14)\n"
"v_mfma_f32_16x16x32_bf16 v[188:191], v[248:251], v[252:255], 0\n"
"ds_read_b128 v[184:187], %0 offset:3712\n"
"s_xor_b32 s97, %20, 4\n"
"s_mul_i32 s97, s97, 1179648\n"
"s_add_u32 s97, s97, %19\n"
"s_add_u32 m0, %18, 34304\n"
"s_waitcnt lgkmcnt(14)\n"
"v_mfma_f32_16x16x32_bf16 v[180:183], v[248:251], v[244:247], 0\n"
"buffer_load_dwordx4 %6, %15, s97 offen offset:0 lds\n"
"s_waitcnt lgkmcnt(13)\n"
"v_mfma_f32_16x16x32_bf16 v[180:183], v[240:243], v[252:255], v[180:183]\n"
"ds_read_b128 v[176:179], %1 offset:128\n"
"v_add_u32 v172, 68608, %10\n"
"v_mfma_f32_16x16x32_bf16 v[168:171], v[240:243], v[244:247], 0\n"
"s_waitcnt lgkmcnt(13)\n"
"v_mfma_f32_16x16x32_bf16 v[168:171], v[248:251], v[236:239], v[168:171]\n"
"ds_read_b128 v[164:167], %0 offset:3200\n"
"v_add_u32 v173, 68608, %11\n"
"s_waitcnt lgkmcnt(13)\n"
"v_mfma_f32_16x16x32_bf16 v[168:171], v[232:235], v[252:255], v[168:171]\n"
"buffer_load_dwordx4 %7, %15, s97 offen offset:1024 lds\n"
"v_mfma_f32_16x16x32_bf16 v[160:163], v[240:243], v[236:239], 0\n"
"ds_read_b128 v[156:159], %1 offset:640\n"
"v_add_u32 v174, 68608, %12\n"
"v_mfma_f32_16x16x32_bf16 v[160:163], v[232:235], v[244:247], v[160:163]\n"
"v_mfma_f32_16x16x32_bf16 v[152:155], v[232:235], v[236:239], 0\n"
"ds_read_b128 v[148:151], %0 offset:2688\n"
"v_add_u32 v175, 68608, %13\n"
"s_waitcnt lgkmcnt(11)\n"
"v_mfma_f32_16x16x32_bf16 v[160:163], v[248:251], v[228:231], v[160:163]\n"
"buffer_load_dwordx4 %8, %15, s97 offen offset:2048 lds\n"
"v_mfma_f32_16x16x32_bf16 v[160:163], v[224:227], v[252:255], v[160:163]\n"
"ds_read_b128 v[144:147], %1 offset:1152\n"
"v_mfma_f32_16x16x32_bf16 v[152:155], v[240:243], v[228:231], v[152:155]\n"
"v_mfma_f32_16x16x32_bf16 v[152:155], v[224:227], v[244:247], v[152:155]\n"
"ds_read_b128 v[140:143], %0 offset:2176\n"
"v_mfma_f32_16x16x32_bf16 v[136:139], v[232:235], v[228:231], 0\n"
"buffer_load_dwordx4 %9, %15, s97 offen offset:3072 lds\n"
"v_mfma_f32_16x16x32_bf16 v[136:139], v[224:227], v[236:239], v[136:139]\n"
"ds_read_b128 v[132:135], %1 offset:1664\n"
"v_mfma_f32_16x16x32_bf16 v[128:131], v[224:227], v[228:231], 0\n"
"s_xor_b32 s97, s97, 128\n"
"v_mfma_f32_16x16x32_bf16 v[152:155], v[248:251], v[220:223], v[152:155]\n"
"ds_read_b128 v[124:127], %0 offset:1664\n"
"s_add_u32 m0, %18, 51456\n"
"v_mfma_f32_16x16x32_bf16 v[152:155], v[216:219], v[252:255], v[152:155]\n"
"buffer_load_dwordx4 %6, %15, s97 offen offset:0 lds\n"
"v_mfma_f32_16x16x32_bf16 v[136:139], v[240:243], v[220:223], v[136:139]\n"
"ds_read_b128 v[120:123], %1 offset:2176\n"
"v_mfma_f32_16x16x32_bf16 v[136:139], v[216:219], v[244:247], v[136:139]\n"
"v_mfma_f32_16x16x32_bf16 v[128:131], v[232:235], v[220:223], v[128:131]\n"
"ds_read_b128 v[116:119], %0 offset:1152\n"
"v_mfma_f32_16x16x32_bf16 v[128:131], v[216:219], v[236:239], v[128:131]\n"
"buffer_load_dwordx4 %7, %15, s97 offen offset:1024 lds\n"
"v_mfma_f32_16x16x32_bf16 v[112:115], v[224:227], v[220:223], 0\n"
"ds_read_b128 v[108:111], %1 offset:2688\n"
"v_mfma_f32_16x16x32_bf16 v[112:115], v[216:219], v[228:231], v[112:115]\n"
"s_waitcnt lgkmcnt(3)\n"
"v_mfma_f32_16x16x32_bf16 v[136:139], v[248:251], v[212:215], v[136:139]\n"
"v_mfma_f32_16x16x32_bf16 v[136:139], v[208:211], v[252:255], v[136:139]\n"
"buffer_load_dwordx4 %8, %15, s97 offen offset:2048 lds\n"
"v_mfma_f32_16x16x32_bf16 v[128:131], v[240:243], v[212:215], v[128:131]\n"
"ds_read_b128 v[228:231], %0 offset:640\n"
"v_mfma_f32_16x16x32_bf16 v[128:131], v[208:211], v[244:247], v[128:131]\n"
"v_mfma_f32_16x16x32_bf16 v[112:115], v[232:235], v[212:215], v[112:115]\n"
"ds_read_b128 v[224:227], %1 offset:3200\n"
"v_mfma_f32_16x16x32_bf16 v[112:115], v[208:211], v[236:239], v[112:115]\n"
"buffer_load_dwordx4 %9, %15, s97 offen offset:3072 lds\n"
"v_mfma_f32_16x16x32_bf16 v[128:131], v[248:251], v[204:207], v[128:131]\n"
"ds_read_b128 v[236:239], %0 offset:128\n"
"v_mfma_f32_16x16x32_bf16 v[128:131], v[200:203], v[252:255], v[128:131]\n"
"s_xor_b32 s97, %20, 5\n"
"s_mul_i32 s97, s97, 1179648\n"
"s_add_u32 s97, s97, %19\n"
"v_mfma_f32_16x16x32_bf16 v[112:115], v[240:243], v[204:207], v[112:115]\n"
"ds_read_b128 v[232:235], %1 offset:3712\n"
"s_add_u32 m0, %18, 68608\n"
"v_mfma_f32_16x16x32_bf16 v[112:115], v[200:203], v[244:247], v[112:115]\n"
"buffer_load_dwordx4 %6, %15, s97 offen offset:0 lds\n"
"v_mfma_f32_16x16x32_bf16 v[112:115], v[248:251], v[196:199], v[112:115]\n"
"ds_read_b128 v[244:247], %0 offset:3648\n"
"v_mfma_f32_16x16x32_bf16 v[112:115], v[192:195], v[252:255], v[112:115]\n"
"v_mfma_f32_16x16x32_bf16 v[188:191], v[176:179], v[184:187], v[188:191]\n"
"ds_read_b128 v[248:251], %1 offset:64\n"
"v_mfma_f32_16x16x32_bf16 v[180:183], v[176:179], v[164:167], v[180:183]\n"
"buffer_load_dwordx4 %7, %15, s97 offen offset:1024 lds\n"
"v_mfma_f32_16x16x32_bf16 v[180:183], v[156:159], v[184:187], v[180:183]\n"
"ds_read_b128 v[252:255], %0 offset:3136\n"
"v_mfma_f32_16x16x32_bf16 v[168:171], v[156:159], v[164:167], v[168:171]\n"
"v_mfma_f32_16x16x32_bf16 v[168:171], v[176:179], v[148:151], v[168:171]\n"
"ds_read_b128 v[240:243], %1 offset:576\n"
"v_mfma_f32_16x16x32_bf16 v[168:171], v[144:147], v[184:187], v[168:171]\n"
"buffer_load_dwordx4 %8, %15, s97 offen offset:2048 lds\n"
"v_mfma_f32_16x16x32_bf16 v[160:163], v[156:159], v[148:151], v[160:163]\n"
"ds_read_b128 v[220:223], %0 offset:2624\n"
"v_mfma_f32_16x16x32_bf16 v[160:163], v[144:147], v[164:167], v[160:163]\n"
"v_mfma_f32_16x16x32_bf16 v[152:155], v[144:147], v[148:151], v[152:155]\n"
"ds_read_b128 v[216:219], %1 offset:1088\n"
"v_mfma_f32_16x16x32_bf16 v[160:163], v[176:179], v[140:143], v[160:163]\n"
"buffer_load_dwordx4 %9, %15, s97 offen offset:3072 lds\n"
"v_mfma_f32_16x16x32_bf16 v[160:163], v[132:135], v[184:187], v[160:163]\n"
"ds_read_b128 v[212:215], %0 offset:2112\n"
"v_mfma_f32_16x16x32_bf16 v[152:155], v[156:159], v[140:143], v[152:155]\n"
"s_xor_b32 s97, s97, 128\n"
"v_mfma_f32_16x16x32_bf16 v[152:155], v[132:135], v[164:167], v[152:155]\n"
"ds_read_b128 v[208:211], %1 offset:1600\n"
"s_add_u32 m0, %18, 0\n"
"v_mfma_f32_16x16x32_bf16 v[136:139], v[144:147], v[140:143], v[136:139]\n"
"buffer_load_dwordx4 %6, %15, s97 offen offset:0 lds\n"
"v_mfma_f32_16x16x32_bf16 v[136:139], v[132:135], v[148:151], v[136:139]\n"
"ds_read_b128 v[204:207], %0 offset:1600\n"
"v_mfma_f32_16x16x32_bf16 v[128:131], v[132:135], v[140:143], v[128:131]\n"
"v_mfma_f32_16x16x32_bf16 v[152:155], v[176:179], v[124:127], v[152:155]\n"
"ds_read_b128 v[200:203], %1 offset:2112\n"
"s_waitcnt lgkmcnt(4)\n"
"v_mfma_f32_16x16x32_bf16 v[152:155], v[120:123], v[184:187], v[152:155]\n"
"buffer_load_dwordx4 %7, %15, s97 offen offset:1024 lds\n"
"v_mfma_f32_16x16x32_bf16 v[136:139], v[156:159], v[124:127], v[136:139]\n"
"v_mfma_f32_16x16x32_bf16 v[136:139], v[120:123], v[164:167], v[136:139]\n"
"v_mfma_f32_16x16x32_bf16 v[128:131], v[144:147], v[124:127], v[128:131]\n"
"ds_read_b128 v[196:199], %0 offset:1088\n"
"v_mfma_f32_16x16x32_bf16 v[128:131], v[120:123], v[148:151], v[128:131]\n"
"buffer_load_dwordx4 %8, %15, s97 offen offset:2048 lds\n"
"v_mfma_f32_16x16x32_bf16 v[112:115], v[132:135], v[124:127], v[112:115]\n"
"ds_read_b128 v[192:195], %1 offset:2624\n"
"v_mfma_f32_16x16x32_bf16 v[112:115], v[120:123], v[140:143], v[112:115]\n"
"v_mfma_f32_16x16x32_bf16 v[136:139], v[176:179], v[116:119], v[136:139]\n"
"v_mfma_f32_16x16x32_bf16 v[136:139], v[108:111], v[184:187], v[136:139]\n"
"s_waitcnt vmcnt(11)\n"
"ds_read_b64_tr_b16 a[128:129], %10 offset:34304\n"
"buffer_load_dwordx4 %9, %15, s97 offen offset:3072 lds\n"
"v_mfma_f32_16x16x32_bf16 v[128:131], v[156:159], v[116:119], v[128:131]\n"
"ds_read_b128 v[140:143], %0 offset:576\n"
"ds_read_b64_tr_b16 a[130:131], %10 offset:34816\n"
"v_mfma_f32_16x16x32_bf16 v[128:131], v[108:111], v[164:167], v[128:131]\n"
"ds_read_b64_tr_b16 a[132:133], %11 offset:34304\n"
"s_xor_b32 s97, %20, 6\n"
"s_mul_i32 s97, s97, 1179648\n"
"s_add_u32 s97, s97, %19\n"
"v_mfma_f32_16x16x32_bf16 v[112:115], v[144:147], v[116:119], v[112:115]\n"
"ds_read_b128 v[132:135], %1 offset:3136\n"
"v_mfma_f32_16x16x32_bf16 v[112:115], v[108:111], v[148:151], v[112:115]\n"
"ds_read_b64_tr_b16 a[134:135], %11 offset:34816\n"
"s_add_u32 m0, %18, 17152\n"
"s_nop 0\n"
"buffer_load_dwordx4 %6, %15, s97 offen offset:0 lds\n"
"v_mfma_f32_16x16x32_bf16 v[128:131], v[176:179], v[228:231], v[128:131]\n"
"ds_read_b128 v[148:151], %0 offset:64\n"
"v_mfma_f32_16x16x32_bf16 v[128:131], v[224:227], v[184:187], v[128:131]\n"
"ds_read_b64_tr_b16 a[136:137], %12 offset:34304\n"
"v_mfma_f32_16x16x32_bf16 v[112:115], v[156:159], v[228:231], v[112:115]\n"
"ds_read_b128 v[144:147], %1 offset:3648\n"
"v_mfma_f32_16x16x32_bf16 v[112:115], v[224:227], v[164:167], v[112:115]\n"
"ds_read_b64_tr_b16 a[138:139], %12 offset:34816\n"
"buffer_load_dwordx4 %7, %15, s97 offen offset:1024 lds\n"
"v_mfma_f32_16x16x32_bf16 v[112:115], v[176:179], v[236:239], v[112:115]\n"
"ds_read_b128 v[228:231], %0 offset:3584\n"
"v_mfma_f32_16x16x32_bf16 v[112:115], v[232:235], v[184:187], v[112:115]\n"
"ds_read_b64_tr_b16 a[140:141], %13 offset:34304\n"
"v_mfma_f32_16x16x32_bf16 v[188:191], v[248:251], v[244:247], v[188:191]\n"
"ds_read_b128 v[236:239], %1 offset:0\n"
"v_mfma_f32_16x16x32_bf16 v[180:183], v[248:251], v[252:255], v[180:183]\n"
"ds_read_b64_tr_b16 a[142:143], %13 offset:34816\n"
"buffer_load_dwordx4 %8, %15, s97 offen offset:2048 lds\n"
"v_mfma_f32_16x16x32_bf16 v[180:183], v[240:243], v[244:247], v[180:183]\n"
"ds_read_b128 v[232:235], %1 offset:512\n"
"v_mfma_f32_16x16x32_bf16 v[168:171], v[240:243], v[252:255], v[168:171]\n"
"v_mfma_f32_16x16x32_bf16 v[168:171], v[248:251], v[220:223], v[168:171]\n"
"ds_read_b128 v[224:227], %0 offset:3072\n"
"v_mfma_f32_16x16x32_bf16 v[168:171], v[216:219], v[244:247], v[168:171]\n"
"s_waitcnt vmcnt(11)\n"
"ds_read_b64_tr_b16 a[144:145], %10 offset:51456\n"
"buffer_load_dwordx4 %9, %15, s97 offen offset:3072 lds\n"
"v_mfma_f32_16x16x32_bf16 v[160:163], v[240:243], v[220:223], v[160:163]\n"
"ds_read_b128 v[184:187], %1 offset:1024\n"
"ds_read_b64_tr_b16 a[146:147], %10 offset:51968\n"
"v_mfma_f32_16x16x32_bf16 v[160:163], v[216:219], v[252:255], v[160:163]\n"
"ds_read_b64_tr_b16 a[148:149], %11 offset:51456\n"
"s_xor_b32 s97, s97, 128\n"
"v_mfma_f32_16x16x32_bf16 v[152:155], v[216:219], v[220:223], v[152:155]\n"
"ds_read_b128 v[176:179], %0 offset:2560\n"
"s_waitcnt lgkmcnt(7)\n"
"v_mfma_f32_16x16x32_bf16 v[160:163], v[248:251], v[212:215], v[160:163]\n"
"ds_read_b64_tr_b16 a[150:151], %11 offset:51968\n"
"s_add_u32 m0, %18, 34304\n"
"s_nop 0\n"
"buffer_load_dwordx4 %6, %15, s97 offen offset:0 lds\n"
"v_mfma_f32_16x16x32_bf16 v[160:163], v[208:211], v[244:247], v[160:163]\n"
"ds_read_b128 v[164:167], %1 offset:1536\n"
"v_mfma_f32_16x16x32_bf16 v[152:155], v[240:243], v[212:215], v[152:155]\n"
"ds_read_b64_tr_b16 a[152:153], %12 offset:51456\n"
"v_mfma_f32_16x16x32_bf16 v[152:155], v[208:211], v[252:255], v[152:155]\n"
"ds_read_b128 v[156:159], %0 offset:2048\n"
"v_mfma_f32_16x16x32_bf16 v[136:139], v[216:219], v[212:215], v[136:139]\n"
"ds_read_b64_tr_b16 a[154:155], %12 offset:51968\n"
"buffer_load_dwordx4 %7, %15, s97 offen offset:1024 lds\n"
"v_mfma_f32_16x16x32_bf16 v[136:139], v[208:211], v[220:223], v[136:139]\n"
"ds_read_b128 v[124:127], %1 offset:2048\n"
"v_mfma_f32_16x16x32_bf16 v[128:131], v[208:211], v[212:215], v[128:131]\n"
"ds_read_b64_tr_b16 a[156:157], %13 offset:51456\n"
"v_mfma_f32_16x16x32_bf16 v[152:155], v[248:251], v[204:207], v[152:155]\n"
"ds_read_b128 v[120:123], %0 offset:1536\n"
"v_mfma_f32_16x16x32_bf16 v[152:155], v[200:203], v[244:247], v[152:155]\n"
"ds_read_b64_tr_b16 a[158:159], %13 offset:51968\n"
"buffer_load_dwordx4 %8, %15, s97 offen offset:2048 lds\n"
"v_mfma_f32_16x16x32_bf16 v[136:139], v[240:243], v[204:207], v[136:139]\n"
"ds_read_b128 v[116:119], %1 offset:2560\n"
"v_mfma_f32_16x16x32_bf16 v[136:139], v[200:203], v[252:255], v[136:139]\n"
"v_mfma_f32_16x16x32_bf16 v[128:131], v[216:219], v[204:207], v[128:131]\n"
"ds_read_b128 v[108:111], %0 offset:1024\n"
"v_mfma_f32_16x16x32_bf16 v[128:131], v[200:203], v[220:223], v[128:131]\n"
"s_waitcnt vmcnt(11)\n"
"ds_read_b64_tr_b16 a[160:161], v172 offset:0\n"
"buffer_load_dwordx4 %9, %15, s97 offen offset:3072 lds\n"
"v_mfma_f32_16x16x32_bf16 v[112:115], v[208:211], v[204:207], v[112:115]\n"
"ds_read_b128 v[104:107], %1 offset:3072\n"
"v_mfma_f32_16x16x32_bf16 v[112:115], v[200:203], v[212:215], v[112:115]\n"
"ds_read_b64_tr_b16 a[162:163], v172 offset:512\n"
"s_xor_b32 s97, %20, 7\n"
"s_mul_i32 s97, s97, 1179648\n"
"s_add_u32 s97, s97, %19\n"
"v_mfma_f32_16x16x32_bf16 v[136:139], v[248:251], v[196:199], v[136:139]\n"
"ds_read_b128 v[212:215], %0 offset:512\n"
"v_mfma_f32_16x16x32_bf16 v[136:139], v[192:195], v[244:247], v[136:139]\n"
"ds_read_b64_tr_b16 a[164:165], v173 offset:0\n"
"s_add_u32 m0, %18, 51456\n"
"s_waitcnt lgkmcnt(7)\n"
"buffer_load_dwordx4 %6, %15, s97 offen offset:0 lds\n"
"v_mfma_f32_16x16x32_bf16 v[128:131], v[240:243], v[196:199], v[128:131]\n"
"ds_read_b64_tr_b16 a[166:167], v173 offset:512\n"
"v_mfma_f32_16x16x32_bf16 v[128:131], v[192:195], v[252:255], v[128:131]\n"
"ds_read_b64_tr_b16 a[168:169], v174 offset:0\n"
"v_mfma_f32_16x16x32_bf16 v[112:115], v[216:219], v[196:199], v[112:115]\n"
"ds_read_b64_tr_b16 a[170:171], v174 offset:512\n"
"v_mov_b32 v208, -1\n"
"v_mfma_f32_16x16x32_bf16 v[112:115], v[192:195], v[220:223], v[112:115]\n"
"ds_read_b64_tr_b16 a[172:173], v175 offset:0\n"
"buffer_load_dwordx4 %7, %15, s97 offen offset:1024 lds\n"
"v_mfma_f32_16x16x32_bf16 v[128:131], v[248:251], v[140:143], v[128:131]\n"
"ds_read_b128 v[220:223], %1 offset:3584\n"
"v_mov_b32 v209, -1\n"
"v_mfma_f32_16x16x32_bf16 v[128:131], v[132:135], v[244:247], v[128:131]\n"
"ds_read_b64_tr_b16 a[174:175], v175 offset:512\n"
"v_mov_b32 v210, -1\n"
"v_mfma_f32_16x16x32_bf16 v[112:115], v[240:243], v[140:143], v[112:115]\n"
"ds_read_b128 v[216:219], %0 offset:0\n"
"v_mov_b32 v211, -1\n"
"v_mfma_f32_16x16x32_bf16 v[112:115], v[132:135], v[252:255], v[112:115]\n"
"buffer_load_dwordx4 %8, %15, s97 offen offset:2048 lds\n"
"v_mfma_f32_16x16x32_bf16 v[112:115], v[248:251], v[148:151], v[112:115]\n"
"v_add_u32 %0, %16, %0\n"
"v_add_u32 %1, %17, %1\n"
"v_mfma_f32_16x16x32_bf16 v[112:115], v[144:147], v[244:247], v[112:115]\n"
"ds_read_b128 v[252:255], %0 offset:3648\n"
"ds_read_b128 v[248:251], %1 offset:64\n"
"v_mfma_f32_16x16x32_bf16 v[188:191], v[236:239], v[228:231], v[188:191]\n"
"ds_read_b128 v[244:247], %1 offset:576\n"
"ds_read_b128 v[240:243], %0 offset:3136\n"
"v_mfma_f32_16x16x32_bf16 v[180:183], v[232:235], v[228:231], v[180:183]\n"
"s_waitcnt vmcnt(11)\n"
"ds_read_b64_tr_b16 a[176:177], %10 offset:0\n"
"buffer_load_dwordx4 %9, %15, s97 offen offset:3072 lds\n"
"v_mfma_f32_16x16x32_bf16 v[180:183], v[236:239], v[224:227], v[180:183]\n"
"ds_read_b64_tr_b16 a[178:179], %10 offset:512\n"
"ds_read_b128 v[204:207], %1 offset:1088\n"
"v_mfma_f32_16x16x32_bf16 v[168:171], v[232:235], v[224:227], v[168:171]\n"
"ds_read_b64_tr_b16 a[180:181], %11 offset:0\n"
"s_xor_b32 s97, s97, 128\n"
"v_mfma_f32_16x16x32_bf16 v[168:171], v[184:187], v[228:231], v[168:171]\n"
"ds_read_b64_tr_b16 a[182:183], %11 offset:512\n"
".Lwk_cv1:\n"
"s_cbranch_execz .Lkg_end\n"
"ds_read_b128 v[200:203], %0 offset:2624\n"
"v_mfma_f32_16x16x32_bf16 v[168:171], v[236:239], v[176:179], v[168:171]\n"
"ds_read_b64_tr_b16 a[184:185], %12 offset:0\n"
"s_add_u32 m0, %18, 68608\n"
"s_waitcnt lgkmcnt(11)\n"
"buffer_load_dwordx4 %6, %15, s97 offen offset:0 lds\n"
"v_mfma_f32_16x16x32_bf16 v[160:163], v[184:187], v[224:227], v[160:163]\n"
"ds_read_b64_tr_b16 a[186:187], %12 offset:512\n"
"ds_read_b128 v[196:199], %1 offset:1600\n"
"v_mfma_f32_16x16x32_bf16 v[160:163], v[232:235], v[176:179], v[160:163]\n"
"ds_read_b64_tr_b16 a[188:189], %13 offset:0\n"
"ds_read_b128 v[192:195], %0 offset:2112\n"
"v_mfma_f32_16x16x32_bf16 v[160:163], v[164:167], v[228:231], v[160:163]\n"
"ds_read_b64_tr_b16 a[190:191], %13 offset:512\n"
"v_mfma_f32_16x16x32_bf16 v[160:163], v[236:239], v[156:159], v[160:163]\n"
"buffer_load_dwordx4 %7, %15, s97 offen offset:1024 lds\n"
"ds_read_b128 v[148:151], %0 offset:3584\n"
"v_mfma_f32_16x16x32_bf16 v[152:155], v[184:187], v[176:179], v[152:155]\n"
"ds_read_b128 v[144:147], %1 offset:0\n"
"v_mfma_f32_16x16x32_bf16 v[152:155], v[164:167], v[224:227], v[152:155]\n"
"ds_read_b128 v[140:143], %1 offset:512\n"
"v_mfma_f32_16x16x32_bf16 v[152:155], v[232:235], v[156:159], v[152:155]\n"
"ds_read_b128 v[132:135], %0 offset:3072\n"
"v_mfma_f32_16x16x32_bf16 v[152:155], v[124:127], v[228:231], v[152:155]\n"
"buffer_load_dwordx4 %8, %15, s97 offen offset:2048 lds\n"
"ds_read_b128 v[100:103], %1 offset:1024\n"
"v_mfma_f32_16x16x32_bf16 v[152:155], v[236:239], v[120:123], v[152:155]\n"
"ds_read_b128 v[96:99], %0 offset:2560\n"
"v_mfma_f32_16x16x32_bf16 v[136:139], v[164:167], v[176:179], v[136:139]\n"
"ds_read_b128 v[92:95], %1 offset:1536\n"
"v_mfma_f32_16x16x32_bf16 v[136:139], v[184:187], v[156:159], v[136:139]\n"
"ds_read_b128 v[88:91], %0 offset:2048\n"
"v_mfma_f32_16x16x32_bf16 v[136:139], v[124:127], v[224:227], v[136:139]\n"
"s_waitcnt vmcnt(11)\n"
"ds_read_b64_tr_b16 a[192:193], %10 offset:17152\n"
"buffer_load_dwordx4 %9, %15, s97 offen offset:3072 lds\n"
"v_mfma_f32_16x16x32_bf16 v[136:139], v[232:235], v[120:123], v[136:139]\n"
"ds_read_b64_tr_b16 a[194:195], %10 offset:17664\n"
"ds_read_b128 v[84:87], %1 offset:2112\n"
"v_mfma_f32_16x16x32_bf16 v[136:139], v[116:119], v[228:231], v[136:139]\n"
"ds_read_b64_tr_b16 a[196:197], %11 offset:17152\n"
"ds_read_b128 v[80:83], %0 offset:1600\n"
"v_mfma_f32_16x16x32_bf16 v[136:139], v[236:239], v[108:111], v[136:139]\n"
"ds_read_b64_tr_b16 a[198:199], %11 offset:17664\n"
"v_mfma_f32_16x16x32_bf16 v[128:131], v[164:167], v[156:159], v[128:131]\n"
"ds_read_b64_tr_b16 a[200:201], %12 offset:17152\n"
"v_mfma_f32_16x16x32_bf16 v[128:131], v[124:127], v[176:179], v[128:131]\n"
"ds_read_b64_tr_b16 a[202:203], %12 offset:17664\n"
"buffer_store_dwordx4 v[208:211], %4, %14, %21 offen offset:0 sc1\n"
"v_mfma_f32_16x16x32_bf16 v[128:131], v[184:187], v[120:123], v[128:131]\n"
"ds_read_b64_tr_b16 a[204:205], %13 offset:17152\n"
"s_add_u32 m0, %18, 0\n"
"s_nop 0\n"
"ds_write_addtid_b32 v188 offset:0\n"
"v_mfma_f32_16x16x32_bf16 v[128:131], v[116:119], v[224:227], v[128:131]\n"
"ds_read_b64_tr_b16 a[206:207], %13 offset:17664\n"
"ds_write_addtid_b32 v189 offset:2112\n"
"v_mfma_f32_16x16x32_bf16 v[128:131], v[232:235], v[108:111], v[128:131]\n"
"ds_write_addtid_b32 v190 offset:17280\n"
"ds_write_addtid_b32 v191 offset:19392\n"
"v_mfma_f32_16x16x32_bf16 v[128:131], v[104:107], v[228:231], v[128:131]\n"
"ds_write_addtid_b32 v180 offset:256\n"
"ds_write_addtid_b32 v181 offset:2368\n"
"v_mfma_f32_16x16x32_bf16 v[128:131], v[236:239], v[212:215], v[128:131]\n"
"ds_write_addtid_b32 v182 offset:17536\n"
"ds_write_addtid_b32 v183 offset:19648\n"
"v_mfma_f32_16x16x32_bf16 v[112:115], v[124:127], v[156:159], v[112:115]\n"
"ds_write_addtid_b32 v168 offset:512\n"
"ds_write_addtid_b32 v169 offset:2624\n"
"v_mfma_f32_16x16x32_bf16 v[112:115], v[164:167], v[120:123], v[112:115]\n"
"ds_write_addtid_b32 v170 offset:17792\n"
"ds_write_addtid_b32 v171 offset:19904\n"
"v_mfma_f32_16x16x32_bf16 v[112:115], v[116:119], v[176:179], v[112:115]\n"
"ds_write_addtid_b32 v160 offset:768\n"
"ds_write_addtid_b32 v161 offset:2880\n"
"v_mfma_f32_16x16x32_bf16 v[112:115], v[184:187], v[108:111], v[112:115]\n"
"ds_write_addtid_b32 v162 offset:18048\n"
"ds_write_addtid_b32 v163 offset:20160\n"
"v_mfma_f32_16x16x32_bf16 v[112:115], v[104:107], v[224:227], v[112:115]\n"
"ds_write_addtid_b32 v152 offset:1024\n"
"ds_write_addtid_b32 v153 offset:3136\n"
"v_mfma_f32_16x16x32_bf16 v[112:115], v[232:235], v[212:215], v[112:115]\n"
"s_waitcnt vmcnt(9)\n"
"ds_read_b64_tr_b16 a[208:209], %10 offset:34304\n"
"ds_write_addtid_b32 v154 offset:18304\n"
"v_mfma_f32_16x16x32_bf16 v[112:115], v[220:223], v[228:231], v[112:115]\n"
"ds_read_b64_tr_b16 a[210:211], %10 offset:34816\n"
"buffer_store_dwordx4 v[208:211], %4, %14, %21 offen offset:1024 sc1\n"
"ds_write_addtid_b32 v155 offset:20416\n"
"v_mfma_f32_16x16x32_bf16 v[112:115], v[236:239], v[216:219], v[112:115]\n"
"ds_read_b64_tr_b16 a[212:213], %11 offset:34304\n"
"ds_write_addtid_b32 v136 offset:1280\n"
"s_waitcnt lgkmcnt(15)\n"
"v_mfma_f32_16x16x32_bf16 v[236:239], v[248:251], v[252:255], 0\n"
"ds_read_b64_tr_b16 a[214:215], %11 offset:34816\n"
"ds_write_addtid_b32 v137 offset:3392\n"
"v_mfma_f32_16x16x32_bf16 v[232:235], v[244:247], v[252:255], 0\n"
"ds_read_b64_tr_b16 a[216:217], %12 offset:34304\n"
"ds_write_addtid_b32 v138 offset:18560\n"
"v_mfma_f32_16x16x32_bf16 v[232:235], v[248:251], v[240:243], v[232:235]\n"
"ds_read_b64_tr_b16 a[218:219], %12 offset:34816\n"
"ds_write_addtid_b32 v139 offset:20672\n"
"v_mfma_f32_16x16x32_bf16 v[228:231], v[244:247], v[240:243], 0\n"
"ds_read_b64_tr_b16 a[220:221], %13 offset:34304\n"
"ds_write_addtid_b32 v128 offset:1536\n"
"v_mfma_f32_16x16x32_bf16 v[228:231], v[204:207], v[252:255], v[228:231]\n"
"ds_read_b64_tr_b16 a[222:223], %13 offset:34816\n"
"ds_write_addtid_b32 v129 offset:3648\n"
"v_mfma_f32_16x16x32_bf16 v[228:231], v[248:251], v[200:203], v[228:231]\n"
"ds_write_addtid_b32 v130 offset:18816\n"
"ds_write_addtid_b32 v131 offset:20928\n"
"v_mfma_f32_16x16x32_bf16 v[224:227], v[204:207], v[240:243], 0\n"
"ds_write_addtid_b32 v112 offset:1792\n"
"ds_write_addtid_b32 v113 offset:3904\n"
"v_mfma_f32_16x16x32_bf16 v[224:227], v[244:247], v[200:203], v[224:227]\n"
"ds_write_addtid_b32 v114 offset:19072\n"
"ds_write_addtid_b32 v115 offset:21184\n"
"v_mfma_f32_16x16x32_bf16 v[224:227], v[196:199], v[252:255], v[224:227]\n"
"buffer_store_dwordx4 v[208:211], %4, %14, %21 offen offset:2048 sc1\n"
"ds_read_b128 v[220:223], %1 offset:2048\n"
"v_mfma_f32_16x16x32_bf16 v[224:227], v[248:251], v[192:195], v[224:227]\n"
"ds_read_b128 v[216:219], %0 offset:1536\n"
"ds_read_b128 v[212:215], %1 offset:2624\n"
"v_mfma_f32_16x16x32_bf16 v[236:239], v[144:147], v[148:151], v[236:239]\n"
"ds_read_b128 v[188:191], %0 offset:1088\n"
"ds_read_b128 v[184:187], %1 offset:2560\n"
"s_waitcnt lgkmcnt(5)\n"
"s_barrier\n"
"v_mfma_f32_16x16x32_bf16 v[232:235], v[140:143], v[148:151], v[232:235]\n"
"ds_read_b128 v[180:183], %0 offset:1024\n"
"ds_read_b64 v[70:71], %2 offset:0\n"
"v_mfma_f32_16x16x32_bf16 v[232:235], v[144:147], v[132:135], v[232:235]\n"
"ds_read_b128 v[176:179], %1 offset:3136\n"
"ds_read_b64 v[72:73], %2 offset:4288\n"
"v_mfma_f32_16x16x32_bf16 v[228:231], v[140:143], v[132:135], v[228:231]\n"
"s_waitcnt vmcnt(7)\n"
"ds_read_b64_tr_b16 a[224:225], %10 offset:51456\n"
"v_mfma_f32_16x16x32_bf16 v[228:231], v[100:103], v[148:151], v[228:231]\n"
"ds_read_b64_tr_b16 a[226:227], %10 offset:51968\n"
"ds_write_addtid_b32 v236 offset:34304\n"
"v_mfma_f32_16x16x32_bf16 v[228:231], v[144:147], v[96:99], v[228:231]\n"
"ds_read_b64_tr_b16 a[228:229], %11 offset:51456\n"
"ds_write_addtid_b32 v237 offset:36416\n"
"v_mfma_f32_16x16x32_bf16 v[224:227], v[100:103], v[132:135], v[224:227]\n"
"ds_read_b64_tr_b16 a[230:231], %11 offset:51968\n"
"ds_read_b64 v[74:75], %2 offset:8576\n"
"v_mfma_f32_16x16x32_bf16 v[224:227], v[140:143], v[96:99], v[224:227]\n"
"ds_read_b64_tr_b16 a[232:233], %12 offset:51456\n"
"ds_read_b64 v[76:77], %2 offset:12864\n"
"v_mfma_f32_16x16x32_bf16 v[224:227], v[92:95], v[148:151], v[224:227]\n"
"ds_read_b64_tr_b16 a[234:235], %12 offset:51968\n"
"ds_read_b64 v[78:79], %2 offset:512\n"
"v_mfma_f32_16x16x32_bf16 v[224:227], v[144:147], v[88:91], v[224:227]\n"
"ds_read_b64_tr_b16 a[236:237], %13 offset:51456\n"
"buffer_store_dwordx4 v[208:211], %4, %14, %21 offen offset:3072 sc1\n"
"ds_read_b64 v[104:105], %2 offset:4800\n"
"v_mfma_f32_16x16x32_bf16 v[168:171], v[204:207], v[200:203], 0\n"
"ds_read_b64_tr_b16 a[238:239], %13 offset:51968\n"
"ds_read_b64 v[106:107], %2 offset:9088\n"
"v_mfma_f32_16x16x32_bf16 v[168:171], v[196:199], v[240:243], v[168:171]\n"
"ds_write_addtid_b32 v238 offset:51584\n"
"ds_write_addtid_b32 v239 offset:53696\n"
"v_mfma_f32_16x16x32_bf16 v[168:171], v[244:247], v[192:195], v[168:171]\n"
"ds_write_addtid_b32 v232 offset:34560\n"
"ds_write_addtid_b32 v233 offset:36672\n"
"v_mfma_f32_16x16x32_bf16 v[168:171], v[84:87], v[252:255], v[168:171]\n"
"ds_write_addtid_b32 v234 offset:51840\n"
"ds_write_addtid_b32 v235 offset:53952\n"
"s_waitcnt lgkmcnt(14)\n"
"v_add_f32 v72, v72, v74\n"
"v_mfma_f32_16x16x32_bf16 v[168:171], v[248:251], v[80:83], v[168:171]\n"
"ds_write_addtid_b32 v228 offset:34816\n"
"ds_write_addtid_b32 v229 offset:36928\n"
"v_add_f32 v73, v73, v75\n"
"v_mfma_f32_16x16x32_bf16 v[168:171], v[100:103], v[96:99], v[168:171]\n"
"ds_write_addtid_b32 v230 offset:52096\n"
"ds_write_addtid_b32 v231 offset:54208\n"
"v_mfma_f32_16x16x32_bf16 v[168:171], v[92:95], v[132:135], v[168:171]\n"
"ds_write_addtid_b32 v224 offset:35072\n"
"ds_write_addtid_b32 v225 offset:37184\n"
"v_mfma_f32_16x16x32_bf16 v[168:171], v[140:143], v[88:91], v[168:171]\n"
"ds_write_addtid_b32 v226 offset:52352\n"
"ds_write_addtid_b32 v227 offset:54464\n"
"v_mfma_f32_16x16x32_bf16 v[168:171], v[220:223], v[148:151], v[168:171]\n"
"ds_read_b128 v[236:239], %0 offset:576\n"
"ds_read_b64 v[74:75], %2 offset:13376\n"
"v_mfma_f32_16x16x32_bf16 v[168:171], v[144:147], v[216:219], v[168:171]\n"
"s_waitcnt vmcnt(4)\n"
"ds_read_b64_tr_b16 a[240:241], v172 offset:0\n"
"v_mfma_f32_16x16x32_bf16 v[232:235], v[196:199], v[200:203], 0\n"
"ds_read_b64_tr_b16 a[242:243], v172 offset:512\n"
"s_waitcnt lgkmcnt(4)\n"
"s_barrier\n"
"v_mfma_f32_16x16x32_bf16 v[232:235], v[204:207], v[192:195], v[232:235]\n"
"ds_read_b64_tr_b16 a[244:245], v173 offset:0\n"
"ds_read_b64 v[108:109], %2 offset:34304\n"
"v_mfma_f32_16x16x32_bf16 v[232:235], v[84:87], v[240:243], v[232:235]\n"
"ds_read_b64_tr_b16 a[246:247], v173 offset:512\n"
"ds_read_b64 v[110:111], %2 offset:38592\n"
"v_mfma_f32_16x16x32_bf16 v[232:235], v[244:247], v[80:83], v[232:235]\n"
"ds_read_b64_tr_b16 a[248:249], v174 offset:0\n"
"ds_read_b64 v[112:113], %2 offset:42880\n"
"v_mfma_f32_16x16x32_bf16 v[232:235], v[212:215], v[252:255], v[232:235]\n"
"ds_read_b64_tr_b16 a[250:251], v174 offset:512\n"
"ds_read_b64 v[114:115], %2 offset:47168\n"
"v_add_f32 v104, v104, v106\n"
"v_mfma_f32_16x16x32_bf16 v[232:235], v[248:251], v[188:191], v[232:235]\n"
"ds_read_b64_tr_b16 a[252:253], v175 offset:0\n"
"ds_read_b64 v[116:117], %2 offset:34816\n"
"v_add_f32 v105, v105, v107\n"
"v_mfma_f32_16x16x32_bf16 v[232:235], v[92:95], v[96:99], v[232:235]\n"
"ds_read_b64_tr_b16 a[254:255], v175 offset:512\n"
"ds_read_b64 v[106:107], %2 offset:39104\n"
"v_mfma_f32_16x16x32_bf16 v[232:235], v[100:103], v[88:91], v[232:235]\n"
"ds_write_addtid_b32 v168 offset:35328\n"
"ds_read_b64 v[118:119], %2 offset:43392\n"
"v_mfma_f32_16x16x32_bf16 v[232:235], v[220:223], v[132:135], v[232:235]\n"
"ds_write_addtid_b32 v169 offset:37440\n"
"ds_read_b64 v[120:121], %2 offset:47680\n"
"v_mfma_f32_16x16x32_bf16 v[232:235], v[140:143], v[216:219], v[232:235]\n"
"ds_write_addtid_b32 v170 offset:52608\n"
"ds_read_b64 v[122:123], %2 offset:1024\n"
"s_waitcnt lgkmcnt(15)\n"
"v_add_f32 v70, v70, v108\n"
"v_mfma_f32_16x16x32_bf16 v[232:235], v[184:187], v[148:151], v[232:235]\n"
"ds_write_addtid_b32 v171 offset:54720\n"
"ds_read_b64 v[124:125], %2 offset:5312\n"
"v_add_f32 v71, v71, v109\n"
"s_waitcnt lgkmcnt(15)\n"
"v_add_f32 v70, v70, v110\n"
"v_mfma_f32_16x16x32_bf16 v[232:235], v[144:147], v[180:183], v[232:235]\n"
"ds_read_b128 v[228:231], %1 offset:3072\n"
"ds_read_b64 v[108:109], %2 offset:9600\n"
"v_add_f32 v71, v71, v111\n"
"v_mfma_f32_16x16x32_bf16 v[224:227], v[196:199], v[192:195], 0\n"
"ds_read_b128 v[208:211], %0 offset:512\n"
"ds_read_b64 v[110:111], %2 offset:13888\n"
"s_waitcnt lgkmcnt(15)\n"
"v_add_f32 v76, v76, v112\n"
"v_mfma_f32_16x16x32_bf16 v[224:227], v[84:87], v[200:203], v[224:227]\n"
"ds_read_b128 v[172:175], %1 offset:3648\n"
"ds_read_b64 v[126:127], %2 offset:1536\n"
"v_add_f32 v77, v77, v113\n"
"v_mfma_f32_16x16x32_bf16 v[224:227], v[204:207], v[80:83], v[224:227]\n"
"ds_read_b128 v[168:171], %0 offset:64\n"
"v_add_f32 v76, v76, v114\n"
"v_add_f32 v77, v77, v115\n"
"v_mfma_f32_16x16x32_bf16 v[224:227], v[212:215], v[240:243], v[224:227]\n"
"ds_write_addtid_b32 v232 offset:35584\n"
"v_cvt_pk_bf16_f32 v165, v76, %24\n"
"v_cvt_pk_bf16_f32 v167, v77, %24\n"
"v_mfma_f32_16x16x32_bf16 v[224:227], v[244:247], v[188:191], v[224:227]\n"
"ds_write_addtid_b32 v233 offset:37696\n"
"v_cvt_pk_bf16_f32 v164, v70, v72\n"
"v_cvt_pk_bf16_f32 v166, v71, v73\n"
"v_mfma_f32_16x16x32_bf16 v[224:227], v[176:179], v[252:255], v[224:227]\n"
"ds_write_addtid_b32 v234 offset:52864\n"
"buffer_store_dwordx4 v[164:167], %5, %14, %23 offen sc1\n"
"s_waitcnt lgkmcnt(15)\n"
"v_add_f32 v78, v78, v116\n"
"v_mfma_f32_16x16x32_bf16 v[224:227], v[248:251], v[236:239], v[224:227]\n"
"ds_write_addtid_b32 v235 offset:54976\n"
"s_waitcnt lgkmcnt(9)\n"
"v_add_f32 v124, v124, v108\n"
"v_add_f32 v79, v79, v117\n"
"v_add_f32 v78, v78, v106\n"
"v_mfma_f32_16x16x32_bf16 v[224:227], v[92:95], v[88:91], v[224:227]\n"
"ds_read_b128 v[232:235], %1 offset:3584\n"
"v_add_f32 v125, v125, v109\n"
"v_add_f32 v79, v79, v107\n"
"v_mfma_f32_16x16x32_bf16 v[224:227], v[220:223], v[96:99], v[224:227]\n"
"ds_read_b128 v[164:167], %0 offset:0\n"
"s_add_u32 s98, %23, 4096\n"
"v_add_f32 v74, v74, v118\n"
"v_mfma_f32_16x16x32_bf16 v[224:227], v[100:103], v[216:219], v[224:227]\n"
"s_waitcnt lgkmcnt(2)\n"
"s_barrier\n"
"v_add_f32 v75, v75, v119\n"
"v_mfma_f32_16x16x32_bf16 v[224:227], v[184:187], v[132:135], v[224:227]\n"
"ds_read_b64 v[70:71], %2 offset:35328\n"
"v_add_f32 v74, v74, v120\n"
"v_add_f32 v75, v75, v121\n"
"v_mfma_f32_16x16x32_bf16 v[224:227], v[140:143], v[180:183], v[224:227]\n"
"ds_read_b64 v[72:73], %2 offset:39616\n"
"v_cvt_pk_bf16_f32 v161, v74, %24\n"
"v_cvt_pk_bf16_f32 v163, v75, %24\n"
"v_mfma_f32_16x16x32_bf16 v[224:227], v[228:231], v[148:151], v[224:227]\n"
"ds_read_b64 v[76:77], %2 offset:43904\n"
"v_cvt_pk_bf16_f32 v160, v78, v104\n"
"v_cvt_pk_bf16_f32 v162, v79, v105\n"
"v_mfma_f32_16x16x32_bf16 v[224:227], v[144:147], v[208:211], v[224:227]\n"
"ds_read_b64 v[74:75], %2 offset:48192\n"
"buffer_store_dwordx4 v[160:163], %5, %14, s98 offen sc1\n"
"v_mfma_f32_16x16x32_bf16 v[156:159], v[84:87], v[192:195], 0\n"
"ds_read_b64 v[78:79], %2 offset:5824\n"
"s_add_u32 s98, %23, 1769472\n"
"v_mfma_f32_16x16x32_bf16 v[156:159], v[196:199], v[80:83], v[156:159]\n"
"ds_read_b64 v[84:85], %2 offset:10112\n"
"v_mfma_f32_16x16x32_bf16 v[156:159], v[212:215], v[200:203], v[156:159]\n"
"ds_read_b64 v[80:81], %2 offset:14400\n"
"v_mfma_f32_16x16x32_bf16 v[156:159], v[204:207], v[188:191], v[156:159]\n"
"ds_write_addtid_b32 v224 offset:35840\n"
"v_mfma_f32_16x16x32_bf16 v[156:159], v[176:179], v[240:243], v[156:159]\n"
"ds_write_addtid_b32 v225 offset:37952\n"
"s_waitcnt lgkmcnt(8)\n"
"v_add_f32 v122, v122, v70\n"
"v_mfma_f32_16x16x32_bf16 v[156:159], v[244:247], v[236:239], v[156:159]\n"
"ds_write_addtid_b32 v226 offset:53120\n"
"v_add_f32 v123, v123, v71\n"
"s_waitcnt lgkmcnt(8)\n"
"v_add_f32 v122, v122, v72\n"
"v_mfma_f32_16x16x32_bf16 v[156:159], v[172:175], v[252:255], v[156:159]\n"
"ds_write_addtid_b32 v227 offset:55232\n"
"v_add_f32 v123, v123, v73\n"
"s_waitcnt lgkmcnt(8)\n"
"v_add_f32 v110, v110, v76\n"
"v_mfma_f32_16x16x32_bf16 v[156:159], v[248:251], v[168:171], v[156:159]\n"
"s_waitcnt lgkmcnt(5)\n"
"v_add_f32 v78, v78, v84\n"
"v_mfma_f32_16x16x32_bf16 v[156:159], v[220:223], v[88:91], v[156:159]\n"
"v_add_f32 v79, v79, v85\n"
"v_add_f32 v111, v111, v77\n"
"v_mfma_f32_16x16x32_bf16 v[156:159], v[92:95], v[216:219], v[156:159]\n"
"v_add_f32 v110, v110, v74\n"
"v_add_f32 v111, v111, v75\n"
"v_mfma_f32_16x16x32_bf16 v[156:159], v[184:187], v[96:99], v[156:159]\n"
"v_cvt_pk_bf16_f32 v253, v110, %24\n"
"v_cvt_pk_bf16_f32 v255, v111, %24\n"
"v_mfma_f32_16x16x32_bf16 v[156:159], v[100:103], v[180:183], v[156:159]\n"
"v_cvt_pk_bf16_f32 v252, v122, v124\n"
"v_cvt_pk_bf16_f32 v254, v123, v125\n"
"v_mfma_f32_16x16x32_bf16 v[156:159], v[228:231], v[132:135], v[156:159]\n"
"buffer_store_dwordx4 v[252:255], %5, %14, s98 offen sc1\n"
"s_add_u32 s98, %23, 1773568\n"
"v_mfma_f32_16x16x32_bf16 v[156:159], v[140:143], v[208:211], v[156:159]\n"
"v_mfma_f32_16x16x32_bf16 v[156:159], v[232:235], v[148:151], v[156:159]\n"
"v_mfma_f32_16x16x32_bf16 v[156:159], v[144:147], v[164:167], v[156:159]\n"
"s_nop 7\n"
"ds_write_addtid_b32 v156 offset:36096\n"
"ds_write_addtid_b32 v157 offset:38208\n"
"ds_write_addtid_b32 v158 offset:53376\n"
"ds_write_addtid_b32 v159 offset:55488\n"
"s_waitcnt lgkmcnt(0)\n"
"s_barrier\n"
"ds_read_b64 v[70:71], %2 offset:35840\n"
"ds_read_b64 v[72:73], %2 offset:40128\n"
"ds_read_b64 v[74:75], %2 offset:44416\n"
"ds_read_b64 v[76:77], %2 offset:48704\n"
"s_waitcnt lgkmcnt(3)\n"
"v_add_f32 v126, v126, v70\n"
"v_add_f32 v127, v127, v71\n"
"s_waitcnt lgkmcnt(2)\n"
"v_add_f32 v126, v126, v72\n"
"v_add_f32 v127, v127, v73\n"
"s_waitcnt lgkmcnt(1)\n"
"v_add_f32 v80, v80, v74\n"
"v_add_f32 v81, v81, v75\n"
"s_waitcnt lgkmcnt(0)\n"
"v_add_f32 v80, v80, v76\n"
"v_add_f32 v81, v81, v77\n"
"v_cvt_pk_bf16_f32 v253, v80, %24\n"
"v_cvt_pk_bf16_f32 v255, v81, %24\n"
"v_cvt_pk_bf16_f32 v252, v126, v78\n"
"v_cvt_pk_bf16_f32 v254, v127, v79\n"
"buffer_store_dwordx4 v[252:255], %5, %14, s98 offen sc1\n"
"s_barrier\n"
        : "+v"(ap0), "+v"(bp0) : "v"(pr0), "v"(pr0), "v"(l16), "v"(l16), "v"(vb1[0]), "v"(vb1[1]), "v"(vb1[2]), "v"(vb1[3]), "v"(rb[0]), "v"(rb[1]), "v"(rb[2]), "v"(rb[3]),
          "s"(sw), "s"(sb1), "s"(sda), "s"(sdb), "s"(swa), "s"(b1_base), "s"(rot1), "s"(smk), "s"(smk), "s"(sao), "s"(tagu)
        : "memory", "scc", "s97", "s98",
          C70UP("v"));
    }
  } else {
    asm volatile(".Lk_sp:" ::: "memory");
    constexpr int E0 = 108 * PFL - 2304, E1 = E0 + PF1_G * 2048, E2 = 9216 - 96 * PFL3;
    touch_a(pfo1, sb1, w1c0);
    __builtin_amdgcn_s_barrier();
    set_m0(sink);
    touch_l(((32 - idx) * 256 + tid - 12) * 128, mk_srd((const void*)pc, plen), 0);
    warm(sink, sb1, w1c0, E0, E1, (idx - 24) * 256 + tid, 2048);
    if (g1_on) {
      marks(ME0 + (blockIdx.x - 192) * 4 + wid, ME1, 96);
      sfor<32>([&](auto I) { ride(I, wbase, IC<I / 4 * 4096>{}); });
      VMCNT(0);
      sfor<32>([&](auto I) { q_frag<32 + I, I / 4 * 4096>(rb[I & 3] + (wbase - (ldsT + CW + wid * WA))); });
      asm volatile("s_waitcnt lgkmcnt(0)" ::: "memory");
    } else {
      warm(sink, sb1, w1c0, E1, E2, (idx - 27) * 256 + tid, 1280);
      marks(ME1 + (blockIdx.x - 216) * 4 + wid, NMARK, 160);
    }
  }

  asm volatile("" : "+v"(bias1[0]), "+v"(bias1[1]), "+v"(bias2[0]), "+v"(bias2[1]));
  unsigned tid_ = tid;
  pin1(tid_);
  {
  const int tid = tid_ & 255, lane = tid & 63, g = lane >> 4, r16 = lane & 15, q4 = r16 >> 2, p4 = lane & 3, ll = lane & 31;
  const unsigned w2o = (kq * 1152 + (idx - 27) * 32 + (tid >> 3)) * ROWB + kh * 2048 + (tid & 7) * 256;
  u32x3 hb[3];
  unsigned in[38], sc[9];
  const unsigned wv = blockIdx.x * 4 + wid;
  auto other = [&](unsigned p) { return p + (p >= kq); };
  auto peer = [&](unsigned q) { return ((blockIdx.x & ~6u) | (q << 1)) * 4 + wid; };
  auto setup = [&](auto KK) {
    constexpr int K = KK;
    if constexpr (K == 0) {
      const unsigned ar = dq1 * 12288 + l16;
      in[0] = l16; in[1] = ar + (1 - kh) * 6144; in[2] = in[1] + 3072; in[3] = ar + kh * 6144; in[4] = in[3] + 3072; in[5] = g1_on ? l16 : OOB;
      pin4(in); pin4(in + 2);
    } else if constexpr (K == 1) {
      for (int k = 0; k < 4; ++k) in[6 + k] = (16 * (lane >> 5) + 4 * k + ((lane >> 3) & 3)) * ROWB + (((lane & 7) ^ (2 * ((g & 1) | ((k & 1) << 1)))) << 4) - k * 1024;
      pin4(in + 6);
    } else if constexpr (K == 2) {
      for (int k = 0; k < 4; ++k) in[10 + k] = rb[k] + (wbase - (ldsT + CW + wid * WA));
      pin4(in + 10);
    } else if constexpr (K == 3) {
      for (int k = 1; k < 3; ++k) in[14 + k] = ldsT + (wid ^ k) * 40960 + 3872 * p4 + 32 * (p4 & 2) + (4 * q4 + g) * 16;
      pin1(in[15]); pin1(in[16]);
      undef1(in[14]); undef1(in[17]);
    } else if constexpr (K == 4) {
      in[18] = ((nt2a * 128 + wid * 32 + 16 * (g & 1) + r16) * D_ + 8 * (g >> 1)) * 2;
      pin1(in[18]);
    } else if constexpr (K == 5) {
      in[19] = tid < 8 ? SY_CNT + ((8 + tid) * 16 + xc) * 256 : OOB; in[20] = SY_CNT + ((8 + xc) * 16 + (lane & 7)) * 256;
      pin1(in[19]); pin1(in[20]);
    } else if constexpr (K == 6) {
      in[21] = (WS_XF + peer(other(lane)) * 128 + kq * 8) | (lane < 3 ? 0u : OOB);
      pin1(in[21]);
    } else if constexpr (K == 7) {
      in[22] = WS_XF + wv * 128 + other(min(p4, 2)) * 8; in[23] = bias1[0] << 16; in[24] = bias1[1] << 16;
      pin4(in + 21);
    } else if constexpr (K == 8) {
      in[25] = kq ? 0u : bias2[0].x; in[26] = kq ? 0u : bias2[0].y; in[27] = kq ? 0u : bias2[1].x; in[28] = kq ? 0u : bias2[1].y;
      pin4(in + 25);
    } else if constexpr (K == 9) {
      const unsigned b = (0x170u >> lane) & 1u, q = lane > 6 ? kq : other(lane - 1 - 3 * b);
      const unsigned t = ((mq & 1 ? 0x82571460u : 0x58247136u) >> (4 * q + 16 * b)) & 15u;
      in[36] = (9 * mq + (lane ? t : 3 * (mq & 1))) * 1024;
      pin1(in[36]);
    } else if constexpr (K == 10) {
      const unsigned q = lane ? other(lane - 1) : wid;
      in[37] = WS_X + peer(q) * 9216 + ((kq ^ q) - 1) * 3072 + (lane ? 1024 : 0);
      pin1(in[37]);
    } else if constexpr (K == 11) {
      sc[0] = ldsT + wid * 40960; sc[1] = yout; sc[2] = WS_AG + (kh ^ 1) * AGH + ((blockIdx.x >> 1) * 4 + (wid ^ 3)) * 4096; sc[3] = nt2b * 256 + (~wid & 2) * 64 + (kq * 1152 + wid * 32) * ROWB;
      pins(sc[0]); pins(sc[1]); pins(sc[2]); pins(sc[3]);
    } else if constexpr (K == 12) {
      sc[4] = gout; sc[5] = WS_X + wv * 9216;
      sc[6] = rot1 << 8 | mq << 5 | ((idx - 27) >> 31 & 1) | (wid == kq ? OOB : 0u);
      pins(sc[4]); pins(sc[5]); pins(sc[6]);
    } else if constexpr (K == 13) {
      sc[7] = WS_HF + (kh * 32 + wid) * 36864; sc[8] = WS_GF + (36 * kq + wid) * 36864;
      pins(sc[7]); pins(sc[8]);
    }
    SB0;
  };
  {
    unsigned ain[3], ago = (((tid >> 2) & 31) * 2 + (tid >> 7)) * AGP + p4 * 16;
#pragma unroll
    for (int j = 0; j < 3; ++j) ain[j] = WS_AG + (tb >> 7) * AGH + j * 64 * AGP + (tb & 127) * 64;
    asm volatile("" : "={v[240:255]}"(tg));
    f32x2 xf[9];
    unsigned aw, ar, hw, hr0, hr1, fo, fo2, fs;
    auto prep = [&](auto K) {
      if constexpr (K < 3) {
        pin3(x3[K]);
#pragma unroll
        for (int i = 0; i < 3; ++i) { xf[3 * K + i] = unpk(x3[K][i]); pin2(xf[3 * K + i]); }
      } else if constexpr (K == 3) {
        aw = ldsT + p4 * (G_WS + 32) + N_A + (tid >> 2) * 16; ar = ldsT + wid * (G_WS + 32) + N_A + ll * 16 + (lane >> 5) * 8;
        pin1(aw); pin1(ar);
      } else if constexpr (K == 4) {
        hw = wbase + N_H + (wid & 1) * 128 + (lane >> 5) * 1216 + ll * 12; hr0 = ldsT + N_H + (q4 >> 1) * (G_WS + 128) + (q4 & 1) * 1216 + wid * 128 + g * 32 + p4 * 8; hr1 = hr0 + 2 * G_WS;
        pin1(hw); pin1(hr0); pin1(hr1);
      } else if constexpr (K == 5) {
        fo = g * 1024 + r16 * 16; fo2 = fo | (wid ? OOB : 0u); fs = WS_HF + ((tb >> 2) * 36 + wid * 4) * 1024 + (tb & 3) * 256;
        pin1(fo); pin1(fo2); pins(fs);
      } else setup(IC<K - 6>{});
      SB0;
    };
    sfor<NA + 1>([&](auto I) {
      if constexpr (I == GATHER_AT) {
        t_ask<0>(tg, ago, sw, ain, idx);
      }
      if constexpr (I < NA) ride2(I);
      sfor<(I + 1) * 20 / (NA + 1) - I * 20 / (NA + 1)>([&](auto J) { prep(IC<I * 20 / (NA + 1) + J>{}); });
    });
    t_ask<1>(tg, ago, sw, ain, idx);
    if (!cv_on) VMCNT(0);
    t_take<3, NA - GATHER_AT>(tg, ago, sw, ain, tagu);
    SB0;
#pragma unroll
    for (int j = 0; j < 3; ++j) ldsw(aw + j * 1024, u32x4{tg[4 * j], tg[4 * j + 1], tg[4 * j + 2], tg[4 * j + 3]});
    SB0;
    if (!g1_on) { set_m0(sink); early(w2o, 0); }
    LDS_BARRIER();
    f32x2 h[9], v[9];
    u32x2 ag[6];
#pragma unroll
    for (int j = 0; j < 6; ++j) ag[j] = ldsr<u32x2>(ar + j * 512);
    SB0;
    sfor<3>([&](auto j) {
      const u32x2 a = ag[2 * j], b = ag[2 * j + 1];
      h[3 * j] = xf[3 * j] + unpk(a.x); h[3 * j + 1] = xf[3 * j + 1] + f32x2{bflo(a.y), bflo(b.x)}; h[3 * j + 2] = xf[3 * j + 2] + f32x2{bfhi(b.x), bflo(b.y)};
    });
    float mean, var;
    ln_stats(h, mean, var);
    float inv = __builtin_amdgcn_rsqf(var + 1e-5f), c = -mean * inv;
#pragma unroll
    for (int i = 0; i < 9; ++i) v[i] = __builtin_elementwise_fma(h[i], f32x2{inv, inv}, f32x2{c, c});
    ln_stats(v, mean, var);
    inv = __builtin_amdgcn_rsqf(var + 1e-5f);
    c = -mean * inv;
#pragma unroll
    for (int i = 0; i < 9; ++i) v[i] = __builtin_elementwise_fma(v[i], f32x2{inv, inv}, f32x2{c, c});
    SB0;
    unsigned o[9];
    sfor<4>([&](auto J) {
      if constexpr (J < 3) { o[3 * J] = pkv(v[3 * J]); o[3 * J + 1] = pkv(v[3 * J + 1]); o[3 * J + 2] = pkv(v[3 * J + 2]); }
      SB0;
      if constexpr (J > 0) put3<96 * J - 96>(hw, o + 3 * J - 3);
    });
    LDS_BARRIER();
    {
      u32x4 f[3];
#pragma unroll
      for (int j = 0; j < 3; ++j) f[j] = cat(trd(hr0 + j * 512), trd(hr1 + j * 512));
      SB0;
#pragma unroll
      for (int i = 0; i < 9; ++i) hb[i / 3][i % 3] = pkv(h[i]);
      SB0;
#pragma unroll
      for (int j = 0; j < 3; ++j) { f[j].y = min(f[j].y, 0xffbfffffu); f[j].w = min(f[j].w, 0xffbfffffu); bstore_s(f[j], j < 2 ? fo : fo2, sw, fs + j * 16384); }
      sfor<3>([&](auto J) { bstore3<384 * J>(hb[J], x3o, sw, WS_H); });
    }
    if (g1_on) sfor<32 - NA>([&](auto I) { ride2(IC<NA + I>{}); });
    if (blockIdx.x == 0 && tid == 0) __builtin_amdgcn_raw_buffer_store_b64(u32x2{tagx, tagy}, rw, SY_EPOCH, 0, SC1);
  }

  undef1(in[34]); undef1(in[35]);
  if (!g1_on) {
    in[34] = r16 < 9 && g < 2 ? sc[8] + 4 + r16 * (4 * 36864) + __builtin_amdgcn_ds_bpermute(4 * g, in[36]) : OOB;
    in[35] = w2o + PFM * 160 * ROWB;
    set_m0(sink);
    sfor<PFM>([&](auto I) { early(w2o, 1 + I); });
    VMCNT(PFM);
    __builtin_amdgcn_s_barrier();
    __builtin_amdgcn_raw_ptr_buffer_atomic_add_i32(1, rw, in[19], 0, SC1);
  }
  {
    u32x16 v0, v1, v2;
    asm volatile("" : "=v"(v2));
    for (int k = 29; k < 34; ++k) undef1(in[k]);
#pragma unroll
    for (int k = 0; k < 16; ++k) { v0[k] = in[k]; v1[k] = in[16 + k]; if (k < 6) v2[k] = in[32 + k]; }
    const u32x2 tg2 = {tagx, tagy};
    u32x4 s2 = {sc[1], sc[2], sc[3], sc[4]};
    u32x4 s3 = {sc[5], sc[6], sc[7], sc[8]};
    const i32x4 so = mk_srd(out, SZ);
#define SCLOB "s48", "s49", C10("s", 5), "s60", "s61", "s62", "s63", "s64", "s65", "s66", "s68", "s69", "s70", "s71", "s72", "s73", "s74", "s75", "s76", "s84", "s85", "s86", "s87", "s92", "s93", "s94", "s95", "s97", "s98"
#define VCLOB "v48", "v49", C50UP("v"), C10("a", ), C10("a", 1), C10("a", 2), C10("a", 3), C10("a", 4), C50UP("a")
    asm volatile("s_nop 4\n"
"s_bitcmp1_b32 s89, 0\n"
"s_cbranch_scc0 .Lgs_spare\n"
"s_waitcnt vmcnt(34)\n"
"ds_read_b64_tr_b16 a[96:97], v10 offset:0\n"
"s_bfe_u32 s65, s89, 0x20008\n"
"s_xor_b32 s66, s65, 0\n"
"ds_read_b64_tr_b16 a[98:99], v10 offset:512\n"
"s_mul_i32 s66, s66, 147456\n"
"s_add_u32 s48, s90, s66\n"
"ds_read_b64_tr_b16 a[100:101], v11 offset:0\n"
"s_xor_b32 s66, s65, 1\n"
"s_mul_i32 s66, s66, 147456\n"
"ds_read_b64_tr_b16 a[102:103], v11 offset:512\n"
"s_add_u32 s49, s90, s66\n"
"s_xor_b32 s66, s65, 2\n"
"ds_read_b64_tr_b16 a[104:105], v12 offset:0\n"
"s_mul_i32 s66, s66, 147456\n"
"s_add_u32 s50, s90, s66\n"
"ds_read_b64_tr_b16 a[106:107], v12 offset:512\n"
"s_xor_b32 s66, s65, 3\n"
"s_mul_i32 s66, s66, 147456\n"
"ds_read_b64_tr_b16 a[108:109], v13 offset:0\n"
"s_add_u32 s51, s90, s66\n"
"s_add_u32 s52, s48, 589824\n"
"ds_read_b64_tr_b16 a[110:111], v13 offset:512\n"
"s_add_u32 s53, s49, 589824\n"
"s_add_u32 s54, s50, 589824\n"
"s_waitcnt vmcnt(30)\n"
"ds_read_b64_tr_b16 a[112:113], v10 offset:4096\n"
"s_add_u32 s55, s51, 589824\n"
"ds_read_b64_tr_b16 a[114:115], v10 offset:4608\n"
"ds_read_b64_tr_b16 a[116:117], v11 offset:4096\n"
"ds_read_b64_tr_b16 a[118:119], v11 offset:4608\n"
"ds_read_b64_tr_b16 a[120:121], v12 offset:4096\n"
"ds_read_b64_tr_b16 a[122:123], v12 offset:4608\n"
"ds_read_b64_tr_b16 a[124:125], v13 offset:4096\n"
"ds_read_b64_tr_b16 a[126:127], v13 offset:4608\n"
"s_waitcnt vmcnt(26)\n"
"ds_read_b64_tr_b16 a[64:65], v10 offset:8192\n"
"ds_read_b64_tr_b16 a[66:67], v10 offset:8704\n"
"ds_read_b64_tr_b16 a[68:69], v11 offset:8192\n"
"ds_read_b64_tr_b16 a[70:71], v11 offset:8704\n"
"ds_read_b64_tr_b16 a[72:73], v12 offset:8192\n"
"ds_read_b64_tr_b16 a[74:75], v12 offset:8704\n"
"ds_read_b64_tr_b16 a[76:77], v13 offset:8192\n"
"ds_read_b64_tr_b16 a[78:79], v13 offset:8704\n"
"s_waitcnt vmcnt(22)\n"
"ds_read_b64_tr_b16 a[80:81], v10 offset:12288\n"
"ds_read_b64_tr_b16 a[82:83], v10 offset:12800\n"
"ds_read_b64_tr_b16 a[84:85], v11 offset:12288\n"
"ds_read_b64_tr_b16 a[86:87], v11 offset:12800\n"
"ds_read_b64_tr_b16 a[88:89], v12 offset:12288\n"
"ds_read_b64_tr_b16 a[90:91], v12 offset:12800\n"
"ds_read_b64_tr_b16 a[92:93], v13 offset:12288\n"
"ds_read_b64_tr_b16 a[94:95], v13 offset:12800\n"
"s_mov_b64 vcc, 0\n"
"s_mov_b64 exec, 0\n"
"s_cmp_lt_u32 s77, 40960\n"
"s_cbranch_scc1 .Lgs_pad0\n"
"s_cmp_lt_u32 s77, 81920\n"
"s_cbranch_scc1 .Lgs_pad1\n"
"s_cmp_lt_u32 s77, 122880\n"
"s_cbranch_scc1 .Lgs_pad2\n"
"s_branch .Lgs_pad3\n"
".p2align 12\n"
".Lgs_al:\n"
"s_mov_b64 exec, -1\n"
"s_cbranch_vccnz .Lgs_al2\n"
"s_sleep 3\n"
"buffer_load_dwordx4 v[252:255], v1, s[36:39], s48 offen offset:0 sc1\n"
"v_readlane_b32 s84, v37, 0\n"
"v_readlane_b32 s85, v37, 1\n"
"buffer_load_dwordx4 v[248:251], v1, s[36:39], s49 offen offset:0 sc1\n"
"v_readlane_b32 s86, v37, 2\n"
"v_readlane_b32 s87, v37, 3\n"
"buffer_load_dwordx4 v[244:247], v1, s[36:39], s50 offen offset:0 sc1\n"
"v_readlane_b32 s68, v36, 0\n"
"v_readlane_b32 s69, v36, 1\n"
"v_readlane_b32 s70, v36, 2\n"
"buffer_load_dwordx4 v[240:243], v1, s[36:39], s51 offen offset:0 sc1\n"
"v_readlane_b32 s71, v36, 3\n"
"v_readlane_b32 s72, v36, 4\n"
"buffer_load_dwordx4 v[236:239], v1, s[36:39], s52 offen offset:0 sc1\n"
"v_readlane_b32 s73, v36, 5\n"
"v_readlane_b32 s74, v36, 6\n"
"buffer_load_dwordx4 v[232:235], v1, s[36:39], s53 offen offset:0 sc1\n"
"v_readlane_b32 s75, v36, 7\n"
"v_readlane_b32 s76, v36, 8\n"
"s_bfe_u32 s65, s89, 0x40004\n"
"buffer_load_dwordx4 v[228:231], v1, s[36:39], s54 offen offset:0 sc1\n"
"s_mul_i32 s66, s65, 147456\n"
"s_add_u32 s56, s91, s66\n"
"buffer_load_dwordx4 v[224:227], v1, s[36:39], s55 offen offset:0 sc1\n"
"s_add_u32 s66, s91, 1327104\n"
"s_add_u32 s57, s56, 147456\n"
"s_add_u32 s58, s57, 147456\n"
"buffer_load_dwordx4 v[220:223], v2, s[36:39], s48 offen offset:0 sc1\n"
"s_add_u32 s59, s58, 147456\n"
"s_cmp_eq_u32 s59, s66\n"
"s_cselect_b32 s59, s91, s59\n"
"buffer_load_dwordx4 v[216:219], v2, s[36:39], s49 offen offset:0 sc1\n"
"s_add_u32 s60, s59, 147456\n"
"s_add_u32 s61, s60, 147456\n"
"buffer_load_dwordx4 v[212:215], v2, s[36:39], s50 offen offset:0 sc1\n"
"s_cmp_eq_u32 s61, s66\n"
"s_cselect_b32 s61, s91, s61\n"
"s_add_u32 s62, s61, 147456\n"
"s_add_u32 s63, s62, 147456\n"
"buffer_load_dwordx4 v[208:211], v2, s[36:39], s51 offen offset:0 sc1\n"
"s_cmp_eq_u32 s63, s66\n"
"s_cselect_b32 s63, s91, s63\n"
"s_add_u32 s64, s63, 147456\n"
"buffer_load_dwordx4 v[204:207], v2, s[36:39], s52 offen offset:0 sc1\n"
"s_lshl_b32 s65, s65, 19\n"
"s_add_u32 s65, s65, s82\n"
"buffer_load_dwordx4 v[200:203], v2, s[36:39], s53 offen offset:0 sc1\n"
"s_add_u32 s66, s82, 4718592\n"
"s_add_u32 s92, s88, 3072\n"
"s_add_u32 s93, s88, 6144\n"
"buffer_load_dwordx4 v[196:199], v2, s[36:39], s54 offen offset:0 sc1\n"
"s_and_b32 s94, s89, 0x7ff00000\n"
"s_xor_b32 s95, s94, 0x7ff00000\n"
"buffer_load_dwordx4 v[192:195], v2, s[36:39], s55 offen offset:0 sc1\n"
"s_mov_b32 s25, 0\n"
"s_mov_b32 s21, 0\n"
"s_mov_b32 s22, 0\n"
"s_waitcnt vmcnt(34)\n"
"ds_read_b64_tr_b16 a[32:33], v10 offset:16384\n"
"ds_read_b64_tr_b16 a[34:35], v10 offset:16896\n"
"ds_read_b64_tr_b16 a[36:37], v11 offset:16384\n"
"ds_read_b64_tr_b16 a[38:39], v11 offset:16896\n"
"ds_read_b64_tr_b16 a[40:41], v12 offset:16384\n"
"ds_read_b64_tr_b16 a[42:43], v12 offset:16896\n"
"ds_read_b64_tr_b16 a[44:45], v13 offset:16384\n"
"ds_read_b64_tr_b16 a[46:47], v13 offset:16896\n"
"s_waitcnt vmcnt(24)\n"
"ds_read_b64_tr_b16 a[48:49], v10 offset:20480\n"
"ds_read_b64_tr_b16 a[50:51], v10 offset:20992\n"
"ds_read_b64_tr_b16 a[52:53], v11 offset:20480\n"
"ds_read_b64_tr_b16 a[54:55], v11 offset:20992\n"
"ds_read_b64_tr_b16 a[56:57], v12 offset:20480\n"
"ds_read_b64_tr_b16 a[58:59], v12 offset:20992\n"
"ds_read_b64_tr_b16 a[60:61], v13 offset:20480\n"
"ds_read_b64_tr_b16 a[62:63], v13 offset:20992\n"
"s_waitcnt vmcnt(20)\n"
"ds_read_b64_tr_b16 a[0:1], v10 offset:24576\n"
"ds_read_b64_tr_b16 a[2:3], v10 offset:25088\n"
"ds_read_b64_tr_b16 a[4:5], v11 offset:24576\n"
"ds_read_b64_tr_b16 a[6:7], v11 offset:25088\n"
"ds_read_b64_tr_b16 a[8:9], v12 offset:24576\n"
"ds_read_b64_tr_b16 a[10:11], v12 offset:25088\n"
"ds_read_b64_tr_b16 a[12:13], v13 offset:24576\n"
"ds_read_b64_tr_b16 a[14:15], v13 offset:25088\n"
"s_waitcnt vmcnt(16)\n"
"ds_read_b64_tr_b16 a[16:17], v10 offset:28672\n"
"ds_read_b64_tr_b16 a[18:19], v10 offset:29184\n"
"ds_read_b64_tr_b16 a[20:21], v11 offset:28672\n"
"ds_read_b64_tr_b16 a[22:23], v11 offset:29184\n"
"ds_read_b64_tr_b16 a[24:25], v12 offset:28672\n"
"ds_read_b64_tr_b16 a[26:27], v12 offset:29184\n"
"ds_read_b64_tr_b16 a[28:29], v13 offset:28672\n"
"ds_read_b64_tr_b16 a[30:31], v13 offset:29184\n"
"s_add_u32 m0, s77, 0\n"
"s_waitcnt vmcnt(13) lgkmcnt(0)\n"
"s_barrier\n"
"s_waitcnt vmcnt(12)\n"
"v_mfma_f32_16x16x32_bf16 v[188:191], a[24:27], v[252:255], 0\n"
"v_max3_u32 v14, v253, v255, v249\n"
"v_mfma_f32_16x16x32_bf16 v[188:191], a[56:59], v[248:251], v[188:191]\n"
"buffer_load_dwordx4 v[184:187], v1, s[36:39], s48 offen offset:1024 sc1\n"
"v_max3_u32 v14, v251, v245, v14\n"
"v_mfma_f32_16x16x32_bf16 v[188:191], a[88:91], v[244:247], v[188:191]\n"
"v_mov_b32 v17, 1\n"
"buffer_atomic_add v17, v19, s[36:39], 0 offen offset:0 sc1\n"
"v_mfma_f32_16x16x32_bf16 v[188:191], a[120:123], v[240:243], v[188:191]\n"
"v_max3_u32 v14, v247, v241, v14\n"
"s_waitcnt vmcnt(10)\n"
"v_mfma_f32_16x16x32_bf16 v[188:191], a[152:155], v[236:239], v[188:191]\n"
"v_max3_u32 v14, v243, v237, v14\n"
"v_max3_u32 v14, v239, v233, v14\n"
"v_mfma_f32_16x16x32_bf16 v[188:191], a[184:187], v[232:235], v[188:191]\n"
"v_max3_u32 v14, v235, v229, v14\n"
"v_max3_u32 v14, v231, v225, v14\n"
"v_mfma_f32_16x16x32_bf16 v[188:191], a[216:219], v[228:231], v[188:191]\n"
"v_mfma_f32_16x16x32_bf16 v[188:191], a[248:251], v[224:227], v[188:191]\n"
"buffer_load_dwordx4 v[180:183], v1, s[36:39], s49 offen offset:1024 sc1\n"
"v_max_u32 v14, v227, v14\n"
"v_mfma_f32_16x16x32_bf16 v[176:179], a[28:31], v[252:255], 0\n"
"v_mfma_f32_16x16x32_bf16 v[176:179], a[60:63], v[248:251], v[176:179]\n"
"v_mfma_f32_16x16x32_bf16 v[176:179], a[92:95], v[244:247], v[176:179]\n"
"v_mfma_f32_16x16x32_bf16 v[176:179], a[124:127], v[240:243], v[176:179]\n"
"buffer_load_dwordx4 v[172:175], v1, s[36:39], s50 offen offset:1024 sc1\n"
"v_cmp_le_u32 vcc, 0xffc00000, v14\n"
"v_mfma_f32_16x16x32_bf16 v[176:179], a[156:159], v[236:239], v[176:179]\n"
"v_mfma_f32_16x16x32_bf16 v[176:179], a[188:191], v[232:235], v[176:179]\n"
"v_mfma_f32_16x16x32_bf16 v[176:179], a[220:223], v[228:231], v[176:179]\n"
"v_mfma_f32_16x16x32_bf16 v[176:179], a[252:255], v[224:227], v[176:179]\n"
"buffer_load_dwordx4 v[168:171], v1, s[36:39], s51 offen offset:1024 sc1\n"
"s_cbranch_vccnz .Lgs_slow1\n"
".Lgs_back1:\n"
"v_mfma_f32_16x16x32_bf16 v[164:167], v[252:255], a[16:19], 0\n"
"ds_write_addtid_b32 v188 offset:24576\n"
"v_mfma_f32_16x16x32_bf16 v[164:167], v[248:251], a[48:51], v[164:167]\n"
"ds_write_addtid_b32 v189 offset:28448\n"
"v_mfma_f32_16x16x32_bf16 v[164:167], v[244:247], a[80:83], v[164:167]\n"
"ds_write_addtid_b32 v190 offset:32384\n"
"v_mfma_f32_16x16x32_bf16 v[164:167], v[240:243], a[112:115], v[164:167]\n"
"ds_write_addtid_b32 v191 offset:36256\n"
"buffer_load_dwordx4 v[160:163], v1, s[36:39], s52 offen offset:1024 sc1\n"
"v_mfma_f32_16x16x32_bf16 v[164:167], v[236:239], a[144:147], v[164:167]\n"
"v_mfma_f32_16x16x32_bf16 v[164:167], v[232:235], a[176:179], v[164:167]\n"
"v_mfma_f32_16x16x32_bf16 v[164:167], v[228:231], a[208:211], v[164:167]\n"
"buffer_load_dwordx4 v[188:191], v1, s[36:39], s53 offen offset:1024 sc1\n"
"v_mfma_f32_16x16x32_bf16 v[164:167], v[224:227], a[240:243], v[164:167]\n"
"ds_write_addtid_b32 v176 offset:24832\n"
"v_mfma_f32_16x16x32_bf16 v[156:159], v[252:255], a[20:23], 0\n"
"ds_write_addtid_b32 v177 offset:28704\n"
"v_mfma_f32_16x16x32_bf16 v[156:159], v[248:251], a[52:55], v[156:159]\n"
"ds_write_addtid_b32 v178 offset:32640\n"
"v_mfma_f32_16x16x32_bf16 v[156:159], v[244:247], a[84:87], v[156:159]\n"
"ds_write_addtid_b32 v179 offset:36512\n"
"buffer_load_dwordx4 v[152:155], v1, s[36:39], s54 offen offset:1024 sc1\n"
"v_mfma_f32_16x16x32_bf16 v[156:159], v[240:243], a[116:119], v[156:159]\n"
"v_mfma_f32_16x16x32_bf16 v[156:159], v[236:239], a[148:151], v[156:159]\n"
"v_mfma_f32_16x16x32_bf16 v[156:159], v[232:235], a[180:183], v[156:159]\n"
"v_mfma_f32_16x16x32_bf16 v[156:159], v[228:231], a[212:215], v[156:159]\n"
"s_mov_b32 s20, s65\n"
"buffer_load_dwordx4 v6, s[40:43], s20 offen offset:0 lds\n"
"v_mfma_f32_16x16x32_bf16 v[156:159], v[224:227], a[244:247], v[156:159]\n"
"ds_write_addtid_b32 v164 offset:25088\n"
"s_waitcnt vmcnt(13)\n"
"v_mfma_f32_16x16x32_bf16 v[176:179], a[24:27], v[220:223], 0\n"
"ds_write_addtid_b32 v165 offset:28960\n"
"v_max3_u32 v14, v221, v223, v217\n"
"v_mfma_f32_16x16x32_bf16 v[176:179], a[56:59], v[216:219], v[176:179]\n"
"ds_write_addtid_b32 v166 offset:32896\n"
"v_max3_u32 v14, v219, v213, v14\n"
"v_mfma_f32_16x16x32_bf16 v[176:179], a[88:91], v[212:215], v[176:179]\n"
"ds_write_addtid_b32 v167 offset:36768\n"
"buffer_load_dwordx4 v[148:151], v1, s[36:39], s55 offen offset:1024 sc1\n"
"v_mfma_f32_16x16x32_bf16 v[176:179], a[120:123], v[208:211], v[176:179]\n"
"v_max3_u32 v14, v215, v209, v14\n"
"s_waitcnt vmcnt(10)\n"
"v_mfma_f32_16x16x32_bf16 v[176:179], a[152:155], v[204:207], v[176:179]\n"
"v_max3_u32 v14, v211, v205, v14\n"
"v_max3_u32 v14, v207, v201, v14\n"
"v_mfma_f32_16x16x32_bf16 v[176:179], a[184:187], v[200:203], v[176:179]\n"
"buffer_load_dwordx4 v[164:167], v2, s[36:39], s48 offen offset:1024 sc1\n"
"v_max3_u32 v14, v203, v197, v14\n"
"v_mfma_f32_16x16x32_bf16 v[176:179], a[216:219], v[196:199], v[176:179]\n"
"v_mfma_f32_16x16x32_bf16 v[176:179], a[248:251], v[192:195], v[176:179]\n"
"ds_write_addtid_b32 v156 offset:25344\n"
"v_max3_u32 v14, v199, v193, v14\n"
"v_mfma_f32_16x16x32_bf16 v[144:147], a[28:31], v[220:223], 0\n"
"ds_write_addtid_b32 v157 offset:29216\n"
"v_max_u32 v14, v195, v14\n"
"v_mfma_f32_16x16x32_bf16 v[144:147], a[60:63], v[216:219], v[144:147]\n"
"ds_write_addtid_b32 v158 offset:33152\n"
"buffer_load_dwordx4 v[140:143], v2, s[36:39], s49 offen offset:1024 sc1\n"
"v_mfma_f32_16x16x32_bf16 v[144:147], a[92:95], v[212:215], v[144:147]\n"
"ds_write_addtid_b32 v159 offset:37024\n"
"v_cmp_le_u32 vcc, 0xffc00000, v14\n"
"v_mfma_f32_16x16x32_bf16 v[144:147], a[124:127], v[208:211], v[144:147]\n"
"v_mfma_f32_16x16x32_bf16 v[144:147], a[156:159], v[204:207], v[144:147]\n"
"v_mfma_f32_16x16x32_bf16 v[144:147], a[188:191], v[200:203], v[144:147]\n"
"buffer_load_dwordx4 v[156:159], v2, s[36:39], s50 offen offset:1024 sc1\n"
"v_mfma_f32_16x16x32_bf16 v[144:147], a[220:223], v[196:199], v[144:147]\n"
"s_waitcnt lgkmcnt(0)\n"
"s_barrier\n"
"v_mfma_f32_16x16x32_bf16 v[144:147], a[252:255], v[192:195], v[144:147]\n"
"s_cbranch_vccnz .Lgs_slow2\n"
".Lgs_back2:\n"
"v_mfma_f32_16x16x32_bf16 v[136:139], v[220:223], a[16:19], 0\n"
"ds_write_addtid_b32 v176 offset:25600\n"
"v_mfma_f32_16x16x32_bf16 v[136:139], v[216:219], a[48:51], v[136:139]\n"
"ds_write_addtid_b32 v177 offset:29472\n"
"buffer_load_dwordx4 v7, s[40:43], s20 offen offset:1024 lds\n"
"v_mfma_f32_16x16x32_bf16 v[136:139], v[212:215], a[80:83], v[136:139]\n"
"ds_write_addtid_b32 v178 offset:33408\n"
"v_mfma_f32_16x16x32_bf16 v[136:139], v[208:211], a[112:115], v[136:139]\n"
"ds_write_addtid_b32 v179 offset:37280\n"
"v_mfma_f32_16x16x32_bf16 v[136:139], v[204:207], a[144:147], v[136:139]\n"
"v_mfma_f32_16x16x32_bf16 v[136:139], v[200:203], a[176:179], v[136:139]\n"
"ds_read_b128 v[176:179], v16 offset:24576\n"
"v_mfma_f32_16x16x32_bf16 v[136:139], v[196:199], a[208:211], v[136:139]\n"
"ds_read_b128 v[132:135], v16 offset:24832\n"
"v_mfma_f32_16x16x32_bf16 v[136:139], v[192:195], a[240:243], v[136:139]\n"
"ds_write_addtid_b32 v144 offset:25856\n"
"v_mfma_f32_16x16x32_bf16 v[128:131], v[220:223], a[20:23], 0\n"
"ds_write_addtid_b32 v145 offset:29728\n"
"v_mfma_f32_16x16x32_bf16 v[128:131], v[216:219], a[52:55], v[128:131]\n"
"ds_write_addtid_b32 v146 offset:33664\n"
"v_mfma_f32_16x16x32_bf16 v[128:131], v[212:215], a[84:87], v[128:131]\n"
"ds_write_addtid_b32 v147 offset:37536\n"
"v_mfma_f32_16x16x32_bf16 v[128:131], v[208:211], a[116:119], v[128:131]\n"
"v_mfma_f32_16x16x32_bf16 v[128:131], v[204:207], a[148:151], v[128:131]\n"
"v_mfma_f32_16x16x32_bf16 v[128:131], v[200:203], a[180:183], v[128:131]\n"
"v_mfma_f32_16x16x32_bf16 v[128:131], v[196:199], a[212:215], v[128:131]\n"
"v_mfma_f32_16x16x32_bf16 v[128:131], v[192:195], a[244:247], v[128:131]\n"
"ds_write_addtid_b32 v136 offset:26112\n"
"s_waitcnt lgkmcnt(5)\n"
"v_mfma_f32_16x16x32_bf16 v[176:179], v[252:255], a[8:11], v[176:179]\n"
"ds_write_addtid_b32 v137 offset:29984\n"
"v_mfma_f32_16x16x32_bf16 v[176:179], v[248:251], a[40:43], v[176:179]\n"
"ds_write_addtid_b32 v138 offset:33920\n"
"buffer_load_dwordx4 v8, s[40:43], s20 offen offset:2048 lds\n"
"v_mfma_f32_16x16x32_bf16 v[176:179], v[244:247], a[72:75], v[176:179]\n"
"ds_write_addtid_b32 v139 offset:37792\n"
"v_mfma_f32_16x16x32_bf16 v[176:179], v[240:243], a[104:107], v[176:179]\n"
"v_mfma_f32_16x16x32_bf16 v[176:179], v[236:239], a[136:139], v[176:179]\n"
"v_mfma_f32_16x16x32_bf16 v[176:179], v[232:235], a[168:171], v[176:179]\n"
"ds_read_b128 v[144:147], v16 offset:25088\n"
"v_mfma_f32_16x16x32_bf16 v[176:179], v[228:231], a[200:203], v[176:179]\n"
"ds_read_b128 v[136:139], v16 offset:25344\n"
"v_mfma_f32_16x16x32_bf16 v[176:179], v[224:227], a[232:235], v[176:179]\n"
"ds_write_addtid_b32 v128 offset:26368\n"
"v_mfma_f32_16x16x32_bf16 v[132:135], v[252:255], a[12:15], v[132:135]\n"
"ds_write_addtid_b32 v129 offset:30240\n"
"v_mfma_f32_16x16x32_bf16 v[132:135], v[248:251], a[44:47], v[132:135]\n"
"ds_write_addtid_b32 v130 offset:34176\n"
"v_mfma_f32_16x16x32_bf16 v[132:135], v[244:247], a[76:79], v[132:135]\n"
"ds_write_addtid_b32 v131 offset:38048\n"
"v_mfma_f32_16x16x32_bf16 v[132:135], v[240:243], a[108:111], v[132:135]\n"
"v_mfma_f32_16x16x32_bf16 v[132:135], v[236:239], a[140:143], v[132:135]\n"
"v_mfma_f32_16x16x32_bf16 v[132:135], v[232:235], a[172:175], v[132:135]\n"
"v_mfma_f32_16x16x32_bf16 v[132:135], v[228:231], a[204:207], v[132:135]\n"
"v_mfma_f32_16x16x32_bf16 v[132:135], v[224:227], a[236:239], v[132:135]\n"
"ds_write_addtid_b32 v176 offset:26624\n"
"s_waitcnt lgkmcnt(5)\n"
"v_mfma_f32_16x16x32_bf16 v[144:147], a[0:3], v[252:255], v[144:147]\n"
"ds_write_addtid_b32 v177 offset:30496\n"
"v_mfma_f32_16x16x32_bf16 v[144:147], a[32:35], v[248:251], v[144:147]\n"
"ds_write_addtid_b32 v178 offset:34432\n"
"buffer_load_dwordx4 v9, s[40:43], s20 offen offset:3072 lds\n"
"v_mfma_f32_16x16x32_bf16 v[144:147], a[64:67], v[244:247], v[144:147]\n"
"ds_write_addtid_b32 v179 offset:38304\n"
"v_mfma_f32_16x16x32_bf16 v[144:147], a[96:99], v[240:243], v[144:147]\n"
"v_mfma_f32_16x16x32_bf16 v[144:147], a[128:131], v[236:239], v[144:147]\n"
"v_mfma_f32_16x16x32_bf16 v[144:147], a[160:163], v[232:235], v[144:147]\n"
"v_mfma_f32_16x16x32_bf16 v[144:147], a[192:195], v[228:231], v[144:147]\n"
"v_mfma_f32_16x16x32_bf16 v[144:147], a[224:227], v[224:227], v[144:147]\n"
"ds_write_addtid_b32 v132 offset:26880\n"
"v_mfma_f32_16x16x32_bf16 v[136:139], a[4:7], v[252:255], v[136:139]\n"
"ds_write_addtid_b32 v133 offset:30752\n"
"v_mfma_f32_16x16x32_bf16 v[136:139], a[36:39], v[248:251], v[136:139]\n"
"ds_write_addtid_b32 v134 offset:34688\n"
"v_mfma_f32_16x16x32_bf16 v[136:139], a[68:71], v[244:247], v[136:139]\n"
"ds_write_addtid_b32 v135 offset:38560\n"
"buffer_load_dwordx4 v[252:255], v2, s[36:39], s51 offen offset:1024 sc1\n"
"v_mfma_f32_16x16x32_bf16 v[136:139], a[100:103], v[240:243], v[136:139]\n"
"v_mfma_f32_16x16x32_bf16 v[136:139], a[132:135], v[236:239], v[136:139]\n"
"v_mfma_f32_16x16x32_bf16 v[136:139], a[164:167], v[232:235], v[136:139]\n"
"buffer_load_dwordx4 v[248:251], v2, s[36:39], s52 offen offset:1024 sc1\n"
"v_mfma_f32_16x16x32_bf16 v[136:139], a[196:199], v[228:231], v[136:139]\n"
"s_waitcnt lgkmcnt(0)\n"
"s_barrier\n"
"v_mfma_f32_16x16x32_bf16 v[136:139], a[228:231], v[224:227], v[136:139]\n"
"ds_read_b128 v[244:247], v15 offset:26624\n"
"s_waitcnt vmcnt(13)\n"
"v_mfma_f32_16x16x32_bf16 v[240:243], a[24:27], v[184:187], 0\n"
"ds_read_b128 v[236:239], v15 offset:26880\n"
"v_max3_u32 v14, v185, v187, v181\n"
"v_mfma_f32_16x16x32_bf16 v[240:243], a[56:59], v[180:183], v[240:243]\n"
"s_add_u32 m0, s77, 4096\n"
"s_add_u32 s20, s20, 524288\n"
"buffer_load_dwordx4 v6, s[40:43], s20 offen offset:0 lds\n"
"v_mfma_f32_16x16x32_bf16 v[240:243], a[88:91], v[172:175], v[240:243]\n"
"v_mfma_f32_16x16x32_bf16 v[240:243], a[120:123], v[168:171], v[240:243]\n"
"v_max3_u32 v14, v183, v173, v14\n"
"s_waitcnt vmcnt(9)\n"
"v_mfma_f32_16x16x32_bf16 v[240:243], a[152:155], v[160:163], v[240:243]\n"
"buffer_load_dwordx4 v[232:235], v2, s[36:39], s53 offen offset:1024 sc1\n"
"v_max3_u32 v14, v175, v169, v14\n"
"v_mfma_f32_16x16x32_bf16 v[240:243], a[184:187], v[188:191], v[240:243]\n"
"v_max3_u32 v14, v171, v161, v14\n"
"v_max3_u32 v14, v163, v189, v14\n"
"v_mfma_f32_16x16x32_bf16 v[240:243], a[216:219], v[152:155], v[240:243]\n"
"v_max3_u32 v14, v191, v153, v14\n"
"v_max3_u32 v14, v155, v149, v14\n"
"v_mfma_f32_16x16x32_bf16 v[240:243], a[248:251], v[148:151], v[240:243]\n"
"s_waitcnt lgkmcnt(0)\n"
"v_add_f32 v144, v144, v244\n"
"v_add_f32 v145, v145, v245\n"
"v_mfma_f32_16x16x32_bf16 v[228:231], a[28:31], v[184:187], 0\n"
"buffer_load_dwordx4 v[224:227], v2, s[36:39], s54 offen offset:1024 sc1\n"
"v_max_u32 v14, v151, v14\n"
"v_mfma_f32_16x16x32_bf16 v[228:231], a[60:63], v[180:183], v[228:231]\n"
"v_add_f32 v146, v146, v246\n"
"v_add_f32 v147, v147, v247\n"
"v_mfma_f32_16x16x32_bf16 v[228:231], a[92:95], v[172:175], v[228:231]\n"
"v_add_f32 v136, v136, v236\n"
"v_add_f32 v137, v137, v237\n"
"v_mfma_f32_16x16x32_bf16 v[228:231], a[124:127], v[168:171], v[228:231]\n"
"v_add_f32 v138, v138, v238\n"
"v_add_f32 v139, v139, v239\n"
"v_mfma_f32_16x16x32_bf16 v[228:231], a[156:159], v[160:163], v[228:231]\n"
"buffer_load_dwordx4 v[244:247], v2, s[36:39], s55 offen offset:1024 sc1\n"
"v_cmp_le_u32 vcc, 0xffc00000, v14\n"
"v_mfma_f32_16x16x32_bf16 v[228:231], a[188:191], v[188:191], v[228:231]\n"
"v_cvt_pk_bf16_f32 v236, v144, v145\n"
"v_cvt_pk_bf16_f32 v237, v146, v147\n"
"v_mfma_f32_16x16x32_bf16 v[228:231], a[220:223], v[152:155], v[228:231]\n"
"v_min_u32 v237, 0xffbfffff, v237\n"
"v_cvt_pk_bf16_f32 v238, v136, v137\n"
"v_mfma_f32_16x16x32_bf16 v[228:231], a[252:255], v[148:151], v[228:231]\n"
"buffer_load_dwordx4 v[176:179], v1, s[36:39], s48 offen offset:2048 sc1\n"
"s_cbranch_vccnz .Lgs_slow3\n"
".Lgs_back3:\n"
"v_mfma_f32_16x16x32_bf16 v[144:147], v[184:187], a[16:19], 0\n"
"ds_write_addtid_b32 v240 offset:20480\n"
"v_cvt_pk_bf16_f32 v239, v138, v139\n"
"v_mfma_f32_16x16x32_bf16 v[144:147], v[180:183], a[48:51], v[144:147]\n"
"ds_write_addtid_b32 v241 offset:24352\n"
"v_min_u32 v239, 0xffbfffff, v239\n"
"v_mfma_f32_16x16x32_bf16 v[144:147], v[172:175], a[80:83], v[144:147]\n"
"ds_write_addtid_b32 v242 offset:28288\n"
"buffer_store_dwordx4 v[236:239], v0, s[36:39], s80 offen offset:0 sc1\n"
"v_mfma_f32_16x16x32_bf16 v[144:147], v[168:171], a[112:115], v[144:147]\n"
"ds_write_addtid_b32 v243 offset:32160\n"
"v_mfma_f32_16x16x32_bf16 v[144:147], v[160:163], a[144:147], v[144:147]\n"
"v_mfma_f32_16x16x32_bf16 v[144:147], v[188:191], a[176:179], v[144:147]\n"
"ds_read_b128 v[240:243], v16 offset:25600\n"
"v_mfma_f32_16x16x32_bf16 v[144:147], v[152:155], a[208:211], v[144:147]\n"
"ds_read_b128 v[236:239], v16 offset:25856\n"
"v_mfma_f32_16x16x32_bf16 v[144:147], v[148:151], a[240:243], v[144:147]\n"
"ds_write_addtid_b32 v228 offset:20736\n"
"v_mfma_f32_16x16x32_bf16 v[136:139], v[184:187], a[20:23], 0\n"
"ds_write_addtid_b32 v229 offset:24608\n"
"v_mfma_f32_16x16x32_bf16 v[136:139], v[180:183], a[52:55], v[136:139]\n"
"ds_write_addtid_b32 v230 offset:28544\n"
"v_mfma_f32_16x16x32_bf16 v[136:139], v[172:175], a[84:87], v[136:139]\n"
"ds_write_addtid_b32 v231 offset:32416\n"
"v_mfma_f32_16x16x32_bf16 v[136:139], v[168:171], a[116:119], v[136:139]\n"
"v_mfma_f32_16x16x32_bf16 v[136:139], v[160:163], a[148:151], v[136:139]\n"
"v_mfma_f32_16x16x32_bf16 v[136:139], v[188:191], a[180:183], v[136:139]\n"
"v_mfma_f32_16x16x32_bf16 v[136:139], v[152:155], a[212:215], v[136:139]\n"
"buffer_load_dwordx4 v7, s[40:43], s20 offen offset:1024 lds\n"
"v_mfma_f32_16x16x32_bf16 v[136:139], v[148:151], a[244:247], v[136:139]\n"
"ds_write_addtid_b32 v144 offset:20992\n"
"s_waitcnt lgkmcnt(5)\n"
"v_mfma_f32_16x16x32_bf16 v[240:243], v[220:223], a[8:11], v[240:243]\n"
"ds_write_addtid_b32 v145 offset:24864\n"
"v_mfma_f32_16x16x32_bf16 v[240:243], v[216:219], a[40:43], v[240:243]\n"
"ds_write_addtid_b32 v146 offset:28800\n"
"v_mfma_f32_16x16x32_bf16 v[240:243], v[212:215], a[72:75], v[240:243]\n"
"ds_write_addtid_b32 v147 offset:32672\n"
"buffer_load_dwordx4 v[228:231], v1, s[36:39], s49 offen offset:2048 sc1\n"
"v_mfma_f32_16x16x32_bf16 v[240:243], v[208:211], a[104:107], v[240:243]\n"
"v_mfma_f32_16x16x32_bf16 v[240:243], v[204:207], a[136:139], v[240:243]\n"
"v_mfma_f32_16x16x32_bf16 v[240:243], v[200:203], a[168:171], v[240:243]\n"
"ds_read_b128 v[144:147], v16 offset:26112\n"
"v_mfma_f32_16x16x32_bf16 v[240:243], v[196:199], a[200:203], v[240:243]\n"
"ds_read_b128 v[132:135], v16 offset:26368\n"
"buffer_load_dwordx4 v[128:131], v1, s[36:39], s50 offen offset:2048 sc1\n"
"v_mfma_f32_16x16x32_bf16 v[240:243], v[192:195], a[232:235], v[240:243]\n"
"ds_write_addtid_b32 v136 offset:21248\n"
"v_mfma_f32_16x16x32_bf16 v[236:239], v[220:223], a[12:15], v[236:239]\n"
"ds_write_addtid_b32 v137 offset:25120\n"
"v_mfma_f32_16x16x32_bf16 v[236:239], v[216:219], a[44:47], v[236:239]\n"
"ds_write_addtid_b32 v138 offset:29056\n"
"v_mfma_f32_16x16x32_bf16 v[236:239], v[212:215], a[76:79], v[236:239]\n"
"ds_write_addtid_b32 v139 offset:32928\n"
"v_mfma_f32_16x16x32_bf16 v[236:239], v[208:211], a[108:111], v[236:239]\n"
"v_mfma_f32_16x16x32_bf16 v[236:239], v[204:207], a[140:143], v[236:239]\n"
"v_mfma_f32_16x16x32_bf16 v[236:239], v[200:203], a[172:175], v[236:239]\n"
"v_mfma_f32_16x16x32_bf16 v[236:239], v[196:199], a[204:207], v[236:239]\n"
"buffer_load_dwordx4 v8, s[40:43], s20 offen offset:2048 lds\n"
"v_mfma_f32_16x16x32_bf16 v[236:239], v[192:195], a[236:239], v[236:239]\n"
"ds_write_addtid_b32 v240 offset:23040\n"
"s_waitcnt lgkmcnt(5)\n"
"v_mfma_f32_16x16x32_bf16 v[144:147], a[0:3], v[220:223], v[144:147]\n"
"ds_write_addtid_b32 v241 offset:26912\n"
"v_mfma_f32_16x16x32_bf16 v[144:147], a[32:35], v[216:219], v[144:147]\n"
"ds_write_addtid_b32 v242 offset:30848\n"
"v_mfma_f32_16x16x32_bf16 v[144:147], a[64:67], v[212:215], v[144:147]\n"
"ds_write_addtid_b32 v243 offset:34720\n"
"v_mfma_f32_16x16x32_bf16 v[144:147], a[96:99], v[208:211], v[144:147]\n"
"v_mfma_f32_16x16x32_bf16 v[144:147], a[128:131], v[204:207], v[144:147]\n"
"v_mfma_f32_16x16x32_bf16 v[144:147], a[160:163], v[200:203], v[144:147]\n"
"v_mfma_f32_16x16x32_bf16 v[144:147], a[192:195], v[196:199], v[144:147]\n"
"v_mfma_f32_16x16x32_bf16 v[144:147], a[224:227], v[192:195], v[144:147]\n"
"ds_write_addtid_b32 v236 offset:23296\n"
"v_mfma_f32_16x16x32_bf16 v[132:135], a[4:7], v[220:223], v[132:135]\n"
"ds_write_addtid_b32 v237 offset:27168\n"
"v_mfma_f32_16x16x32_bf16 v[132:135], a[36:39], v[216:219], v[132:135]\n"
"ds_write_addtid_b32 v238 offset:31104\n"
"v_mfma_f32_16x16x32_bf16 v[132:135], a[68:71], v[212:215], v[132:135]\n"
"ds_write_addtid_b32 v239 offset:34976\n"
"buffer_load_dwordx4 v[240:243], v1, s[36:39], s51 offen offset:2048 sc1\n"
"v_mfma_f32_16x16x32_bf16 v[132:135], a[100:103], v[208:211], v[132:135]\n"
"v_mfma_f32_16x16x32_bf16 v[132:135], a[132:135], v[204:207], v[132:135]\n"
"v_mfma_f32_16x16x32_bf16 v[132:135], a[164:167], v[200:203], v[132:135]\n"
"buffer_load_dwordx4 v[236:239], v1, s[36:39], s52 offen offset:2048 sc1\n"
"v_mfma_f32_16x16x32_bf16 v[132:135], a[196:199], v[196:199], v[132:135]\n"
"s_waitcnt lgkmcnt(0)\n"
"s_barrier\n"
"v_mfma_f32_16x16x32_bf16 v[132:135], a[228:231], v[192:195], v[132:135]\n"
"ds_read_b128 v[220:223], v15 offset:27136\n"
"s_waitcnt vmcnt(13)\n"
"v_mfma_f32_16x16x32_bf16 v[216:219], a[24:27], v[164:167], 0\n"
"ds_read_b128 v[212:215], v15 offset:27392\n"
"v_max3_u32 v14, v165, v167, v141\n"
"v_mfma_f32_16x16x32_bf16 v[216:219], a[56:59], v[140:143], v[216:219]\n"
"ds_read_b64_tr_b16 v[208:209], v12 offset:0\n"
"buffer_load_dwordx4 v9, s[40:43], s20 offen offset:3072 lds\n"
"v_mfma_f32_16x16x32_bf16 v[216:219], a[88:91], v[156:159], v[216:219]\n"
"ds_read_b64_tr_b16 v[210:211], v12 offset:512\n"
"v_max3_u32 v14, v143, v157, v14\n"
"v_mfma_f32_16x16x32_bf16 v[216:219], a[120:123], v[252:255], v[216:219]\n"
"ds_read_b64_tr_b16 v[204:205], v13 offset:0\n"
"s_waitcnt vmcnt(9)\n"
"v_mfma_f32_16x16x32_bf16 v[216:219], a[152:155], v[248:251], v[216:219]\n"
"ds_read_b64_tr_b16 v[206:207], v13 offset:512\n"
"v_max3_u32 v14, v159, v253, v14\n"
"v_mfma_f32_16x16x32_bf16 v[216:219], a[184:187], v[232:235], v[216:219]\n"
"ds_read_b64_tr_b16 v[200:201], v10 offset:0\n"
"buffer_load_dwordx4 v[196:199], v1, s[36:39], s53 offen offset:2048 sc1\n"
"v_mfma_f32_16x16x32_bf16 v[216:219], a[216:219], v[224:227], v[216:219]\n"
"ds_read_b64_tr_b16 v[202:203], v10 offset:512\n"
"v_max3_u32 v14, v255, v249, v14\n"
"v_mfma_f32_16x16x32_bf16 v[216:219], a[248:251], v[244:247], v[216:219]\n"
"ds_read_b64_tr_b16 v[192:193], v11 offset:0\n"
"v_max3_u32 v14, v251, v233, v14\n"
"s_waitcnt lgkmcnt(7)\n"
"v_add_f32 v144, v144, v220\n"
"v_mfma_f32_16x16x32_bf16 v[136:139], a[28:31], v[164:167], 0\n"
"ds_read_b64_tr_b16 v[194:195], v11 offset:512\n"
"buffer_load_dwordx4 v[124:127], v1, s[36:39], s54 offen offset:2048 sc1\n"
"v_add_f32 v132, v132, v212\n"
"v_mfma_f32_16x16x32_bf16 v[136:139], a[60:63], v[140:143], v[136:139]\n"
"v_max3_u32 v14, v235, v225, v14\n"
"v_max3_u32 v14, v227, v245, v14\n"
"v_add_f32 v133, v133, v213\n"
"v_mfma_f32_16x16x32_bf16 v[136:139], a[92:95], v[156:159], v[136:139]\n"
"v_max_u32 v14, v247, v14\n"
"v_cmp_le_u32 vcc, 0xffc00000, v14\n"
"v_mfma_f32_16x16x32_bf16 v[136:139], a[124:127], v[252:255], v[136:139]\n"
"buffer_load_dwordx4 v[120:123], v1, s[36:39], s55 offen offset:2048 sc1\n"
"v_add_f32 v145, v145, v221\n"
"v_mfma_f32_16x16x32_bf16 v[136:139], a[156:159], v[248:251], v[136:139]\n"
"v_add_f32 v146, v146, v222\n"
"v_add_f32 v147, v147, v223\n"
"v_mfma_f32_16x16x32_bf16 v[136:139], a[188:191], v[232:235], v[136:139]\n"
"v_add_f32 v134, v134, v214\n"
"v_add_f32 v135, v135, v215\n"
"v_mfma_f32_16x16x32_bf16 v[136:139], a[220:223], v[224:227], v[136:139]\n"
"v_cvt_pk_bf16_f32 v220, v144, v145\n"
"v_cvt_pk_bf16_f32 v221, v146, v147\n"
"v_mfma_f32_16x16x32_bf16 v[136:139], a[252:255], v[244:247], v[136:139]\n"
"s_add_u32 m0, s77, 8192\n"
"s_add_u32 s20, s20, 524288\n"
"buffer_load_dwordx4 v6, s[40:43], s20 offen offset:0 lds\n"
"s_cbranch_vccnz .Lgs_slow4\n"
".Lgs_back4:\n"
"v_mfma_f32_16x16x32_bf16 v[212:215], v[164:167], a[16:19], 0\n"
"ds_write_addtid_b32 v216 offset:17408\n"
"v_min_u32 v221, 0xffbfffff, v221\n"
"v_mfma_f32_16x16x32_bf16 v[212:215], v[140:143], a[48:51], v[212:215]\n"
"ds_write_addtid_b32 v217 offset:21280\n"
"v_cvt_pk_bf16_f32 v222, v132, v133\n"
"v_mfma_f32_16x16x32_bf16 v[212:215], v[156:159], a[80:83], v[212:215]\n"
"ds_write_addtid_b32 v218 offset:25216\n"
"buffer_load_dwordx4 v[144:147], v2, s[36:39], s48 offen offset:2048 sc1\n"
"v_mfma_f32_16x16x32_bf16 v[212:215], v[252:255], a[112:115], v[212:215]\n"
"ds_write_addtid_b32 v219 offset:29088\n"
"v_cvt_pk_bf16_f32 v223, v134, v135\n"
"v_mfma_f32_16x16x32_bf16 v[212:215], v[248:251], a[144:147], v[212:215]\n"
"v_min_u32 v223, 0xffbfffff, v223\n"
"v_mfma_f32_16x16x32_bf16 v[212:215], v[232:235], a[176:179], v[212:215]\n"
"ds_read_b128 v[216:219], v16 offset:24576\n"
"buffer_store_dwordx4 v[220:223], v0, s[36:39], s80 offen offset:1024 sc1\n"
"v_mfma_f32_16x16x32_bf16 v[212:215], v[224:227], a[208:211], v[212:215]\n"
"ds_read_b128 v[132:135], v16 offset:24832\n"
"buffer_load_dword v14, v20, s[36:39], 0 offen offset:0 sc1\n"
"v_mfma_f32_16x16x32_bf16 v[212:215], v[244:247], a[240:243], v[212:215]\n"
"ds_write_addtid_b32 v136 offset:17664\n"
"v_mfma_f32_16x16x32_bf16 v[220:223], v[164:167], a[20:23], 0\n"
"ds_write_addtid_b32 v137 offset:21536\n"
"v_mfma_f32_16x16x32_bf16 v[220:223], v[140:143], a[52:55], v[220:223]\n"
"ds_write_addtid_b32 v138 offset:25472\n"
"v_mfma_f32_16x16x32_bf16 v[220:223], v[156:159], a[84:87], v[220:223]\n"
"ds_write_addtid_b32 v139 offset:29344\n"
"v_mfma_f32_16x16x32_bf16 v[220:223], v[252:255], a[116:119], v[220:223]\n"
"v_mfma_f32_16x16x32_bf16 v[220:223], v[248:251], a[148:151], v[220:223]\n"
"v_mfma_f32_16x16x32_bf16 v[220:223], v[232:235], a[180:183], v[220:223]\n"
"v_mfma_f32_16x16x32_bf16 v[220:223], v[224:227], a[212:215], v[220:223]\n"
"v_mfma_f32_16x16x32_bf16 v[220:223], v[244:247], a[244:247], v[220:223]\n"
"ds_write_addtid_b32 v212 offset:17920\n"
"s_waitcnt lgkmcnt(5)\n"
"v_mfma_f32_16x16x32_bf16 v[216:219], v[184:187], a[8:11], v[216:219]\n"
"ds_write_addtid_b32 v213 offset:21792\n"
"v_mfma_f32_16x16x32_bf16 v[216:219], v[180:183], a[40:43], v[216:219]\n"
"ds_write_addtid_b32 v214 offset:25728\n"
"v_mfma_f32_16x16x32_bf16 v[216:219], v[172:175], a[72:75], v[216:219]\n"
"ds_write_addtid_b32 v215 offset:29600\n"
"v_mfma_f32_16x16x32_bf16 v[216:219], v[168:171], a[104:107], v[216:219]\n"
"v_mfma_f32_16x16x32_bf16 v[216:219], v[160:163], a[136:139], v[216:219]\n"
"buffer_load_dwordx4 v7, s[40:43], s20 offen offset:1024 lds\n"
"v_mfma_f32_16x16x32_bf16 v[216:219], v[188:191], a[168:171], v[216:219]\n"
"ds_read_b128 v[212:215], v16 offset:25088\n"
"v_mfma_f32_16x16x32_bf16 v[216:219], v[152:155], a[200:203], v[216:219]\n"
"ds_read_b128 v[136:139], v16 offset:25344\n"
"v_mfma_f32_16x16x32_bf16 v[216:219], v[148:151], a[232:235], v[216:219]\n"
"ds_write_addtid_b32 v220 offset:18176\n"
"v_mfma_f32_16x16x32_bf16 v[132:135], v[184:187], a[12:15], v[132:135]\n"
"ds_write_addtid_b32 v221 offset:22048\n"
"buffer_load_dwordx4 v[116:119], v2, s[36:39], s49 offen offset:2048 sc1\n"
"v_mfma_f32_16x16x32_bf16 v[132:135], v[180:183], a[44:47], v[132:135]\n"
"ds_write_addtid_b32 v222 offset:25984\n"
"v_mfma_f32_16x16x32_bf16 v[132:135], v[172:175], a[76:79], v[132:135]\n"
"ds_write_addtid_b32 v223 offset:29856\n"
"v_mfma_f32_16x16x32_bf16 v[132:135], v[168:171], a[108:111], v[132:135]\n"
"v_mfma_f32_16x16x32_bf16 v[132:135], v[160:163], a[140:143], v[132:135]\n"
"buffer_load_dwordx4 v8, s[40:43], s20 offen offset:2048 lds\n"
"v_mfma_f32_16x16x32_bf16 v[132:135], v[188:191], a[172:175], v[132:135]\n"
"v_mfma_f32_16x16x32_bf16 v[132:135], v[152:155], a[204:207], v[132:135]\n"
"v_mfma_f32_16x16x32_bf16 v[132:135], v[148:151], a[236:239], v[132:135]\n"
".Lgs_pad0:\n"
"s_cbranch_execz .Lgs_pad4\n"
"ds_write_addtid_b32 v216 offset:18432\n"
"s_waitcnt lgkmcnt(5)\n"
"v_mfma_f32_16x16x32_bf16 v[212:215], a[0:3], v[184:187], v[212:215]\n"
"ds_write_addtid_b32 v217 offset:22304\n"
"buffer_load_dwordx4 v[220:223], v2, s[36:39], s50 offen offset:2048 sc1\n"
"v_mfma_f32_16x16x32_bf16 v[212:215], a[32:35], v[180:183], v[212:215]\n"
"ds_write_addtid_b32 v218 offset:26240\n"
"v_mfma_f32_16x16x32_bf16 v[212:215], a[64:67], v[172:175], v[212:215]\n"
"ds_write_addtid_b32 v219 offset:30112\n"
"v_mfma_f32_16x16x32_bf16 v[212:215], a[96:99], v[168:171], v[212:215]\n"
"buffer_load_dwordx4 v9, s[40:43], s20 offen offset:3072 lds\n"
"v_mfma_f32_16x16x32_bf16 v[212:215], a[128:131], v[160:163], v[212:215]\n"
"v_mfma_f32_16x16x32_bf16 v[212:215], a[160:163], v[188:191], v[212:215]\n"
"v_mfma_f32_16x16x32_bf16 v[212:215], a[192:195], v[152:155], v[212:215]\n"
"v_mfma_f32_16x16x32_bf16 v[212:215], a[224:227], v[148:151], v[212:215]\n"
"ds_write_addtid_b32 v132 offset:18688\n"
"v_mfma_f32_16x16x32_bf16 v[136:139], a[4:7], v[184:187], v[136:139]\n"
"ds_write_addtid_b32 v133 offset:22560\n"
"v_mfma_f32_16x16x32_bf16 v[136:139], a[36:39], v[180:183], v[136:139]\n"
"ds_write_addtid_b32 v134 offset:26496\n"
"v_mfma_f32_16x16x32_bf16 v[136:139], a[68:71], v[172:175], v[136:139]\n"
"ds_write_addtid_b32 v135 offset:30368\n"
"s_add_u32 m0, s77, 12288\n"
"s_add_u32 s20, s20, 524288\n"
"s_cmp_eq_u32 s20, s66\n"
"s_cselect_b32 s20, s82, s20\n"
"buffer_load_dwordx4 v6, s[40:43], s20 offen offset:0 lds\n"
"v_mfma_f32_16x16x32_bf16 v[136:139], a[100:103], v[168:171], v[136:139]\n"
"v_mfma_f32_16x16x32_bf16 v[136:139], a[132:135], v[160:163], v[136:139]\n"
"v_mfma_f32_16x16x32_bf16 v[136:139], a[164:167], v[188:191], v[136:139]\n"
"buffer_load_dwordx4 v[216:219], v2, s[36:39], s51 offen offset:2048 sc1\n"
"v_mfma_f32_16x16x32_bf16 v[136:139], a[196:199], v[152:155], v[136:139]\n"
"s_waitcnt lgkmcnt(0)\n"
"s_barrier\n"
"v_mfma_f32_16x16x32_bf16 v[136:139], a[228:231], v[148:151], v[136:139]\n"
"ds_read_b128 v[188:191], v15 offset:26624\n"
"s_waitcnt vmcnt(16)\n"
"v_mfma_f32_16x16x32_bf16 v[184:187], a[24:27], v[176:179], 0\n"
"ds_read_b128 v[180:183], v15 offset:26880\n"
"v_max3_u32 v17, v177, v179, v229\n"
"v_mfma_f32_16x16x32_bf16 v[184:187], a[56:59], v[228:231], v[184:187]\n"
"buffer_load_dwordx4 v7, s[40:43], s20 offen offset:1024 lds\n"
"v_max3_u32 v17, v231, v129, v17\n"
"v_mfma_f32_16x16x32_bf16 v[184:187], a[88:91], v[128:131], v[184:187]\n"
"v_mfma_f32_16x16x32_bf16 v[184:187], a[120:123], v[240:243], v[184:187]\n"
"v_max3_u32 v17, v131, v241, v17\n"
"s_waitcnt vmcnt(12)\n"
"v_mfma_f32_16x16x32_bf16 v[184:187], a[152:155], v[236:239], v[184:187]\n"
"v_max3_u32 v17, v243, v237, v17\n"
"v_max3_u32 v17, v239, v197, v17\n"
"v_mfma_f32_16x16x32_bf16 v[184:187], a[184:187], v[196:199], v[184:187]\n"
"buffer_load_dwordx4 v[172:175], v2, s[36:39], s52 offen offset:2048 sc1\n"
"v_max3_u32 v17, v199, v125, v17\n"
"v_mfma_f32_16x16x32_bf16 v[184:187], a[216:219], v[124:127], v[184:187]\n"
"v_max3_u32 v17, v127, v121, v17\n"
"v_max_u32 v17, v123, v17\n"
"v_mfma_f32_16x16x32_bf16 v[184:187], a[248:251], v[120:123], v[184:187]\n"
"s_waitcnt lgkmcnt(0)\n"
"v_add_f32 v212, v212, v188\n"
"v_add_f32 v213, v213, v189\n"
"v_mfma_f32_16x16x32_bf16 v[168:171], a[28:31], v[176:179], 0\n"
"buffer_load_dwordx4 v[160:163], v2, s[36:39], s53 offen offset:2048 sc1\n"
"v_cmp_le_u32 vcc, 0xffc00000, v17\n"
"v_mfma_f32_16x16x32_bf16 v[168:171], a[60:63], v[228:231], v[168:171]\n"
"v_add_f32 v214, v214, v190\n"
"v_add_f32 v215, v215, v191\n"
"v_mfma_f32_16x16x32_bf16 v[168:171], a[92:95], v[128:131], v[168:171]\n"
"v_add_f32 v136, v136, v180\n"
"v_add_f32 v137, v137, v181\n"
"v_mfma_f32_16x16x32_bf16 v[168:171], a[124:127], v[240:243], v[168:171]\n"
"v_add_f32 v138, v138, v182\n"
"v_add_f32 v139, v139, v183\n"
"v_mfma_f32_16x16x32_bf16 v[168:171], a[156:159], v[236:239], v[168:171]\n"
"buffer_load_dwordx4 v[188:191], v2, s[36:39], s54 offen offset:2048 sc1\n"
"v_cvt_pk_bf16_f32 v180, v212, v213\n"
"v_mfma_f32_16x16x32_bf16 v[168:171], a[188:191], v[196:199], v[168:171]\n"
"v_cvt_pk_bf16_f32 v181, v214, v215\n"
"v_min_u32 v181, 0xffbfffff, v181\n"
"v_mfma_f32_16x16x32_bf16 v[168:171], a[220:223], v[124:127], v[168:171]\n"
"v_cvt_pk_bf16_f32 v182, v136, v137\n"
"v_cvt_pk_bf16_f32 v183, v138, v139\n"
"v_mfma_f32_16x16x32_bf16 v[168:171], a[252:255], v[120:123], v[168:171]\n"
"s_cbranch_vccnz .Lgs_slow5\n"
".Lgs_back5:\n"
"v_min_u32 v183, 0xffbfffff, v183\n"
"v_mfma_f32_16x16x32_bf16 v[212:215], v[176:179], a[16:19], 0\n"
"ds_write_addtid_b32 v184 offset:12288\n"
"buffer_load_dwordx4 v[152:155], v2, s[36:39], s55 offen offset:2048 sc1\n"
"v_mfma_f32_16x16x32_bf16 v[212:215], v[228:231], a[48:51], v[212:215]\n"
"ds_write_addtid_b32 v185 offset:16160\n"
"buffer_store_dwordx4 v[180:183], v0, s[36:39], s80 offen offset:2048 sc1\n"
"v_mfma_f32_16x16x32_bf16 v[212:215], v[128:131], a[80:83], v[212:215]\n"
"ds_write_addtid_b32 v186 offset:20096\n"
"v_mfma_f32_16x16x32_bf16 v[212:215], v[240:243], a[112:115], v[212:215]\n"
"ds_write_addtid_b32 v187 offset:23968\n"
"buffer_load_dwordx4 v8, s[40:43], s20 offen offset:2048 lds\n"
"v_mfma_f32_16x16x32_bf16 v[212:215], v[236:239], a[144:147], v[212:215]\n"
"ds_read_b64_tr_b16 v[184:185], v12 offset:4096\n"
"v_mfma_f32_16x16x32_bf16 v[212:215], v[196:199], a[176:179], v[212:215]\n"
"ds_read_b128 v[180:183], v16 offset:25600\n"
"v_mfma_f32_16x16x32_bf16 v[212:215], v[124:127], a[208:211], v[212:215]\n"
"ds_read_b128 v[148:151], v16 offset:25856\n"
"v_mfma_f32_16x16x32_bf16 v[212:215], v[120:123], a[240:243], v[212:215]\n"
"ds_write_addtid_b32 v168 offset:12544\n"
"v_mfma_f32_16x16x32_bf16 v[136:139], v[176:179], a[20:23], 0\n"
"ds_write_addtid_b32 v169 offset:16416\n"
"v_mfma_f32_16x16x32_bf16 v[136:139], v[228:231], a[52:55], v[136:139]\n"
"ds_write_addtid_b32 v170 offset:20352\n"
"v_mfma_f32_16x16x32_bf16 v[136:139], v[128:131], a[84:87], v[136:139]\n"
"ds_write_addtid_b32 v171 offset:24224\n"
"v_mfma_f32_16x16x32_bf16 v[136:139], v[240:243], a[116:119], v[136:139]\n"
"ds_read_b64_tr_b16 v[186:187], v12 offset:4608\n"
"v_mfma_f32_16x16x32_bf16 v[136:139], v[236:239], a[148:151], v[136:139]\n"
"ds_read_b64_tr_b16 v[168:169], v13 offset:4096\n"
"s_waitcnt vmcnt(14)\n"
"v_cmp_ne_u32 vcc, 32, v14\n"
"v_mfma_f32_16x16x32_bf16 v[136:139], v[196:199], a[180:183], v[136:139]\n"
"ds_read_b64_tr_b16 v[170:171], v13 offset:4608\n"
"v_mfma_f32_16x16x32_bf16 v[136:139], v[124:127], a[212:215], v[136:139]\n"
"ds_read_b64_tr_b16 v[132:133], v10 offset:4096\n"
"s_cbranch_vccnz .Lgs_slow17\n"
".Lgs_back17:\n"
"v_mfma_f32_16x16x32_bf16 v[136:139], v[120:123], a[244:247], v[136:139]\n"
"ds_write_addtid_b32 v212 offset:12800\n"
"s_waitcnt lgkmcnt(9)\n"
"v_mfma_f32_16x16x32_bf16 v[180:183], v[164:167], a[8:11], v[180:183]\n"
"ds_write_addtid_b32 v213 offset:16672\n"
"v_mfma_f32_16x16x32_bf16 v[180:183], v[140:143], a[40:43], v[180:183]\n"
"ds_write_addtid_b32 v214 offset:20608\n"
"v_mfma_f32_16x16x32_bf16 v[180:183], v[156:159], a[72:75], v[180:183]\n"
"ds_write_addtid_b32 v215 offset:24480\n"
"buffer_load_dwordx4 v9, s[40:43], s20 offen offset:3072 lds\n"
"v_mfma_f32_16x16x32_bf16 v[180:183], v[252:255], a[104:107], v[180:183]\n"
"ds_read_b64_tr_b16 v[134:135], v10 offset:4608\n"
"v_mfma_f32_16x16x32_bf16 v[180:183], v[248:251], a[136:139], v[180:183]\n"
"ds_read_b64_tr_b16 v[212:213], v11 offset:4096\n"
"v_mfma_f32_16x16x32_bf16 v[180:183], v[232:235], a[168:171], v[180:183]\n"
"ds_read_b128 v[112:115], v16 offset:26112\n"
"v_mfma_f32_16x16x32_bf16 v[180:183], v[224:227], a[200:203], v[180:183]\n"
"ds_read_b128 v[108:111], v16 offset:26368\n"
"buffer_load_dwordx4 v[104:107], v3, s[36:39], s48 offen offset:0 sc1\n"
"v_mfma_f32_16x16x32_bf16 v[180:183], v[244:247], a[232:235], v[180:183]\n"
"ds_write_addtid_b32 v136 offset:13056\n"
"v_mfma_f32_16x16x32_bf16 v[148:151], v[164:167], a[12:15], v[148:151]\n"
"ds_write_addtid_b32 v137 offset:16928\n"
"v_mfma_f32_16x16x32_bf16 v[148:151], v[140:143], a[44:47], v[148:151]\n"
"ds_write_addtid_b32 v138 offset:20864\n"
"s_add_u32 m0, s77, 16384\n"
"s_add_u32 s20, s20, 524288\n"
"buffer_load_dwordx4 v6, s[40:43], s20 offen offset:0 lds\n"
"v_mfma_f32_16x16x32_bf16 v[148:151], v[156:159], a[76:79], v[148:151]\n"
"ds_write_addtid_b32 v139 offset:20640\n"
"v_mfma_f32_16x16x32_bf16 v[148:151], v[252:255], a[108:111], v[148:151]\n"
"ds_read_b64_tr_b16 v[214:215], v11 offset:4608\n"
"buffer_load_dwordx4 v[136:139], v5, s[36:39], s81 offen offset:0 sc1\n"
"v_mfma_f32_16x16x32_bf16 v[148:151], v[248:251], a[140:143], v[148:151]\n"
"v_mfma_f32_16x16x32_bf16 v[148:151], v[232:235], a[172:175], v[148:151]\n"
"v_mfma_f32_16x16x32_bf16 v[148:151], v[224:227], a[204:207], v[148:151]\n"
"v_mfma_f32_16x16x32_bf16 v[148:151], v[244:247], a[236:239], v[148:151]\n"
"ds_write_addtid_b32 v180 offset:10752\n"
"s_waitcnt lgkmcnt(6)\n"
"v_mfma_f32_16x16x32_bf16 v[112:115], a[0:3], v[164:167], v[112:115]\n"
"ds_write_addtid_b32 v181 offset:14624\n"
"v_mfma_f32_16x16x32_bf16 v[112:115], a[32:35], v[140:143], v[112:115]\n"
"ds_write_addtid_b32 v182 offset:18560\n"
"buffer_load_dwordx4 v7, s[40:43], s20 offen offset:1024 lds\n"
"v_mfma_f32_16x16x32_bf16 v[112:115], a[64:67], v[156:159], v[112:115]\n"
"ds_write_addtid_b32 v183 offset:22432\n"
"v_mfma_f32_16x16x32_bf16 v[112:115], a[96:99], v[252:255], v[112:115]\n"
"v_mfma_f32_16x16x32_bf16 v[112:115], a[128:131], v[248:251], v[112:115]\n"
"buffer_load_dwordx4 v[180:183], v3, s[36:39], s49 offen offset:0 sc1\n"
"v_mfma_f32_16x16x32_bf16 v[112:115], a[160:163], v[232:235], v[112:115]\n"
"v_mfma_f32_16x16x32_bf16 v[112:115], a[192:195], v[224:227], v[112:115]\n"
"v_mfma_f32_16x16x32_bf16 v[112:115], a[224:227], v[244:247], v[112:115]\n"
"ds_write_addtid_b32 v148 offset:11008\n"
"v_mfma_f32_16x16x32_bf16 v[108:111], a[4:7], v[164:167], v[108:111]\n"
"ds_write_addtid_b32 v149 offset:14880\n"
"buffer_load_dwordx4 v8, s[40:43], s20 offen offset:2048 lds\n"
"v_mfma_f32_16x16x32_bf16 v[108:111], a[36:39], v[140:143], v[108:111]\n"
"ds_write_addtid_b32 v150 offset:18816\n"
"v_mfma_f32_16x16x32_bf16 v[108:111], a[68:71], v[156:159], v[108:111]\n"
"ds_write_addtid_b32 v151 offset:22688\n"
"v_mfma_f32_16x16x32_bf16 v[108:111], a[100:103], v[252:255], v[108:111]\n"
"v_mfma_f32_16x16x32_bf16 v[108:111], a[132:135], v[248:251], v[108:111]\n"
"buffer_load_dwordx4 v[164:167], v3, s[36:39], s50 offen offset:0 sc1\n"
"v_mfma_f32_16x16x32_bf16 v[108:111], a[164:167], v[232:235], v[108:111]\n"
"v_mfma_f32_16x16x32_bf16 v[108:111], a[196:199], v[224:227], v[108:111]\n"
"s_waitcnt lgkmcnt(0)\n"
"s_barrier\n"
"v_mfma_f32_16x16x32_bf16 v[108:111], a[228:231], v[244:247], v[108:111]\n"
"ds_read_b128 v[252:255], v15 offset:27136\n"
"s_waitcnt vmcnt(15)\n"
"v_mfma_f32_16x16x32_bf16 v[248:251], a[24:27], v[144:147], 0\n"
"ds_read_b128 v[244:247], v15 offset:27392\n"
"buffer_load_dwordx4 v9, s[40:43], s20 offen offset:3072 lds\n"
"v_mfma_f32_16x16x32_bf16 v[248:251], a[56:59], v[116:119], v[248:251]\n"
"v_max3_u32 v1, v145, v147, v117\n"
"v_max3_u32 v1, v119, v221, v1\n"
"v_mfma_f32_16x16x32_bf16 v[248:251], a[88:91], v[220:223], v[248:251]\n"
"v_mfma_f32_16x16x32_bf16 v[248:251], a[120:123], v[216:219], v[248:251]\n"
"ds_read_b64_tr_b16 v[232:233], v12 offset:8192\n"
"buffer_load_dwordx4 v[224:227], v3, s[36:39], s51 offen offset:0 sc1\n"
"s_waitcnt vmcnt(12)\n"
"v_mfma_f32_16x16x32_bf16 v[248:251], a[152:155], v[172:175], v[248:251]\n"
"ds_read_b64_tr_b16 v[234:235], v12 offset:8704\n"
"v_max3_u32 v1, v223, v217, v1\n"
"v_mfma_f32_16x16x32_bf16 v[248:251], a[184:187], v[160:163], v[248:251]\n"
"ds_read_b64_tr_b16 v[156:157], v13 offset:8192\n"
"v_max3_u32 v1, v219, v173, v1\n"
"v_mfma_f32_16x16x32_bf16 v[248:251], a[216:219], v[188:191], v[248:251]\n"
"ds_read_b64_tr_b16 v[158:159], v13 offset:8704\n"
"v_max3_u32 v1, v175, v161, v1\n"
"v_mfma_f32_16x16x32_bf16 v[248:251], a[248:251], v[152:155], v[248:251]\n"
"ds_read_b64_tr_b16 v[148:149], v10 offset:8192\n"
"buffer_load_dwordx4 v[140:143], v3, s[36:39], s52 offen offset:0 sc1\n"
"s_waitcnt lgkmcnt(5)\n"
"v_add_f32 v112, v112, v252\n"
"v_mfma_f32_16x16x32_bf16 v[100:103], a[28:31], v[144:147], 0\n"
"ds_read_b64_tr_b16 v[150:151], v10 offset:8704\n"
"v_max3_u32 v1, v163, v189, v1\n"
"v_add_f32 v113, v113, v253\n"
"v_mfma_f32_16x16x32_bf16 v[100:103], a[60:63], v[116:119], v[100:103]\n"
"ds_read_b64_tr_b16 v[96:97], v11 offset:8192\n"
"v_max3_u32 v1, v191, v153, v1\n"
"v_add_f32 v108, v108, v244\n"
"v_mfma_f32_16x16x32_bf16 v[100:103], a[92:95], v[220:223], v[100:103]\n"
"ds_read_b64_tr_b16 v[98:99], v11 offset:8704\n"
"buffer_load_dwordx4 v[92:95], v3, s[36:39], s53 offen offset:0 sc1\n"
"v_add_f32 v109, v109, v245\n"
"v_mfma_f32_16x16x32_bf16 v[100:103], a[124:127], v[216:219], v[100:103]\n"
"v_max_u32 v1, v155, v1\n"
"v_cmp_le_u32 vcc, 0xffc00000, v1\n"
"v_mfma_f32_16x16x32_bf16 v[100:103], a[156:159], v[172:175], v[100:103]\n"
"v_add_f32 v114, v114, v254\n"
"v_add_f32 v115, v115, v255\n"
"v_mfma_f32_16x16x32_bf16 v[100:103], a[188:191], v[160:163], v[100:103]\n"
"v_add_f32 v110, v110, v246\n"
"v_add_f32 v111, v111, v247\n"
"v_mfma_f32_16x16x32_bf16 v[100:103], a[220:223], v[188:191], v[100:103]\n"
"buffer_load_dwordx4 v[252:255], v3, s[36:39], s54 offen offset:0 sc1\n"
"v_cvt_pk_bf16_f32 v244, v112, v113\n"
"v_mfma_f32_16x16x32_bf16 v[100:103], a[252:255], v[152:155], v[100:103]\n"
"s_cbranch_vccnz .Lgs_slow6\n"
".Lgs_back6:\n"
"v_cvt_pk_bf16_f32 v245, v114, v115\n"
"v_mfma_f32_16x16x32_bf16 v[88:91], v[144:147], a[16:19], 0\n"
"ds_write_addtid_b32 v248 offset:9216\n"
"v_min_u32 v245, 0xffbfffff, v245\n"
"v_mfma_f32_16x16x32_bf16 v[88:91], v[116:119], a[48:51], v[88:91]\n"
"ds_write_addtid_b32 v249 offset:13088\n"
"buffer_load_dwordx4 v[112:115], v3, s[36:39], s55 offen offset:0 sc1\n"
"v_mfma_f32_16x16x32_bf16 v[88:91], v[220:223], a[80:83], v[88:91]\n"
"ds_write_addtid_b32 v250 offset:17024\n"
"v_cvt_pk_bf16_f32 v246, v108, v109\n"
"v_mfma_f32_16x16x32_bf16 v[88:91], v[216:219], a[112:115], v[88:91]\n"
"ds_write_addtid_b32 v251 offset:20896\n"
"v_cvt_pk_bf16_f32 v247, v110, v111\n"
"v_mfma_f32_16x16x32_bf16 v[88:91], v[172:175], a[144:147], v[88:91]\n"
"v_min_u32 v247, 0xffbfffff, v247\n"
"v_mfma_f32_16x16x32_bf16 v[88:91], v[160:163], a[176:179], v[88:91]\n"
"ds_read_b128 v[248:251], v16 offset:24576\n"
"buffer_store_dwordx4 v[244:247], v0, s[36:39], s80 offen offset:3072 sc1\n"
"v_mfma_f32_16x16x32_bf16 v[88:91], v[188:191], a[208:211], v[88:91]\n"
"ds_read_b128 v[108:111], v16 offset:24832\n"
"v_mfma_f32_16x16x32_bf16 v[88:91], v[152:155], a[240:243], v[88:91]\n"
"ds_write_addtid_b32 v100 offset:9472\n"
"v_mfma_f32_16x16x32_bf16 v[244:247], v[144:147], a[20:23], 0\n"
"ds_write_addtid_b32 v101 offset:13344\n"
"v_mfma_f32_16x16x32_bf16 v[244:247], v[116:119], a[52:55], v[244:247]\n"
"ds_write_addtid_b32 v102 offset:17280\n"
"v_mfma_f32_16x16x32_bf16 v[244:247], v[220:223], a[84:87], v[244:247]\n"
"ds_write_addtid_b32 v103 offset:21152\n"
"v_mfma_f32_16x16x32_bf16 v[244:247], v[216:219], a[116:119], v[244:247]\n"
"v_mfma_f32_16x16x32_bf16 v[244:247], v[172:175], a[148:151], v[244:247]\n"
"v_mfma_f32_16x16x32_bf16 v[244:247], v[160:163], a[180:183], v[244:247]\n"
"v_mfma_f32_16x16x32_bf16 v[244:247], v[188:191], a[212:215], v[244:247]\n"
"v_mfma_f32_16x16x32_bf16 v[244:247], v[152:155], a[244:247], v[244:247]\n"
"ds_write_addtid_b32 v88 offset:9728\n"
"s_waitcnt lgkmcnt(5)\n"
"v_mfma_f32_16x16x32_bf16 v[248:251], v[176:179], a[8:11], v[248:251]\n"
"ds_write_addtid_b32 v89 offset:13600\n"
"s_add_u32 m0, s77, 20480\n"
"s_add_u32 s20, s20, 524288\n"
"s_cmp_eq_u32 s20, s66\n"
"s_cselect_b32 s20, s82, s20\n"
"buffer_load_dwordx4 v6, s[40:43], s20 offen offset:0 lds\n"
"v_mfma_f32_16x16x32_bf16 v[248:251], v[228:231], a[40:43], v[248:251]\n"
"ds_write_addtid_b32 v90 offset:13440\n"
"v_mfma_f32_16x16x32_bf16 v[248:251], v[128:131], a[72:75], v[248:251]\n"
"ds_write_addtid_b32 v91 offset:17312\n"
"v_mfma_f32_16x16x32_bf16 v[248:251], v[240:243], a[104:107], v[248:251]\n"
"buffer_load_dwordx4 v[100:103], v4, s[36:39], s48 offen offset:0 sc1\n"
"v_mfma_f32_16x16x32_bf16 v[248:251], v[236:239], a[136:139], v[248:251]\n"
"v_mfma_f32_16x16x32_bf16 v[248:251], v[196:199], a[168:171], v[248:251]\n"
"ds_read_b128 v[88:91], v16 offset:25088\n"
"v_mfma_f32_16x16x32_bf16 v[248:251], v[124:127], a[200:203], v[248:251]\n"
"ds_read_b128 v[84:87], v16 offset:25344\n"
"v_mfma_f32_16x16x32_bf16 v[248:251], v[120:123], a[232:235], v[248:251]\n"
"ds_write_addtid_b32 v244 offset:5888\n"
"buffer_load_dwordx4 v7, s[40:43], s20 offen offset:1024 lds\n"
"v_mfma_f32_16x16x32_bf16 v[108:111], v[176:179], a[12:15], v[108:111]\n"
"ds_write_addtid_b32 v245 offset:9760\n"
"v_mfma_f32_16x16x32_bf16 v[108:111], v[228:231], a[44:47], v[108:111]\n"
"ds_write_addtid_b32 v246 offset:13696\n"
"v_mfma_f32_16x16x32_bf16 v[108:111], v[128:131], a[76:79], v[108:111]\n"
"ds_write_addtid_b32 v247 offset:17568\n"
"buffer_load_dwordx4 v[80:83], v5, s[36:39], s81 offen offset:1024 sc1\n"
"v_mfma_f32_16x16x32_bf16 v[108:111], v[240:243], a[108:111], v[108:111]\n"
"v_mfma_f32_16x16x32_bf16 v[108:111], v[236:239], a[140:143], v[108:111]\n"
"v_mfma_f32_16x16x32_bf16 v[108:111], v[196:199], a[172:175], v[108:111]\n"
"v_mfma_f32_16x16x32_bf16 v[108:111], v[124:127], a[204:207], v[108:111]\n"
"v_mfma_f32_16x16x32_bf16 v[108:111], v[120:123], a[236:239], v[108:111]\n"
"ds_write_addtid_b32 v248 offset:6144\n"
"buffer_load_dwordx4 v8, s[40:43], s20 offen offset:2048 lds\n"
"s_waitcnt lgkmcnt(5)\n"
"v_mfma_f32_16x16x32_bf16 v[88:91], a[0:3], v[176:179], v[88:91]\n"
"ds_write_addtid_b32 v249 offset:10016\n"
"s_waitcnt vmcnt(16)\n"
"v_max_u32 v14, v137, v139\n"
"v_mfma_f32_16x16x32_bf16 v[88:91], a[32:35], v[228:231], v[88:91]\n"
"ds_write_addtid_b32 v250 offset:13952\n"
"v_cmp_le_u32 vcc, 0xffc00000, v14\n"
"v_mfma_f32_16x16x32_bf16 v[88:91], a[64:67], v[128:131], v[88:91]\n"
"ds_write_addtid_b32 v251 offset:17824\n"
"buffer_load_dwordx4 v[244:247], v4, s[36:39], s49 offen offset:0 sc1\n"
"v_mfma_f32_16x16x32_bf16 v[88:91], a[96:99], v[240:243], v[88:91]\n"
"ds_read_b64_tr_b16 v[248:249], v12 offset:12288\n"
"s_cbranch_vccnz .Lgs_slow18\n"
".Lgs_back18:\n"
"v_mfma_f32_16x16x32_bf16 v[88:91], a[128:131], v[236:239], v[88:91]\n"
"ds_read_b64_tr_b16 v[250:251], v12 offset:12800\n"
"v_lshlrev_b32 v76, 16, v136\n"
"v_and_b32 v77, 0xffff0000, v136\n"
"v_mov_b32 v72, v23\n"
"v_mfma_f32_16x16x32_bf16 v[88:91], a[160:163], v[196:199], v[88:91]\n"
"ds_read_b64_tr_b16 v[68:69], v13 offset:12288\n"
"buffer_load_dwordx4 v[64:67], v4, s[36:39], s50 offen offset:0 sc1\n"
"v_mov_b32 v73, v23\n"
"v_mov_b32 v74, v23\n"
"v_mfma_f32_16x16x32_bf16 v[88:91], a[192:195], v[124:127], v[88:91]\n"
"ds_read_b64_tr_b16 v[70:71], v13 offset:12800\n"
"v_lshlrev_b32 v78, 16, v137\n"
"v_mov_b32 v75, v23\n"
"v_mfma_f32_16x16x32_bf16 v[88:91], a[224:227], v[120:123], v[88:91]\n"
"ds_write_addtid_b32 v108 offset:6400\n"
"v_and_b32 v79, 0xffff0000, v137\n"
"v_mov_b32 v60, v24\n"
"v_mfma_f32_16x16x32_bf16 v[84:87], a[4:7], v[176:179], v[84:87]\n"
"ds_write_addtid_b32 v109 offset:10272\n"
"buffer_load_dwordx4 v9, s[40:43], s20 offen offset:3072 lds\n"
"v_mov_b32 v61, v24\n"
"v_mfma_f32_16x16x32_bf16 v[84:87], a[36:39], v[228:231], v[84:87]\n"
"ds_write_addtid_b32 v110 offset:14208\n"
"v_lshlrev_b32 v176, 16, v138\n"
"v_mov_b32 v62, v24\n"
"v_mfma_f32_16x16x32_bf16 v[84:87], a[68:71], v[128:131], v[84:87]\n"
"ds_write_addtid_b32 v111 offset:18080\n"
"v_and_b32 v177, 0xffff0000, v138\n"
"v_mov_b32 v63, v24\n"
"v_mfma_f32_16x16x32_bf16 v[84:87], a[100:103], v[240:243], v[84:87]\n"
"ds_read_b64_tr_b16 v[228:229], v10 offset:12288\n"
"buffer_load_dwordx4 v[128:131], v4, s[36:39], s51 offen offset:0 sc1\n"
"v_mov_b32 v14, 0xbdd2d3e8\n"
"v_mfma_f32_16x16x32_bf16 v[84:87], a[132:135], v[236:239], v[84:87]\n"
"ds_read_b64_tr_b16 v[230:231], v10 offset:12800\n"
"v_lshlrev_b32 v178, 16, v139\n"
"v_mfma_f32_16x16x32_bf16 v[84:87], a[164:167], v[196:199], v[84:87]\n"
"ds_read_b64_tr_b16 v[240:241], v11 offset:12288\n"
"v_and_b32 v179, 0xffff0000, v139\n"
"v_mfma_f32_16x16x32_bf16 v[84:87], a[196:199], v[124:127], v[84:87]\n"
"s_waitcnt lgkmcnt(3)\n"
"s_barrier\n"
"buffer_load_dwordx4 v[236:239], v4, s[36:39], s52 offen offset:0 sc1\n"
"v_mfma_f32_16x16x32_bf16 v[84:87], a[228:231], v[120:123], v[84:87]\n"
"ds_read_b128 v[196:199], v15 offset:26624\n"
"s_waitcnt vmcnt(15)\n"
"v_mfma_f32_16x16x32_bf16 v[136:139], a[24:27], v[104:107], v[76:79]\n"
"ds_read_b128 v[124:127], v15 offset:26880\n"
"v_max3_u32 v24, v105, v107, v181\n"
"v_mfma_f32_16x16x32_bf16 v[136:139], a[56:59], v[180:183], v[136:139]\n"
"v_max3_u32 v24, v183, v165, v24\n"
"v_max3_u32 v24, v167, v225, v24\n"
"v_mfma_f32_16x16x32_bf16 v[136:139], a[88:91], v[164:167], v[136:139]\n"
"s_add_u32 m0, s77, 0\n"
"s_add_u32 s20, s20, 524288\n"
"buffer_load_dwordx4 v6, s[40:43], s20 offen offset:0 lds\n"
"v_mfma_f32_16x16x32_bf16 v[136:139], a[120:123], v[224:227], v[136:139]\n"
"ds_read_b64_tr_b16 v[242:243], v11 offset:12800\n"
"s_waitcnt vmcnt(12)\n"
"v_mfma_f32_16x16x32_bf16 v[136:139], a[152:155], v[140:143], v[136:139]\n"
"v_max3_u32 v24, v227, v141, v24\n"
"v_max3_u32 v24, v143, v93, v24\n"
"v_mfma_f32_16x16x32_bf16 v[136:139], a[184:187], v[92:95], v[136:139]\n"
"buffer_load_dwordx4 v[120:123], v4, s[36:39], s53 offen offset:0 sc1\n"
"v_max3_u32 v24, v95, v253, v24\n"
"v_mfma_f32_16x16x32_bf16 v[136:139], a[216:219], v[252:255], v[136:139]\n"
"v_max3_u32 v24, v255, v113, v24\n"
"v_max_u32 v24, v115, v24\n"
"v_mfma_f32_16x16x32_bf16 v[136:139], a[248:251], v[112:115], v[136:139]\n"
"s_waitcnt lgkmcnt(1)\n"
"v_add_f32 v88, v88, v196\n"
"v_add_f32 v89, v89, v197\n"
"v_mfma_f32_16x16x32_bf16 v[108:111], a[28:31], v[104:107], v[176:179]\n"
"v_add_f32 v90, v90, v198\n"
"v_add_f32 v91, v91, v199\n"
"v_mfma_f32_16x16x32_bf16 v[108:111], a[60:63], v[180:183], v[108:111]\n"
"buffer_load_dwordx4 v[56:59], v4, s[36:39], s54 offen offset:0 sc1\n"
"v_cmp_le_u32 vcc, 0xffc00000, v24\n"
"v_mfma_f32_16x16x32_bf16 v[108:111], a[92:95], v[164:167], v[108:111]\n"
"v_add_f32 v84, v84, v124\n"
"v_add_f32 v85, v85, v125\n"
"v_mfma_f32_16x16x32_bf16 v[108:111], a[124:127], v[224:227], v[108:111]\n"
"v_add_f32 v86, v86, v126\n"
"v_add_f32 v87, v87, v127\n"
"v_mfma_f32_16x16x32_bf16 v[108:111], a[156:159], v[140:143], v[108:111]\n"
"buffer_load_dwordx4 v[196:199], v4, s[36:39], s55 offen offset:0 sc1\n"
"v_cvt_pk_bf16_f32 v124, v88, v89\n"
"s_lshr_b32 s21, s80, 1\n"
"s_add_u32 s21, s21, 14745600\n"
"v_mfma_f32_16x16x32_bf16 v[108:111], a[188:191], v[92:95], v[108:111]\n"
"v_cvt_pk_bf16_f32 v125, v90, v91\n"
"v_min_u32 v125, 0xffbfffff, v125\n"
"v_mfma_f32_16x16x32_bf16 v[108:111], a[220:223], v[252:255], v[108:111]\n"
"v_cvt_pk_bf16_f32 v126, v84, v85\n"
"v_cvt_pk_bf16_f32 v127, v86, v87\n"
"v_mfma_f32_16x16x32_bf16 v[108:111], a[252:255], v[112:115], v[108:111]\n"
"s_cbranch_vccnz .Lgs_slow7\n"
".Lgs_back7:\n"
"v_min_u32 v127, 0xffbfffff, v127\n"
"v_mfma_f32_16x16x32_bf16 v[88:91], v[104:107], a[16:19], v[72:75]\n"
"ds_write_addtid_b32 v136 offset:24576\n"
"buffer_load_dwordx4 v7, s[40:43], s20 offen offset:1024 lds\n"
"v_mfma_f32_16x16x32_bf16 v[88:91], v[180:183], a[48:51], v[88:91]\n"
"ds_write_addtid_b32 v137 offset:28448\n"
"v_mfma_f32_16x16x32_bf16 v[88:91], v[164:167], a[80:83], v[88:91]\n"
"ds_write_addtid_b32 v138 offset:32384\n"
"v_mfma_f32_16x16x32_bf16 v[88:91], v[224:227], a[112:115], v[88:91]\n"
"ds_write_addtid_b32 v139 offset:36256\n"
"buffer_store_dwordx4 v[124:127], v0, s[36:39], s21 offen offset:0 sc1\n"
"v_mfma_f32_16x16x32_bf16 v[88:91], v[140:143], a[144:147], v[88:91]\n"
"v_mfma_f32_16x16x32_bf16 v[88:91], v[92:95], a[176:179], v[88:91]\n"
"ds_read_b128 v[176:179], v16 offset:25600\n"
"v_mfma_f32_16x16x32_bf16 v[88:91], v[252:255], a[208:211], v[88:91]\n"
"ds_read_b128 v[136:139], v16 offset:25856\n"
"v_mfma_f32_16x16x32_bf16 v[88:91], v[112:115], a[240:243], v[88:91]\n"
"ds_write_addtid_b32 v108 offset:24832\n"
"v_mfma_f32_16x16x32_bf16 v[124:127], v[104:107], a[20:23], v[60:63]\n"
"ds_write_addtid_b32 v109 offset:28704\n"
"v_mfma_f32_16x16x32_bf16 v[124:127], v[180:183], a[52:55], v[124:127]\n"
"ds_write_addtid_b32 v110 offset:32640\n"
"v_mfma_f32_16x16x32_bf16 v[124:127], v[164:167], a[84:87], v[124:127]\n"
"ds_write_addtid_b32 v111 offset:36512\n"
"v_mfma_f32_16x16x32_bf16 v[124:127], v[224:227], a[116:119], v[124:127]\n"
"v_mfma_f32_16x16x32_bf16 v[124:127], v[140:143], a[148:151], v[124:127]\n"
"v_mfma_f32_16x16x32_bf16 v[124:127], v[92:95], a[180:183], v[124:127]\n"
"v_mfma_f32_16x16x32_bf16 v[124:127], v[252:255], a[212:215], v[124:127]\n"
"v_mfma_f32_16x16x32_bf16 v[124:127], v[112:115], a[244:247], v[124:127]\n"
"ds_write_addtid_b32 v88 offset:25088\n"
"buffer_load_dwordx4 v8, s[40:43], s20 offen offset:2048 lds\n"
"s_waitcnt lgkmcnt(5)\n"
"v_mfma_f32_16x16x32_bf16 v[176:179], v[144:147], a[8:11], v[176:179]\n"
"ds_write_addtid_b32 v89 offset:28960\n"
"v_mfma_f32_16x16x32_bf16 v[176:179], v[116:119], a[40:43], v[176:179]\n"
"ds_write_addtid_b32 v90 offset:32896\n"
"v_mfma_f32_16x16x32_bf16 v[176:179], v[220:223], a[72:75], v[176:179]\n"
"ds_write_addtid_b32 v91 offset:36768\n"
"v_mfma_f32_16x16x32_bf16 v[176:179], v[216:219], a[104:107], v[176:179]\n"
"buffer_load_dwordx4 v[108:111], v3, s[36:39], s48 offen offset:1024 sc1\n"
"v_mfma_f32_16x16x32_bf16 v[176:179], v[172:175], a[136:139], v[176:179]\n"
"v_mfma_f32_16x16x32_bf16 v[176:179], v[160:163], a[168:171], v[176:179]\n"
"ds_read_b128 v[88:91], v16 offset:26112\n"
"v_mfma_f32_16x16x32_bf16 v[176:179], v[188:191], a[200:203], v[176:179]\n"
"ds_read_b128 v[84:87], v16 offset:26368\n"
"v_mfma_f32_16x16x32_bf16 v[176:179], v[152:155], a[232:235], v[176:179]\n"
"ds_write_addtid_b32 v124 offset:25344\n"
"buffer_load_dwordx4 v9, s[40:43], s20 offen offset:3072 lds\n"
"v_mfma_f32_16x16x32_bf16 v[136:139], v[144:147], a[12:15], v[136:139]\n"
"ds_write_addtid_b32 v125 offset:29216\n"
"v_mfma_f32_16x16x32_bf16 v[136:139], v[116:119], a[44:47], v[136:139]\n"
"ds_write_addtid_b32 v126 offset:33152\n"
"v_mfma_f32_16x16x32_bf16 v[136:139], v[220:223], a[76:79], v[136:139]\n"
"ds_write_addtid_b32 v127 offset:37024\n"
"buffer_load_dwordx4 v[76:79], v3, s[36:39], s49 offen offset:1024 sc1\n"
"v_mfma_f32_16x16x32_bf16 v[136:139], v[216:219], a[108:111], v[136:139]\n"
"buffer_load_dwordx4 v[124:127], v5, s[36:39], s81 offen offset:2048 sc1\n"
"v_mfma_f32_16x16x32_bf16 v[136:139], v[172:175], a[140:143], v[136:139]\n"
"v_mfma_f32_16x16x32_bf16 v[136:139], v[160:163], a[172:175], v[136:139]\n"
"v_mfma_f32_16x16x32_bf16 v[136:139], v[188:191], a[204:207], v[136:139]\n"
"v_mfma_f32_16x16x32_bf16 v[136:139], v[152:155], a[236:239], v[136:139]\n"
"ds_write_addtid_b32 v176 offset:27136\n"
"s_waitcnt lgkmcnt(5)\n"
"v_mfma_f32_16x16x32_bf16 v[88:91], a[0:3], v[144:147], v[88:91]\n"
"ds_write_addtid_b32 v177 offset:31008\n"
"s_waitcnt vmcnt(17)\n"
"v_max_u32 v24, v81, v83\n"
"v_mfma_f32_16x16x32_bf16 v[88:91], a[32:35], v[116:119], v[88:91]\n"
"ds_write_addtid_b32 v178 offset:34944\n"
"s_add_u32 m0, s77, 4096\n"
"s_add_u32 s20, s20, 524288\n"
"s_cmp_eq_u32 s20, s66\n"
"s_cselect_b32 s20, s82, s20\n"
"buffer_load_dwordx4 v6, s[40:43], s20 offen offset:0 lds\n"
"v_mfma_f32_16x16x32_bf16 v[88:91], a[64:67], v[220:223], v[88:91]\n"
"ds_write_addtid_b32 v179 offset:34720\n"
"v_cmp_le_u32 vcc, 0xffc00000, v24\n"
"v_mfma_f32_16x16x32_bf16 v[88:91], a[96:99], v[216:219], v[88:91]\n"
"v_mfma_f32_16x16x32_bf16 v[88:91], a[128:131], v[172:175], v[88:91]\n"
"s_cbranch_vccnz .Lgs_slow19\n"
".Lgs_back19:\n"
"v_mfma_f32_16x16x32_bf16 v[88:91], a[160:163], v[160:163], v[88:91]\n"
"buffer_load_dwordx4 v[176:179], v3, s[36:39], s50 offen offset:1024 sc1\n"
"v_lshlrev_b32 v52, 16, v80\n"
"v_mfma_f32_16x16x32_bf16 v[88:91], a[192:195], v[188:191], v[88:91]\n"
"v_and_b32 v53, 0xffff0000, v80\n"
"v_lshlrev_b32 v54, 16, v81\n"
"v_mfma_f32_16x16x32_bf16 v[88:91], a[224:227], v[152:155], v[88:91]\n"
"ds_write_addtid_b32 v136 offset:23296\n"
"v_and_b32 v55, 0xffff0000, v81\n"
"v_mfma_f32_16x16x32_bf16 v[84:87], a[4:7], v[144:147], v[84:87]\n"
"ds_write_addtid_b32 v137 offset:27168\n"
"buffer_load_dwordx4 v7, s[40:43], s20 offen offset:1024 lds\n"
"v_mfma_f32_16x16x32_bf16 v[84:87], a[36:39], v[116:119], v[84:87]\n"
"ds_write_addtid_b32 v138 offset:31104\n"
"v_lshlrev_b32 v144, 16, v82\n"
"v_mfma_f32_16x16x32_bf16 v[84:87], a[68:71], v[220:223], v[84:87]\n"
"ds_write_addtid_b32 v139 offset:34976\n"
"v_and_b32 v145, 0xffff0000, v82\n"
"v_mfma_f32_16x16x32_bf16 v[84:87], a[100:103], v[216:219], v[84:87]\n"
"v_mfma_f32_16x16x32_bf16 v[84:87], a[132:135], v[172:175], v[84:87]\n"
"buffer_load_dwordx4 v[220:223], v3, s[36:39], s51 offen offset:1024 sc1\n"
"v_lshlrev_b32 v146, 16, v83\n"
"v_mfma_f32_16x16x32_bf16 v[84:87], a[164:167], v[160:163], v[84:87]\n"
"v_mfma_f32_16x16x32_bf16 v[84:87], a[196:199], v[188:191], v[84:87]\n"
"s_waitcnt lgkmcnt(0)\n"
"s_barrier\n"
"v_mfma_f32_16x16x32_bf16 v[84:87], a[228:231], v[152:155], v[84:87]\n"
"ds_read_b128 v[216:219], v15 offset:27136\n"
"s_waitcnt vmcnt(16)\n"
"v_mfma_f32_16x16x32_bf16 v[188:191], a[24:27], v[100:103], v[52:55]\n"
"ds_read_b128 v[172:175], v15 offset:27392\n"
"buffer_load_dwordx4 v8, s[40:43], s20 offen offset:2048 lds\n"
"v_mfma_f32_16x16x32_bf16 v[188:191], a[56:59], v[244:247], v[188:191]\n"
"v_mfma_f32_16x16x32_bf16 v[188:191], a[88:91], v[64:67], v[188:191]\n"
"v_mfma_f32_16x16x32_bf16 v[188:191], a[120:123], v[128:131], v[188:191]\n"
"v_and_b32 v147, 0xffff0000, v83\n"
"s_waitcnt vmcnt(12)\n"
"v_mfma_f32_16x16x32_bf16 v[188:191], a[152:155], v[236:239], v[188:191]\n"
"buffer_load_dwordx4 v[160:163], v3, s[36:39], s52 offen offset:1024 sc1\n"
"v_mfma_f32_16x16x32_bf16 v[188:191], a[184:187], v[120:123], v[188:191]\n"
"v_mfma_f32_16x16x32_bf16 v[188:191], a[216:219], v[56:59], v[188:191]\n"
"s_waitcnt lgkmcnt(0)\n"
"v_add_f32 v88, v88, v216\n"
"v_mfma_f32_16x16x32_bf16 v[188:191], a[248:251], v[196:199], v[188:191]\n"
"buffer_load_dwordx4 v[152:155], v3, s[36:39], s53 offen offset:1024 sc1\n"
"v_add_f32 v89, v89, v217\n"
"v_mfma_f32_16x16x32_bf16 v[136:139], a[28:31], v[100:103], v[144:147]\n"
"v_add_f32 v90, v90, v218\n"
"v_add_f32 v91, v91, v219\n"
"v_mfma_f32_16x16x32_bf16 v[136:139], a[60:63], v[244:247], v[136:139]\n"
"v_add_f32 v84, v84, v172\n"
"v_add_f32 v85, v85, v173\n"
"v_mfma_f32_16x16x32_bf16 v[136:139], a[92:95], v[64:67], v[136:139]\n"
"v_add_f32 v86, v86, v174\n"
"v_add_f32 v87, v87, v175\n"
"v_mfma_f32_16x16x32_bf16 v[136:139], a[124:127], v[128:131], v[136:139]\n"
"buffer_load_dwordx4 v[216:219], v3, s[36:39], s54 offen offset:1024 sc1\n"
".Lgs_pad1:\n"
"s_cbranch_execz .Lgs_pad5\n"
"v_cvt_pk_bf16_f32 v172, v88, v89\n"
"v_mfma_f32_16x16x32_bf16 v[136:139], a[156:159], v[236:239], v[136:139]\n"
"v_cvt_pk_bf16_f32 v173, v90, v91\n"
"v_min_u32 v173, 0xffbfffff, v173\n"
"v_mfma_f32_16x16x32_bf16 v[136:139], a[188:191], v[120:123], v[136:139]\n"
"v_cvt_pk_bf16_f32 v174, v84, v85\n"
"v_cvt_pk_bf16_f32 v175, v86, v87\n"
"v_mfma_f32_16x16x32_bf16 v[136:139], a[220:223], v[56:59], v[136:139]\n"
"s_lshr_b32 s21, s80, 1\n"
"s_add_u32 s21, s21, 14745600\n"
"v_mfma_f32_16x16x32_bf16 v[136:139], a[252:255], v[196:199], v[136:139]\n"
"ds_write_addtid_b32 v188 offset:21504\n"
"buffer_load_dwordx4 v[144:147], v3, s[36:39], s55 offen offset:1024 sc1\n"
"v_mfma_f32_16x16x32_bf16 v[116:119], v[100:103], a[16:19], v[72:75]\n"
"ds_write_addtid_b32 v189 offset:25376\n"
"v_min_u32 v175, 0xffbfffff, v175\n"
"v_mfma_f32_16x16x32_bf16 v[116:119], v[244:247], a[48:51], v[116:119]\n"
"ds_write_addtid_b32 v190 offset:29312\n"
"v_mfma_f32_16x16x32_bf16 v[116:119], v[64:67], a[80:83], v[116:119]\n"
"ds_write_addtid_b32 v191 offset:33184\n"
"buffer_store_dwordx4 v[172:175], v0, s[36:39], s21 offen offset:1024 sc1\n"
"v_mfma_f32_16x16x32_bf16 v[116:119], v[128:131], a[112:115], v[116:119]\n"
"v_mfma_f32_16x16x32_bf16 v[116:119], v[236:239], a[144:147], v[116:119]\n"
"v_mfma_f32_16x16x32_bf16 v[116:119], v[120:123], a[176:179], v[116:119]\n"
"ds_read_b128 v[188:191], v16 offset:24576\n"
"v_mfma_f32_16x16x32_bf16 v[116:119], v[56:59], a[208:211], v[116:119]\n"
"ds_read_b128 v[172:175], v16 offset:24832\n"
"v_mfma_f32_16x16x32_bf16 v[116:119], v[196:199], a[240:243], v[116:119]\n"
"ds_write_addtid_b32 v136 offset:21760\n"
"v_mfma_f32_16x16x32_bf16 v[88:91], v[100:103], a[20:23], v[60:63]\n"
"ds_write_addtid_b32 v137 offset:25632\n"
"v_mfma_f32_16x16x32_bf16 v[88:91], v[244:247], a[52:55], v[88:91]\n"
"ds_write_addtid_b32 v138 offset:29568\n"
"v_mfma_f32_16x16x32_bf16 v[88:91], v[64:67], a[84:87], v[88:91]\n"
"ds_write_addtid_b32 v139 offset:33440\n"
"v_mfma_f32_16x16x32_bf16 v[88:91], v[128:131], a[116:119], v[88:91]\n"
"v_mfma_f32_16x16x32_bf16 v[88:91], v[236:239], a[148:151], v[88:91]\n"
"v_mfma_f32_16x16x32_bf16 v[88:91], v[120:123], a[180:183], v[88:91]\n"
"v_mfma_f32_16x16x32_bf16 v[88:91], v[56:59], a[212:215], v[88:91]\n"
"buffer_load_dwordx4 v9, s[40:43], s20 offen offset:3072 lds\n"
"v_mfma_f32_16x16x32_bf16 v[88:91], v[196:199], a[244:247], v[88:91]\n"
"ds_write_addtid_b32 v116 offset:22016\n"
"s_waitcnt lgkmcnt(5)\n"
"v_mfma_f32_16x16x32_bf16 v[188:191], v[104:107], a[8:11], v[188:191]\n"
"ds_write_addtid_b32 v117 offset:25888\n"
"v_mfma_f32_16x16x32_bf16 v[188:191], v[180:183], a[40:43], v[188:191]\n"
"ds_write_addtid_b32 v118 offset:29824\n"
"v_mfma_f32_16x16x32_bf16 v[188:191], v[164:167], a[72:75], v[188:191]\n"
"ds_write_addtid_b32 v119 offset:33696\n"
"buffer_load_dwordx4 v[136:139], v4, s[36:39], s48 offen offset:1024 sc1\n"
"v_mfma_f32_16x16x32_bf16 v[188:191], v[224:227], a[104:107], v[188:191]\n"
"v_mfma_f32_16x16x32_bf16 v[188:191], v[140:143], a[136:139], v[188:191]\n"
"v_mfma_f32_16x16x32_bf16 v[188:191], v[92:95], a[168:171], v[188:191]\n"
"ds_read_b128 v[116:119], v16 offset:25088\n"
"s_add_u32 m0, s77, 8192\n"
"s_add_u32 s20, s20, 524288\n"
"buffer_load_dwordx4 v6, s[40:43], s20 offen offset:0 lds\n"
"v_mfma_f32_16x16x32_bf16 v[188:191], v[252:255], a[200:203], v[188:191]\n"
"ds_read_b128 v[84:87], v16 offset:25344\n"
"v_mfma_f32_16x16x32_bf16 v[188:191], v[112:115], a[232:235], v[188:191]\n"
"ds_write_addtid_b32 v88 offset:18176\n"
"v_mfma_f32_16x16x32_bf16 v[172:175], v[104:107], a[12:15], v[172:175]\n"
"ds_write_addtid_b32 v89 offset:22048\n"
"v_mfma_f32_16x16x32_bf16 v[172:175], v[180:183], a[44:47], v[172:175]\n"
"ds_write_addtid_b32 v90 offset:25984\n"
"buffer_load_dwordx4 v[80:83], v4, s[36:39], s49 offen offset:1024 sc1\n"
"v_mfma_f32_16x16x32_bf16 v[172:175], v[164:167], a[76:79], v[172:175]\n"
"ds_write_addtid_b32 v91 offset:29856\n"
"buffer_load_dwordx4 v[52:55], v5, s[36:39], s81 offen offset:3072 sc1\n"
"v_mfma_f32_16x16x32_bf16 v[172:175], v[224:227], a[108:111], v[172:175]\n"
"v_mfma_f32_16x16x32_bf16 v[172:175], v[140:143], a[140:143], v[172:175]\n"
"v_mfma_f32_16x16x32_bf16 v[172:175], v[92:95], a[172:175], v[172:175]\n"
"v_mfma_f32_16x16x32_bf16 v[172:175], v[252:255], a[204:207], v[172:175]\n"
"v_mfma_f32_16x16x32_bf16 v[172:175], v[112:115], a[236:239], v[172:175]\n"
"ds_write_addtid_b32 v188 offset:18432\n"
"s_waitcnt lgkmcnt(5)\n"
"v_mfma_f32_16x16x32_bf16 v[116:119], a[0:3], v[104:107], v[116:119]\n"
"ds_write_addtid_b32 v189 offset:22304\n"
"s_waitcnt vmcnt(15)\n"
"v_max_u32 v24, v125, v127\n"
"v_mfma_f32_16x16x32_bf16 v[116:119], a[32:35], v[180:183], v[116:119]\n"
"ds_write_addtid_b32 v190 offset:26240\n"
"buffer_load_dwordx4 v7, s[40:43], s20 offen offset:1024 lds\n"
"v_mfma_f32_16x16x32_bf16 v[116:119], a[64:67], v[164:167], v[116:119]\n"
"ds_write_addtid_b32 v191 offset:30112\n"
"v_cmp_le_u32 vcc, 0xffc00000, v24\n"
"v_mfma_f32_16x16x32_bf16 v[116:119], a[96:99], v[224:227], v[116:119]\n"
"v_mfma_f32_16x16x32_bf16 v[116:119], a[128:131], v[140:143], v[116:119]\n"
"buffer_load_dwordx4 v[188:191], v4, s[36:39], s50 offen offset:1024 sc1\n"
"s_cbranch_vccnz .Lgs_slow20\n"
".Lgs_back20:\n"
"v_mfma_f32_16x16x32_bf16 v[116:119], a[160:163], v[92:95], v[116:119]\n"
"v_lshlrev_b32 v88, 16, v124\n"
"v_and_b32 v89, 0xffff0000, v124\n"
"v_mfma_f32_16x16x32_bf16 v[116:119], a[192:195], v[252:255], v[116:119]\n"
"v_lshlrev_b32 v90, 16, v125\n"
"v_and_b32 v91, 0xffff0000, v125\n"
"v_mfma_f32_16x16x32_bf16 v[116:119], a[224:227], v[112:115], v[116:119]\n"
"ds_write_addtid_b32 v172 offset:18688\n"
"v_lshlrev_b32 v48, 16, v126\n"
"v_mfma_f32_16x16x32_bf16 v[84:87], a[4:7], v[104:107], v[84:87]\n"
"ds_write_addtid_b32 v173 offset:22560\n"
"buffer_load_dwordx4 v8, s[40:43], s20 offen offset:2048 lds\n"
"v_mfma_f32_16x16x32_bf16 v[84:87], a[36:39], v[180:183], v[84:87]\n"
"ds_write_addtid_b32 v174 offset:26496\n"
"v_and_b32 v49, 0xffff0000, v126\n"
"v_mfma_f32_16x16x32_bf16 v[84:87], a[68:71], v[164:167], v[84:87]\n"
"ds_write_addtid_b32 v175 offset:30368\n"
"v_lshlrev_b32 v50, 16, v127\n"
"v_mfma_f32_16x16x32_bf16 v[84:87], a[100:103], v[224:227], v[84:87]\n"
"buffer_load_dwordx4 v[180:183], v4, s[36:39], s51 offen offset:1024 sc1\n"
"v_and_b32 v51, 0xffff0000, v127\n"
"v_mfma_f32_16x16x32_bf16 v[84:87], a[132:135], v[140:143], v[84:87]\n"
"v_mfma_f32_16x16x32_bf16 v[84:87], a[164:167], v[92:95], v[84:87]\n"
"v_mfma_f32_16x16x32_bf16 v[84:87], a[196:199], v[252:255], v[84:87]\n"
"s_waitcnt lgkmcnt(0)\n"
"s_barrier\n"
"v_mfma_f32_16x16x32_bf16 v[84:87], a[228:231], v[112:115], v[84:87]\n"
"ds_read_b128 v[252:255], v15 offset:26624\n"
"buffer_load_dwordx4 v9, s[40:43], s20 offen offset:3072 lds\n"
"s_waitcnt vmcnt(16)\n"
"v_mfma_f32_16x16x32_bf16 v[224:227], a[24:27], v[108:111], v[88:91]\n"
"ds_read_b128 v[172:175], v15 offset:26880\n"
"v_mfma_f32_16x16x32_bf16 v[224:227], a[56:59], v[76:79], v[224:227]\n"
"v_mfma_f32_16x16x32_bf16 v[224:227], a[88:91], v[176:179], v[224:227]\n"
"v_mfma_f32_16x16x32_bf16 v[224:227], a[120:123], v[220:223], v[224:227]\n"
"buffer_load_dwordx4 v[164:167], v4, s[36:39], s52 offen offset:1024 sc1\n"
"s_waitcnt vmcnt(12)\n"
"v_mfma_f32_16x16x32_bf16 v[224:227], a[152:155], v[160:163], v[224:227]\n"
"v_mfma_f32_16x16x32_bf16 v[224:227], a[184:187], v[152:155], v[224:227]\n"
"v_mfma_f32_16x16x32_bf16 v[224:227], a[216:219], v[216:219], v[224:227]\n"
"s_waitcnt lgkmcnt(0)\n"
"v_add_f32 v116, v116, v252\n"
"v_mfma_f32_16x16x32_bf16 v[224:227], a[248:251], v[144:147], v[224:227]\n"
"buffer_load_dwordx4 v[140:143], v4, s[36:39], s53 offen offset:1024 sc1\n"
"v_add_f32 v117, v117, v253\n"
"v_mfma_f32_16x16x32_bf16 v[124:127], a[28:31], v[108:111], v[48:51]\n"
"v_add_f32 v118, v118, v254\n"
"v_add_f32 v119, v119, v255\n"
"v_mfma_f32_16x16x32_bf16 v[124:127], a[60:63], v[76:79], v[124:127]\n"
"v_add_f32 v84, v84, v172\n"
"v_add_f32 v85, v85, v173\n"
"v_mul_f32 v252, v116, v116\n"
"v_mul_f32 v253, v117, v117\n"
"v_mul_f32 v254, v118, v118\n"
"v_mfma_f32_16x16x32_bf16 v[124:127], a[92:95], v[176:179], v[124:127]\n"
"buffer_load_dwordx4 v[112:115], v4, s[36:39], s54 offen offset:1024 sc1\n"
"v_add_f32 v86, v86, v174\n"
"v_mul_f32 v255, v119, v119\n"
"v_fmaak_f32 v252, v252, v14, 0xc0135761\n"
"v_mfma_f32_16x16x32_bf16 v[124:127], a[124:127], v[220:223], v[124:127]\n"
"v_add_f32 v87, v87, v175\n"
"v_fmaak_f32 v253, v253, v14, 0xc0135761\n"
"v_fmaak_f32 v254, v254, v14, 0xc0135761\n"
"v_fmaak_f32 v255, v255, v14, 0xc0135761\n"
"v_mfma_f32_16x16x32_bf16 v[124:127], a[156:159], v[160:163], v[124:127]\n"
"v_mul_f32 v252, v252, v116\n"
"v_mul_f32 v253, v253, v117\n"
"v_mul_f32 v254, v254, v118\n"
"v_mul_f32 v255, v255, v119\n"
"v_mfma_f32_16x16x32_bf16 v[124:127], a[188:191], v[152:155], v[124:127]\n"
"v_exp_f32 v252, v252\n"
"v_exp_f32 v253, v253\n"
"v_mfma_f32_16x16x32_bf16 v[124:127], a[220:223], v[216:219], v[124:127]\n"
"buffer_load_dwordx4 v[172:175], v4, s[36:39], s55 offen offset:1024 sc1\n"
"v_exp_f32 v254, v254\n"
"v_mfma_f32_16x16x32_bf16 v[124:127], a[252:255], v[144:147], v[124:127]\n"
"ds_write_addtid_b32 v224 offset:16384\n"
"v_exp_f32 v255, v255\n"
"v_add_f32 v252, 1.0, v252\n"
"v_mfma_f32_16x16x32_bf16 v[104:107], v[108:111], a[16:19], v[72:75]\n"
"ds_write_addtid_b32 v225 offset:20256\n"
"s_add_u32 m0, s77, 12288\n"
"s_xor_b32 s20, s65, s67\n"
"s_xor_b32 s82, s82, s67\n"
"s_xor_b32 s66, s66, s67\n"
"buffer_load_dwordx4 v6, s[40:43], s20 offen offset:0 lds\n"
"v_mfma_f32_16x16x32_bf16 v[104:107], v[76:79], a[48:51], v[104:107]\n"
"ds_write_addtid_b32 v226 offset:20096\n"
"v_add_f32 v253, 1.0, v253\n"
"v_add_f32 v254, 1.0, v254\n"
"v_add_f32 v255, 1.0, v255\n"
"v_mfma_f32_16x16x32_bf16 v[104:107], v[176:179], a[80:83], v[104:107]\n"
"ds_write_addtid_b32 v227 offset:23968\n"
"v_rcp_f32 v252, v252\n"
"v_mfma_f32_16x16x32_bf16 v[104:107], v[220:223], a[112:115], v[104:107]\n"
"buffer_load_dwordx4 v[224:227], v3, s[36:39], s48 offen offset:2048 sc1\n"
"v_rcp_f32 v253, v253\n"
"v_mfma_f32_16x16x32_bf16 v[104:107], v[160:163], a[144:147], v[104:107]\n"
"v_rcp_f32 v254, v254\n"
"v_rcp_f32 v255, v255\n"
"v_mfma_f32_16x16x32_bf16 v[104:107], v[152:155], a[176:179], v[104:107]\n"
"ds_read_b128 v[92:95], v16 offset:25600\n"
"buffer_load_dwordx4 v[88:91], v3, s[36:39], s49 offen offset:2048 sc1\n"
"v_mul_f32 v252, v252, v116\n"
"v_mfma_f32_16x16x32_bf16 v[104:107], v[216:219], a[208:211], v[104:107]\n"
"ds_read_b128 v[48:51], v16 offset:25856\n"
"v_mul_f32 v253, v253, v117\n"
"v_mul_f32 v254, v254, v118\n"
"v_mfma_f32_16x16x32_bf16 v[104:107], v[144:147], a[240:243], v[104:107]\n"
"ds_write_addtid_b32 v124 offset:12544\n"
"v_mul_f32 v255, v255, v119\n"
"v_cvt_pk_bf16_f32 v44, v252, v253\n"
"v_mfma_f32_16x16x32_bf16 v[40:43], v[108:111], a[20:23], v[60:63]\n"
"ds_write_addtid_b32 v125 offset:16416\n"
"buffer_load_dwordx4 v7, s[40:43], s20 offen offset:1024 lds\n"
"v_cvt_pk_bf16_f32 v45, v254, v255\n"
"v_mfma_f32_16x16x32_bf16 v[40:43], v[76:79], a[52:55], v[40:43]\n"
"ds_write_addtid_b32 v126 offset:20352\n"
"v_min_u32 v45, 0xffbfffff, v45\n"
"v_mul_f32 v252, v84, v84\n"
"v_mfma_f32_16x16x32_bf16 v[40:43], v[176:179], a[84:87], v[40:43]\n"
"ds_write_addtid_b32 v127 offset:24224\n"
"v_mul_f32 v253, v85, v85\n"
"v_mul_f32 v254, v86, v86\n"
"v_mfma_f32_16x16x32_bf16 v[40:43], v[220:223], a[116:119], v[40:43]\n"
"v_mul_f32 v255, v87, v87\n"
"v_fmaak_f32 v252, v252, v14, 0xc0135761\n"
"v_mfma_f32_16x16x32_bf16 v[40:43], v[160:163], a[148:151], v[40:43]\n"
"buffer_load_dwordx4 v[124:127], v3, s[36:39], s50 offen offset:2048 sc1\n"
"v_fmaak_f32 v253, v253, v14, 0xc0135761\n"
"v_mfma_f32_16x16x32_bf16 v[40:43], v[152:155], a[180:183], v[40:43]\n"
"v_fmaak_f32 v254, v254, v14, 0xc0135761\n"
"v_fmaak_f32 v255, v255, v14, 0xc0135761\n"
"v_mfma_f32_16x16x32_bf16 v[40:43], v[216:219], a[212:215], v[40:43]\n"
"v_mul_f32 v252, v252, v84\n"
"v_mul_f32 v253, v253, v85\n"
"v_mfma_f32_16x16x32_bf16 v[40:43], v[144:147], a[244:247], v[40:43]\n"
"ds_write_addtid_b32 v104 offset:12800\n"
"buffer_load_dwordx4 v8, s[40:43], s20 offen offset:2048 lds\n"
"s_waitcnt lgkmcnt(5)\n"
"v_mfma_f32_16x16x32_bf16 v[92:95], v[100:103], a[8:11], v[92:95]\n"
"ds_write_addtid_b32 v105 offset:16672\n"
"v_mul_f32 v254, v254, v86\n"
"v_mfma_f32_16x16x32_bf16 v[92:95], v[244:247], a[40:43], v[92:95]\n"
"ds_write_addtid_b32 v106 offset:20608\n"
"v_mul_f32 v255, v255, v87\n"
"v_mfma_f32_16x16x32_bf16 v[92:95], v[64:67], a[72:75], v[92:95]\n"
"ds_write_addtid_b32 v107 offset:24480\n"
"v_exp_f32 v252, v252\n"
"v_mfma_f32_16x16x32_bf16 v[92:95], v[128:131], a[104:107], v[92:95]\n"
"v_exp_f32 v253, v253\n"
"v_mfma_f32_16x16x32_bf16 v[92:95], v[236:239], a[136:139], v[92:95]\n"
"v_exp_f32 v254, v254\n"
"v_mfma_f32_16x16x32_bf16 v[92:95], v[120:123], a[168:171], v[92:95]\n"
"ds_read_b128 v[116:119], v16 offset:26112\n"
"v_exp_f32 v255, v255\n"
"s_lshr_b32 s22, s81, 1\n"
"s_add_u32 s22, s22, 14745600\n"
"v_mfma_f32_16x16x32_bf16 v[92:95], v[56:59], a[200:203], v[92:95]\n"
"ds_read_b128 v[104:107], v16 offset:26368\n"
"buffer_load_dwordx4 v9, s[40:43], s20 offen offset:3072 lds\n"
"v_mfma_f32_16x16x32_bf16 v[92:95], v[196:199], a[232:235], v[92:95]\n"
"ds_write_addtid_b32 v40 offset:13056\n"
"v_add_f32 v252, 1.0, v252\n"
"v_mfma_f32_16x16x32_bf16 v[48:51], v[100:103], a[12:15], v[48:51]\n"
"ds_write_addtid_b32 v41 offset:16928\n"
"v_add_f32 v253, 1.0, v253\n"
"v_mfma_f32_16x16x32_bf16 v[48:51], v[244:247], a[44:47], v[48:51]\n"
"ds_write_addtid_b32 v42 offset:20864\n"
"v_add_f32 v254, 1.0, v254\n"
"v_mfma_f32_16x16x32_bf16 v[48:51], v[64:67], a[76:79], v[48:51]\n"
"ds_write_addtid_b32 v43 offset:24736\n"
"v_add_f32 v255, 1.0, v255\n"
"buffer_load_dwordx4 v[36:39], v5, s[36:39], s22 offen offset:0 sc1\n"
"v_mfma_f32_16x16x32_bf16 v[48:51], v[128:131], a[108:111], v[48:51]\n"
"v_rcp_f32 v252, v252\n"
"v_mfma_f32_16x16x32_bf16 v[48:51], v[236:239], a[140:143], v[48:51]\n"
"v_rcp_f32 v253, v253\n"
"v_mfma_f32_16x16x32_bf16 v[48:51], v[120:123], a[172:175], v[48:51]\n"
"v_rcp_f32 v254, v254\n"
"v_mfma_f32_16x16x32_bf16 v[48:51], v[56:59], a[204:207], v[48:51]\n"
"v_rcp_f32 v255, v255\n"
"v_mul_f32 v252, v252, v84\n"
"v_mfma_f32_16x16x32_bf16 v[48:51], v[196:199], a[236:239], v[48:51]\n"
"ds_write_addtid_b32 v92 offset:14848\n"
"v_mul_f32 v253, v253, v85\n"
"s_waitcnt lgkmcnt(5)\n"
"v_mfma_f32_16x16x32_bf16 v[116:119], a[0:3], v[100:103], v[116:119]\n"
"ds_write_addtid_b32 v93 offset:18720\n"
"v_mul_f32 v254, v254, v86\n"
"s_waitcnt vmcnt(17)\n"
"v_max_u32 v24, v53, v55\n"
"v_mfma_f32_16x16x32_bf16 v[116:119], a[32:35], v[244:247], v[116:119]\n"
"ds_write_addtid_b32 v94 offset:22656\n"
"v_mul_f32 v255, v255, v87\n"
"v_cmp_le_u32 vcc, 0xffc00000, v24\n"
"v_mfma_f32_16x16x32_bf16 v[116:119], a[64:67], v[64:67], v[116:119]\n"
"ds_write_addtid_b32 v95 offset:26528\n"
"v_cvt_pk_bf16_f32 v46, v252, v253\n"
"v_mfma_f32_16x16x32_bf16 v[116:119], a[96:99], v[128:131], v[116:119]\n"
"v_cvt_pk_bf16_f32 v47, v254, v255\n"
"v_min_u32 v47, 0xffbfffff, v47\n"
"s_cbranch_vccnz .Lgs_slow21\n"
".Lgs_back21:\n"
"v_mfma_f32_16x16x32_bf16 v[116:119], a[128:131], v[236:239], v[116:119]\n"
"v_lshlrev_b32 v252, 16, v52\n"
"v_and_b32 v253, 0xffff0000, v52\n"
"v_mfma_f32_16x16x32_bf16 v[116:119], a[160:163], v[120:123], v[116:119]\n"
"v_mfma_f32_16x16x32_bf16 v[116:119], a[192:195], v[56:59], v[116:119]\n"
"v_mfma_f32_16x16x32_bf16 v[116:119], a[224:227], v[196:199], v[116:119]\n"
"ds_write_addtid_b32 v48 offset:15104\n"
"v_lshlrev_b32 v254, 16, v53\n"
"v_mfma_f32_16x16x32_bf16 v[104:107], a[4:7], v[100:103], v[104:107]\n"
"ds_write_addtid_b32 v49 offset:18976\n"
"buffer_store_dwordx4 v[44:47], v5, s[36:39], s83 offen offset:0 sc1\n"
"v_mfma_f32_16x16x32_bf16 v[104:107], a[36:39], v[244:247], v[104:107]\n"
"ds_write_addtid_b32 v50 offset:22912\n"
"v_and_b32 v255, 0xffff0000, v53\n"
"v_mfma_f32_16x16x32_bf16 v[104:107], a[68:71], v[64:67], v[104:107]\n"
"ds_write_addtid_b32 v51 offset:26784\n"
"v_lshlrev_b32 v244, 16, v54\n"
"v_mfma_f32_16x16x32_bf16 v[104:107], a[100:103], v[128:131], v[104:107]\n"
"v_mov_b32 v100, -1\n"
"v_mov_b32 v101, -1\n"
"v_mfma_f32_16x16x32_bf16 v[104:107], a[132:135], v[236:239], v[104:107]\n"
"v_mov_b32 v102, -1\n"
"v_mov_b32 v103, -1\n"
"v_mfma_f32_16x16x32_bf16 v[104:107], a[164:167], v[120:123], v[104:107]\n"
"v_or_b32 v24, s94, v0\n"
"v_mfma_f32_16x16x32_bf16 v[104:107], a[196:199], v[56:59], v[104:107]\n"
"s_waitcnt lgkmcnt(0)\n"
"s_barrier\n"
"v_mfma_f32_16x16x32_bf16 v[104:107], a[228:231], v[196:199], v[104:107]\n"
"ds_read_b128 v[236:239], v15 offset:27136\n"
"s_waitcnt vmcnt(14)\n"
"v_mfma_f32_16x16x32_bf16 v[196:199], a[24:27], v[136:139], v[252:255]\n"
"ds_read_b128 v[128:131], v15 offset:27392\n"
"v_and_b32 v245, 0xffff0000, v54\n"
"v_mfma_f32_16x16x32_bf16 v[196:199], a[56:59], v[80:83], v[196:199]\n"
"v_mfma_f32_16x16x32_bf16 v[196:199], a[88:91], v[188:191], v[196:199]\n"
"buffer_load_dwordx4 v[252:255], v3, s[36:39], s51 offen offset:2048 sc1\n"
"v_lshlrev_b32 v246, 16, v55\n"
"v_mfma_f32_16x16x32_bf16 v[196:199], a[120:123], v[180:183], v[196:199]\n"
"v_and_b32 v247, 0xffff0000, v55\n"
"s_waitcnt vmcnt(10)\n"
"v_mfma_f32_16x16x32_bf16 v[196:199], a[152:155], v[164:167], v[196:199]\n"
"buffer_store_dwordx4 v[100:103], v24, s[36:39], s84 offen offset:0 sc1\n"
"v_mfma_f32_16x16x32_bf16 v[196:199], a[184:187], v[140:143], v[196:199]\n"
"v_mfma_f32_16x16x32_bf16 v[196:199], a[216:219], v[112:115], v[196:199]\n"
"s_waitcnt lgkmcnt(0)\n"
"v_add_f32 v116, v116, v236\n"
"v_mfma_f32_16x16x32_bf16 v[196:199], a[248:251], v[172:175], v[196:199]\n"
"v_add_f32 v117, v117, v237\n"
"v_add_f32 v118, v118, v238\n"
"v_mfma_f32_16x16x32_bf16 v[120:123], a[28:31], v[136:139], v[244:247]\n"
"v_add_f32 v119, v119, v239\n"
"v_add_f32 v104, v104, v128\n"
"v_mfma_f32_16x16x32_bf16 v[120:123], a[60:63], v[80:83], v[120:123]\n"
"v_add_f32 v105, v105, v129\n"
"v_add_f32 v106, v106, v130\n"
"v_mul_f32 v244, v116, v116\n"
"v_mul_f32 v245, v117, v117\n"
"v_mfma_f32_16x16x32_bf16 v[120:123], a[92:95], v[188:191], v[120:123]\n"
"v_add_f32 v107, v107, v131\n"
"v_mul_f32 v246, v118, v118\n"
"v_mul_f32 v247, v119, v119\n"
"v_fmaak_f32 v244, v244, v14, 0xc0135761\n"
"v_mfma_f32_16x16x32_bf16 v[120:123], a[124:127], v[180:183], v[120:123]\n"
"v_fmaak_f32 v245, v245, v14, 0xc0135761\n"
"v_fmaak_f32 v246, v246, v14, 0xc0135761\n"
"v_fmaak_f32 v247, v247, v14, 0xc0135761\n"
"v_mul_f32 v244, v244, v116\n"
"v_mfma_f32_16x16x32_bf16 v[120:123], a[156:159], v[164:167], v[120:123]\n"
"v_mul_f32 v245, v245, v117\n"
"v_mul_f32 v246, v246, v118\n"
"v_mul_f32 v247, v247, v119\n"
"v_mfma_f32_16x16x32_bf16 v[120:123], a[188:191], v[140:143], v[120:123]\n"
"v_exp_f32 v244, v244\n"
"v_exp_f32 v245, v245\n"
"buffer_store_dwordx4 v[100:103], v0, s[36:39], s85 offen offset:0 sc1\n"
"v_mfma_f32_16x16x32_bf16 v[120:123], a[220:223], v[112:115], v[120:123]\n"
"v_exp_f32 v246, v246\n"
"v_exp_f32 v247, v247\n"
"v_mfma_f32_16x16x32_bf16 v[120:123], a[252:255], v[172:175], v[120:123]\n"
"ds_write_addtid_b32 v196 offset:13312\n"
"v_add_f32 v244, 1.0, v244\n"
"v_add_f32 v245, 1.0, v245\n"
"v_add_f32 v246, 1.0, v246\n"
"v_mfma_f32_16x16x32_bf16 v[236:239], v[136:139], a[16:19], v[72:75]\n"
"ds_write_addtid_b32 v197 offset:17184\n"
"v_add_f32 v247, 1.0, v247\n"
"v_rcp_f32 v244, v244\n"
"v_mfma_f32_16x16x32_bf16 v[236:239], v[80:83], a[48:51], v[236:239]\n"
"ds_write_addtid_b32 v198 offset:21120\n"
"v_rcp_f32 v245, v245\n"
"v_mfma_f32_16x16x32_bf16 v[236:239], v[188:191], a[80:83], v[236:239]\n"
"ds_write_addtid_b32 v199 offset:24992\n"
"v_rcp_f32 v246, v246\n"
"v_mfma_f32_16x16x32_bf16 v[236:239], v[180:183], a[112:115], v[236:239]\n"
"v_rcp_f32 v247, v247\n"
"v_mul_f32 v244, v244, v116\n"
"v_mfma_f32_16x16x32_bf16 v[236:239], v[164:167], a[144:147], v[236:239]\n"
"v_mul_f32 v245, v245, v117\n"
"v_mul_f32 v246, v246, v118\n"
"v_mul_f32 v247, v247, v119\n"
"v_mfma_f32_16x16x32_bf16 v[236:239], v[140:143], a[176:179], v[236:239]\n"
"ds_read_b128 v[196:199], v16 offset:24576\n"
"v_cvt_pk_bf16_f32 v128, v244, v245\n"
"v_cvt_pk_bf16_f32 v129, v246, v247\n"
"v_mfma_f32_16x16x32_bf16 v[236:239], v[112:115], a[208:211], v[236:239]\n"
"ds_read_b128 v[116:119], v16 offset:24832\n"
"v_min_u32 v129, 0xffbfffff, v129\n"
"v_mul_f32 v244, v104, v104\n"
"v_mfma_f32_16x16x32_bf16 v[236:239], v[172:175], a[240:243], v[236:239]\n"
"ds_write_addtid_b32 v120 offset:13568\n"
"v_mul_f32 v245, v105, v105\n"
"v_mfma_f32_16x16x32_bf16 v[92:95], v[136:139], a[20:23], v[60:63]\n"
"ds_write_addtid_b32 v121 offset:17440\n"
"buffer_load_dwordx4 v[84:87], v3, s[36:39], s52 offen offset:2048 sc1\n"
"v_mfma_f32_16x16x32_bf16 v[92:95], v[80:83], a[52:55], v[92:95]\n"
"ds_write_addtid_b32 v122 offset:21376\n"
"v_mul_f32 v246, v106, v106\n"
"v_mfma_f32_16x16x32_bf16 v[92:95], v[188:191], a[84:87], v[92:95]\n"
"ds_write_addtid_b32 v123 offset:25248\n"
"v_mul_f32 v247, v107, v107\n"
"v_mfma_f32_16x16x32_bf16 v[92:95], v[180:183], a[116:119], v[92:95]\n"
"buffer_load_dwordx4 v[120:123], v3, s[36:39], s53 offen offset:2048 sc1\n"
"v_fmaak_f32 v244, v244, v14, 0xc0135761\n"
"v_mfma_f32_16x16x32_bf16 v[92:95], v[164:167], a[148:151], v[92:95]\n"
"v_fmaak_f32 v245, v245, v14, 0xc0135761\n"
"v_fmaak_f32 v246, v246, v14, 0xc0135761\n"
"v_mfma_f32_16x16x32_bf16 v[92:95], v[140:143], a[180:183], v[92:95]\n"
"v_fmaak_f32 v247, v247, v14, 0xc0135761\n"
"v_mul_f32 v244, v244, v104\n"
"v_mfma_f32_16x16x32_bf16 v[92:95], v[112:115], a[212:215], v[92:95]\n"
"v_mul_f32 v245, v245, v105\n"
"v_mul_f32 v246, v246, v106\n"
"v_mfma_f32_16x16x32_bf16 v[92:95], v[172:175], a[244:247], v[92:95]\n"
"ds_write_addtid_b32 v236 offset:13824\n"
"buffer_load_dwordx4 v[64:67], v3, s[36:39], s54 offen offset:2048 sc1\n"
"s_waitcnt lgkmcnt(5)\n"
"v_mfma_f32_16x16x32_bf16 v[196:199], v[108:111], a[8:11], v[196:199]\n"
"ds_write_addtid_b32 v237 offset:17696\n"
"v_mul_f32 v247, v247, v107\n"
"v_mfma_f32_16x16x32_bf16 v[196:199], v[76:79], a[40:43], v[196:199]\n"
"ds_write_addtid_b32 v238 offset:21632\n"
"v_exp_f32 v244, v244\n"
"v_mfma_f32_16x16x32_bf16 v[196:199], v[176:179], a[72:75], v[196:199]\n"
"ds_write_addtid_b32 v239 offset:25504\n"
"v_exp_f32 v245, v245\n"
"v_mfma_f32_16x16x32_bf16 v[196:199], v[220:223], a[104:107], v[196:199]\n"
"buffer_load_dwordx4 v[236:239], v3, s[36:39], s55 offen offset:2048 sc1\n"
"v_exp_f32 v246, v246\n"
"v_mfma_f32_16x16x32_bf16 v[196:199], v[160:163], a[136:139], v[196:199]\n"
"v_exp_f32 v247, v247\n"
"v_mfma_f32_16x16x32_bf16 v[196:199], v[152:155], a[168:171], v[196:199]\n"
"ds_read_b128 v[56:59], v16 offset:25088\n"
"v_add_f32 v244, 1.0, v244\n"
"s_lshr_b32 s22, s81, 1\n"
"s_add_u32 s22, s22, 14745600\n"
"v_mfma_f32_16x16x32_bf16 v[196:199], v[216:219], a[200:203], v[196:199]\n"
"ds_read_b128 v[52:55], v16 offset:25344\n"
"buffer_load_dwordx4 v[48:51], v4, s[36:39], s48 offen offset:2048 sc1\n"
"v_mfma_f32_16x16x32_bf16 v[196:199], v[144:147], a[232:235], v[196:199]\n"
"ds_write_addtid_b32 v92 offset:14080\n"
"v_add_f32 v245, 1.0, v245\n"
"v_mfma_f32_16x16x32_bf16 v[116:119], v[108:111], a[12:15], v[116:119]\n"
"ds_write_addtid_b32 v93 offset:17952\n"
"v_add_f32 v246, 1.0, v246\n"
"v_mfma_f32_16x16x32_bf16 v[116:119], v[76:79], a[44:47], v[116:119]\n"
"ds_write_addtid_b32 v94 offset:21888\n"
"v_add_f32 v247, 1.0, v247\n"
"v_mfma_f32_16x16x32_bf16 v[116:119], v[176:179], a[76:79], v[116:119]\n"
"ds_write_addtid_b32 v95 offset:25760\n"
"v_rcp_f32 v244, v244\n"
"buffer_load_dwordx4 v[44:47], v5, s[36:39], s22 offen offset:1024 sc1\n"
"v_mfma_f32_16x16x32_bf16 v[116:119], v[220:223], a[108:111], v[116:119]\n"
"v_rcp_f32 v245, v245\n"
"v_mfma_f32_16x16x32_bf16 v[116:119], v[160:163], a[140:143], v[116:119]\n"
"v_rcp_f32 v246, v246\n"
"v_mfma_f32_16x16x32_bf16 v[116:119], v[152:155], a[172:175], v[116:119]\n"
"buffer_load_dwordx4 v[92:95], v4, s[36:39], s49 offen offset:2048 sc1\n"
"v_rcp_f32 v247, v247\n"
"v_mfma_f32_16x16x32_bf16 v[116:119], v[216:219], a[204:207], v[116:119]\n"
"v_mul_f32 v244, v244, v104\n"
"v_mul_f32 v245, v245, v105\n"
"v_mfma_f32_16x16x32_bf16 v[116:119], v[144:147], a[236:239], v[116:119]\n"
"ds_write_addtid_b32 v196 offset:14336\n"
"v_mul_f32 v246, v246, v106\n"
"s_waitcnt lgkmcnt(5)\n"
"v_mfma_f32_16x16x32_bf16 v[56:59], a[0:3], v[108:111], v[56:59]\n"
"ds_write_addtid_b32 v197 offset:18208\n"
"buffer_load_dwordx4 v[40:43], v4, s[36:39], s50 offen offset:2048 sc1\n"
"s_waitcnt vmcnt(12)\n"
"v_max_u32 v24, v37, v39\n"
"v_mfma_f32_16x16x32_bf16 v[56:59], a[32:35], v[76:79], v[56:59]\n"
"ds_write_addtid_b32 v198 offset:22144\n"
"v_mul_f32 v247, v247, v107\n"
"v_cmp_le_u32 vcc, 0xffc00000, v24\n"
"v_mfma_f32_16x16x32_bf16 v[56:59], a[64:67], v[176:179], v[56:59]\n"
"ds_write_addtid_b32 v199 offset:26016\n"
"v_cvt_pk_bf16_f32 v130, v244, v245\n"
"v_mfma_f32_16x16x32_bf16 v[56:59], a[96:99], v[220:223], v[56:59]\n"
"v_cvt_pk_bf16_f32 v131, v246, v247\n"
"v_min_u32 v131, 0xffbfffff, v131\n"
"s_cbranch_vccnz .Lgs_slow22\n"
".Lgs_back22:\n"
"v_mfma_f32_16x16x32_bf16 v[56:59], a[128:131], v[160:163], v[56:59]\n"
"v_lshlrev_b32 v244, 16, v36\n"
"v_and_b32 v245, 0xffff0000, v36\n"
"v_mfma_f32_16x16x32_bf16 v[56:59], a[160:163], v[152:155], v[56:59]\n"
"v_mfma_f32_16x16x32_bf16 v[56:59], a[192:195], v[216:219], v[56:59]\n"
"v_mfma_f32_16x16x32_bf16 v[56:59], a[224:227], v[144:147], v[56:59]\n"
"ds_write_addtid_b32 v116 offset:14592\n"
"v_lshlrev_b32 v246, 16, v37\n"
"v_mfma_f32_16x16x32_bf16 v[52:55], a[4:7], v[108:111], v[52:55]\n"
"ds_write_addtid_b32 v117 offset:18464\n"
"buffer_store_dwordx4 v[128:131], v5, s[36:39], s83 offen offset:3072 sc1\n"
"v_mfma_f32_16x16x32_bf16 v[52:55], a[36:39], v[76:79], v[52:55]\n"
"ds_write_addtid_b32 v118 offset:22400\n"
"v_and_b32 v247, 0xffff0000, v37\n"
"v_mfma_f32_16x16x32_bf16 v[52:55], a[68:71], v[176:179], v[52:55]\n"
"ds_write_addtid_b32 v119 offset:26272\n"
"v_lshlrev_b32 v196, 16, v38\n"
"v_mfma_f32_16x16x32_bf16 v[52:55], a[100:103], v[220:223], v[52:55]\n"
"v_mfma_f32_16x16x32_bf16 v[52:55], a[132:135], v[160:163], v[52:55]\n"
"v_mfma_f32_16x16x32_bf16 v[52:55], a[164:167], v[152:155], v[52:55]\n"
"v_mfma_f32_16x16x32_bf16 v[52:55], a[196:199], v[216:219], v[52:55]\n"
"s_waitcnt lgkmcnt(0)\n"
"s_barrier\n"
"v_mfma_f32_16x16x32_bf16 v[52:55], a[228:231], v[144:147], v[52:55]\n"
"ds_read_b128 v[220:223], v15 offset:26624\n"
"s_waitcnt vmcnt(11)\n"
"v_mfma_f32_16x16x32_bf16 v[216:219], a[24:27], v[224:227], v[244:247]\n"
"ds_read_b128 v[176:179], v15 offset:26880\n"
"v_and_b32 v197, 0xffff0000, v38\n"
"v_mfma_f32_16x16x32_bf16 v[216:219], a[56:59], v[88:91], v[216:219]\n"
"v_mfma_f32_16x16x32_bf16 v[216:219], a[88:91], v[124:127], v[216:219]\n"
"buffer_load_dwordx4 v[244:247], v4, s[36:39], s51 offen offset:2048 sc1\n"
"v_lshlrev_b32 v198, 16, v39\n"
"v_mfma_f32_16x16x32_bf16 v[216:219], a[120:123], v[252:255], v[216:219]\n"
"v_and_b32 v199, 0xffff0000, v39\n"
"s_waitcnt vmcnt(6)\n"
"v_mfma_f32_16x16x32_bf16 v[216:219], a[152:155], v[84:87], v[216:219]\n"
"buffer_store_dwordx4 v[100:103], v0, s[36:39], s86 offen offset:0 sc1\n"
"v_mfma_f32_16x16x32_bf16 v[216:219], a[184:187], v[120:123], v[216:219]\n"
"v_mfma_f32_16x16x32_bf16 v[216:219], a[216:219], v[64:67], v[216:219]\n"
"s_waitcnt lgkmcnt(0)\n"
"v_add_f32 v56, v56, v220\n"
"v_mfma_f32_16x16x32_bf16 v[216:219], a[248:251], v[236:239], v[216:219]\n"
"v_add_f32 v57, v57, v221\n"
"v_add_f32 v58, v58, v222\n"
"v_mfma_f32_16x16x32_bf16 v[160:163], a[28:31], v[224:227], v[196:199]\n"
"v_add_f32 v59, v59, v223\n"
"v_add_f32 v52, v52, v176\n"
"v_mfma_f32_16x16x32_bf16 v[160:163], a[60:63], v[88:91], v[160:163]\n"
"v_add_f32 v53, v53, v177\n"
"v_add_f32 v54, v54, v178\n"
"v_mul_f32 v220, v56, v56\n"
"v_mul_f32 v221, v57, v57\n"
"v_mfma_f32_16x16x32_bf16 v[160:163], a[92:95], v[124:127], v[160:163]\n"
"v_add_f32 v55, v55, v179\n"
"v_mul_f32 v222, v58, v58\n"
"v_mul_f32 v223, v59, v59\n"
"v_fmaak_f32 v220, v220, v14, 0xc0135761\n"
"v_mfma_f32_16x16x32_bf16 v[160:163], a[124:127], v[252:255], v[160:163]\n"
"v_fmaak_f32 v221, v221, v14, 0xc0135761\n"
"v_fmaak_f32 v222, v222, v14, 0xc0135761\n"
"v_fmaak_f32 v223, v223, v14, 0xc0135761\n"
"v_mul_f32 v220, v220, v56\n"
"v_mfma_f32_16x16x32_bf16 v[160:163], a[156:159], v[84:87], v[160:163]\n"
"v_mul_f32 v221, v221, v57\n"
"v_mul_f32 v222, v222, v58\n"
"v_mul_f32 v223, v223, v59\n"
"v_mfma_f32_16x16x32_bf16 v[160:163], a[188:191], v[120:123], v[160:163]\n"
"v_exp_f32 v220, v220\n"
"v_exp_f32 v221, v221\n"
"buffer_store_dwordx4 v[100:103], v0, s[36:39], s87 offen offset:0 sc1\n"
"v_mfma_f32_16x16x32_bf16 v[160:163], a[220:223], v[64:67], v[160:163]\n"
"v_exp_f32 v222, v222\n"
"v_exp_f32 v223, v223\n"
"v_mfma_f32_16x16x32_bf16 v[160:163], a[252:255], v[236:239], v[160:163]\n"
"ds_write_addtid_b32 v216 offset:12288\n"
"v_add_f32 v220, 1.0, v220\n"
"v_add_f32 v221, 1.0, v221\n"
"v_add_f32 v222, 1.0, v222\n"
"v_mfma_f32_16x16x32_bf16 v[196:199], v[224:227], a[16:19], v[72:75]\n"
"ds_write_addtid_b32 v217 offset:16160\n"
"v_add_f32 v223, 1.0, v223\n"
"v_rcp_f32 v220, v220\n"
"v_mfma_f32_16x16x32_bf16 v[196:199], v[88:91], a[48:51], v[196:199]\n"
"ds_write_addtid_b32 v218 offset:20096\n"
"v_rcp_f32 v221, v221\n"
"v_mfma_f32_16x16x32_bf16 v[196:199], v[124:127], a[80:83], v[196:199]\n"
"ds_write_addtid_b32 v219 offset:23968\n"
"v_rcp_f32 v222, v222\n"
"v_mfma_f32_16x16x32_bf16 v[196:199], v[252:255], a[112:115], v[196:199]\n"
"v_rcp_f32 v223, v223\n"
"v_mul_f32 v220, v220, v56\n"
"v_mul_f32 v221, v221, v57\n"
"v_mfma_f32_16x16x32_bf16 v[196:199], v[84:87], a[144:147], v[196:199]\n"
"v_mul_f32 v222, v222, v58\n"
"v_mul_f32 v223, v223, v59\n"
"v_cvt_pk_bf16_f32 v216, v220, v221\n"
"v_mfma_f32_16x16x32_bf16 v[196:199], v[120:123], a[176:179], v[196:199]\n"
"ds_read_b128 v[176:179], v16 offset:25600\n"
"v_cvt_pk_bf16_f32 v217, v222, v223\n"
"v_min_u32 v217, 0xffbfffff, v217\n"
"v_mfma_f32_16x16x32_bf16 v[196:199], v[64:67], a[208:211], v[196:199]\n"
"ds_read_b128 v[220:223], v16 offset:25856\n"
"v_mul_f32 v152, v52, v52\n"
"v_mul_f32 v153, v53, v53\n"
"v_mfma_f32_16x16x32_bf16 v[196:199], v[236:239], a[240:243], v[196:199]\n"
"ds_write_addtid_b32 v160 offset:12544\n"
"v_mul_f32 v154, v54, v54\n"
"v_mul_f32 v155, v55, v55\n"
"v_mfma_f32_16x16x32_bf16 v[144:147], v[224:227], a[20:23], v[60:63]\n"
"ds_write_addtid_b32 v161 offset:16416\n"
"v_fmaak_f32 v152, v152, v14, 0xc0135761\n"
"v_fmaak_f32 v153, v153, v14, 0xc0135761\n"
"v_mfma_f32_16x16x32_bf16 v[144:147], v[88:91], a[52:55], v[144:147]\n"
"ds_write_addtid_b32 v162 offset:20352\n"
"buffer_load_dwordx4 v[128:131], v4, s[36:39], s52 offen offset:2048 sc1\n"
"v_mfma_f32_16x16x32_bf16 v[144:147], v[124:127], a[84:87], v[144:147]\n"
".Lgs_pad2:\n"
"s_cbranch_execz .Lgs_al\n"
"ds_write_addtid_b32 v163 offset:24224\n"
"v_fmaak_f32 v154, v154, v14, 0xc0135761\n"
"v_mfma_f32_16x16x32_bf16 v[144:147], v[252:255], a[116:119], v[144:147]\n"
"v_fmaak_f32 v155, v155, v14, 0xc0135761\n"
"v_mul_f32 v152, v152, v52\n"
"v_mfma_f32_16x16x32_bf16 v[144:147], v[84:87], a[148:151], v[144:147]\n"
"buffer_load_dwordx4 v[160:163], v4, s[36:39], s53 offen offset:2048 sc1\n"
"v_mul_f32 v153, v153, v53\n"
"v_mfma_f32_16x16x32_bf16 v[144:147], v[120:123], a[180:183], v[144:147]\n"
"v_mul_f32 v154, v154, v54\n"
"v_mul_f32 v155, v155, v55\n"
"v_mfma_f32_16x16x32_bf16 v[144:147], v[64:67], a[212:215], v[144:147]\n"
"v_exp_f32 v152, v152\n"
"v_mfma_f32_16x16x32_bf16 v[144:147], v[236:239], a[244:247], v[144:147]\n"
"ds_write_addtid_b32 v196 offset:12800\n"
"buffer_load_dwordx4 v[116:119], v4, s[36:39], s54 offen offset:2048 sc1\n"
"s_waitcnt lgkmcnt(5)\n"
"v_mfma_f32_16x16x32_bf16 v[176:179], v[136:139], a[8:11], v[176:179]\n"
"ds_write_addtid_b32 v197 offset:16672\n"
"v_exp_f32 v153, v153\n"
"v_mfma_f32_16x16x32_bf16 v[176:179], v[80:83], a[40:43], v[176:179]\n"
"ds_write_addtid_b32 v198 offset:20608\n"
"v_exp_f32 v154, v154\n"
"v_mfma_f32_16x16x32_bf16 v[176:179], v[188:191], a[72:75], v[176:179]\n"
"ds_write_addtid_b32 v199 offset:24480\n"
"v_exp_f32 v155, v155\n"
"v_mfma_f32_16x16x32_bf16 v[176:179], v[180:183], a[104:107], v[176:179]\n"
"buffer_load_dwordx4 v[196:199], v4, s[36:39], s55 offen offset:2048 sc1\n"
"v_add_u32 v24, s68, v0\n"
"v_mfma_f32_16x16x32_bf16 v[176:179], v[164:167], a[136:139], v[176:179]\n"
"v_add_f32 v152, 1.0, v152\n"
"v_add_f32 v153, 1.0, v153\n"
"v_mfma_f32_16x16x32_bf16 v[176:179], v[140:143], a[168:171], v[176:179]\n"
"ds_read_b128 v[108:111], v16 offset:26112\n"
"v_add_f32 v154, 1.0, v154\n"
"v_mfma_f32_16x16x32_bf16 v[176:179], v[112:115], a[200:203], v[176:179]\n"
"ds_read_b128 v[104:107], v16 offset:26368\n"
"v_add_f32 v155, 1.0, v155\n"
"v_mfma_f32_16x16x32_bf16 v[176:179], v[172:175], a[232:235], v[176:179]\n"
"ds_write_addtid_b32 v144 offset:13056\n"
"buffer_load_dwordx4 v[100:103], v24, s[36:39], s56 offen sc1\n"
"v_mfma_f32_16x16x32_bf16 v[220:223], v[136:139], a[12:15], v[220:223]\n"
"ds_write_addtid_b32 v145 offset:16928\n"
"v_rcp_f32 v152, v152\n"
"v_mfma_f32_16x16x32_bf16 v[220:223], v[80:83], a[44:47], v[220:223]\n"
"ds_write_addtid_b32 v146 offset:20864\n"
"v_rcp_f32 v153, v153\n"
"v_mfma_f32_16x16x32_bf16 v[220:223], v[188:191], a[76:79], v[220:223]\n"
"ds_write_addtid_b32 v147 offset:24736\n"
"buffer_load_dwordx4 v[76:79], v24, s[36:39], s57 offen sc1\n"
"v_mfma_f32_16x16x32_bf16 v[220:223], v[180:183], a[108:111], v[220:223]\n"
"v_rcp_f32 v154, v154\n"
"v_mfma_f32_16x16x32_bf16 v[220:223], v[164:167], a[140:143], v[220:223]\n"
"v_rcp_f32 v155, v155\n"
"v_mfma_f32_16x16x32_bf16 v[220:223], v[140:143], a[172:175], v[220:223]\n"
"buffer_load_dwordx4 v[144:147], v24, s[36:39], s58 offen sc1\n"
"v_mul_f32 v152, v152, v52\n"
"v_mfma_f32_16x16x32_bf16 v[220:223], v[112:115], a[204:207], v[220:223]\n"
"v_mul_f32 v153, v153, v53\n"
"v_mul_f32 v154, v154, v54\n"
"v_mfma_f32_16x16x32_bf16 v[220:223], v[172:175], a[236:239], v[220:223]\n"
"ds_write_addtid_b32 v176 offset:14848\n"
"v_mul_f32 v155, v155, v55\n"
"s_waitcnt lgkmcnt(5)\n"
"v_mfma_f32_16x16x32_bf16 v[108:111], a[0:3], v[136:139], v[108:111]\n"
"ds_write_addtid_b32 v177 offset:18720\n"
"buffer_load_dwordx4 v[56:59], v24, s[36:39], s59 offen sc1\n"
"s_waitcnt vmcnt(14)\n"
"v_max_u32 v4, v45, v47\n"
"v_mfma_f32_16x16x32_bf16 v[108:111], a[32:35], v[80:83], v[108:111]\n"
"ds_write_addtid_b32 v178 offset:22656\n"
"v_cvt_pk_bf16_f32 v218, v152, v153\n"
"v_cmp_le_u32 vcc, 0xffc00000, v4\n"
"v_mfma_f32_16x16x32_bf16 v[108:111], a[64:67], v[188:191], v[108:111]\n"
"ds_write_addtid_b32 v179 offset:26528\n"
"v_cvt_pk_bf16_f32 v219, v154, v155\n"
"v_mfma_f32_16x16x32_bf16 v[108:111], a[96:99], v[180:183], v[108:111]\n"
"buffer_load_dwordx4 v[176:179], v24, s[36:39], s60 offen sc1\n"
"v_min_u32 v219, 0xffbfffff, v219\n"
"s_cbranch_vccnz .Lgs_slow23\n"
".Lgs_back23:\n"
"v_mfma_f32_16x16x32_bf16 v[108:111], a[128:131], v[164:167], v[108:111]\n"
"v_lshlrev_b32 v152, 16, v44\n"
"v_and_b32 v153, 0xffff0000, v44\n"
"v_mfma_f32_16x16x32_bf16 v[108:111], a[160:163], v[140:143], v[108:111]\n"
"v_mfma_f32_16x16x32_bf16 v[108:111], a[192:195], v[112:115], v[108:111]\n"
"v_mfma_f32_16x16x32_bf16 v[108:111], a[224:227], v[172:175], v[108:111]\n"
"ds_write_addtid_b32 v220 offset:15104\n"
"v_lshlrev_b32 v154, 16, v45\n"
"v_mfma_f32_16x16x32_bf16 v[104:107], a[4:7], v[136:139], v[104:107]\n"
"ds_write_addtid_b32 v221 offset:18976\n"
"buffer_store_dwordx4 v[216:219], v5, s[36:39], s83 offen offset:1024 sc1\n"
"v_mfma_f32_16x16x32_bf16 v[104:107], a[36:39], v[80:83], v[104:107]\n"
"ds_write_addtid_b32 v222 offset:22912\n"
"v_and_b32 v155, 0xffff0000, v45\n"
"v_mfma_f32_16x16x32_bf16 v[104:107], a[68:71], v[188:191], v[104:107]\n"
"ds_write_addtid_b32 v223 offset:26784\n"
"v_lshlrev_b32 v216, 16, v46\n"
"v_mfma_f32_16x16x32_bf16 v[104:107], a[100:103], v[180:183], v[104:107]\n"
"v_mfma_f32_16x16x32_bf16 v[104:107], a[132:135], v[164:167], v[104:107]\n"
"v_mfma_f32_16x16x32_bf16 v[104:107], a[164:167], v[140:143], v[104:107]\n"
"v_mfma_f32_16x16x32_bf16 v[104:107], a[196:199], v[112:115], v[104:107]\n"
"s_waitcnt lgkmcnt(0)\n"
"s_barrier\n"
"v_mfma_f32_16x16x32_bf16 v[104:107], a[228:231], v[172:175], v[104:107]\n"
"ds_read_b128 v[220:223], v15 offset:27136\n"
"s_waitcnt vmcnt(12)\n"
"v_mfma_f32_16x16x32_bf16 v[188:191], a[24:27], v[48:51], v[152:155]\n"
"ds_read_b128 v[180:183], v15 offset:27392\n"
"v_and_b32 v217, 0xffff0000, v46\n"
"v_mfma_f32_16x16x32_bf16 v[188:191], a[56:59], v[92:95], v[188:191]\n"
"v_mfma_f32_16x16x32_bf16 v[188:191], a[88:91], v[40:43], v[188:191]\n"
"buffer_load_dwordx4 v[172:175], v24, s[36:39], s61 offen sc1\n"
"v_lshlrev_b32 v218, 16, v47\n"
"v_mfma_f32_16x16x32_bf16 v[188:191], a[120:123], v[244:247], v[188:191]\n"
"v_and_b32 v219, 0xffff0000, v47\n"
"s_waitcnt vmcnt(7)\n"
"v_mfma_f32_16x16x32_bf16 v[188:191], a[152:155], v[128:131], v[188:191]\n"
"ds_read_b64_tr_b16 a[24:25], v12 offset:16384\n"
"v_mfma_f32_16x16x32_bf16 v[188:191], a[184:187], v[160:163], v[188:191]\n"
"ds_read_b64_tr_b16 a[26:27], v12 offset:16896\n"
"v_mfma_f32_16x16x32_bf16 v[188:191], a[216:219], v[116:119], v[188:191]\n"
"ds_read_b64_tr_b16 a[56:57], v13 offset:16384\n"
"v_mfma_f32_16x16x32_bf16 v[188:191], a[248:251], v[196:199], v[188:191]\n"
"ds_read_b64_tr_b16 a[58:59], v13 offset:16896\n"
"buffer_load_dwordx4 v[164:167], v24, s[36:39], s62 offen sc1\n"
"s_waitcnt lgkmcnt(4)\n"
"v_add_f32 v104, v104, v180\n"
"v_mfma_f32_16x16x32_bf16 v[152:155], a[28:31], v[48:51], v[216:219]\n"
"ds_read_b64_tr_b16 a[88:89], v10 offset:16384\n"
"v_add_f32 v108, v108, v220\n"
"v_add_f32 v105, v105, v181\n"
"v_mfma_f32_16x16x32_bf16 v[152:155], a[60:63], v[92:95], v[152:155]\n"
"ds_read_b64_tr_b16 a[90:91], v10 offset:16896\n"
"v_add_f32 v109, v109, v221\n"
"v_add_f32 v106, v106, v182\n"
"v_mfma_f32_16x16x32_bf16 v[152:155], a[92:95], v[40:43], v[152:155]\n"
"ds_read_b64_tr_b16 a[120:121], v11 offset:16384\n"
"buffer_load_dwordx4 v[216:219], v24, s[36:39], s63 offen sc1\n"
"v_add_f32 v107, v107, v183\n"
"v_mfma_f32_16x16x32_bf16 v[152:155], a[124:127], v[244:247], v[152:155]\n"
"ds_read_b64_tr_b16 a[122:123], v11 offset:16896\n"
"ds_read_b64_tr_b16 a[152:153], v12 offset:20480\n"
"ds_read_b64_tr_b16 a[154:155], v12 offset:20992\n"
"v_add_f32 v110, v110, v222\n"
"v_mfma_f32_16x16x32_bf16 v[152:155], a[156:159], v[128:131], v[152:155]\n"
"ds_read_b64_tr_b16 a[184:185], v13 offset:20480\n"
"ds_read_b64_tr_b16 a[186:187], v13 offset:20992\n"
"v_add_f32 v111, v111, v223\n"
"v_mfma_f32_16x16x32_bf16 v[152:155], a[188:191], v[160:163], v[152:155]\n"
"ds_read_b64_tr_b16 a[216:217], v10 offset:20480\n"
"buffer_load_dwordx4 v[220:223], v24, s[36:39], s64 offen sc1\n"
"v_mul_f32 v180, v108, v108\n"
"v_mul_f32 v181, v109, v109\n"
"v_mul_f32 v182, v110, v110\n"
"v_mfma_f32_16x16x32_bf16 v[152:155], a[220:223], v[116:119], v[152:155]\n"
"ds_read_b64_tr_b16 a[218:219], v10 offset:20992\n"
"v_add_u32 v4, s69, v0\n"
"v_mul_f32 v183, v111, v111\n"
"v_fmaak_f32 v180, v180, v14, 0xc0135761\n"
"v_fmaak_f32 v181, v181, v14, 0xc0135761\n"
"v_mfma_f32_16x16x32_bf16 v[152:155], a[252:255], v[196:199], v[152:155]\n"
"ds_write_addtid_b32 v188 offset:13312\n"
"v_fmaak_f32 v182, v182, v14, 0xc0135761\n"
"v_fmaak_f32 v183, v183, v14, 0xc0135761\n"
"v_mul_f32 v180, v180, v108\n"
"v_mul_f32 v181, v181, v109\n"
"v_mfma_f32_16x16x32_bf16 v[140:143], v[48:51], a[16:19], v[72:75]\n"
"ds_write_addtid_b32 v189 offset:17184\n"
"s_add_u32 m0, s77, 16384\n"
"s_add_u32 s20, s20, 524288\n"
"s_waitcnt lgkmcnt(8)\n"
"buffer_load_dwordx4 v6, s[40:43], s20 offen offset:0 lds\n"
"v_mul_f32 v182, v182, v110\n"
"v_mul_f32 v183, v183, v111\n"
"v_exp_f32 v180, v180\n"
"v_exp_f32 v181, v181\n"
"v_exp_f32 v182, v182\n"
"v_exp_f32 v183, v183\n"
"v_mfma_f32_16x16x32_bf16 v[140:143], v[92:95], a[48:51], v[140:143]\n"
"ds_write_addtid_b32 v190 offset:17024\n"
"buffer_load_dwordx4 v[136:139], v4, s[36:39], s56 offen sc1\n"
"v_add_f32 v180, 1.0, v180\n"
"v_add_f32 v181, 1.0, v181\n"
"v_add_f32 v182, 1.0, v182\n"
"v_mfma_f32_16x16x32_bf16 v[140:143], v[40:43], a[80:83], v[140:143]\n"
"ds_write_addtid_b32 v191 offset:20896\n"
"v_add_f32 v183, 1.0, v183\n"
"v_rcp_f32 v180, v180\n"
"v_mfma_f32_16x16x32_bf16 v[140:143], v[244:247], a[112:115], v[140:143]\n"
"ds_read_b64_tr_b16 a[248:249], v11 offset:20480\n"
"v_rcp_f32 v181, v181\n"
"v_rcp_f32 v182, v182\n"
"v_mfma_f32_16x16x32_bf16 v[140:143], v[128:131], a[144:147], v[140:143]\n"
"ds_read_b64_tr_b16 a[250:251], v11 offset:20992\n"
"ds_read_b64_tr_b16 a[28:29], v12 offset:0\n"
"ds_read_b64_tr_b16 a[30:31], v12 offset:512\n"
"buffer_load_dwordx4 v7, s[40:43], s20 offen offset:1024 lds\n"
"v_mfma_f32_16x16x32_bf16 v[140:143], v[160:163], a[176:179], v[140:143]\n"
"ds_read_b128 v[188:191], v16 offset:24576\n"
"ds_read_b64_tr_b16 a[60:61], v13 offset:0\n"
"ds_read_b64_tr_b16 a[62:63], v13 offset:512\n"
"v_rcp_f32 v183, v183\n"
"v_mfma_f32_16x16x32_bf16 v[140:143], v[116:119], a[208:211], v[140:143]\n"
"ds_read_b128 v[112:115], v16 offset:24832\n"
"buffer_load_dwordx4 v[80:83], v4, s[36:39], s57 offen sc1\n"
"v_mul_f32 v180, v180, v108\n"
"v_mul_f32 v181, v181, v109\n"
"v_mfma_f32_16x16x32_bf16 v[140:143], v[196:199], a[240:243], v[140:143]\n"
"ds_write_addtid_b32 v152 offset:9472\n"
"v_mul_f32 v182, v182, v110\n"
"v_mul_f32 v183, v183, v111\n"
"v_cvt_pk_bf16_f32 v72, v180, v181\n"
"v_mfma_f32_16x16x32_bf16 v[52:55], v[48:51], a[20:23], v[60:63]\n"
"ds_write_addtid_b32 v153 offset:13344\n"
"v_cvt_pk_bf16_f32 v73, v182, v183\n"
"v_min_u32 v73, 0xffbfffff, v73\n"
"v_mul_f32 v108, v104, v104\n"
"v_mfma_f32_16x16x32_bf16 v[52:55], v[92:95], a[52:55], v[52:55]\n"
"ds_write_addtid_b32 v154 offset:17280\n"
"buffer_load_dwordx4 v8, s[40:43], s20 offen offset:2048 lds\n"
"v_mul_f32 v109, v105, v105\n"
"v_mul_f32 v110, v106, v106\n"
"v_mfma_f32_16x16x32_bf16 v[52:55], v[40:43], a[84:87], v[52:55]\n"
"ds_write_addtid_b32 v155 offset:21152\n"
"v_mul_f32 v111, v107, v107\n"
"v_fmaak_f32 v108, v108, v14, 0xc0135761\n"
"v_fmaak_f32 v109, v109, v14, 0xc0135761\n"
"v_mfma_f32_16x16x32_bf16 v[52:55], v[244:247], a[116:119], v[52:55]\n"
"ds_read_b64_tr_b16 a[92:93], v10 offset:0\n"
"v_fmaak_f32 v110, v110, v14, 0xc0135761\n"
"v_fmaak_f32 v111, v111, v14, 0xc0135761\n"
"v_mfma_f32_16x16x32_bf16 v[52:55], v[128:131], a[148:151], v[52:55]\n"
"ds_read_b64_tr_b16 a[94:95], v10 offset:512\n"
"buffer_load_dwordx4 v[180:183], v4, s[36:39], s58 offen sc1\n"
"v_mul_f32 v108, v108, v104\n"
"v_mfma_f32_16x16x32_bf16 v[52:55], v[160:163], a[180:183], v[52:55]\n"
"ds_read_b64_tr_b16 a[124:125], v11 offset:0\n"
"v_mul_f32 v109, v109, v105\n"
"v_mul_f32 v110, v110, v106\n"
"v_mfma_f32_16x16x32_bf16 v[52:55], v[116:119], a[212:215], v[52:55]\n"
"ds_read_b64_tr_b16 a[126:127], v11 offset:512\n"
"ds_read_b64_tr_b16 a[156:157], v12 offset:4096\n"
"ds_read_b64_tr_b16 a[158:159], v12 offset:4608\n"
"v_mul_f32 v111, v111, v107\n"
"v_mfma_f32_16x16x32_bf16 v[52:55], v[196:199], a[244:247], v[52:55]\n"
"ds_write_addtid_b32 v140 offset:9728\n"
"ds_read_b64_tr_b16 a[188:189], v13 offset:4096\n"
"ds_read_b64_tr_b16 a[190:191], v13 offset:4608\n"
"v_exp_f32 v108, v108\n"
"s_waitcnt lgkmcnt(13)\n"
"v_mfma_f32_16x16x32_bf16 v[188:191], v[224:227], a[8:11], v[188:191]\n"
"ds_write_addtid_b32 v141 offset:13600\n"
"ds_read_b64_tr_b16 a[220:221], v10 offset:4096\n"
"ds_read_b64_tr_b16 a[222:223], v10 offset:4608\n"
"buffer_load_dwordx4 v9, s[40:43], s20 offen offset:3072 lds\n"
"v_mfma_f32_16x16x32_bf16 v[188:191], v[88:91], a[40:43], v[188:191]\n"
"ds_write_addtid_b32 v142 offset:17536\n"
"v_exp_f32 v109, v109\n"
"v_mfma_f32_16x16x32_bf16 v[188:191], v[124:127], a[72:75], v[188:191]\n"
"ds_write_addtid_b32 v143 offset:21408\n"
"v_exp_f32 v110, v110\n"
"v_mfma_f32_16x16x32_bf16 v[188:191], v[252:255], a[104:107], v[188:191]\n"
"ds_read_b64_tr_b16 a[252:253], v11 offset:4096\n"
"buffer_load_dwordx4 v[152:155], v4, s[36:39], s59 offen sc1\n"
"v_exp_f32 v111, v111\n"
"v_mfma_f32_16x16x32_bf16 v[188:191], v[84:87], a[136:139], v[188:191]\n"
"ds_read_b64_tr_b16 a[254:255], v11 offset:4608\n"
"ds_read_b64_tr_b16 a[16:17], v12 offset:8192\n"
"ds_read_b64_tr_b16 a[18:19], v12 offset:8704\n"
"v_add_f32 v108, 1.0, v108\n"
"v_mfma_f32_16x16x32_bf16 v[188:191], v[120:123], a[168:171], v[188:191]\n"
"ds_read_b128 v[140:143], v16 offset:25088\n"
"ds_read_b64_tr_b16 a[48:49], v13 offset:8192\n"
"ds_read_b64_tr_b16 a[50:51], v13 offset:8704\n"
"v_add_f32 v109, 1.0, v109\n"
"v_mfma_f32_16x16x32_bf16 v[188:191], v[64:67], a[200:203], v[188:191]\n"
"ds_read_b128 v[60:63], v16 offset:25344\n"
"ds_read_b64_tr_b16 a[80:81], v10 offset:8192\n"
"s_add_u32 m0, s77, 20480\n"
"s_add_u32 s20, s20, 524288\n"
"buffer_load_dwordx4 v6, s[40:43], s20 offen offset:0 lds\n"
"v_mfma_f32_16x16x32_bf16 v[188:191], v[236:239], a[232:235], v[188:191]\n"
"ds_write_addtid_b32 v52 offset:5888\n"
"v_add_f32 v110, 1.0, v110\n"
"v_mfma_f32_16x16x32_bf16 v[112:115], v[224:227], a[12:15], v[112:115]\n"
"ds_write_addtid_b32 v53 offset:9760\n"
"v_add_f32 v111, 1.0, v111\n"
"v_mfma_f32_16x16x32_bf16 v[112:115], v[88:91], a[44:47], v[112:115]\n"
"ds_write_addtid_b32 v54 offset:13696\n"
"v_rcp_f32 v108, v108\n"
"v_mfma_f32_16x16x32_bf16 v[112:115], v[124:127], a[76:79], v[112:115]\n"
"ds_write_addtid_b32 v55 offset:17568\n"
"buffer_load_dwordx4 v7, s[40:43], s20 offen offset:1024 lds\n"
"v_mfma_f32_16x16x32_bf16 v[112:115], v[252:255], a[108:111], v[112:115]\n"
"ds_read_b64_tr_b16 a[82:83], v10 offset:8704\n"
"v_rcp_f32 v109, v109\n"
"v_mfma_f32_16x16x32_bf16 v[112:115], v[84:87], a[140:143], v[112:115]\n"
"ds_read_b64_tr_b16 a[112:113], v11 offset:8192\n"
"v_rcp_f32 v110, v110\n"
"v_mfma_f32_16x16x32_bf16 v[112:115], v[120:123], a[172:175], v[112:115]\n"
"ds_read_b64_tr_b16 a[114:115], v11 offset:8704\n"
"ds_read_b64_tr_b16 a[144:145], v12 offset:12288\n"
"ds_read_b64_tr_b16 a[146:147], v12 offset:12800\n"
"v_rcp_f32 v111, v111\n"
"v_mfma_f32_16x16x32_bf16 v[112:115], v[64:67], a[204:207], v[112:115]\n"
"s_waitcnt lgkmcnt(5)\n"
"s_barrier\n"
"ds_read_b64_tr_b16 a[176:177], v13 offset:12288\n"
"buffer_load_dwordx4 v8, s[40:43], s20 offen offset:2048 lds\n"
"v_mfma_f32_16x16x32_bf16 v[112:115], v[236:239], a[236:239], v[112:115]\n"
"ds_write_addtid_b32 v188 offset:4096\n"
"ds_read_b64_tr_b16 a[178:179], v13 offset:12800\n"
"ds_read_b64_tr_b16 a[208:209], v10 offset:12288\n"
"v_mul_f32 v108, v108, v104\n"
"v_mfma_f32_16x16x32_bf16 v[140:143], a[0:3], v[224:227], v[140:143]\n"
"ds_write_addtid_b32 v189 offset:7968\n"
"ds_read_b64_tr_b16 a[210:211], v10 offset:12800\n"
"v_mul_f32 v109, v109, v105\n"
"v_mfma_f32_16x16x32_bf16 v[140:143], a[32:35], v[88:91], v[140:143]\n"
"ds_write_addtid_b32 v190 offset:11904\n"
"v_mul_f32 v110, v110, v106\n"
"v_mfma_f32_16x16x32_bf16 v[140:143], a[64:67], v[124:127], v[140:143]\n"
"ds_write_addtid_b32 v191 offset:15776\n"
"buffer_load_dwordx4 v9, s[40:43], s20 offen offset:3072 lds\n"
"v_mfma_f32_16x16x32_bf16 v[140:143], a[96:99], v[252:255], v[140:143]\n"
"ds_read_b64_tr_b16 a[240:241], v11 offset:12288\n"
"v_mul_f32 v111, v111, v107\n"
"v_mfma_f32_16x16x32_bf16 v[140:143], a[128:131], v[84:87], v[140:143]\n"
"ds_read_b64_tr_b16 a[242:243], v11 offset:12800\n"
"v_cvt_pk_bf16_f32 v74, v108, v109\n"
"s_add_u32 s21, s83, 4096\n"
"v_mfma_f32_16x16x32_bf16 v[140:143], a[160:163], v[120:123], v[140:143]\n"
"ds_read_b128 v[188:191], v16 offset:25600\n"
"v_cvt_pk_bf16_f32 v75, v110, v111\n"
"v_mfma_f32_16x16x32_bf16 v[140:143], a[192:195], v[64:67], v[140:143]\n"
"ds_read_b128 v[104:107], v16 offset:25856\n"
"s_add_u32 m0, s77, 0\n"
"s_add_u32 s20, s20, 524288\n"
"s_cmp_eq_u32 s20, s66\n"
"s_cselect_b32 s20, s82, s20\n"
"buffer_load_dwordx4 v6, s[40:43], s20 offen offset:0 lds\n"
"v_mfma_f32_16x16x32_bf16 v[140:143], a[224:227], v[236:239], v[140:143]\n"
"ds_write_addtid_b32 v112 offset:24832\n"
"v_min_u32 v75, 0xffbfffff, v75\n"
"v_mfma_f32_16x16x32_bf16 v[60:63], a[4:7], v[224:227], v[60:63]\n"
"ds_write_addtid_b32 v113 offset:28704\n"
"buffer_store_dwordx4 v[72:75], v5, s[36:39], s21 offen offset:0 sc1\n"
"v_mfma_f32_16x16x32_bf16 v[60:63], a[36:39], v[88:91], v[60:63]\n"
"ds_write_addtid_b32 v114 offset:32640\n"
"v_mfma_f32_16x16x32_bf16 v[60:63], a[68:71], v[124:127], v[60:63]\n"
"ds_write_addtid_b32 v115 offset:36512\n"
"v_mfma_f32_16x16x32_bf16 v[60:63], a[100:103], v[252:255], v[60:63]\n"
"v_mfma_f32_16x16x32_bf16 v[60:63], a[132:135], v[84:87], v[60:63]\n"
"v_mfma_f32_16x16x32_bf16 v[60:63], a[164:167], v[120:123], v[60:63]\n"
"v_mfma_f32_16x16x32_bf16 v[60:63], a[196:199], v[64:67], v[60:63]\n"
"s_waitcnt lgkmcnt(0)\n"
"s_barrier\n"
"v_mfma_f32_16x16x32_bf16 v[60:63], a[228:231], v[236:239], v[60:63]\n"
"ds_read_b128 v[252:255], v15 offset:24576\n"
"v_mfma_f32_16x16x32_bf16 v[188:191], v[48:51], a[8:11], v[188:191]\n"
"ds_read_b128 v[236:239], v15 offset:24832\n"
"v_mfma_f32_16x16x32_bf16 v[188:191], v[92:95], a[40:43], v[188:191]\n"
"v_mfma_f32_16x16x32_bf16 v[188:191], v[40:43], a[72:75], v[188:191]\n"
"v_mfma_f32_16x16x32_bf16 v[188:191], v[244:247], a[104:107], v[188:191]\n"
"v_mfma_f32_16x16x32_bf16 v[188:191], v[128:131], a[136:139], v[188:191]\n"
"v_mfma_f32_16x16x32_bf16 v[188:191], v[160:163], a[168:171], v[188:191]\n"
"ds_read_b128 v[224:227], v16 offset:26112\n"
"buffer_load_dwordx4 v7, s[40:43], s20 offen offset:1024 lds\n"
"v_mfma_f32_16x16x32_bf16 v[188:191], v[116:119], a[200:203], v[188:191]\n"
"ds_read_b128 v[124:127], v16 offset:26368\n"
"v_mfma_f32_16x16x32_bf16 v[188:191], v[196:199], a[232:235], v[188:191]\n"
"s_waitcnt lgkmcnt(2)\n"
"v_add_f32 v140, v140, v252\n"
"v_add_f32 v141, v141, v253\n"
"v_mfma_f32_16x16x32_bf16 v[104:107], v[48:51], a[12:15], v[104:107]\n"
"v_add_f32 v142, v142, v254\n"
"v_add_f32 v143, v143, v255\n"
"v_mfma_f32_16x16x32_bf16 v[104:107], v[92:95], a[44:47], v[104:107]\n"
"buffer_load_dwordx4 v8, s[40:43], s20 offen offset:2048 lds\n"
"v_add_f32 v60, v60, v236\n"
"v_mul_f32 v252, v140, v140\n"
"v_mul_f32 v253, v141, v141\n"
"v_mfma_f32_16x16x32_bf16 v[104:107], v[40:43], a[76:79], v[104:107]\n"
"v_add_f32 v61, v61, v237\n"
"v_add_f32 v62, v62, v238\n"
"v_mul_f32 v254, v142, v142\n"
"v_mul_f32 v255, v143, v143\n"
"v_mfma_f32_16x16x32_bf16 v[104:107], v[244:247], a[108:111], v[104:107]\n"
"v_add_f32 v63, v63, v239\n"
"v_fmaak_f32 v252, v252, v14, 0xc0135761\n"
"v_fmaak_f32 v253, v253, v14, 0xc0135761\n"
"v_fmaak_f32 v254, v254, v14, 0xc0135761\n"
"v_mfma_f32_16x16x32_bf16 v[104:107], v[128:131], a[140:143], v[104:107]\n"
"v_fmaak_f32 v255, v255, v14, 0xc0135761\n"
"v_mul_f32 v252, v252, v140\n"
"v_mul_f32 v253, v253, v141\n"
"v_mul_f32 v254, v254, v142\n"
"v_mfma_f32_16x16x32_bf16 v[104:107], v[160:163], a[172:175], v[104:107]\n"
"s_waitcnt vmcnt(9)\n"
"ds_read_b64_tr_b16 a[20:21], v12 offset:16384\n"
"ds_read_b64_tr_b16 a[22:23], v12 offset:16896\n"
"ds_read_b64_tr_b16 a[52:53], v13 offset:16384\n"
"buffer_load_dwordx4 v9, s[40:43], s20 offen offset:3072 lds\n"
"v_mul_f32 v255, v255, v143\n"
"v_exp_f32 v252, v252\n"
"v_exp_f32 v253, v253\n"
"v_exp_f32 v254, v254\n"
"v_exp_f32 v255, v255\n"
"v_add_f32 v252, 1.0, v252\n"
"v_mfma_f32_16x16x32_bf16 v[104:107], v[116:119], a[204:207], v[104:107]\n"
"ds_read_b64_tr_b16 a[54:55], v13 offset:16896\n"
"v_add_f32 v253, 1.0, v253\n"
"v_add_f32 v254, 1.0, v254\n"
"v_add_f32 v255, 1.0, v255\n"
"v_mfma_f32_16x16x32_bf16 v[104:107], v[196:199], a[236:239], v[104:107]\n"
"ds_write_addtid_b32 v188 offset:25088\n"
"buffer_load_dwordx4 v[236:239], v4, s[36:39], s60 offen sc1\n"
"v_rcp_f32 v252, v252\n"
"v_rcp_f32 v253, v253\n"
"v_rcp_f32 v254, v254\n"
"v_rcp_f32 v255, v255\n"
"v_mul_f32 v252, v252, v140\n"
"v_mul_f32 v253, v253, v141\n"
"s_waitcnt lgkmcnt(5)\n"
"v_mfma_f32_16x16x32_bf16 v[224:227], a[0:3], v[48:51], v[224:227]\n"
"ds_write_addtid_b32 v189 offset:28960\n"
"v_mul_f32 v254, v254, v142\n"
"v_mul_f32 v255, v255, v143\n"
"v_cvt_pk_bf16_f32 v120, v252, v253\n"
"v_cvt_pk_bf16_f32 v121, v254, v255\n"
"v_mfma_f32_16x16x32_bf16 v[224:227], a[32:35], v[92:95], v[224:227]\n"
"ds_write_addtid_b32 v190 offset:32896\n"
"buffer_load_dwordx4 v[252:255], v4, s[36:39], s61 offen sc1\n"
"v_min_u32 v121, 0xffbfffff, v121\n"
"v_mul_f32 v140, v60, v60\n"
"v_mul_f32 v141, v61, v61\n"
"v_mfma_f32_16x16x32_bf16 v[224:227], a[64:67], v[40:43], v[224:227]\n"
"ds_write_addtid_b32 v191 offset:36768\n"
"v_mul_f32 v142, v62, v62\n"
"v_mul_f32 v143, v63, v63\n"
"v_fmaak_f32 v140, v140, v14, 0xc0135761\n"
"v_fmaak_f32 v141, v141, v14, 0xc0135761\n"
"v_mfma_f32_16x16x32_bf16 v[224:227], a[96:99], v[244:247], v[224:227]\n"
"ds_read_b64_tr_b16 a[84:85], v10 offset:16384\n"
"buffer_load_dwordx4 v[188:191], v4, s[36:39], s62 offen sc1\n"
"v_fmaak_f32 v142, v142, v14, 0xc0135761\n"
"v_fmaak_f32 v143, v143, v14, 0xc0135761\n"
"v_mul_f32 v140, v140, v60\n"
"v_mfma_f32_16x16x32_bf16 v[224:227], a[128:131], v[128:131], v[224:227]\n"
"ds_read_b64_tr_b16 a[86:87], v10 offset:16896\n"
"v_mul_f32 v141, v141, v61\n"
"v_mul_f32 v142, v142, v62\n"
"v_mul_f32 v143, v143, v63\n"
"v_mfma_f32_16x16x32_bf16 v[224:227], a[160:163], v[160:163], v[224:227]\n"
"ds_read_b64_tr_b16 a[116:117], v11 offset:16384\n"
"v_exp_f32 v140, v140\n"
"v_exp_f32 v141, v141\n"
"v_mfma_f32_16x16x32_bf16 v[224:227], a[192:195], v[116:119], v[224:227]\n"
"ds_read_b64_tr_b16 a[118:119], v11 offset:16896\n"
"buffer_load_dwordx4 v[112:115], v4, s[36:39], s63 offen sc1\n"
"v_exp_f32 v142, v142\n"
"v_mfma_f32_16x16x32_bf16 v[224:227], a[224:227], v[196:199], v[224:227]\n"
"ds_write_addtid_b32 v104 offset:25344\n"
"v_exp_f32 v143, v143\n"
"v_add_f32 v140, 1.0, v140\n"
"v_mfma_f32_16x16x32_bf16 v[124:127], a[4:7], v[48:51], v[124:127]\n"
"ds_write_addtid_b32 v105 offset:29216\n"
"v_add_f32 v141, 1.0, v141\n"
"v_add_f32 v142, 1.0, v142\n"
"v_add_f32 v143, 1.0, v143\n"
"v_mfma_f32_16x16x32_bf16 v[124:127], a[36:39], v[92:95], v[124:127]\n"
"ds_write_addtid_b32 v106 offset:33152\n"
"buffer_load_dwordx4 v[108:111], v4, s[36:39], s64 offen sc1\n"
"v_rcp_f32 v140, v140\n"
"v_mfma_f32_16x16x32_bf16 v[124:127], a[68:71], v[40:43], v[124:127]\n"
"ds_write_addtid_b32 v107 offset:37024\n"
"v_add_u32 v17, s70, v0\n"
"v_rcp_f32 v141, v141\n"
"v_mfma_f32_16x16x32_bf16 v[124:127], a[100:103], v[244:247], v[124:127]\n"
"v_rcp_f32 v142, v142\n"
"v_rcp_f32 v143, v143\n"
"v_mfma_f32_16x16x32_bf16 v[124:127], a[132:135], v[128:131], v[124:127]\n"
"s_add_u32 m0, s77, 4096\n"
"s_add_u32 s20, s20, 524288\n"
"buffer_load_dwordx4 v6, s[40:43], s20 offen offset:0 lds\n"
"v_mul_f32 v140, v140, v60\n"
"v_mfma_f32_16x16x32_bf16 v[124:127], a[164:167], v[160:163], v[124:127]\n"
"v_mul_f32 v141, v141, v61\n"
"v_mul_f32 v142, v142, v62\n"
"v_mul_f32 v143, v143, v63\n"
"v_mfma_f32_16x16x32_bf16 v[124:127], a[196:199], v[116:119], v[124:127]\n"
"s_waitcnt lgkmcnt(0)\n"
"s_barrier\n"
"buffer_load_dwordx4 v[244:247], v17, s[36:39], s56 offen sc1\n"
"v_mfma_f32_16x16x32_bf16 v[124:127], a[228:231], v[196:199], v[124:127]\n"
"ds_read_b128 v[160:163], v15 offset:25088\n"
"v_cvt_pk_bf16_f32 v122, v140, v141\n"
".Lgs_merge:\n"
"v_mfma_f32_16x16x32_bf16 v[196:199], v[100:103], v[208:211], 0\n"
"ds_read_b128 v[128:131], v15 offset:25344\n"
"v_max3_u32 v19, v101, v103, v77\n"
"v_cvt_pk_bf16_f32 v123, v142, v143\n"
"v_mfma_f32_16x16x32_bf16 v[196:199], v[76:79], v[184:187], v[196:199]\n"
"s_waitcnt vmcnt(12)\n"
"ds_read_b64_tr_b16 a[148:149], v12 offset:20480\n"
"v_max3_u32 v19, v79, v145, v19\n"
"v_min_u32 v123, 0xffbfffff, v123\n"
"v_mfma_f32_16x16x32_bf16 v[196:199], v[144:147], v[232:235], v[196:199]\n"
"ds_read_b64_tr_b16 a[150:151], v12 offset:20992\n"
"buffer_load_dwordx4 v7, s[40:43], s20 offen offset:1024 lds\n"
"v_lshlrev_b32 v140, 16, v25\n"
"v_mfma_f32_16x16x32_bf16 v[196:199], v[56:59], v[248:251], v[196:199]\n"
"ds_read_b64_tr_b16 a[180:181], v13 offset:20480\n"
"v_and_b32 v141, 0xffff0000, v25\n"
"v_mfma_f32_16x16x32_bf16 v[196:199], v[176:179], a[24:27], v[196:199]\n"
"ds_read_b64_tr_b16 a[182:183], v13 offset:20992\n"
"v_max3_u32 v19, v147, v57, v19\n"
"v_lshlrev_b32 v142, 16, v26\n"
"v_mfma_f32_16x16x32_bf16 v[196:199], v[172:175], a[152:155], v[196:199]\n"
"ds_read_b64_tr_b16 a[212:213], v10 offset:20480\n"
"buffer_load_dwordx4 v[116:119], v17, s[36:39], s57 offen sc1\n"
"v_and_b32 v143, 0xffff0000, v26\n"
"v_lshlrev_b32 v104, 16, v27\n"
"v_mfma_f32_16x16x32_bf16 v[196:199], v[164:167], a[28:31], v[196:199]\n"
"ds_read_b64_tr_b16 a[214:215], v10 offset:20992\n"
"v_max3_u32 v19, v59, v177, v19\n"
"buffer_store_dwordx4 v[120:123], v5, s[36:39], s83 offen offset:2048 sc1\n"
"v_mfma_f32_16x16x32_bf16 v[196:199], v[216:219], a[156:159], v[196:199]\n"
"ds_read_b64_tr_b16 a[244:245], v11 offset:20480\n"
"s_waitcnt lgkmcnt(7)\n"
"v_add_f32 v124, v124, v128\n"
"v_and_b32 v105, 0xffff0000, v27\n"
"v_mfma_f32_16x16x32_bf16 v[196:199], v[220:223], a[16:19], v[196:199]\n"
"ds_read_b64_tr_b16 a[246:247], v11 offset:20992\n"
"v_max3_u32 v19, v179, v173, v19\n"
"v_add_f32 v125, v125, v129\n"
"v_lshlrev_b32 v106, 16, v28\n"
"v_mfma_f32_16x16x32_bf16 v[120:123], v[100:103], v[204:207], 0\n"
"v_max3_u32 v19, v175, v165, v19\n"
"v_max3_u32 v19, v167, v217, v19\n"
"v_and_b32 v107, 0xffff0000, v28\n"
"v_mfma_f32_16x16x32_bf16 v[120:123], v[76:79], v[168:171], v[120:123]\n"
"v_max3_u32 v19, v219, v221, v19\n"
"v_max_u32 v19, v223, v19\n"
"v_mfma_f32_16x16x32_bf16 v[120:123], v[144:147], v[156:159], v[120:123]\n"
"v_add_f32 v224, v224, v160\n"
"v_add_f32 v225, v225, v161\n"
"v_mfma_f32_16x16x32_bf16 v[120:123], v[56:59], v[68:71], v[120:123]\n"
"v_add_f32 v226, v226, v162\n"
"v_add_f32 v227, v227, v163\n"
"v_mfma_f32_16x16x32_bf16 v[120:123], v[176:179], a[56:59], v[120:123]\n"
"v_add_f32 v126, v126, v130\n"
"v_add_f32 v127, v127, v131\n"
"v_mul_f32 v160, v224, v224\n"
"v_mul_f32 v161, v225, v225\n"
"v_mul_f32 v162, v226, v226\n"
"v_mfma_f32_16x16x32_bf16 v[120:123], v[172:175], a[184:187], v[120:123]\n"
"buffer_load_dwordx4 v8, s[40:43], s20 offen offset:2048 lds\n"
"v_cmp_le_u32 vcc, 0xffc00000, v19\n"
"v_mul_f32 v163, v227, v227\n"
"v_fmaak_f32 v160, v160, v14, 0xc0135761\n"
"v_fmaak_f32 v161, v161, v14, 0xc0135761\n"
"v_mfma_f32_16x16x32_bf16 v[120:123], v[164:167], a[60:63], v[120:123]\n"
"v_fmaak_f32 v162, v162, v14, 0xc0135761\n"
"v_fmaak_f32 v163, v163, v14, 0xc0135761\n"
"v_mul_f32 v160, v160, v224\n"
"v_mul_f32 v161, v161, v225\n"
"v_mul_f32 v162, v162, v226\n"
"v_mfma_f32_16x16x32_bf16 v[120:123], v[216:219], a[188:191], v[120:123]\n"
"buffer_load_dwordx4 v9, s[40:43], s20 offen offset:3072 lds\n"
"v_mul_f32 v163, v163, v227\n"
"v_exp_f32 v160, v160\n"
"v_exp_f32 v161, v161\n"
"v_exp_f32 v162, v162\n"
"v_exp_f32 v163, v163\n"
"v_add_f32 v160, 1.0, v160\n"
"v_mfma_f32_16x16x32_bf16 v[120:123], v[220:223], a[48:51], v[120:123]\n"
"s_cbranch_vccnz .Lgs_slow8\n"
".Lgs_back8:\n"
"v_add_f32 v161, 1.0, v161\n"
"v_add_f32 v162, 1.0, v162\n"
"v_add_f32 v163, 1.0, v163\n"
"v_mfma_f32_16x16x32_bf16 v[128:131], v[200:203], v[100:103], v[140:143]\n"
"ds_write_addtid_b32 v196 offset:20480\n"
"s_add_u32 m0, s77, 8192\n"
"s_add_u32 s20, s20, 524288\n"
"s_cmp_eq_u32 s20, s66\n"
"s_cselect_b32 s20, s82, s20\n"
"buffer_load_dwordx4 v6, s[40:43], s20 offen offset:0 lds\n"
"v_rcp_f32 v160, v160\n"
"v_rcp_f32 v161, v161\n"
"v_rcp_f32 v162, v162\n"
"v_rcp_f32 v163, v163\n"
"v_mul_f32 v160, v160, v224\n"
"v_mul_f32 v161, v161, v225\n"
"v_mfma_f32_16x16x32_bf16 v[128:131], v[132:135], v[76:79], v[128:131]\n"
"ds_write_addtid_b32 v197 offset:20256\n"
"buffer_load_dwordx4 v[92:95], v17, s[36:39], s58 offen sc1\n"
"v_mul_f32 v162, v162, v226\n"
"v_mul_f32 v163, v163, v227\n"
"v_cvt_pk_bf16_f32 v88, v160, v161\n"
"v_mfma_f32_16x16x32_bf16 v[128:131], v[148:151], v[144:147], v[128:131]\n"
"ds_write_addtid_b32 v198 offset:24192\n"
"buffer_load_dwordx4 v7, s[40:43], s20 offen offset:1024 lds\n"
"v_cvt_pk_bf16_f32 v89, v162, v163\n"
"v_min_u32 v89, 0xffbfffff, v89\n"
"v_mul_f32 v224, v124, v124\n"
"v_mfma_f32_16x16x32_bf16 v[128:131], v[228:231], v[56:59], v[128:131]\n"
"ds_write_addtid_b32 v199 offset:28064\n"
"v_mul_f32 v225, v125, v125\n"
"v_mul_f32 v226, v126, v126\n"
"v_mul_f32 v227, v127, v127\n"
"v_mfma_f32_16x16x32_bf16 v[128:131], a[88:91], v[176:179], v[128:131]\n"
"buffer_load_dwordx4 v8, s[40:43], s20 offen offset:2048 lds\n"
"v_fmaak_f32 v224, v224, v14, 0xc0135761\n"
"v_fmaak_f32 v225, v225, v14, 0xc0135761\n"
"v_fmaak_f32 v226, v226, v14, 0xc0135761\n"
"v_mfma_f32_16x16x32_bf16 v[128:131], a[216:219], v[172:175], v[128:131]\n"
".Lgs_pad3:\n"
"s_cbranch_execz .Lgs_al\n"
"v_fmaak_f32 v227, v227, v14, 0xc0135761\n"
"v_mul_f32 v224, v224, v124\n"
"v_mul_f32 v225, v225, v125\n"
"v_mul_f32 v226, v226, v126\n"
"v_mfma_f32_16x16x32_bf16 v[128:131], a[92:95], v[164:167], v[128:131]\n"
"buffer_load_dwordx4 v9, s[40:43], s20 offen offset:3072 lds\n"
"v_mul_f32 v227, v227, v127\n"
"v_exp_f32 v224, v224\n"
"v_mfma_f32_16x16x32_bf16 v[128:131], a[220:223], v[216:219], v[128:131]\n"
"ds_write_addtid_b32 v120 offset:16640\n"
"v_exp_f32 v225, v225\n"
"v_exp_f32 v226, v226\n"
"v_mfma_f32_16x16x32_bf16 v[128:131], a[80:83], v[220:223], v[128:131]\n"
"ds_write_addtid_b32 v121 offset:20512\n"
"s_add_u32 m0, s77, 12288\n"
"s_add_u32 s20, s20, 524288\n"
"buffer_load_dwordx4 v6, s[40:43], s20 offen offset:0 lds\n"
"v_mfma_f32_16x16x32_bf16 v[196:199], v[192:195], v[100:103], v[104:107]\n"
"ds_write_addtid_b32 v122 offset:20352\n"
"v_exp_f32 v227, v227\n"
"v_add_f32 v224, 1.0, v224\n"
"v_mfma_f32_16x16x32_bf16 v[196:199], v[212:215], v[76:79], v[196:199]\n"
"ds_write_addtid_b32 v123 offset:24224\n"
"buffer_load_dwordx4 v7, s[40:43], s20 offen offset:1024 lds\n"
"v_add_f32 v225, 1.0, v225\n"
"v_add_f32 v226, 1.0, v226\n"
"v_mfma_f32_16x16x32_bf16 v[196:199], v[96:99], v[144:147], v[196:199]\n"
"s_waitcnt vmcnt(19)\n"
"ds_read_b64_tr_b16 a[8:9], v12 offset:0\n"
"v_add_f32 v227, 1.0, v227\n"
"v_rcp_f32 v224, v224\n"
"v_mfma_f32_16x16x32_bf16 v[196:199], v[240:243], v[56:59], v[196:199]\n"
"ds_read_b64_tr_b16 a[10:11], v12 offset:512\n"
"buffer_load_dwordx4 v8, s[40:43], s20 offen offset:2048 lds\n"
"v_rcp_f32 v225, v225\n"
"v_mfma_f32_16x16x32_bf16 v[196:199], a[120:123], v[176:179], v[196:199]\n"
"ds_read_b64_tr_b16 a[40:41], v13 offset:0\n"
"v_rcp_f32 v226, v226\n"
"v_mfma_f32_16x16x32_bf16 v[196:199], a[248:251], v[172:175], v[196:199]\n"
"ds_read_b64_tr_b16 a[42:43], v13 offset:512\n"
"buffer_load_dwordx4 v9, s[40:43], s20 offen offset:3072 lds\n"
"v_rcp_f32 v227, v227\n"
"v_mfma_f32_16x16x32_bf16 v[196:199], a[124:127], v[164:167], v[196:199]\n"
"ds_read_b64_tr_b16 a[72:73], v10 offset:0\n"
"v_mul_f32 v224, v224, v124\n"
"v_mul_f32 v225, v225, v125\n"
"v_mul_f32 v226, v226, v126\n"
"v_mfma_f32_16x16x32_bf16 v[196:199], a[252:255], v[216:219], v[196:199]\n"
"ds_write_addtid_b32 v128 offset:13312\n"
"s_add_u32 m0, s77, 16384\n"
"s_add_u32 s20, s20, 524288\n"
"s_cmp_eq_u32 s20, s66\n"
"s_cselect_b32 s20, s82, s20\n"
"buffer_load_dwordx4 v6, s[40:43], s20 offen offset:0 lds\n"
"v_mfma_f32_16x16x32_bf16 v[196:199], a[112:115], v[220:223], v[196:199]\n"
"ds_write_addtid_b32 v129 offset:13088\n"
"v_mul_f32 v227, v227, v127\n"
"s_add_u32 s21, s83, 4096\n"
"v_mfma_f32_16x16x32_bf16 v[160:163], v[136:139], v[208:211], 0\n"
"ds_write_addtid_b32 v130 offset:17024\n"
"buffer_load_dwordx4 v7, s[40:43], s20 offen offset:1024 lds\n"
"v_cvt_pk_bf16_f32 v90, v224, v225\n"
"v_mfma_f32_16x16x32_bf16 v[160:163], v[80:83], v[184:187], v[160:163]\n"
"ds_write_addtid_b32 v131 offset:20896\n"
"v_max3_u32 v14, v137, v139, v81\n"
"v_cvt_pk_bf16_f32 v91, v226, v227\n"
"v_mfma_f32_16x16x32_bf16 v[160:163], v[180:183], v[232:235], v[160:163]\n"
"ds_read_b64_tr_b16 a[74:75], v10 offset:512\n"
"buffer_load_dwordx4 v8, s[40:43], s20 offen offset:2048 lds\n"
"v_min_u32 v91, 0xffbfffff, v91\n"
"v_mfma_f32_16x16x32_bf16 v[160:163], v[152:155], v[248:251], v[160:163]\n"
"ds_read_b64_tr_b16 a[104:105], v11 offset:0\n"
"buffer_store_dwordx4 v[88:91], v5, s[36:39], s21 offen offset:1024 sc1\n"
"s_waitcnt vmcnt(21)\n"
"v_mfma_f32_16x16x32_bf16 v[160:163], v[236:239], a[24:27], v[160:163]\n"
"ds_read_b64_tr_b16 a[106:107], v11 offset:512\n"
"buffer_load_dwordx4 v9, s[40:43], s20 offen offset:3072 lds\n"
"v_mfma_f32_16x16x32_bf16 v[160:163], v[252:255], a[152:155], v[160:163]\n"
"v_max3_u32 v14, v83, v181, v14\n"
"v_max3_u32 v14, v183, v153, v14\n"
"v_mfma_f32_16x16x32_bf16 v[160:163], v[188:191], a[28:31], v[160:163]\n"
"s_add_u32 m0, s77, 20480\n"
"s_add_u32 s20, s20, 524288\n"
"s_waitcnt lgkmcnt(15)\n"
"buffer_load_dwordx4 v6, s[40:43], s20 offen offset:0 lds\n"
"v_mfma_f32_16x16x32_bf16 v[160:163], v[112:115], a[156:159], v[160:163]\n"
"ds_write_addtid_b32 v196 offset:5376\n"
"s_waitcnt vmcnt(22)\n"
"v_mfma_f32_16x16x32_bf16 v[160:163], v[108:111], a[16:19], v[160:163]\n"
"ds_write_addtid_b32 v197 offset:9248\n"
"buffer_load_dwordx4 v7, s[40:43], s20 offen offset:1024 lds\n"
"v_mfma_f32_16x16x32_bf16 v[224:227], v[136:139], v[204:207], 0\n"
"ds_write_addtid_b32 v198 offset:13184\n"
"v_max3_u32 v14, v155, v237, v14\n"
"v_mfma_f32_16x16x32_bf16 v[224:227], v[80:83], v[168:171], v[224:227]\n"
"ds_write_addtid_b32 v199 offset:17056\n"
"buffer_load_dwordx4 v8, s[40:43], s20 offen offset:2048 lds\n"
"v_mfma_f32_16x16x32_bf16 v[224:227], v[180:183], v[156:159], v[224:227]\n"
"v_max3_u32 v14, v239, v253, v14\n"
"v_max3_u32 v14, v255, v189, v14\n"
"v_mfma_f32_16x16x32_bf16 v[224:227], v[152:155], v[68:71], v[224:227]\n"
"buffer_load_dwordx4 v9, s[40:43], s20 offen offset:3072 lds\n"
"v_max3_u32 v14, v191, v113, v14\n"
"v_mfma_f32_16x16x32_bf16 v[224:227], v[236:239], a[56:59], v[224:227]\n"
"v_max3_u32 v14, v115, v109, v14\n"
"v_max_u32 v14, v111, v14\n"
"v_mfma_f32_16x16x32_bf16 v[224:227], v[252:255], a[184:187], v[224:227]\n"
"s_waitcnt lgkmcnt(0)\n"
"s_barrier\n"
"v_mfma_f32_16x16x32_bf16 v[224:227], v[188:191], a[60:63], v[224:227]\n"
"buffer_load_dwordx4 v[196:199], v17, s[36:39], s59 offen sc1\n"
"v_cmp_le_u32 vcc, 0xffc00000, v14\n"
"v_mfma_f32_16x16x32_bf16 v[224:227], v[112:115], a[188:191], v[224:227]\n"
"v_mfma_f32_16x16x32_bf16 v[224:227], v[108:111], a[48:51], v[224:227]\n"
"s_cbranch_vccnz .Lgs_slow9\n"
".Lgs_back9:\n"
"v_mfma_f32_16x16x32_bf16 v[128:131], v[200:203], v[136:139], v[140:143]\n"
"ds_write_addtid_b32 v160 offset:4608\n"
"v_mfma_f32_16x16x32_bf16 v[128:131], v[132:135], v[80:83], v[128:131]\n"
"ds_write_addtid_b32 v161 offset:8480\n"
"v_mfma_f32_16x16x32_bf16 v[128:131], v[148:151], v[180:183], v[128:131]\n"
"ds_write_addtid_b32 v162 offset:12416\n"
"buffer_load_dwordx4 v[124:127], v17, s[36:39], s60 offen sc1\n"
"v_mfma_f32_16x16x32_bf16 v[128:131], v[228:231], v[152:155], v[128:131]\n"
"ds_write_addtid_b32 v163 offset:16288\n"
"v_mfma_f32_16x16x32_bf16 v[128:131], a[88:91], v[236:239], v[128:131]\n"
"v_mfma_f32_16x16x32_bf16 v[128:131], a[216:219], v[252:255], v[128:131]\n"
"ds_read_b128 v[160:163], v16 offset:24832\n"
"v_mfma_f32_16x16x32_bf16 v[128:131], a[92:95], v[188:191], v[128:131]\n"
"ds_read_b128 v[120:123], v16 offset:24576\n"
"buffer_load_dwordx4 v[88:91], v17, s[36:39], s61 offen sc1\n"
"v_mfma_f32_16x16x32_bf16 v[128:131], a[220:223], v[112:115], v[128:131]\n"
"ds_write_addtid_b32 v224 offset:4864\n"
"v_mfma_f32_16x16x32_bf16 v[128:131], a[80:83], v[108:111], v[128:131]\n"
"ds_write_addtid_b32 v225 offset:8736\n"
"v_mfma_f32_16x16x32_bf16 v[84:87], v[192:195], v[136:139], v[104:107]\n"
"ds_write_addtid_b32 v226 offset:12672\n"
"buffer_load_dwordx4 v[72:75], v17, s[36:39], s62 offen sc1\n"
"v_mfma_f32_16x16x32_bf16 v[84:87], v[212:215], v[80:83], v[84:87]\n"
"ds_write_addtid_b32 v227 offset:16544\n"
"v_mfma_f32_16x16x32_bf16 v[84:87], v[96:99], v[180:183], v[84:87]\n"
"s_waitcnt vmcnt(22)\n"
"ds_read_b64_tr_b16 a[136:137], v12 offset:4096\n"
"v_mfma_f32_16x16x32_bf16 v[84:87], v[240:243], v[152:155], v[84:87]\n"
"ds_read_b64_tr_b16 a[138:139], v12 offset:4608\n"
"v_mov_b32 v226, s78\n"
"v_mfma_f32_16x16x32_bf16 v[84:87], a[120:123], v[236:239], v[84:87]\n"
"ds_read_b64_tr_b16 a[168:169], v13 offset:4096\n"
"buffer_load_dwordx4 v[64:67], v17, s[36:39], s63 offen sc1\n"
"v_mfma_f32_16x16x32_bf16 v[84:87], a[248:251], v[252:255], v[84:87]\n"
"ds_read_b64_tr_b16 a[170:171], v13 offset:4608\n"
"v_mov_b32 v227, s79\n"
"v_mfma_f32_16x16x32_bf16 v[84:87], a[124:127], v[188:191], v[84:87]\n"
"ds_read_b64_tr_b16 a[200:201], v10 offset:4096\n"
"s_waitcnt vmcnt(18)\n"
"ds_read_b64_tr_b16 a[12:13], v12 offset:8192\n"
"ds_read_b64_tr_b16 a[14:15], v12 offset:8704\n"
"v_add_u32 v14, s71, v0\n"
"v_mfma_f32_16x16x32_bf16 v[84:87], a[252:255], v[112:115], v[84:87]\n"
"ds_write_addtid_b32 v128 offset:5632\n"
"ds_read_b64_tr_b16 a[44:45], v13 offset:8192\n"
"ds_read_b64_tr_b16 a[46:47], v13 offset:8704\n"
"buffer_load_dwordx4 v[60:63], v17, s[36:39], s64 offen sc1\n"
"v_mfma_f32_16x16x32_bf16 v[84:87], a[112:115], v[108:111], v[84:87]\n"
"ds_write_addtid_b32 v129 offset:9504\n"
"s_waitcnt lgkmcnt(15)\n"
"v_mfma_f32_16x16x32_bf16 v[120:123], a[144:147], v[100:103], v[120:123]\n"
"ds_write_addtid_b32 v130 offset:13440\n"
"v_mfma_f32_16x16x32_bf16 v[120:123], a[20:23], v[76:79], v[120:123]\n"
"ds_write_addtid_b32 v131 offset:17312\n"
"s_waitcnt vmcnt(15)\n"
"ds_read_b64_tr_b16 a[140:141], v12 offset:12288\n"
"ds_read_b64_tr_b16 a[142:143], v12 offset:12800\n"
"buffer_load_dwordx4 v[128:131], v14, s[36:39], s56 offen sc1\n"
"v_mfma_f32_16x16x32_bf16 v[120:123], a[148:151], v[144:147], v[120:123]\n"
"ds_read_b64_tr_b16 a[202:203], v10 offset:4608\n"
"ds_read_b64_tr_b16 a[172:173], v13 offset:12288\n"
"ds_read_b64_tr_b16 a[174:175], v13 offset:12800\n"
"s_waitcnt vmcnt(11)\n"
"ds_read_b64_tr_b16 a[0:1], v12 offset:16384\n"
"ds_read_b64_tr_b16 a[2:3], v12 offset:16896\n"
"v_mfma_f32_16x16x32_bf16 v[120:123], a[8:11], v[56:59], v[120:123]\n"
"ds_read_b64_tr_b16 a[232:233], v11 offset:4096\n"
"ds_read_b64_tr_b16 a[204:205], v10 offset:12288\n"
"ds_read_b64_tr_b16 a[32:33], v13 offset:16384\n"
"s_waitcnt vmcnt(7)\n"
"ds_read_b64_tr_b16 a[128:129], v12 offset:20480\n"
"ds_read_b64_tr_b16 a[130:131], v12 offset:20992\n"
"buffer_load_dwordx4 v[52:55], v14, s[36:39], s57 offen sc1\n"
"s_waitcnt lgkmcnt(15)\n"
"v_mfma_f32_16x16x32_bf16 v[120:123], a[136:139], v[176:179], v[120:123]\n"
"ds_read_b64_tr_b16 a[234:235], v11 offset:4608\n"
"ds_read_b64_tr_b16 a[34:35], v13 offset:16896\n"
"ds_read_b64_tr_b16 a[64:65], v10 offset:16384\n"
"v_mfma_f32_16x16x32_bf16 v[120:123], a[12:15], v[172:175], v[120:123]\n"
"ds_read_b128 v[48:51], v16 offset:25856\n"
"ds_read_b64_tr_b16 a[66:67], v10 offset:16896\n"
"ds_read_b64_tr_b16 a[160:161], v13 offset:20480\n"
"s_waitcnt lgkmcnt(15)\n"
"v_mfma_f32_16x16x32_bf16 v[120:123], a[140:143], v[164:167], v[120:123]\n"
"ds_read_b128 v[44:47], v16 offset:25600\n"
"ds_read_b64_tr_b16 a[162:163], v13 offset:20992\n"
"ds_read_b64_tr_b16 a[192:193], v10 offset:20480\n"
"buffer_load_dwordx4 v[40:43], v14, s[36:39], s58 offen sc1\n"
"s_waitcnt lgkmcnt(14)\n"
"v_mfma_f32_16x16x32_bf16 v[120:123], a[0:3], v[216:219], v[120:123]\n"
"ds_write_addtid_b32 v84 offset:5888\n"
"ds_read_b64_tr_b16 a[194:195], v10 offset:20992\n"
"s_waitcnt lgkmcnt(11)\n"
"v_mfma_f32_16x16x32_bf16 v[120:123], a[128:131], v[220:223], v[120:123]\n"
"ds_write_addtid_b32 v85 offset:9760\n"
"v_mfma_f32_16x16x32_bf16 v[160:163], a[176:179], v[100:103], v[160:163]\n"
"ds_write_addtid_b32 v86 offset:13696\n"
"v_mfma_f32_16x16x32_bf16 v[160:163], a[52:55], v[76:79], v[160:163]\n"
"ds_write_addtid_b32 v87 offset:17568\n"
"buffer_store_dwordx2 v[226:227], v21, s[36:39], 0 offen offset:0 sc1\n"
"v_mfma_f32_16x16x32_bf16 v[160:163], a[180:183], v[144:147], v[160:163]\n"
"ds_read_b64_tr_b16 a[76:77], v10 offset:8192\n"
"v_mfma_f32_16x16x32_bf16 v[160:163], a[40:43], v[56:59], v[160:163]\n"
"ds_read_b64_tr_b16 a[78:79], v10 offset:8704\n"
"v_mfma_f32_16x16x32_bf16 v[160:163], a[168:171], v[176:179], v[160:163]\n"
"ds_read_b64_tr_b16 a[108:109], v11 offset:8192\n"
"v_mfma_f32_16x16x32_bf16 v[160:163], a[44:47], v[172:175], v[160:163]\n"
"ds_read_b64_tr_b16 a[110:111], v11 offset:8704\n"
"v_mfma_f32_16x16x32_bf16 v[160:163], a[172:175], v[164:167], v[160:163]\n"
"ds_read_b64_tr_b16 a[206:207], v10 offset:12800\n"
"s_waitcnt lgkmcnt(15)\n"
"v_mfma_f32_16x16x32_bf16 v[160:163], a[32:35], v[216:219], v[160:163]\n"
"ds_write_addtid_b32 v120 offset:6144\n"
"s_waitcnt lgkmcnt(12)\n"
"v_mfma_f32_16x16x32_bf16 v[160:163], a[160:163], v[220:223], v[160:163]\n"
"ds_write_addtid_b32 v121 offset:10016\n"
"v_mfma_f32_16x16x32_bf16 v[44:47], v[100:103], a[208:211], v[44:47]\n"
"ds_write_addtid_b32 v122 offset:13952\n"
"v_mfma_f32_16x16x32_bf16 v[44:47], v[76:79], a[84:87], v[44:47]\n"
"ds_write_addtid_b32 v123 offset:17824\n"
"v_mfma_f32_16x16x32_bf16 v[44:47], v[144:147], a[212:215], v[44:47]\n"
"ds_read_b64_tr_b16 a[236:237], v11 offset:12288\n"
"v_mfma_f32_16x16x32_bf16 v[44:47], v[56:59], a[72:75], v[44:47]\n"
"ds_read_b64_tr_b16 a[238:239], v11 offset:12800\n"
"v_mfma_f32_16x16x32_bf16 v[44:47], v[176:179], a[200:203], v[44:47]\n"
"ds_read_b64_tr_b16 a[96:97], v11 offset:16384\n"
"s_waitcnt lgkmcnt(10)\n"
"v_mfma_f32_16x16x32_bf16 v[44:47], v[172:175], a[76:79], v[44:47]\n"
"ds_read_b64_tr_b16 a[98:99], v11 offset:16896\n"
"s_waitcnt lgkmcnt(8)\n"
"v_mfma_f32_16x16x32_bf16 v[44:47], v[164:167], a[204:207], v[44:47]\n"
"ds_read_b64_tr_b16 a[224:225], v11 offset:20480\n"
"v_mfma_f32_16x16x32_bf16 v[44:47], v[216:219], a[64:67], v[44:47]\n"
"ds_write_addtid_b32 v160 offset:6400\n"
"v_mfma_f32_16x16x32_bf16 v[44:47], v[220:223], a[192:195], v[44:47]\n"
"ds_write_addtid_b32 v161 offset:10272\n"
"v_mfma_f32_16x16x32_bf16 v[48:51], v[100:103], a[240:243], v[48:51]\n"
"ds_write_addtid_b32 v162 offset:14208\n"
"v_mfma_f32_16x16x32_bf16 v[48:51], v[76:79], a[116:119], v[48:51]\n"
"ds_write_addtid_b32 v163 offset:18080\n"
"v_mfma_f32_16x16x32_bf16 v[48:51], v[144:147], a[244:247], v[48:51]\n"
"ds_read_b64_tr_b16 a[226:227], v11 offset:20992\n"
"buffer_load_dwordx4 v[224:227], v14, s[36:39], s59 offen sc1\n"
"v_mfma_f32_16x16x32_bf16 v[48:51], v[56:59], a[104:107], v[48:51]\n"
"v_mfma_f32_16x16x32_bf16 v[48:51], v[176:179], a[232:235], v[48:51]\n"
"v_mfma_f32_16x16x32_bf16 v[48:51], v[172:175], a[108:111], v[48:51]\n"
"s_waitcnt lgkmcnt(1)\n"
"s_barrier\n"
"buffer_load_dwordx4 v[176:179], v14, s[36:39], s60 offen sc1\n"
"v_mfma_f32_16x16x32_bf16 v[48:51], v[164:167], a[236:239], v[48:51]\n"
"ds_read_b128 v[172:175], v15 offset:26624\n"
"v_mfma_f32_16x16x32_bf16 v[48:51], v[216:219], a[96:99], v[48:51]\n"
"ds_read_b128 v[164:167], v15 offset:26880\n"
"s_waitcnt lgkmcnt(2)\n"
"v_mfma_f32_16x16x32_bf16 v[48:51], v[220:223], a[224:227], v[48:51]\n"
"s_waitcnt vmcnt(11)\n"
"v_mfma_f32_16x16x32_bf16 v[216:219], v[244:247], v[208:211], 0\n"
"buffer_load_dwordx4 v[220:223], v14, s[36:39], s61 offen sc1\n"
"v_max3_u32 v19, v245, v247, v117\n"
"v_mfma_f32_16x16x32_bf16 v[216:219], v[116:119], v[184:187], v[216:219]\n"
"v_max3_u32 v19, v119, v93, v19\n"
"v_max3_u32 v19, v95, v197, v19\n"
"v_mfma_f32_16x16x32_bf16 v[216:219], v[92:95], v[232:235], v[216:219]\n"
"v_mfma_f32_16x16x32_bf16 v[216:219], v[196:199], v[248:251], v[216:219]\n"
"buffer_load_dwordx4 v[160:163], v14, s[36:39], s62 offen sc1\n"
"s_waitcnt vmcnt(9)\n"
"v_mfma_f32_16x16x32_bf16 v[216:219], v[124:127], a[24:27], v[216:219]\n"
"v_max3_u32 v19, v199, v125, v19\n"
"v_max3_u32 v19, v127, v89, v19\n"
"v_mfma_f32_16x16x32_bf16 v[216:219], v[88:91], a[152:155], v[216:219]\n"
"v_max3_u32 v19, v91, v73, v19\n"
"v_max3_u32 v19, v75, v65, v19\n"
"v_mfma_f32_16x16x32_bf16 v[216:219], v[72:75], a[28:31], v[216:219]\n"
"s_waitcnt lgkmcnt(0)\n"
"v_add_f32 v44, v44, v172\n"
"v_add_f32 v45, v45, v173\n"
"v_mfma_f32_16x16x32_bf16 v[216:219], v[64:67], a[156:159], v[216:219]\n"
"buffer_load_dwordx4 v[144:147], v14, s[36:39], s63 offen sc1\n"
"s_waitcnt vmcnt(9)\n"
"v_mfma_f32_16x16x32_bf16 v[216:219], v[60:63], a[16:19], v[216:219]\n"
"v_max3_u32 v19, v67, v61, v19\n"
"v_max_u32 v19, v63, v19\n"
"v_or_b32 v12, s94, v0\n"
"v_mfma_f32_16x16x32_bf16 v[120:123], v[244:247], v[204:207], 0\n"
"v_add_f32 v46, v46, v174\n"
"v_add_f32 v47, v47, v175\n"
"v_mfma_f32_16x16x32_bf16 v[120:123], v[116:119], v[168:171], v[120:123]\n"
"buffer_load_dwordx4 v[100:103], v14, s[36:39], s64 offen sc1\n"
"v_add_u32 v13, s72, v0\n"
"v_cvt_pk_bf16_f32 v172, v44, v45\n"
"v_mfma_f32_16x16x32_bf16 v[120:123], v[92:95], v[156:159], v[120:123]\n"
"v_add_f32 v48, v48, v164\n"
"v_add_f32 v49, v49, v165\n"
"v_mfma_f32_16x16x32_bf16 v[120:123], v[196:199], v[68:71], v[120:123]\n"
"v_add_f32 v50, v50, v166\n"
"v_add_f32 v51, v51, v167\n"
"v_mfma_f32_16x16x32_bf16 v[120:123], v[124:127], a[56:59], v[120:123]\n"
"v_cvt_pk_bf16_f32 v173, v46, v47\n"
"v_min_u32 v173, 0xffbfffff, v173\n"
"v_mfma_f32_16x16x32_bf16 v[120:123], v[88:91], a[184:187], v[120:123]\n"
"buffer_load_dwordx4 v[164:167], v13, s[36:39], s56 offen sc1\n"
"v_cmp_le_u32 vcc, 0xffc00000, v19\n"
"v_mfma_f32_16x16x32_bf16 v[120:123], v[72:75], a[60:63], v[120:123]\n"
"v_cvt_pk_bf16_f32 v174, v48, v49\n"
"v_cvt_pk_bf16_f32 v175, v50, v51\n"
"v_mfma_f32_16x16x32_bf16 v[120:123], v[64:67], a[188:191], v[120:123]\n"
"v_mfma_f32_16x16x32_bf16 v[120:123], v[60:63], a[48:51], v[120:123]\n"
"s_cbranch_vccnz .Lgs_slow10\n"
".Lgs_back10:\n"
"v_min_u32 v175, 0xffbfffff, v175\n"
"v_mfma_f32_16x16x32_bf16 v[84:87], v[200:203], v[244:247], v[140:143]\n"
"ds_write_addtid_b32 v216 offset:4096\n"
"buffer_load_dwordx4 v[76:79], v13, s[36:39], s57 offen sc1\n"
"v_mfma_f32_16x16x32_bf16 v[84:87], v[132:135], v[116:119], v[84:87]\n"
"ds_write_addtid_b32 v217 offset:7968\n"
"buffer_store_dwordx4 v[172:175], v12, s[36:39], s84 offen offset:0 sc1\n"
"v_mfma_f32_16x16x32_bf16 v[84:87], v[148:151], v[92:95], v[84:87]\n"
"ds_write_addtid_b32 v218 offset:11904\n"
"v_mfma_f32_16x16x32_bf16 v[84:87], v[228:231], v[196:199], v[84:87]\n"
"ds_write_addtid_b32 v219 offset:15776\n"
"v_mov_b32 v172, -1\n"
"v_mfma_f32_16x16x32_bf16 v[84:87], a[88:91], v[124:127], v[84:87]\n"
"v_mfma_f32_16x16x32_bf16 v[84:87], a[216:219], v[88:91], v[84:87]\n"
"ds_read_b128 v[216:219], v16 offset:25344\n"
"v_mov_b32 v173, -1\n"
"v_mfma_f32_16x16x32_bf16 v[84:87], a[92:95], v[72:75], v[84:87]\n"
"ds_read_b128 v[56:59], v16 offset:25088\n"
"v_mov_b32 v174, -1\n"
"v_mfma_f32_16x16x32_bf16 v[84:87], a[220:223], v[64:67], v[84:87]\n"
"ds_write_addtid_b32 v120 offset:4352\n"
"v_mov_b32 v175, -1\n"
"v_mfma_f32_16x16x32_bf16 v[84:87], a[80:83], v[60:63], v[84:87]\n"
"ds_write_addtid_b32 v121 offset:8224\n"
"v_mfma_f32_16x16x32_bf16 v[36:39], v[192:195], v[244:247], v[104:107]\n"
"ds_write_addtid_b32 v122 offset:12160\n"
"v_mfma_f32_16x16x32_bf16 v[36:39], v[212:215], v[116:119], v[36:39]\n"
"ds_write_addtid_b32 v123 offset:16032\n"
"v_mfma_f32_16x16x32_bf16 v[36:39], v[96:99], v[92:95], v[36:39]\n"
"v_mfma_f32_16x16x32_bf16 v[36:39], v[240:243], v[196:199], v[36:39]\n"
"v_mfma_f32_16x16x32_bf16 v[36:39], a[120:123], v[124:127], v[36:39]\n"
"buffer_store_dwordx4 v[172:175], v0, s[36:39], s85 offen offset:1024 sc1\n"
"v_mfma_f32_16x16x32_bf16 v[36:39], a[248:251], v[88:91], v[36:39]\n"
"v_mfma_f32_16x16x32_bf16 v[36:39], a[124:127], v[72:75], v[36:39]\n"
"v_mfma_f32_16x16x32_bf16 v[36:39], a[252:255], v[64:67], v[36:39]\n"
"ds_write_addtid_b32 v84 offset:5120\n"
"v_mfma_f32_16x16x32_bf16 v[36:39], a[112:115], v[60:63], v[36:39]\n"
"ds_write_addtid_b32 v85 offset:8992\n"
"s_waitcnt lgkmcnt(6)\n"
"v_mfma_f32_16x16x32_bf16 v[56:59], a[144:147], v[136:139], v[56:59]\n"
"ds_write_addtid_b32 v86 offset:12928\n"
"v_mfma_f32_16x16x32_bf16 v[56:59], a[20:23], v[80:83], v[56:59]\n"
"ds_write_addtid_b32 v87 offset:16800\n"
"v_mfma_f32_16x16x32_bf16 v[56:59], a[148:151], v[180:183], v[56:59]\n"
"v_mfma_f32_16x16x32_bf16 v[56:59], a[8:11], v[152:155], v[56:59]\n"
"v_mfma_f32_16x16x32_bf16 v[56:59], a[136:139], v[236:239], v[56:59]\n"
"v_mfma_f32_16x16x32_bf16 v[56:59], a[12:15], v[252:255], v[56:59]\n"
"ds_read_b128 v[120:123], v16 offset:26368\n"
"v_mfma_f32_16x16x32_bf16 v[56:59], a[140:143], v[188:191], v[56:59]\n"
"ds_read_b128 v[84:87], v16 offset:26112\n"
"v_mfma_f32_16x16x32_bf16 v[56:59], a[0:3], v[112:115], v[56:59]\n"
"ds_write_addtid_b32 v36 offset:5376\n"
"buffer_store_dwordx4 v[172:175], v0, s[36:39], s86 offen offset:1024 sc1\n"
"v_mfma_f32_16x16x32_bf16 v[56:59], a[128:131], v[108:111], v[56:59]\n"
"ds_write_addtid_b32 v37 offset:9248\n"
"v_mfma_f32_16x16x32_bf16 v[216:219], a[176:179], v[136:139], v[216:219]\n"
"ds_write_addtid_b32 v38 offset:13184\n"
"v_mfma_f32_16x16x32_bf16 v[216:219], a[52:55], v[80:83], v[216:219]\n"
"ds_write_addtid_b32 v39 offset:17056\n"
"v_mfma_f32_16x16x32_bf16 v[216:219], a[180:183], v[180:183], v[216:219]\n"
"v_mfma_f32_16x16x32_bf16 v[216:219], a[40:43], v[152:155], v[216:219]\n"
"v_mfma_f32_16x16x32_bf16 v[216:219], a[168:171], v[236:239], v[216:219]\n"
"v_mfma_f32_16x16x32_bf16 v[216:219], a[44:47], v[252:255], v[216:219]\n"
"v_mfma_f32_16x16x32_bf16 v[216:219], a[172:175], v[188:191], v[216:219]\n"
"v_mfma_f32_16x16x32_bf16 v[216:219], a[32:35], v[112:115], v[216:219]\n"
"ds_write_addtid_b32 v56 offset:6656\n"
"v_mfma_f32_16x16x32_bf16 v[216:219], a[160:163], v[108:111], v[216:219]\n"
"ds_write_addtid_b32 v57 offset:10528\n"
"s_waitcnt lgkmcnt(6)\n"
"v_mfma_f32_16x16x32_bf16 v[84:87], v[136:139], a[208:211], v[84:87]\n"
"ds_write_addtid_b32 v58 offset:14464\n"
"v_mfma_f32_16x16x32_bf16 v[84:87], v[80:83], a[84:87], v[84:87]\n"
"ds_write_addtid_b32 v59 offset:18336\n"
"v_mfma_f32_16x16x32_bf16 v[84:87], v[180:183], a[212:215], v[84:87]\n"
"buffer_load_dwordx4 v[36:39], v13, s[36:39], s58 offen sc1\n"
"v_mfma_f32_16x16x32_bf16 v[84:87], v[152:155], a[72:75], v[84:87]\n"
"v_mfma_f32_16x16x32_bf16 v[84:87], v[236:239], a[200:203], v[84:87]\n"
"v_mfma_f32_16x16x32_bf16 v[84:87], v[252:255], a[76:79], v[84:87]\n"
"v_mfma_f32_16x16x32_bf16 v[84:87], v[188:191], a[204:207], v[84:87]\n"
"v_mfma_f32_16x16x32_bf16 v[84:87], v[112:115], a[64:67], v[84:87]\n"
"ds_write_addtid_b32 v216 offset:6912\n"
"v_mfma_f32_16x16x32_bf16 v[84:87], v[108:111], a[192:195], v[84:87]\n"
"ds_write_addtid_b32 v217 offset:10784\n"
"v_mfma_f32_16x16x32_bf16 v[120:123], v[136:139], a[240:243], v[120:123]\n"
"ds_write_addtid_b32 v218 offset:14720\n"
"v_mfma_f32_16x16x32_bf16 v[120:123], v[80:83], a[116:119], v[120:123]\n"
"ds_write_addtid_b32 v219 offset:18592\n"
"v_mfma_f32_16x16x32_bf16 v[120:123], v[180:183], a[244:247], v[120:123]\n"
"buffer_load_dwordx4 v[136:139], v13, s[36:39], s59 offen sc1\n"
"v_mfma_f32_16x16x32_bf16 v[120:123], v[152:155], a[104:107], v[120:123]\n"
"v_mfma_f32_16x16x32_bf16 v[120:123], v[236:239], a[232:235], v[120:123]\n"
"v_mfma_f32_16x16x32_bf16 v[120:123], v[252:255], a[108:111], v[120:123]\n"
"s_waitcnt lgkmcnt(0)\n"
"s_barrier\n"
"buffer_load_dwordx4 v[252:255], v13, s[36:39], s60 offen sc1\n"
"v_mfma_f32_16x16x32_bf16 v[120:123], v[188:191], a[236:239], v[120:123]\n"
"ds_read_b128 v[236:239], v15 offset:27136\n"
"v_mfma_f32_16x16x32_bf16 v[120:123], v[112:115], a[96:99], v[120:123]\n"
"ds_read_b128 v[216:219], v15 offset:27392\n"
"v_mfma_f32_16x16x32_bf16 v[120:123], v[108:111], a[224:227], v[120:123]\n"
"s_waitcnt vmcnt(13)\n"
"v_mfma_f32_16x16x32_bf16 v[188:191], v[128:131], v[208:211], 0\n"
"buffer_load_dwordx4 v[180:183], v13, s[36:39], s61 offen sc1\n"
"v_max3_u32 v12, v129, v131, v53\n"
"v_mfma_f32_16x16x32_bf16 v[188:191], v[52:55], v[184:187], v[188:191]\n"
"v_max3_u32 v12, v55, v41, v12\n"
"v_max3_u32 v12, v43, v225, v12\n"
"v_mfma_f32_16x16x32_bf16 v[188:191], v[40:43], v[232:235], v[188:191]\n"
"v_mfma_f32_16x16x32_bf16 v[188:191], v[224:227], v[248:251], v[188:191]\n"
"buffer_load_dwordx4 v[152:155], v13, s[36:39], s62 offen sc1\n"
"s_waitcnt vmcnt(11)\n"
"v_mfma_f32_16x16x32_bf16 v[188:191], v[176:179], a[24:27], v[188:191]\n"
"v_max3_u32 v12, v227, v177, v12\n"
"v_max3_u32 v12, v179, v221, v12\n"
"v_mfma_f32_16x16x32_bf16 v[188:191], v[220:223], a[152:155], v[188:191]\n"
"v_max3_u32 v12, v223, v161, v12\n"
"v_max3_u32 v12, v163, v145, v12\n"
"v_mfma_f32_16x16x32_bf16 v[188:191], v[160:163], a[28:31], v[188:191]\n"
"s_waitcnt lgkmcnt(0)\n"
"v_add_f32 v84, v84, v236\n"
"v_add_f32 v85, v85, v237\n"
"v_mfma_f32_16x16x32_bf16 v[188:191], v[144:147], a[156:159], v[188:191]\n"
"buffer_load_dwordx4 v[112:115], v13, s[36:39], s63 offen sc1\n"
"s_waitcnt vmcnt(11)\n"
"v_mfma_f32_16x16x32_bf16 v[188:191], v[100:103], a[16:19], v[188:191]\n"
"v_max3_u32 v12, v147, v101, v12\n"
"v_max_u32 v12, v103, v12\n"
"v_mfma_f32_16x16x32_bf16 v[108:111], v[128:131], v[204:207], 0\n"
"v_add_f32 v86, v86, v238\n"
"v_add_f32 v87, v87, v239\n"
"v_mfma_f32_16x16x32_bf16 v[108:111], v[52:55], v[168:171], v[108:111]\n"
"v_add_f32 v120, v120, v216\n"
"v_add_f32 v121, v121, v217\n"
"v_cvt_pk_bf16_f32 v236, v84, v85\n"
"v_mfma_f32_16x16x32_bf16 v[108:111], v[40:43], v[156:159], v[108:111]\n"
"buffer_load_dwordx4 v[80:83], v13, s[36:39], s64 offen sc1\n"
"v_add_u32 v17, s73, v0\n"
"v_mfma_f32_16x16x32_bf16 v[108:111], v[224:227], v[68:71], v[108:111]\n"
"v_add_f32 v122, v122, v218\n"
"v_add_f32 v123, v123, v219\n"
"v_mfma_f32_16x16x32_bf16 v[108:111], v[176:179], a[56:59], v[108:111]\n"
"v_cvt_pk_bf16_f32 v237, v86, v87\n"
"v_min_u32 v237, 0xffbfffff, v237\n"
"v_mfma_f32_16x16x32_bf16 v[108:111], v[220:223], a[184:187], v[108:111]\n"
"buffer_load_dwordx4 v[216:219], v17, s[36:39], s56 offen sc1\n"
"v_cmp_le_u32 vcc, 0xffc00000, v12\n"
"v_mfma_f32_16x16x32_bf16 v[108:111], v[160:163], a[60:63], v[108:111]\n"
"v_cvt_pk_bf16_f32 v238, v120, v121\n"
"v_cvt_pk_bf16_f32 v239, v122, v123\n"
"v_mfma_f32_16x16x32_bf16 v[108:111], v[144:147], a[188:191], v[108:111]\n"
"v_mfma_f32_16x16x32_bf16 v[108:111], v[100:103], a[48:51], v[108:111]\n"
"s_cbranch_vccnz .Lgs_slow11\n"
".Lgs_back11:\n"
"v_min_u32 v239, 0xffbfffff, v239\n"
"v_mfma_f32_16x16x32_bf16 v[120:123], v[200:203], v[128:131], v[140:143]\n"
"ds_write_addtid_b32 v188 offset:4608\n"
"buffer_load_dwordx2 v[86:87], v22, s[36:39], 0 offen offset:0 sc1\n"
"v_mfma_f32_16x16x32_bf16 v[120:123], v[132:135], v[52:55], v[120:123]\n"
"ds_write_addtid_b32 v189 offset:8480\n"
"buffer_store_dwordx4 v[236:239], v0, s[36:39], s85 offen offset:0 sc1\n"
"v_mfma_f32_16x16x32_bf16 v[120:123], v[148:151], v[40:43], v[120:123]\n"
"ds_write_addtid_b32 v190 offset:12416\n"
"v_mfma_f32_16x16x32_bf16 v[120:123], v[228:231], v[224:227], v[120:123]\n"
"ds_write_addtid_b32 v191 offset:16288\n"
"v_mfma_f32_16x16x32_bf16 v[120:123], a[88:91], v[176:179], v[120:123]\n"
"v_mfma_f32_16x16x32_bf16 v[120:123], a[216:219], v[220:223], v[120:123]\n"
"ds_read_b128 v[236:239], v16 offset:24832\n"
"v_mfma_f32_16x16x32_bf16 v[120:123], a[92:95], v[160:163], v[120:123]\n"
"ds_read_b128 v[188:191], v16 offset:24576\n"
"v_mfma_f32_16x16x32_bf16 v[120:123], a[220:223], v[144:147], v[120:123]\n"
"ds_write_addtid_b32 v108 offset:4864\n"
"v_mfma_f32_16x16x32_bf16 v[120:123], a[80:83], v[100:103], v[120:123]\n"
"ds_write_addtid_b32 v109 offset:8736\n"
"v_mfma_f32_16x16x32_bf16 v[56:59], v[192:195], v[128:131], v[104:107]\n"
"ds_write_addtid_b32 v110 offset:12672\n"
"v_mfma_f32_16x16x32_bf16 v[56:59], v[212:215], v[52:55], v[56:59]\n"
"ds_write_addtid_b32 v111 offset:16544\n"
"v_mfma_f32_16x16x32_bf16 v[56:59], v[96:99], v[40:43], v[56:59]\n"
"v_mfma_f32_16x16x32_bf16 v[56:59], v[240:243], v[224:227], v[56:59]\n"
"v_mfma_f32_16x16x32_bf16 v[56:59], a[120:123], v[176:179], v[56:59]\n"
"buffer_store_dwordx4 v[172:175], v0, s[36:39], s87 offen offset:1024 sc1\n"
"v_mfma_f32_16x16x32_bf16 v[56:59], a[248:251], v[220:223], v[56:59]\n"
"v_mfma_f32_16x16x32_bf16 v[56:59], a[124:127], v[160:163], v[56:59]\n"
"v_mfma_f32_16x16x32_bf16 v[56:59], a[252:255], v[144:147], v[56:59]\n"
"ds_write_addtid_b32 v120 offset:5632\n"
"v_mfma_f32_16x16x32_bf16 v[56:59], a[112:115], v[100:103], v[56:59]\n"
"ds_write_addtid_b32 v121 offset:9504\n"
"s_waitcnt lgkmcnt(6)\n"
"v_mfma_f32_16x16x32_bf16 v[188:191], a[144:147], v[244:247], v[188:191]\n"
"ds_write_addtid_b32 v122 offset:13440\n"
"v_mfma_f32_16x16x32_bf16 v[188:191], a[20:23], v[116:119], v[188:191]\n"
"ds_write_addtid_b32 v123 offset:17312\n"
"v_mfma_f32_16x16x32_bf16 v[188:191], a[148:151], v[92:95], v[188:191]\n"
"v_mfma_f32_16x16x32_bf16 v[188:191], a[8:11], v[196:199], v[188:191]\n"
"v_mfma_f32_16x16x32_bf16 v[188:191], a[136:139], v[124:127], v[188:191]\n"
"v_mfma_f32_16x16x32_bf16 v[188:191], a[12:15], v[88:91], v[188:191]\n"
"ds_read_b128 v[172:175], v16 offset:25856\n"
"v_mfma_f32_16x16x32_bf16 v[188:191], a[140:143], v[72:75], v[188:191]\n"
"ds_read_b128 v[120:123], v16 offset:25600\n"
"v_mfma_f32_16x16x32_bf16 v[188:191], a[0:3], v[64:67], v[188:191]\n"
"ds_write_addtid_b32 v56 offset:5888\n"
"v_mfma_f32_16x16x32_bf16 v[188:191], a[128:131], v[60:63], v[188:191]\n"
"ds_write_addtid_b32 v57 offset:9760\n"
"buffer_load_dwordx4 v[108:111], v17, s[36:39], s57 offen sc1\n"
"v_mfma_f32_16x16x32_bf16 v[236:239], a[176:179], v[244:247], v[236:239]\n"
"ds_write_addtid_b32 v58 offset:13696\n"
"v_mfma_f32_16x16x32_bf16 v[236:239], a[52:55], v[116:119], v[236:239]\n"
"ds_write_addtid_b32 v59 offset:17568\n"
"v_mfma_f32_16x16x32_bf16 v[236:239], a[180:183], v[92:95], v[236:239]\n"
"buffer_load_dwordx4 v[32:35], v17, s[36:39], s58 offen sc1\n"
"v_mfma_f32_16x16x32_bf16 v[236:239], a[40:43], v[196:199], v[236:239]\n"
"v_mfma_f32_16x16x32_bf16 v[236:239], a[168:171], v[124:127], v[236:239]\n"
"v_mfma_f32_16x16x32_bf16 v[236:239], a[44:47], v[88:91], v[236:239]\n"
"v_mfma_f32_16x16x32_bf16 v[236:239], a[172:175], v[72:75], v[236:239]\n"
"v_mfma_f32_16x16x32_bf16 v[236:239], a[32:35], v[64:67], v[236:239]\n"
"ds_write_addtid_b32 v188 offset:6144\n"
"v_mfma_f32_16x16x32_bf16 v[236:239], a[160:163], v[60:63], v[236:239]\n"
"ds_write_addtid_b32 v189 offset:10016\n"
"s_waitcnt lgkmcnt(6)\n"
"v_mfma_f32_16x16x32_bf16 v[120:123], v[244:247], a[208:211], v[120:123]\n"
"ds_write_addtid_b32 v190 offset:13952\n"
"v_mfma_f32_16x16x32_bf16 v[120:123], v[116:119], a[84:87], v[120:123]\n"
"ds_write_addtid_b32 v191 offset:17824\n"
"v_mfma_f32_16x16x32_bf16 v[120:123], v[92:95], a[212:215], v[120:123]\n"
"v_mfma_f32_16x16x32_bf16 v[120:123], v[196:199], a[72:75], v[120:123]\n"
"v_mfma_f32_16x16x32_bf16 v[120:123], v[124:127], a[200:203], v[120:123]\n"
"v_mfma_f32_16x16x32_bf16 v[120:123], v[88:91], a[76:79], v[120:123]\n"
"v_mfma_f32_16x16x32_bf16 v[120:123], v[72:75], a[204:207], v[120:123]\n"
"v_mfma_f32_16x16x32_bf16 v[120:123], v[64:67], a[64:67], v[120:123]\n"
"ds_write_addtid_b32 v236 offset:6400\n"
"v_mfma_f32_16x16x32_bf16 v[120:123], v[60:63], a[192:195], v[120:123]\n"
"ds_write_addtid_b32 v237 offset:10272\n"
"v_mfma_f32_16x16x32_bf16 v[172:175], v[244:247], a[240:243], v[172:175]\n"
"ds_write_addtid_b32 v238 offset:14208\n"
"v_mfma_f32_16x16x32_bf16 v[172:175], v[116:119], a[116:119], v[172:175]\n"
".Lgs_pad4:\n"
"s_cbranch_execz .Lgs_al\n"
"ds_write_addtid_b32 v239 offset:18080\n"
"v_mfma_f32_16x16x32_bf16 v[172:175], v[92:95], a[244:247], v[172:175]\n"
"buffer_load_dwordx4 v[244:247], v17, s[36:39], s59 offen sc1\n"
"v_mfma_f32_16x16x32_bf16 v[172:175], v[196:199], a[104:107], v[172:175]\n"
"v_mfma_f32_16x16x32_bf16 v[172:175], v[124:127], a[232:235], v[172:175]\n"
"v_mfma_f32_16x16x32_bf16 v[172:175], v[88:91], a[108:111], v[172:175]\n"
"s_waitcnt lgkmcnt(0)\n"
"s_barrier\n"
"buffer_load_dwordx4 v[236:239], v17, s[36:39], s60 offen sc1\n"
"v_mfma_f32_16x16x32_bf16 v[172:175], v[72:75], a[236:239], v[172:175]\n"
"ds_read_b128 v[196:199], v15 offset:26624\n"
"v_mfma_f32_16x16x32_bf16 v[172:175], v[64:67], a[96:99], v[172:175]\n"
"ds_read_b128 v[188:191], v15 offset:26880\n"
"v_mfma_f32_16x16x32_bf16 v[172:175], v[60:63], a[224:227], v[172:175]\n"
"s_waitcnt vmcnt(13)\n"
"v_mfma_f32_16x16x32_bf16 v[124:127], v[164:167], v[208:211], 0\n"
"buffer_load_dwordx4 v[116:119], v17, s[36:39], s61 offen sc1\n"
"v_max3_u32 v19, v165, v167, v77\n"
"v_mfma_f32_16x16x32_bf16 v[124:127], v[76:79], v[184:187], v[124:127]\n"
"v_max3_u32 v19, v79, v37, v19\n"
"v_max3_u32 v19, v39, v137, v19\n"
"v_mfma_f32_16x16x32_bf16 v[124:127], v[36:39], v[232:235], v[124:127]\n"
"v_mfma_f32_16x16x32_bf16 v[124:127], v[136:139], v[248:251], v[124:127]\n"
"buffer_load_dwordx4 v[92:95], v17, s[36:39], s62 offen sc1\n"
"s_waitcnt vmcnt(11)\n"
"v_mfma_f32_16x16x32_bf16 v[124:127], v[252:255], a[24:27], v[124:127]\n"
"v_max3_u32 v19, v139, v253, v19\n"
"v_max3_u32 v19, v255, v181, v19\n"
"v_mfma_f32_16x16x32_bf16 v[124:127], v[180:183], a[152:155], v[124:127]\n"
"v_max3_u32 v19, v183, v153, v19\n"
"v_max3_u32 v19, v155, v113, v19\n"
"v_mfma_f32_16x16x32_bf16 v[124:127], v[152:155], a[28:31], v[124:127]\n"
"s_waitcnt lgkmcnt(0)\n"
"v_add_f32 v120, v120, v196\n"
"v_add_f32 v121, v121, v197\n"
"v_mfma_f32_16x16x32_bf16 v[124:127], v[112:115], a[156:159], v[124:127]\n"
"buffer_load_dwordx4 v[88:91], v17, s[36:39], s63 offen sc1\n"
"s_waitcnt vmcnt(11)\n"
"v_mfma_f32_16x16x32_bf16 v[124:127], v[80:83], a[16:19], v[124:127]\n"
"v_max3_u32 v19, v115, v81, v19\n"
"v_max_u32 v19, v83, v19\n"
"v_or_b32 v12, s95, v0\n"
"v_mfma_f32_16x16x32_bf16 v[72:75], v[164:167], v[204:207], 0\n"
"v_add_f32 v122, v122, v198\n"
"v_add_f32 v123, v123, v199\n"
"v_or_b32 v14, s95, v18\n"
"v_mfma_f32_16x16x32_bf16 v[72:75], v[76:79], v[168:171], v[72:75]\n"
"buffer_load_dwordx4 v[196:199], v17, s[36:39], s64 offen sc1\n"
"v_add_u32 v20, s74, v0\n"
"v_cvt_pk_bf16_f32 v64, v120, v121\n"
"v_mfma_f32_16x16x32_bf16 v[72:75], v[36:39], v[156:159], v[72:75]\n"
"v_add_f32 v172, v172, v188\n"
"v_add_f32 v173, v173, v189\n"
"v_mfma_f32_16x16x32_bf16 v[72:75], v[136:139], v[68:71], v[72:75]\n"
"v_add_f32 v174, v174, v190\n"
"v_add_f32 v175, v175, v191\n"
"v_mfma_f32_16x16x32_bf16 v[72:75], v[252:255], a[56:59], v[72:75]\n"
"v_cvt_pk_bf16_f32 v65, v122, v123\n"
"v_min_u32 v65, 0xffbfffff, v65\n"
"s_lshr_b32 s21, s68, 5\n"
"v_mfma_f32_16x16x32_bf16 v[72:75], v[180:183], a[184:187], v[72:75]\n"
"buffer_load_dwordx4 v[188:191], v20, s[36:39], s56 offen sc1\n"
"v_cmp_le_u32 vcc, 0xffc00000, v19\n"
"v_mfma_f32_16x16x32_bf16 v[72:75], v[152:155], a[60:63], v[72:75]\n"
"v_cvt_pk_bf16_f32 v66, v172, v173\n"
"v_cvt_pk_bf16_f32 v67, v174, v175\n"
"v_mfma_f32_16x16x32_bf16 v[72:75], v[112:115], a[188:191], v[72:75]\n"
"v_min_u32 v67, 0xffbfffff, v67\n"
"s_add_u32 s26, s21, 4718592\n"
"v_mfma_f32_16x16x32_bf16 v[72:75], v[80:83], a[48:51], v[72:75]\n"
"buffer_load_dwordx4 v[172:175], v20, s[36:39], s57 offen sc1\n"
"s_cbranch_vccnz .Lgs_slow12\n"
".Lgs_back12:\n"
"v_mfma_f32_16x16x32_bf16 v[120:123], v[200:203], v[164:167], v[140:143]\n"
"ds_write_addtid_b32 v124 offset:4096\n"
"buffer_load_dwordx4 v[60:63], v14, s[36:39], s26 offen offset:0 sc1\n"
"v_mfma_f32_16x16x32_bf16 v[120:123], v[132:135], v[76:79], v[120:123]\n"
"ds_write_addtid_b32 v125 offset:7968\n"
"buffer_store_dwordx4 v[64:67], v0, s[36:39], s86 offen offset:0 sc1\n"
"s_waitcnt vmcnt(14)\n"
"v_cmp_ne_u64 vcc, s[78:79], v[86:87]\n"
"v_mfma_f32_16x16x32_bf16 v[120:123], v[148:151], v[36:39], v[120:123]\n"
"ds_write_addtid_b32 v126 offset:11904\n"
"v_mfma_f32_16x16x32_bf16 v[120:123], v[228:231], v[136:139], v[120:123]\n"
"ds_write_addtid_b32 v127 offset:15776\n"
"s_cbranch_vccnz .Lgs_slow24\n"
".Lgs_back24:\n"
"v_mfma_f32_16x16x32_bf16 v[120:123], a[88:91], v[252:255], v[120:123]\n"
"v_mfma_f32_16x16x32_bf16 v[120:123], a[216:219], v[180:183], v[120:123]\n"
"ds_read_b128 v[124:127], v16 offset:25344\n"
"v_mfma_f32_16x16x32_bf16 v[120:123], a[92:95], v[152:155], v[120:123]\n"
"ds_read_b128 v[84:87], v16 offset:25088\n"
"v_mfma_f32_16x16x32_bf16 v[120:123], a[220:223], v[112:115], v[120:123]\n"
"ds_write_addtid_b32 v72 offset:4352\n"
"v_mfma_f32_16x16x32_bf16 v[120:123], a[80:83], v[80:83], v[120:123]\n"
"ds_write_addtid_b32 v73 offset:8224\n"
"v_mfma_f32_16x16x32_bf16 v[64:67], v[192:195], v[164:167], v[104:107]\n"
"ds_write_addtid_b32 v74 offset:12160\n"
"v_mfma_f32_16x16x32_bf16 v[64:67], v[212:215], v[76:79], v[64:67]\n"
"ds_write_addtid_b32 v75 offset:16032\n"
"buffer_load_dwordx4 v[56:59], v12, s[36:39], s88 offen offset:0 sc1\n"
"v_mfma_f32_16x16x32_bf16 v[64:67], v[96:99], v[36:39], v[64:67]\n"
"v_mfma_f32_16x16x32_bf16 v[64:67], v[240:243], v[136:139], v[64:67]\n"
"v_mfma_f32_16x16x32_bf16 v[64:67], a[120:123], v[252:255], v[64:67]\n"
"v_mfma_f32_16x16x32_bf16 v[64:67], a[248:251], v[180:183], v[64:67]\n"
"buffer_load_dwordx4 v[72:75], v12, s[36:39], s92 offen offset:0 sc1\n"
"v_mfma_f32_16x16x32_bf16 v[64:67], a[124:127], v[152:155], v[64:67]\n"
"v_mfma_f32_16x16x32_bf16 v[64:67], a[252:255], v[112:115], v[64:67]\n"
"ds_write_addtid_b32 v120 offset:5120\n"
"v_mfma_f32_16x16x32_bf16 v[64:67], a[112:115], v[80:83], v[64:67]\n"
"ds_write_addtid_b32 v121 offset:8992\n"
"s_waitcnt lgkmcnt(6)\n"
"v_mfma_f32_16x16x32_bf16 v[84:87], a[144:147], v[128:131], v[84:87]\n"
"ds_write_addtid_b32 v122 offset:12928\n"
"buffer_load_dwordx4 v[28:31], v12, s[36:39], s93 offen offset:0 sc1\n"
"v_mfma_f32_16x16x32_bf16 v[84:87], a[20:23], v[52:55], v[84:87]\n"
"ds_write_addtid_b32 v123 offset:16800\n"
"v_mfma_f32_16x16x32_bf16 v[84:87], a[148:151], v[40:43], v[84:87]\n"
"v_mfma_f32_16x16x32_bf16 v[84:87], a[8:11], v[224:227], v[84:87]\n"
"v_mfma_f32_16x16x32_bf16 v[84:87], a[136:139], v[176:179], v[84:87]\n"
"v_mfma_f32_16x16x32_bf16 v[84:87], a[12:15], v[220:223], v[84:87]\n"
"ds_read_b128 v[120:123], v16 offset:26368\n"
"v_mfma_f32_16x16x32_bf16 v[84:87], a[140:143], v[160:163], v[84:87]\n"
"ds_read_b128 v[24:27], v16 offset:26112\n"
"v_mfma_f32_16x16x32_bf16 v[84:87], a[0:3], v[144:147], v[84:87]\n"
"ds_write_addtid_b32 v64 offset:5376\n"
"v_mfma_f32_16x16x32_bf16 v[84:87], a[128:131], v[100:103], v[84:87]\n"
"ds_write_addtid_b32 v65 offset:9248\n"
"v_mfma_f32_16x16x32_bf16 v[124:127], a[176:179], v[128:131], v[124:127]\n"
"ds_write_addtid_b32 v66 offset:13184\n"
"v_mfma_f32_16x16x32_bf16 v[124:127], a[52:55], v[52:55], v[124:127]\n"
"ds_write_addtid_b32 v67 offset:17056\n"
"v_mfma_f32_16x16x32_bf16 v[124:127], a[180:183], v[40:43], v[124:127]\n"
"buffer_load_dwordx4 v[8:11], v20, s[36:39], s58 offen sc1\n"
"v_mfma_f32_16x16x32_bf16 v[124:127], a[40:43], v[224:227], v[124:127]\n"
"v_mfma_f32_16x16x32_bf16 v[124:127], a[168:171], v[176:179], v[124:127]\n"
"v_mfma_f32_16x16x32_bf16 v[124:127], a[44:47], v[220:223], v[124:127]\n"
"v_mfma_f32_16x16x32_bf16 v[124:127], a[172:175], v[160:163], v[124:127]\n"
"v_mfma_f32_16x16x32_bf16 v[124:127], a[32:35], v[144:147], v[124:127]\n"
"ds_write_addtid_b32 v84 offset:6656\n"
"v_mfma_f32_16x16x32_bf16 v[124:127], a[160:163], v[100:103], v[124:127]\n"
"ds_write_addtid_b32 v85 offset:10528\n"
"s_waitcnt lgkmcnt(6)\n"
"v_mfma_f32_16x16x32_bf16 v[24:27], v[128:131], a[208:211], v[24:27]\n"
"ds_write_addtid_b32 v86 offset:14464\n"
"v_mfma_f32_16x16x32_bf16 v[24:27], v[52:55], a[84:87], v[24:27]\n"
"ds_write_addtid_b32 v87 offset:18336\n"
"v_mfma_f32_16x16x32_bf16 v[24:27], v[40:43], a[212:215], v[24:27]\n"
"v_mfma_f32_16x16x32_bf16 v[24:27], v[224:227], a[72:75], v[24:27]\n"
"v_mfma_f32_16x16x32_bf16 v[24:27], v[176:179], a[200:203], v[24:27]\n"
"v_mov_b32 v86, s78\n"
"v_mov_b32 v87, s79\n"
"v_mfma_f32_16x16x32_bf16 v[24:27], v[220:223], a[76:79], v[24:27]\n"
"v_mfma_f32_16x16x32_bf16 v[24:27], v[160:163], a[204:207], v[24:27]\n"
"v_mfma_f32_16x16x32_bf16 v[24:27], v[144:147], a[64:67], v[24:27]\n"
"ds_write_addtid_b32 v124 offset:6912\n"
"v_mfma_f32_16x16x32_bf16 v[24:27], v[100:103], a[192:195], v[24:27]\n"
"ds_write_addtid_b32 v125 offset:10784\n"
"v_mfma_f32_16x16x32_bf16 v[120:123], v[128:131], a[240:243], v[120:123]\n"
"ds_write_addtid_b32 v126 offset:14720\n"
"v_mfma_f32_16x16x32_bf16 v[120:123], v[52:55], a[116:119], v[120:123]\n"
"ds_write_addtid_b32 v127 offset:18592\n"
"s_waitcnt vmcnt(16)\n"
"buffer_store_dwordx2 v[86:87], v21, s[36:39], 0 offen offset:32 sc1\n"
"v_mfma_f32_16x16x32_bf16 v[120:123], v[40:43], a[244:247], v[120:123]\n"
"v_mfma_f32_16x16x32_bf16 v[120:123], v[224:227], a[104:107], v[120:123]\n"
"buffer_load_dwordx4 v[128:131], v20, s[36:39], s59 offen sc1\n"
"v_mfma_f32_16x16x32_bf16 v[120:123], v[176:179], a[232:235], v[120:123]\n"
"v_mfma_f32_16x16x32_bf16 v[120:123], v[220:223], a[108:111], v[120:123]\n"
"s_waitcnt lgkmcnt(0)\n"
"s_barrier\n"
"v_mfma_f32_16x16x32_bf16 v[120:123], v[160:163], a[236:239], v[120:123]\n"
"ds_read_b128 v[224:227], v15 offset:27136\n"
"v_mfma_f32_16x16x32_bf16 v[120:123], v[144:147], a[96:99], v[120:123]\n"
"ds_read_b128 v[220:223], v15 offset:27392\n"
"buffer_load_dwordx4 v[176:179], v20, s[36:39], s60 offen sc1\n"
"v_mfma_f32_16x16x32_bf16 v[120:123], v[100:103], a[224:227], v[120:123]\n"
"s_waitcnt vmcnt(16)\n"
"v_mfma_f32_16x16x32_bf16 v[160:163], v[216:219], v[208:211], 0\n"
"v_max3_u32 v13, v217, v219, v109\n"
"v_max3_u32 v13, v111, v33, v13\n"
"v_mfma_f32_16x16x32_bf16 v[160:163], v[108:111], v[184:187], v[160:163]\n"
"buffer_load_dwordx4 v[144:147], v20, s[36:39], s61 offen sc1\n"
"v_max3_u32 v13, v35, v245, v13\n"
"v_mfma_f32_16x16x32_bf16 v[160:163], v[32:35], v[232:235], v[160:163]\n"
"v_mfma_f32_16x16x32_bf16 v[160:163], v[244:247], v[248:251], v[160:163]\n"
"s_waitcnt vmcnt(13)\n"
"v_mfma_f32_16x16x32_bf16 v[160:163], v[236:239], a[24:27], v[160:163]\n"
"v_max3_u32 v13, v247, v237, v13\n"
"v_max3_u32 v13, v239, v117, v13\n"
"v_mfma_f32_16x16x32_bf16 v[160:163], v[116:119], a[152:155], v[160:163]\n"
"buffer_load_dwordx4 v[124:127], v20, s[36:39], s62 offen sc1\n"
"v_max3_u32 v13, v119, v93, v13\n"
"v_mfma_f32_16x16x32_bf16 v[160:163], v[92:95], a[28:31], v[160:163]\n"
"s_waitcnt lgkmcnt(0)\n"
"v_add_f32 v24, v24, v224\n"
"v_add_f32 v25, v25, v225\n"
"v_mfma_f32_16x16x32_bf16 v[160:163], v[88:91], a[156:159], v[160:163]\n"
"v_max3_u32 v13, v95, v89, v13\n"
"s_waitcnt vmcnt(13)\n"
"v_mfma_f32_16x16x32_bf16 v[160:163], v[196:199], a[16:19], v[160:163]\n"
"v_max3_u32 v13, v91, v197, v13\n"
"v_max_u32 v13, v199, v13\n"
"v_mfma_f32_16x16x32_bf16 v[100:103], v[216:219], v[204:207], 0\n"
"buffer_load_dwordx4 v[84:87], v20, s[36:39], s63 offen sc1\n"
"v_cmp_le_u32 vcc, 0xffc00000, v13\n"
"v_mfma_f32_16x16x32_bf16 v[100:103], v[108:111], v[168:171], v[100:103]\n"
"v_add_f32 v26, v26, v226\n"
"v_add_f32 v27, v27, v227\n"
"v_mfma_f32_16x16x32_bf16 v[100:103], v[32:35], v[156:159], v[100:103]\n"
"v_add_f32 v120, v120, v220\n"
"v_add_f32 v121, v121, v221\n"
"v_mfma_f32_16x16x32_bf16 v[100:103], v[244:247], v[68:71], v[100:103]\n"
"buffer_load_dwordx4 v[224:227], v20, s[36:39], s64 offen sc1\n"
"v_add_u32 v19, s75, v0\n"
"v_mfma_f32_16x16x32_bf16 v[100:103], v[236:239], a[56:59], v[100:103]\n"
"v_add_f32 v122, v122, v222\n"
"v_add_f32 v123, v123, v223\n"
"v_mfma_f32_16x16x32_bf16 v[100:103], v[116:119], a[184:187], v[100:103]\n"
"v_cvt_pk_bf16_f32 v64, v24, v25\n"
"v_cvt_pk_bf16_f32 v65, v26, v27\n"
"v_mfma_f32_16x16x32_bf16 v[100:103], v[92:95], a[60:63], v[100:103]\n"
"v_min_u32 v65, 0xffbfffff, v65\n"
"v_cvt_pk_bf16_f32 v66, v120, v121\n"
"v_mfma_f32_16x16x32_bf16 v[100:103], v[88:91], a[188:191], v[100:103]\n"
"buffer_load_dwordx4 v[220:223], v19, s[36:39], s56 offen sc1\n"
"v_cvt_pk_bf16_f32 v67, v122, v123\n"
"v_mfma_f32_16x16x32_bf16 v[100:103], v[196:199], a[48:51], v[100:103]\n"
"s_cbranch_vccnz .Lgs_slow13\n"
".Lgs_back13:\n"
"v_min_u32 v67, 0xffbfffff, v67\n"
"v_mfma_f32_16x16x32_bf16 v[120:123], v[200:203], v[216:219], v[140:143]\n"
"ds_write_addtid_b32 v160 offset:4608\n"
"s_waitcnt vmcnt(13)\n"
"v_permlane16_swap_b32 v60, v62\n"
"v_mfma_f32_16x16x32_bf16 v[120:123], v[132:135], v[108:111], v[120:123]\n"
"ds_write_addtid_b32 v161 offset:8480\n"
"buffer_store_dwordx4 v[64:67], v0, s[36:39], s87 offen offset:0 sc1\n"
"v_mfma_f32_16x16x32_bf16 v[120:123], v[148:151], v[32:35], v[120:123]\n"
"ds_write_addtid_b32 v162 offset:12416\n"
"v_permlane16_swap_b32 v61, v63\n"
"v_mfma_f32_16x16x32_bf16 v[120:123], v[228:231], v[244:247], v[120:123]\n"
"ds_write_addtid_b32 v163 offset:16288\n"
"v_lshlrev_b32 v64, 16, v60\n"
"v_mfma_f32_16x16x32_bf16 v[120:123], a[88:91], v[236:239], v[120:123]\n"
"v_and_b32 v65, 0xffff0000, v60\n"
"v_lshlrev_b32 v66, 16, v61\n"
"v_mfma_f32_16x16x32_bf16 v[120:123], a[216:219], v[116:119], v[120:123]\n"
"ds_read_b128 v[160:163], v16 offset:24832\n"
"v_and_b32 v67, 0xffff0000, v61\n"
"v_mfma_f32_16x16x32_bf16 v[120:123], a[92:95], v[92:95], v[120:123]\n"
"ds_read_b128 v[52:55], v16 offset:24576\n"
"v_add_f32 v64, v64, v44\n"
"v_mfma_f32_16x16x32_bf16 v[120:123], a[220:223], v[88:91], v[120:123]\n"
"ds_write_addtid_b32 v100 offset:4864\n"
"v_add_f32 v65, v65, v45\n"
"v_mfma_f32_16x16x32_bf16 v[120:123], a[80:83], v[196:199], v[120:123]\n"
"ds_write_addtid_b32 v101 offset:8736\n"
"v_add_f32 v66, v66, v46\n"
"v_mfma_f32_16x16x32_bf16 v[40:43], v[192:195], v[216:219], v[104:107]\n"
"ds_write_addtid_b32 v102 offset:12672\n"
"v_add_f32 v67, v67, v47\n"
"v_mfma_f32_16x16x32_bf16 v[40:43], v[212:215], v[108:111], v[40:43]\n"
"ds_write_addtid_b32 v103 offset:16544\n"
"v_lshlrev_b32 v44, 16, v62\n"
"v_mfma_f32_16x16x32_bf16 v[40:43], v[96:99], v[32:35], v[40:43]\n"
"v_and_b32 v45, 0xffff0000, v62\n"
"v_lshlrev_b32 v46, 16, v63\n"
"v_mfma_f32_16x16x32_bf16 v[40:43], v[240:243], v[244:247], v[40:43]\n"
"v_and_b32 v47, 0xffff0000, v63\n"
"v_add_f32 v44, v44, v48\n"
"v_mfma_f32_16x16x32_bf16 v[40:43], a[120:123], v[236:239], v[40:43]\n"
"buffer_load_dwordx4 v[100:103], v19, s[36:39], s57 offen sc1\n"
"v_add_f32 v45, v45, v49\n"
"v_mfma_f32_16x16x32_bf16 v[40:43], a[248:251], v[116:119], v[40:43]\n"
"v_add_f32 v46, v46, v50\n"
"v_add_f32 v47, v47, v51\n"
"v_mfma_f32_16x16x32_bf16 v[40:43], a[124:127], v[92:95], v[40:43]\n"
"v_mfma_f32_16x16x32_bf16 v[40:43], a[252:255], v[88:91], v[40:43]\n"
"ds_write_addtid_b32 v120 offset:5632\n"
"buffer_load_dwordx4 v[60:63], v19, s[36:39], s58 offen sc1\n"
"s_waitcnt vmcnt(13)\n"
"v_max3_u32 v13, v57, v59, v73\n"
"v_mfma_f32_16x16x32_bf16 v[40:43], a[112:115], v[196:199], v[40:43]\n"
"ds_write_addtid_b32 v121 offset:9504\n"
"s_waitcnt vmcnt(12)\n"
"v_max3_u32 v13, v75, v29, v13\n"
"s_waitcnt lgkmcnt(6)\n"
"v_mfma_f32_16x16x32_bf16 v[52:55], a[144:147], v[164:167], v[52:55]\n"
"ds_write_addtid_b32 v122 offset:13440\n"
"v_max_u32 v13, v31, v13\n"
"v_mfma_f32_16x16x32_bf16 v[52:55], a[20:23], v[76:79], v[52:55]\n"
"ds_write_addtid_b32 v123 offset:17312\n"
"v_cmp_le_u32 vcc, 0xffc00000, v13\n"
"v_mfma_f32_16x16x32_bf16 v[52:55], a[148:151], v[36:39], v[52:55]\n"
"v_mfma_f32_16x16x32_bf16 v[52:55], a[8:11], v[136:139], v[52:55]\n"
"s_cbranch_vccnz .Lgs_slow26\n"
".Lgs_back26:\n"
"v_mfma_f32_16x16x32_bf16 v[52:55], a[136:139], v[252:255], v[52:55]\n"
"v_lshlrev_b32 v120, 16, v56\n"
"v_and_b32 v121, 0xffff0000, v56\n"
"v_lshlrev_b32 v122, 16, v57\n"
"v_mfma_f32_16x16x32_bf16 v[52:55], a[12:15], v[180:183], v[52:55]\n"
"ds_read_b128 v[48:51], v16 offset:25856\n"
"v_and_b32 v123, 0xffff0000, v57\n"
"v_mfma_f32_16x16x32_bf16 v[52:55], a[140:143], v[152:155], v[52:55]\n"
"ds_read_b128 v[24:27], v16 offset:25600\n"
"v_add_f32 v64, v64, v120\n"
"v_mfma_f32_16x16x32_bf16 v[52:55], a[0:3], v[112:115], v[52:55]\n"
"ds_write_addtid_b32 v40 offset:5888\n"
"v_add_f32 v65, v65, v121\n"
"v_mfma_f32_16x16x32_bf16 v[52:55], a[128:131], v[80:83], v[52:55]\n"
"ds_write_addtid_b32 v41 offset:9760\n"
"v_add_f32 v66, v66, v122\n"
"v_mfma_f32_16x16x32_bf16 v[160:163], a[176:179], v[164:167], v[160:163]\n"
"ds_write_addtid_b32 v42 offset:13696\n"
"v_add_f32 v67, v67, v123\n"
"v_mfma_f32_16x16x32_bf16 v[160:163], a[52:55], v[76:79], v[160:163]\n"
"ds_write_addtid_b32 v43 offset:17568\n"
"v_lshlrev_b32 v120, 16, v72\n"
"v_mfma_f32_16x16x32_bf16 v[160:163], a[180:183], v[36:39], v[160:163]\n"
"v_and_b32 v121, 0xffff0000, v72\n"
"v_lshlrev_b32 v122, 16, v73\n"
"v_mfma_f32_16x16x32_bf16 v[160:163], a[40:43], v[136:139], v[160:163]\n"
"v_and_b32 v123, 0xffff0000, v73\n"
"v_add_f32 v64, v64, v120\n"
"v_mfma_f32_16x16x32_bf16 v[160:163], a[168:171], v[252:255], v[160:163]\n"
"v_add_f32 v65, v65, v121\n"
"v_add_f32 v66, v66, v122\n"
"v_mfma_f32_16x16x32_bf16 v[160:163], a[44:47], v[180:183], v[160:163]\n"
"v_add_f32 v67, v67, v123\n"
"v_lshlrev_b32 v40, 16, v28\n"
"v_mfma_f32_16x16x32_bf16 v[160:163], a[172:175], v[152:155], v[160:163]\n"
"v_and_b32 v41, 0xffff0000, v28\n"
"v_lshlrev_b32 v42, 16, v29\n"
"v_mfma_f32_16x16x32_bf16 v[160:163], a[32:35], v[112:115], v[160:163]\n"
"ds_write_addtid_b32 v52 offset:6144\n"
"v_and_b32 v43, 0xffff0000, v29\n"
"v_mfma_f32_16x16x32_bf16 v[160:163], a[160:163], v[80:83], v[160:163]\n"
"ds_write_addtid_b32 v53 offset:10016\n"
"v_add_f32 v64, v64, v40\n"
"s_waitcnt lgkmcnt(6)\n"
"v_mfma_f32_16x16x32_bf16 v[24:27], v[164:167], a[208:211], v[24:27]\n"
"ds_write_addtid_b32 v54 offset:13952\n"
"v_add_f32 v65, v65, v41\n"
"v_mfma_f32_16x16x32_bf16 v[24:27], v[76:79], a[84:87], v[24:27]\n"
"ds_write_addtid_b32 v55 offset:17824\n"
"v_add_f32 v66, v66, v42\n"
"v_mfma_f32_16x16x32_bf16 v[24:27], v[36:39], a[212:215], v[24:27]\n"
"v_add_f32 v67, v67, v43\n"
"v_lshlrev_b32 v120, 16, v58\n"
"v_mfma_f32_16x16x32_bf16 v[24:27], v[136:139], a[72:75], v[24:27]\n"
"v_and_b32 v121, 0xffff0000, v58\n"
"v_lshlrev_b32 v122, 16, v59\n"
"v_mfma_f32_16x16x32_bf16 v[24:27], v[252:255], a[200:203], v[24:27]\n"
"v_and_b32 v123, 0xffff0000, v59\n"
"v_add_f32 v44, v44, v120\n"
"v_mfma_f32_16x16x32_bf16 v[24:27], v[180:183], a[76:79], v[24:27]\n"
"v_add_f32 v45, v45, v121\n"
"v_add_f32 v46, v46, v122\n"
"v_mfma_f32_16x16x32_bf16 v[24:27], v[152:155], a[204:207], v[24:27]\n"
"v_add_f32 v47, v47, v123\n"
"v_lshlrev_b32 v56, 16, v74\n"
"v_mfma_f32_16x16x32_bf16 v[24:27], v[112:115], a[64:67], v[24:27]\n"
"ds_write_addtid_b32 v160 offset:6400\n"
"v_and_b32 v57, 0xffff0000, v74\n"
"v_mfma_f32_16x16x32_bf16 v[24:27], v[80:83], a[192:195], v[24:27]\n"
"ds_write_addtid_b32 v161 offset:10272\n"
"v_lshlrev_b32 v58, 16, v75\n"
"v_mfma_f32_16x16x32_bf16 v[48:51], v[164:167], a[240:243], v[48:51]\n"
"ds_write_addtid_b32 v162 offset:14208\n"
"v_and_b32 v59, 0xffff0000, v75\n"
"v_mfma_f32_16x16x32_bf16 v[48:51], v[76:79], a[116:119], v[48:51]\n"
"ds_write_addtid_b32 v163 offset:18080\n"
"v_add_f32 v44, v44, v56\n"
"v_mfma_f32_16x16x32_bf16 v[48:51], v[36:39], a[244:247], v[48:51]\n"
"buffer_load_dwordx4 v[164:167], v19, s[36:39], s59 offen sc1\n"
"v_add_f32 v45, v45, v57\n"
"v_mfma_f32_16x16x32_bf16 v[48:51], v[136:139], a[104:107], v[48:51]\n"
"v_add_f32 v46, v46, v58\n"
"v_add_f32 v47, v47, v59\n"
"v_mfma_f32_16x16x32_bf16 v[48:51], v[252:255], a[232:235], v[48:51]\n"
"v_lshlrev_b32 v160, 16, v30\n"
"v_and_b32 v161, 0xffff0000, v30\n"
"v_mfma_f32_16x16x32_bf16 v[48:51], v[180:183], a[108:111], v[48:51]\n"
"s_waitcnt lgkmcnt(0)\n"
"s_barrier\n"
"buffer_load_dwordx4 v[252:255], v19, s[36:39], s60 offen sc1\n"
"v_mfma_f32_16x16x32_bf16 v[48:51], v[152:155], a[236:239], v[48:51]\n"
"ds_read_b128 v[180:183], v15 offset:26624\n"
"v_lshlrev_b32 v162, 16, v31\n"
"v_mfma_f32_16x16x32_bf16 v[48:51], v[112:115], a[96:99], v[48:51]\n"
"ds_read_b128 v[152:155], v15 offset:26880\n"
"v_and_b32 v163, 0xffff0000, v31\n"
"v_mfma_f32_16x16x32_bf16 v[48:51], v[80:83], a[224:227], v[48:51]\n"
"buffer_load_dwordx4 v[136:139], v19, s[36:39], s61 offen sc1\n"
"s_waitcnt vmcnt(12)\n"
"v_mfma_f32_16x16x32_bf16 v[120:123], v[188:191], v[208:211], 0\n"
"v_max3_u32 v17, v189, v191, v173\n"
"v_max3_u32 v17, v175, v9, v17\n"
"v_mfma_f32_16x16x32_bf16 v[120:123], v[172:175], v[184:187], v[120:123]\n"
"v_add_f32 v44, v44, v160\n"
"v_add_f32 v45, v45, v161\n"
"v_mfma_f32_16x16x32_bf16 v[120:123], v[8:11], v[232:235], v[120:123]\n"
"v_add_f32 v46, v46, v162\n"
"v_add_f32 v47, v47, v163\n"
"v_cvt_pk_bf16_f32 v112, v64, v65\n"
"v_mfma_f32_16x16x32_bf16 v[120:123], v[128:131], v[248:251], v[120:123]\n"
"buffer_load_dwordx4 v[160:163], v19, s[36:39], s62 offen sc1\n"
"v_cvt_pk_bf16_f32 v113, v66, v67\n"
"s_waitcnt vmcnt(9)\n"
"v_mfma_f32_16x16x32_bf16 v[120:123], v[176:179], a[24:27], v[120:123]\n"
"v_max3_u32 v17, v11, v129, v17\n"
"v_max3_u32 v17, v131, v177, v17\n"
"v_mfma_f32_16x16x32_bf16 v[120:123], v[144:147], a[152:155], v[120:123]\n"
"s_waitcnt lgkmcnt(0)\n"
"v_add_f32 v24, v24, v180\n"
"v_add_f32 v25, v25, v181\n"
"v_cvt_pk_bf16_f32 v114, v44, v45\n"
"v_mfma_f32_16x16x32_bf16 v[120:123], v[124:127], a[28:31], v[120:123]\n"
"buffer_load_dwordx4 v[80:83], v19, s[36:39], s63 offen sc1\n"
"v_max3_u32 v17, v179, v145, v17\n"
"v_cvt_pk_bf16_f32 v115, v46, v47\n"
"v_mfma_f32_16x16x32_bf16 v[120:123], v[84:87], a[156:159], v[120:123]\n"
"v_max3_u32 v17, v147, v125, v17\n"
"v_permlane16_swap_b32 v112, v114\n"
"s_waitcnt vmcnt(9)\n"
"v_mfma_f32_16x16x32_bf16 v[120:123], v[224:227], a[16:19], v[120:123]\n"
"v_max3_u32 v17, v127, v85, v17\n"
"v_max3_u32 v17, v87, v225, v17\n"
"v_permlane16_swap_b32 v113, v115\n"
"buffer_store_dwordx4 v[112:115], v14, s[44:47], s21 offen offset:0 sc1\n"
"v_mfma_f32_16x16x32_bf16 v[76:79], v[188:191], v[204:207], 0\n"
"v_max_u32 v17, v227, v17\n"
"v_cmp_le_u32 vcc, 0xffc00000, v17\n"
"v_mfma_f32_16x16x32_bf16 v[76:79], v[172:175], v[168:171], v[76:79]\n"
"v_add_f32 v26, v26, v182\n"
"v_add_f32 v27, v27, v183\n"
"v_mfma_f32_16x16x32_bf16 v[76:79], v[8:11], v[156:159], v[76:79]\n"
"v_add_f32 v48, v48, v152\n"
"v_add_f32 v49, v49, v153\n"
"v_mfma_f32_16x16x32_bf16 v[76:79], v[128:131], v[68:71], v[76:79]\n"
"v_add_f32 v50, v50, v154\n"
"v_add_f32 v51, v51, v155\n"
"v_mfma_f32_16x16x32_bf16 v[76:79], v[176:179], a[56:59], v[76:79]\n"
"v_cvt_pk_bf16_f32 v180, v24, v25\n"
"v_cvt_pk_bf16_f32 v181, v26, v27\n"
"v_mfma_f32_16x16x32_bf16 v[76:79], v[144:147], a[184:187], v[76:79]\n"
"v_min_u32 v181, 0xffbfffff, v181\n"
"v_cvt_pk_bf16_f32 v182, v48, v49\n"
"v_mfma_f32_16x16x32_bf16 v[76:79], v[124:127], a[60:63], v[76:79]\n"
"v_mfma_f32_16x16x32_bf16 v[76:79], v[84:87], a[188:191], v[76:79]\n"
"v_mfma_f32_16x16x32_bf16 v[76:79], v[224:227], a[48:51], v[76:79]\n"
"s_cbranch_vccnz .Lgs_slow14\n"
".Lgs_back14:\n"
"v_cvt_pk_bf16_f32 v183, v50, v51\n"
"v_mfma_f32_16x16x32_bf16 v[152:155], v[200:203], v[188:191], v[140:143]\n"
"ds_write_addtid_b32 v120 offset:4096\n"
"v_min_u32 v183, 0xffbfffff, v183\n"
"v_mfma_f32_16x16x32_bf16 v[152:155], v[132:135], v[172:175], v[152:155]\n"
"ds_write_addtid_b32 v121 offset:7968\n"
"buffer_store_dwordx4 v[180:183], v0, s[36:39], s85 offen offset:1024 sc1\n"
"v_mfma_f32_16x16x32_bf16 v[152:155], v[148:151], v[8:11], v[152:155]\n"
"ds_write_addtid_b32 v122 offset:11904\n"
"v_mfma_f32_16x16x32_bf16 v[152:155], v[228:231], v[128:131], v[152:155]\n"
"ds_write_addtid_b32 v123 offset:15776\n"
"v_mfma_f32_16x16x32_bf16 v[152:155], a[88:91], v[176:179], v[152:155]\n"
"v_mfma_f32_16x16x32_bf16 v[152:155], a[216:219], v[144:147], v[152:155]\n"
"ds_read_b128 v[180:183], v16 offset:25344\n"
"v_mfma_f32_16x16x32_bf16 v[152:155], a[92:95], v[124:127], v[152:155]\n"
"ds_read_b128 v[120:123], v16 offset:25088\n"
"v_mfma_f32_16x16x32_bf16 v[152:155], a[220:223], v[84:87], v[152:155]\n"
"ds_write_addtid_b32 v76 offset:4352\n"
"v_mfma_f32_16x16x32_bf16 v[152:155], a[80:83], v[224:227], v[152:155]\n"
"ds_write_addtid_b32 v77 offset:8224\n"
"v_mfma_f32_16x16x32_bf16 v[112:115], v[192:195], v[188:191], v[104:107]\n"
"ds_write_addtid_b32 v78 offset:12160\n"
"v_mfma_f32_16x16x32_bf16 v[112:115], v[212:215], v[172:175], v[112:115]\n"
"ds_write_addtid_b32 v79 offset:16032\n"
"v_mfma_f32_16x16x32_bf16 v[112:115], v[96:99], v[8:11], v[112:115]\n"
"v_mfma_f32_16x16x32_bf16 v[112:115], v[240:243], v[128:131], v[112:115]\n"
"v_mfma_f32_16x16x32_bf16 v[112:115], a[120:123], v[176:179], v[112:115]\n"
"buffer_load_dwordx4 v[76:79], v19, s[36:39], s64 offen sc1\n"
"v_add_u32 v17, s76, v0\n"
"v_mfma_f32_16x16x32_bf16 v[112:115], a[248:251], v[144:147], v[112:115]\n"
"v_mfma_f32_16x16x32_bf16 v[112:115], a[124:127], v[124:127], v[112:115]\n"
"v_mfma_f32_16x16x32_bf16 v[112:115], a[252:255], v[84:87], v[112:115]\n"
"ds_write_addtid_b32 v152 offset:5120\n"
"v_mfma_f32_16x16x32_bf16 v[112:115], a[112:115], v[224:227], v[112:115]\n"
"ds_write_addtid_b32 v153 offset:8992\n"
"buffer_load_dwordx4 v[72:75], v17, s[36:39], s56 offen sc1\n"
"s_waitcnt lgkmcnt(6)\n"
"v_mfma_f32_16x16x32_bf16 v[120:123], a[144:147], v[216:219], v[120:123]\n"
"ds_write_addtid_b32 v154 offset:12928\n"
"v_mfma_f32_16x16x32_bf16 v[120:123], a[20:23], v[108:111], v[120:123]\n"
"ds_write_addtid_b32 v155 offset:16800\n"
"v_mfma_f32_16x16x32_bf16 v[120:123], a[148:151], v[32:35], v[120:123]\n"
"v_mfma_f32_16x16x32_bf16 v[120:123], a[8:11], v[244:247], v[120:123]\n"
"buffer_load_dwordx4 v[152:155], v17, s[36:39], s57 offen sc1\n"
"v_mfma_f32_16x16x32_bf16 v[120:123], a[136:139], v[236:239], v[120:123]\n"
"v_mfma_f32_16x16x32_bf16 v[120:123], a[12:15], v[116:119], v[120:123]\n"
"ds_read_b128 v[64:67], v16 offset:26368\n"
"v_mfma_f32_16x16x32_bf16 v[120:123], a[140:143], v[92:95], v[120:123]\n"
"ds_read_b128 v[56:59], v16 offset:26112\n"
"v_mfma_f32_16x16x32_bf16 v[120:123], a[0:3], v[88:91], v[120:123]\n"
"ds_write_addtid_b32 v112 offset:5376\n"
"buffer_load_dwordx4 v[52:55], v17, s[36:39], s58 offen sc1\n"
"v_mfma_f32_16x16x32_bf16 v[120:123], a[128:131], v[196:199], v[120:123]\n"
"ds_write_addtid_b32 v113 offset:9248\n"
"v_mfma_f32_16x16x32_bf16 v[180:183], a[176:179], v[216:219], v[180:183]\n"
"ds_write_addtid_b32 v114 offset:13184\n"
"s_lshr_b32 s22, s75, 5\n"
"v_mfma_f32_16x16x32_bf16 v[180:183], a[52:55], v[108:111], v[180:183]\n"
"ds_write_addtid_b32 v115 offset:17056\n"
"s_add_u32 s27, s22, 4718592\n"
"v_mfma_f32_16x16x32_bf16 v[180:183], a[180:183], v[32:35], v[180:183]\n"
"v_mfma_f32_16x16x32_bf16 v[180:183], a[40:43], v[244:247], v[180:183]\n"
"v_mfma_f32_16x16x32_bf16 v[180:183], a[168:171], v[236:239], v[180:183]\n"
"v_mfma_f32_16x16x32_bf16 v[180:183], a[44:47], v[116:119], v[180:183]\n"
"s_waitcnt lgkmcnt(0)\n"
"s_barrier\n"
"buffer_load_dwordx4 v[112:115], v18, s[36:39], s27 offen offset:0 sc1\n"
"v_mfma_f32_16x16x32_bf16 v[180:183], a[172:175], v[92:95], v[180:183]\n"
"v_mfma_f32_16x16x32_bf16 v[180:183], a[32:35], v[88:91], v[180:183]\n"
"ds_write_addtid_b32 v120 offset:4608\n"
"v_mfma_f32_16x16x32_bf16 v[180:183], a[160:163], v[196:199], v[180:183]\n"
"ds_write_addtid_b32 v121 offset:8480\n"
"v_mfma_f32_16x16x32_bf16 v[56:59], v[216:219], a[208:211], v[56:59]\n"
"ds_write_addtid_b32 v122 offset:12416\n"
"buffer_load_dwordx4 v[48:51], v0, s[36:39], s88 offen offset:1024 sc1\n"
"v_mfma_f32_16x16x32_bf16 v[56:59], v[108:111], a[84:87], v[56:59]\n"
"ds_write_addtid_b32 v123 offset:16288\n"
"v_mfma_f32_16x16x32_bf16 v[56:59], v[32:35], a[212:215], v[56:59]\n"
"v_mfma_f32_16x16x32_bf16 v[56:59], v[244:247], a[72:75], v[56:59]\n"
"v_mfma_f32_16x16x32_bf16 v[56:59], v[236:239], a[200:203], v[56:59]\n"
"buffer_load_dwordx4 v[120:123], v0, s[36:39], s92 offen offset:1024 sc1\n"
"v_mfma_f32_16x16x32_bf16 v[56:59], v[116:119], a[76:79], v[56:59]\n"
"ds_read_b128 v[44:47], v16 offset:24832\n"
"v_mfma_f32_16x16x32_bf16 v[56:59], v[92:95], a[204:207], v[56:59]\n"
"ds_read_b128 v[40:43], v16 offset:24576\n"
"v_mfma_f32_16x16x32_bf16 v[56:59], v[88:91], a[64:67], v[56:59]\n"
"ds_write_addtid_b32 v180 offset:4864\n"
"v_mfma_f32_16x16x32_bf16 v[56:59], v[196:199], a[192:195], v[56:59]\n"
"ds_write_addtid_b32 v181 offset:8736\n"
"buffer_load_dwordx4 v[36:39], v0, s[36:39], s93 offen offset:1024 sc1\n"
"v_mfma_f32_16x16x32_bf16 v[64:67], v[216:219], a[240:243], v[64:67]\n"
"ds_write_addtid_b32 v182 offset:12672\n"
"v_mfma_f32_16x16x32_bf16 v[64:67], v[108:111], a[116:119], v[64:67]\n"
"ds_write_addtid_b32 v183 offset:16544\n"
"buffer_load_dwordx2 v[218:219], v22, s[36:39], 0 offen offset:32 sc1\n"
"v_mfma_f32_16x16x32_bf16 v[64:67], v[32:35], a[244:247], v[64:67]\n"
"v_mfma_f32_16x16x32_bf16 v[64:67], v[244:247], a[104:107], v[64:67]\n"
"v_mfma_f32_16x16x32_bf16 v[64:67], v[236:239], a[232:235], v[64:67]\n"
"v_mfma_f32_16x16x32_bf16 v[64:67], v[116:119], a[108:111], v[64:67]\n"
"s_waitcnt lgkmcnt(0)\n"
"s_barrier\n"
"buffer_load_dwordx4 v[244:247], v17, s[36:39], s59 offen sc1\n"
"v_mfma_f32_16x16x32_bf16 v[64:67], v[92:95], a[236:239], v[64:67]\n"
"ds_read_b128 v[236:239], v15 offset:25088\n"
"v_mfma_f32_16x16x32_bf16 v[64:67], v[88:91], a[96:99], v[64:67]\n"
"ds_read_b128 v[180:183], v15 offset:25344\n"
"v_mfma_f32_16x16x32_bf16 v[64:67], v[196:199], a[224:227], v[64:67]\n"
"v_mfma_f32_16x16x32_bf16 v[40:43], a[144:147], v[188:191], v[40:43]\n"
"buffer_load_dwordx4 v[116:119], v17, s[36:39], s60 offen sc1\n"
"v_mfma_f32_16x16x32_bf16 v[40:43], a[20:23], v[172:175], v[40:43]\n"
"v_mfma_f32_16x16x32_bf16 v[40:43], a[148:151], v[8:11], v[40:43]\n"
"v_mfma_f32_16x16x32_bf16 v[40:43], a[8:11], v[128:131], v[40:43]\n"
"v_mfma_f32_16x16x32_bf16 v[40:43], a[136:139], v[176:179], v[40:43]\n"
"buffer_load_dwordx4 v[196:199], v17, s[36:39], s61 offen sc1\n"
"v_mfma_f32_16x16x32_bf16 v[40:43], a[12:15], v[144:147], v[40:43]\n"
"s_waitcnt lgkmcnt(0)\n"
"v_add_f32 v56, v56, v236\n"
"v_add_f32 v57, v57, v237\n"
"v_mfma_f32_16x16x32_bf16 v[40:43], a[140:143], v[124:127], v[40:43]\n"
"v_add_f32 v58, v58, v238\n"
"v_add_f32 v59, v59, v239\n"
"v_mfma_f32_16x16x32_bf16 v[40:43], a[0:3], v[84:87], v[40:43]\n"
"ds_read_b128 v[108:111], v16 offset:25600\n"
"buffer_load_dwordx4 v[236:239], v17, s[36:39], s62 offen sc1\n"
"v_cvt_pk_bf16_f32 v92, v56, v57\n"
"v_mfma_f32_16x16x32_bf16 v[40:43], a[128:131], v[224:227], v[40:43]\n"
"ds_read_b128 v[88:91], v16 offset:25856\n"
"v_add_f32 v64, v64, v180\n"
".Lgs_pad5:\n"
"s_cbranch_execz .Lgs_al\n"
"v_mfma_f32_16x16x32_bf16 v[44:47], a[176:179], v[188:191], v[44:47]\n"
"v_add_f32 v65, v65, v181\n"
"v_add_f32 v66, v66, v182\n"
"v_mfma_f32_16x16x32_bf16 v[44:47], a[52:55], v[172:175], v[44:47]\n"
"v_cvt_pk_bf16_f32 v93, v58, v59\n"
"v_min_u32 v93, 0xffbfffff, v93\n"
"v_mfma_f32_16x16x32_bf16 v[44:47], a[180:183], v[8:11], v[44:47]\n"
"buffer_load_dwordx4 v[56:59], v17, s[36:39], s63 offen sc1\n"
"v_add_f32 v67, v67, v183\n"
"v_mfma_f32_16x16x32_bf16 v[44:47], a[40:43], v[128:131], v[44:47]\n"
"v_cvt_pk_bf16_f32 v94, v64, v65\n"
"v_cvt_pk_bf16_f32 v95, v66, v67\n"
"v_mfma_f32_16x16x32_bf16 v[44:47], a[168:171], v[176:179], v[44:47]\n"
"v_mfma_f32_16x16x32_bf16 v[44:47], a[44:47], v[144:147], v[44:47]\n"
"v_mfma_f32_16x16x32_bf16 v[44:47], a[172:175], v[124:127], v[44:47]\n"
"buffer_load_dwordx4 v[180:183], v17, s[36:39], s64 offen sc1\n"
"v_min_u32 v95, 0xffbfffff, v95\n"
"v_mfma_f32_16x16x32_bf16 v[44:47], a[32:35], v[84:87], v[44:47]\n"
"ds_write_addtid_b32 v40 offset:4096\n"
"v_mfma_f32_16x16x32_bf16 v[44:47], a[160:163], v[224:227], v[44:47]\n"
"ds_write_addtid_b32 v41 offset:7968\n"
"buffer_store_dwordx4 v[92:95], v0, s[36:39], s86 offen offset:1024 sc1\n"
"s_waitcnt lgkmcnt(2)\n"
"v_mfma_f32_16x16x32_bf16 v[108:111], v[188:191], a[208:211], v[108:111]\n"
"ds_write_addtid_b32 v42 offset:11904\n"
"v_mfma_f32_16x16x32_bf16 v[108:111], v[172:175], a[84:87], v[108:111]\n"
"ds_write_addtid_b32 v43 offset:15776\n"
"v_mfma_f32_16x16x32_bf16 v[108:111], v[8:11], a[212:215], v[108:111]\n"
"v_mfma_f32_16x16x32_bf16 v[108:111], v[128:131], a[72:75], v[108:111]\n"
"v_mfma_f32_16x16x32_bf16 v[108:111], v[176:179], a[200:203], v[108:111]\n"
"v_mfma_f32_16x16x32_bf16 v[108:111], v[144:147], a[76:79], v[108:111]\n"
"v_mfma_f32_16x16x32_bf16 v[108:111], v[124:127], a[204:207], v[108:111]\n"
"v_mfma_f32_16x16x32_bf16 v[108:111], v[84:87], a[64:67], v[108:111]\n"
"ds_write_addtid_b32 v44 offset:4352\n"
"v_mfma_f32_16x16x32_bf16 v[108:111], v[224:227], a[192:195], v[108:111]\n"
"ds_write_addtid_b32 v45 offset:8224\n"
"v_mfma_f32_16x16x32_bf16 v[88:91], v[188:191], a[240:243], v[88:91]\n"
"ds_write_addtid_b32 v46 offset:12160\n"
"s_lshr_b32 s23, s76, 5\n"
"v_mfma_f32_16x16x32_bf16 v[88:91], v[172:175], a[116:119], v[88:91]\n"
"ds_write_addtid_b32 v47 offset:16032\n"
"s_add_u32 s20, s23, 4718592\n"
"v_mfma_f32_16x16x32_bf16 v[88:91], v[8:11], a[244:247], v[88:91]\n"
"v_mfma_f32_16x16x32_bf16 v[88:91], v[128:131], a[104:107], v[88:91]\n"
"v_mfma_f32_16x16x32_bf16 v[88:91], v[176:179], a[232:235], v[88:91]\n"
"v_mfma_f32_16x16x32_bf16 v[88:91], v[144:147], a[108:111], v[88:91]\n"
"s_waitcnt lgkmcnt(0)\n"
"s_barrier\n"
"buffer_load_dwordx4 v[188:191], v18, s[36:39], s20 offen offset:0 sc1\n"
"v_mfma_f32_16x16x32_bf16 v[88:91], v[124:127], a[236:239], v[88:91]\n"
"ds_read_b128 v[176:179], v15 offset:24576\n"
"v_mfma_f32_16x16x32_bf16 v[88:91], v[84:87], a[96:99], v[88:91]\n"
"ds_read_b128 v[172:175], v15 offset:24832\n"
"v_mfma_f32_16x16x32_bf16 v[88:91], v[224:227], a[224:227], v[88:91]\n"
"s_waitcnt vmcnt(23)\n"
"v_mfma_f32_16x16x32_bf16 v[144:147], v[220:223], v[208:211], 0\n"
"v_max3_u32 v216, v221, v223, v101\n"
"v_max3_u32 v216, v103, v61, v216\n"
"v_mfma_f32_16x16x32_bf16 v[144:147], v[100:103], v[184:187], v[144:147]\n"
"v_mfma_f32_16x16x32_bf16 v[144:147], v[60:63], v[232:235], v[144:147]\n"
"v_mfma_f32_16x16x32_bf16 v[144:147], v[164:167], v[248:251], v[144:147]\n"
"v_max3_u32 v216, v63, v165, v216\n"
"s_waitcnt vmcnt(19)\n"
"v_mfma_f32_16x16x32_bf16 v[144:147], v[252:255], a[24:27], v[144:147]\n"
"v_max3_u32 v216, v167, v253, v216\n"
"v_max3_u32 v216, v255, v137, v216\n"
"v_mfma_f32_16x16x32_bf16 v[144:147], v[136:139], a[152:155], v[144:147]\n"
"s_waitcnt lgkmcnt(0)\n"
"v_add_f32 v108, v108, v176\n"
"v_add_f32 v109, v109, v177\n"
"v_mfma_f32_16x16x32_bf16 v[144:147], v[160:163], a[28:31], v[144:147]\n"
"v_add_f32 v110, v110, v178\n"
"v_add_f32 v111, v111, v179\n"
"v_mfma_f32_16x16x32_bf16 v[144:147], v[80:83], a[156:159], v[144:147]\n"
"v_max3_u32 v216, v139, v161, v216\n"
"v_cvt_pk_bf16_f32 v224, v108, v109\n"
"s_waitcnt vmcnt(16)\n"
"v_mfma_f32_16x16x32_bf16 v[144:147], v[76:79], a[16:19], v[144:147]\n"
"v_max3_u32 v216, v163, v81, v216\n"
"v_max3_u32 v216, v83, v77, v216\n"
"v_cvt_pk_bf16_f32 v225, v110, v111\n"
"v_mfma_f32_16x16x32_bf16 v[176:179], v[220:223], v[204:207], 0\n"
"v_max_u32 v216, v79, v216\n"
"v_cmp_le_u32 vcc, 0xffc00000, v216\n"
"v_min_u32 v225, 0xffbfffff, v225\n"
"v_mfma_f32_16x16x32_bf16 v[176:179], v[100:103], v[168:171], v[176:179]\n"
"v_add_f32 v88, v88, v172\n"
"v_add_f32 v89, v89, v173\n"
"v_mfma_f32_16x16x32_bf16 v[176:179], v[60:63], v[156:159], v[176:179]\n"
"v_add_f32 v90, v90, v174\n"
"v_add_f32 v91, v91, v175\n"
"v_mfma_f32_16x16x32_bf16 v[176:179], v[164:167], v[68:71], v[176:179]\n"
"v_cvt_pk_bf16_f32 v226, v88, v89\n"
"v_cvt_pk_bf16_f32 v227, v90, v91\n"
"v_mfma_f32_16x16x32_bf16 v[176:179], v[252:255], a[56:59], v[176:179]\n"
"v_min_u32 v227, 0xffbfffff, v227\n"
"s_waitcnt vmcnt(12)\n"
"v_permlane16_swap_b32 v112, v114\n"
"v_mfma_f32_16x16x32_bf16 v[176:179], v[136:139], a[184:187], v[176:179]\n"
"buffer_store_dwordx4 v[224:227], v0, s[36:39], s87 offen offset:1024 sc1\n"
"v_permlane16_swap_b32 v113, v115\n"
"v_mfma_f32_16x16x32_bf16 v[176:179], v[160:163], a[60:63], v[176:179]\n"
"v_lshlrev_b32 v224, 16, v112\n"
"v_and_b32 v225, 0xffff0000, v112\n"
"v_mfma_f32_16x16x32_bf16 v[176:179], v[80:83], a[188:191], v[176:179]\n"
"v_lshlrev_b32 v226, 16, v113\n"
"v_and_b32 v227, 0xffff0000, v113\n"
"v_mfma_f32_16x16x32_bf16 v[176:179], v[76:79], a[48:51], v[176:179]\n"
"s_cbranch_vccnz .Lgs_slow15\n"
".Lgs_back15:\n"
"v_lshlrev_b32 v172, 16, v114\n"
"v_mfma_f32_16x16x32_bf16 v[128:131], v[200:203], v[220:223], v[140:143]\n"
"ds_write_addtid_b32 v144 offset:4608\n"
"v_and_b32 v173, 0xffff0000, v114\n"
"v_mfma_f32_16x16x32_bf16 v[128:131], v[132:135], v[100:103], v[128:131]\n"
"ds_write_addtid_b32 v145 offset:8480\n"
"v_lshlrev_b32 v174, 16, v115\n"
"v_mfma_f32_16x16x32_bf16 v[128:131], v[148:151], v[60:63], v[128:131]\n"
"ds_write_addtid_b32 v146 offset:12416\n"
"v_and_b32 v175, 0xffff0000, v115\n"
"v_mfma_f32_16x16x32_bf16 v[128:131], v[228:231], v[164:167], v[128:131]\n"
"ds_write_addtid_b32 v147 offset:16288\n"
"v_mfma_f32_16x16x32_bf16 v[128:131], a[88:91], v[252:255], v[128:131]\n"
"v_mfma_f32_16x16x32_bf16 v[128:131], a[216:219], v[136:139], v[128:131]\n"
"v_mfma_f32_16x16x32_bf16 v[128:131], a[92:95], v[160:163], v[128:131]\n"
"v_mfma_f32_16x16x32_bf16 v[128:131], a[220:223], v[80:83], v[128:131]\n"
"ds_write_addtid_b32 v176 offset:4864\n"
"v_mfma_f32_16x16x32_bf16 v[128:131], a[80:83], v[76:79], v[128:131]\n"
"ds_write_addtid_b32 v177 offset:8736\n"
"s_waitcnt vmcnt(9)\n"
"v_cmp_ne_u64 vcc, s[78:79], v[218:219]\n"
"v_mfma_f32_16x16x32_bf16 v[144:147], v[192:195], v[220:223], v[104:107]\n"
"ds_write_addtid_b32 v178 offset:12672\n"
"v_mfma_f32_16x16x32_bf16 v[144:147], v[212:215], v[100:103], v[144:147]\n"
"ds_write_addtid_b32 v179 offset:16544\n"
"s_cbranch_vccnz .Lgs_slow25\n"
".Lgs_back25:\n"
"v_mfma_f32_16x16x32_bf16 v[144:147], v[96:99], v[60:63], v[144:147]\n"
"v_mfma_f32_16x16x32_bf16 v[144:147], v[240:243], v[164:167], v[144:147]\n"
"v_mfma_f32_16x16x32_bf16 v[144:147], a[120:123], v[252:255], v[144:147]\n"
"v_mfma_f32_16x16x32_bf16 v[144:147], a[248:251], v[136:139], v[144:147]\n"
"v_max3_u32 v19, v49, v51, v121\n"
"v_max3_u32 v19, v123, v37, v19\n"
"v_mfma_f32_16x16x32_bf16 v[144:147], a[124:127], v[160:163], v[144:147]\n"
"v_max_u32 v19, v39, v19\n"
"v_cmp_le_u32 vcc, 0xffc00000, v19\n"
"v_mfma_f32_16x16x32_bf16 v[144:147], a[252:255], v[80:83], v[144:147]\n"
"ds_write_addtid_b32 v128 offset:5120\n"
"v_mfma_f32_16x16x32_bf16 v[144:147], a[112:115], v[76:79], v[144:147]\n"
"ds_write_addtid_b32 v129 offset:8992\n"
"s_cbranch_vccnz .Lgs_slow27\n"
".Lgs_back27:\n"
"s_waitcnt vmcnt(8)\n"
"v_mfma_f32_16x16x32_bf16 v[216:219], v[72:75], v[208:211], 0\n"
"ds_write_addtid_b32 v130 offset:12928\n"
"v_max3_u32 v19, v73, v75, v153\n"
"v_mfma_f32_16x16x32_bf16 v[216:219], v[152:155], v[184:187], v[216:219]\n"
"ds_write_addtid_b32 v131 offset:16800\n"
"v_max3_u32 v19, v155, v53, v19\n"
"v_mfma_f32_16x16x32_bf16 v[216:219], v[52:55], v[232:235], v[216:219]\n"
"v_lshlrev_b32 v176, 16, v48\n"
"v_and_b32 v177, 0xffff0000, v48\n"
"v_mfma_f32_16x16x32_bf16 v[216:219], v[244:247], v[248:251], v[216:219]\n"
"v_max3_u32 v19, v55, v245, v19\n"
"s_waitcnt vmcnt(4)\n"
"v_mfma_f32_16x16x32_bf16 v[216:219], v[116:119], a[24:27], v[216:219]\n"
"v_max3_u32 v19, v247, v117, v19\n"
"v_max3_u32 v19, v119, v197, v19\n"
"v_mfma_f32_16x16x32_bf16 v[216:219], v[196:199], a[152:155], v[216:219]\n"
"v_max3_u32 v19, v199, v237, v19\n"
"v_max3_u32 v19, v239, v57, v19\n"
"v_mfma_f32_16x16x32_bf16 v[216:219], v[236:239], a[28:31], v[216:219]\n"
"v_lshlrev_b32 v178, 16, v49\n"
"v_and_b32 v179, 0xffff0000, v49\n"
"v_mfma_f32_16x16x32_bf16 v[216:219], v[56:59], a[156:159], v[216:219]\n"
"ds_write_addtid_b32 v144 offset:5376\n"
"s_waitcnt vmcnt(3)\n"
"v_mfma_f32_16x16x32_bf16 v[216:219], v[180:183], a[16:19], v[216:219]\n"
"ds_write_addtid_b32 v145 offset:9248\n"
"v_max3_u32 v19, v59, v181, v19\n"
"v_mfma_f32_16x16x32_bf16 v[128:131], v[72:75], v[204:207], 0\n"
"ds_write_addtid_b32 v146 offset:13184\n"
"v_max_u32 v19, v183, v19\n"
"v_mfma_f32_16x16x32_bf16 v[128:131], v[152:155], v[168:171], v[128:131]\n"
"ds_write_addtid_b32 v147 offset:17056\n"
"v_cmp_le_u32 vcc, 0xffc00000, v19\n"
"v_mfma_f32_16x16x32_bf16 v[128:131], v[52:55], v[156:159], v[128:131]\n"
"v_add_f32 v224, v224, v176\n"
"v_add_f32 v225, v225, v177\n"
"v_mfma_f32_16x16x32_bf16 v[128:131], v[244:247], v[68:71], v[128:131]\n"
"v_add_f32 v226, v226, v178\n"
"v_add_f32 v227, v227, v179\n"
"v_mfma_f32_16x16x32_bf16 v[128:131], v[116:119], a[56:59], v[128:131]\n"
"v_lshlrev_b32 v144, 16, v120\n"
"v_and_b32 v145, 0xffff0000, v120\n"
"v_mfma_f32_16x16x32_bf16 v[128:131], v[196:199], a[184:187], v[128:131]\n"
"s_waitcnt lgkmcnt(0)\n"
"s_barrier\n"
"v_mfma_f32_16x16x32_bf16 v[128:131], v[236:239], a[60:63], v[128:131]\n"
"v_lshlrev_b32 v146, 16, v121\n"
"v_and_b32 v147, 0xffff0000, v121\n"
"v_mfma_f32_16x16x32_bf16 v[128:131], v[56:59], a[188:191], v[128:131]\n"
"v_add_f32 v224, v224, v144\n"
"v_add_f32 v225, v225, v145\n"
"v_mfma_f32_16x16x32_bf16 v[128:131], v[180:183], a[48:51], v[128:131]\n"
"s_cbranch_vccnz .Lgs_slow16\n"
".Lgs_back16:\n"
"v_add_f32 v226, v226, v146\n"
"v_mfma_f32_16x16x32_bf16 v[176:179], v[200:203], v[72:75], v[140:143]\n"
"ds_write_addtid_b32 v216 offset:4096\n"
"v_add_f32 v227, v227, v147\n"
"v_mfma_f32_16x16x32_bf16 v[176:179], v[132:135], v[152:155], v[176:179]\n"
"ds_write_addtid_b32 v217 offset:7968\n"
"v_lshlrev_b32 v248, 16, v36\n"
"v_mfma_f32_16x16x32_bf16 v[176:179], v[148:151], v[52:55], v[176:179]\n"
"ds_write_addtid_b32 v218 offset:11904\n"
"v_and_b32 v249, 0xffff0000, v36\n"
"v_mfma_f32_16x16x32_bf16 v[176:179], v[228:231], v[244:247], v[176:179]\n"
"ds_write_addtid_b32 v219 offset:15776\n"
"v_lshlrev_b32 v250, 16, v37\n"
"v_mfma_f32_16x16x32_bf16 v[176:179], a[88:91], v[116:119], v[176:179]\n"
"v_and_b32 v251, 0xffff0000, v37\n"
"v_add_f32 v224, v224, v248\n"
"v_mfma_f32_16x16x32_bf16 v[176:179], a[216:219], v[196:199], v[176:179]\n"
"ds_read_b128 v[232:235], v16 offset:25344\n"
"v_add_f32 v225, v225, v249\n"
"v_mfma_f32_16x16x32_bf16 v[176:179], a[92:95], v[236:239], v[176:179]\n"
"ds_read_b128 v[228:231], v16 offset:25088\n"
"v_add_f32 v226, v226, v250\n"
"v_mfma_f32_16x16x32_bf16 v[176:179], a[220:223], v[56:59], v[176:179]\n"
"ds_write_addtid_b32 v128 offset:4352\n"
"v_add_f32 v227, v227, v251\n"
"v_mfma_f32_16x16x32_bf16 v[176:179], a[80:83], v[180:183], v[176:179]\n"
"ds_write_addtid_b32 v129 offset:8224\n"
"v_lshlrev_b32 v248, 16, v50\n"
"v_mfma_f32_16x16x32_bf16 v[216:219], v[192:195], v[72:75], v[104:107]\n"
"ds_write_addtid_b32 v130 offset:12160\n"
"v_and_b32 v249, 0xffff0000, v50\n"
"v_mfma_f32_16x16x32_bf16 v[216:219], v[212:215], v[152:155], v[216:219]\n"
"ds_write_addtid_b32 v131 offset:16032\n"
"v_lshlrev_b32 v250, 16, v51\n"
"v_mfma_f32_16x16x32_bf16 v[216:219], v[96:99], v[52:55], v[216:219]\n"
"v_and_b32 v251, 0xffff0000, v51\n"
"v_add_f32 v172, v172, v248\n"
"v_mfma_f32_16x16x32_bf16 v[216:219], v[240:243], v[244:247], v[216:219]\n"
"v_add_f32 v173, v173, v249\n"
"v_add_f32 v174, v174, v250\n"
"v_mfma_f32_16x16x32_bf16 v[216:219], a[120:123], v[116:119], v[216:219]\n"
"v_add_f32 v175, v175, v251\n"
"v_lshlrev_b32 v240, 16, v122\n"
"v_mfma_f32_16x16x32_bf16 v[216:219], a[248:251], v[196:199], v[216:219]\n"
"v_and_b32 v241, 0xffff0000, v122\n"
"v_lshlrev_b32 v242, 16, v123\n"
"v_mfma_f32_16x16x32_bf16 v[216:219], a[124:127], v[236:239], v[216:219]\n"
"v_and_b32 v243, 0xffff0000, v123\n"
"v_add_f32 v172, v172, v240\n"
"v_mfma_f32_16x16x32_bf16 v[216:219], a[252:255], v[56:59], v[216:219]\n"
"ds_write_addtid_b32 v176 offset:5632\n"
"v_add_f32 v173, v173, v241\n"
"v_mfma_f32_16x16x32_bf16 v[216:219], a[112:115], v[180:183], v[216:219]\n"
"ds_write_addtid_b32 v177 offset:9504\n"
"v_add_f32 v174, v174, v242\n"
"s_waitcnt lgkmcnt(6)\n"
"v_mfma_f32_16x16x32_bf16 v[228:231], a[144:147], v[220:223], v[228:231]\n"
"ds_write_addtid_b32 v178 offset:13440\n"
"v_add_f32 v175, v175, v243\n"
"v_mfma_f32_16x16x32_bf16 v[228:231], a[20:23], v[100:103], v[228:231]\n"
"ds_write_addtid_b32 v179 offset:17312\n"
"v_lshlrev_b32 v248, 16, v38\n"
"v_mfma_f32_16x16x32_bf16 v[228:231], a[148:151], v[60:63], v[228:231]\n"
"v_and_b32 v249, 0xffff0000, v38\n"
"v_lshlrev_b32 v250, 16, v39\n"
"v_mfma_f32_16x16x32_bf16 v[228:231], a[8:11], v[164:167], v[228:231]\n"
"v_and_b32 v251, 0xffff0000, v39\n"
"v_add_f32 v172, v172, v248\n"
"v_mfma_f32_16x16x32_bf16 v[228:231], a[136:139], v[252:255], v[228:231]\n"
"buffer_load_dwordx4 v[240:243], v0, s[36:39], s88 offen offset:2048 sc1\n"
"v_mfma_f32_16x16x32_bf16 v[228:231], a[12:15], v[136:139], v[228:231]\n"
"ds_read_b128 v[212:215], v16 offset:25856\n"
"v_add_f32 v173, v173, v249\n"
"v_mfma_f32_16x16x32_bf16 v[228:231], a[140:143], v[160:163], v[228:231]\n"
"ds_read_b128 v[208:211], v16 offset:25600\n"
"v_add_f32 v174, v174, v250\n"
"v_mfma_f32_16x16x32_bf16 v[228:231], a[0:3], v[80:83], v[228:231]\n"
"ds_write_addtid_b32 v216 offset:5888\n"
"v_add_f32 v175, v175, v251\n"
"v_mfma_f32_16x16x32_bf16 v[228:231], a[128:131], v[76:79], v[228:231]\n"
"ds_write_addtid_b32 v217 offset:9760\n"
"buffer_load_dwordx4 v[248:251], v0, s[36:39], s92 offen offset:2048 sc1\n"
"v_mfma_f32_16x16x32_bf16 v[232:235], a[176:179], v[220:223], v[232:235]\n"
"ds_write_addtid_b32 v218 offset:13696\n"
"s_waitcnt vmcnt(3)\n"
"v_permlane16_swap_b32 v188, v190\n"
"v_mfma_f32_16x16x32_bf16 v[232:235], a[52:55], v[100:103], v[232:235]\n"
"ds_write_addtid_b32 v219 offset:17568\n"
"v_permlane16_swap_b32 v189, v191\n"
"v_mfma_f32_16x16x32_bf16 v[232:235], a[180:183], v[60:63], v[232:235]\n"
"v_lshlrev_b32 v216, 16, v188\n"
"v_and_b32 v217, 0xffff0000, v188\n"
"v_mfma_f32_16x16x32_bf16 v[232:235], a[40:43], v[164:167], v[232:235]\n"
"buffer_load_dwordx4 v[204:207], v0, s[36:39], s93 offen offset:2048 sc1\n"
"v_lshlrev_b32 v218, 16, v189\n"
"v_mfma_f32_16x16x32_bf16 v[232:235], a[168:171], v[252:255], v[232:235]\n"
"v_and_b32 v219, 0xffff0000, v189\n"
"v_lshlrev_b32 v200, 16, v190\n"
"v_mfma_f32_16x16x32_bf16 v[232:235], a[44:47], v[136:139], v[232:235]\n"
"s_waitcnt lgkmcnt(0)\n"
"s_barrier\n"
"v_mfma_f32_16x16x32_bf16 v[232:235], a[172:175], v[160:163], v[232:235]\n"
"v_mfma_f32_16x16x32_bf16 v[232:235], a[32:35], v[80:83], v[232:235]\n"
"ds_write_addtid_b32 v228 offset:4608\n"
"v_and_b32 v201, 0xffff0000, v190\n"
"v_mfma_f32_16x16x32_bf16 v[232:235], a[160:163], v[76:79], v[232:235]\n"
"ds_write_addtid_b32 v229 offset:8480\n"
"v_lshlrev_b32 v202, 16, v191\n"
"v_mfma_f32_16x16x32_bf16 v[208:211], v[220:223], a[208:211], v[208:211]\n"
"ds_write_addtid_b32 v230 offset:12416\n"
"v_and_b32 v203, 0xffff0000, v191\n"
"v_mfma_f32_16x16x32_bf16 v[208:211], v[100:103], a[84:87], v[208:211]\n"
"ds_write_addtid_b32 v231 offset:16288\n"
"v_mfma_f32_16x16x32_bf16 v[208:211], v[60:63], a[212:215], v[208:211]\n"
"v_mfma_f32_16x16x32_bf16 v[208:211], v[164:167], a[72:75], v[208:211]\n"
"v_mfma_f32_16x16x32_bf16 v[208:211], v[252:255], a[200:203], v[208:211]\n"
"v_mfma_f32_16x16x32_bf16 v[208:211], v[136:139], a[76:79], v[208:211]\n"
"ds_read_b128 v[228:231], v16 offset:24832\n"
"v_mfma_f32_16x16x32_bf16 v[208:211], v[160:163], a[204:207], v[208:211]\n"
"ds_read_b128 v[192:195], v16 offset:24576\n"
"v_mfma_f32_16x16x32_bf16 v[208:211], v[80:83], a[64:67], v[208:211]\n"
"ds_write_addtid_b32 v232 offset:4864\n"
"v_mfma_f32_16x16x32_bf16 v[208:211], v[76:79], a[192:195], v[208:211]\n"
"ds_write_addtid_b32 v233 offset:8736\n"
"v_mfma_f32_16x16x32_bf16 v[212:215], v[220:223], a[240:243], v[212:215]\n"
"ds_write_addtid_b32 v234 offset:12672\n"
"v_mfma_f32_16x16x32_bf16 v[212:215], v[100:103], a[116:119], v[212:215]\n"
"ds_write_addtid_b32 v235 offset:16544\n"
"v_mfma_f32_16x16x32_bf16 v[212:215], v[60:63], a[244:247], v[212:215]\n"
"v_mfma_f32_16x16x32_bf16 v[212:215], v[164:167], a[104:107], v[212:215]\n"
"v_mfma_f32_16x16x32_bf16 v[212:215], v[252:255], a[232:235], v[212:215]\n"
"v_mfma_f32_16x16x32_bf16 v[212:215], v[136:139], a[108:111], v[212:215]\n"
"s_waitcnt lgkmcnt(0)\n"
"s_barrier\n"
"v_mfma_f32_16x16x32_bf16 v[212:215], v[160:163], a[236:239], v[212:215]\n"
"ds_read_b128 v[252:255], v15 offset:25088\n"
"v_mfma_f32_16x16x32_bf16 v[212:215], v[80:83], a[96:99], v[212:215]\n"
"ds_read_b128 v[232:235], v15 offset:25344\n"
"v_mfma_f32_16x16x32_bf16 v[212:215], v[76:79], a[224:227], v[212:215]\n"
"v_mfma_f32_16x16x32_bf16 v[192:195], a[144:147], v[72:75], v[192:195]\n"
"v_mfma_f32_16x16x32_bf16 v[192:195], a[20:23], v[152:155], v[192:195]\n"
"v_mfma_f32_16x16x32_bf16 v[192:195], a[148:151], v[52:55], v[192:195]\n"
"v_mfma_f32_16x16x32_bf16 v[192:195], a[8:11], v[244:247], v[192:195]\n"
"v_mfma_f32_16x16x32_bf16 v[192:195], a[136:139], v[116:119], v[192:195]\n"
"s_waitcnt lgkmcnt(0)\n"
"v_add_f32 v208, v208, v252\n"
"v_mfma_f32_16x16x32_bf16 v[192:195], a[12:15], v[196:199], v[192:195]\n"
"v_add_f32 v209, v209, v253\n"
"v_add_f32 v210, v210, v254\n"
"v_mfma_f32_16x16x32_bf16 v[192:195], a[140:143], v[236:239], v[192:195]\n"
"v_mfma_f32_16x16x32_bf16 v[192:195], a[0:3], v[56:59], v[192:195]\n"
"ds_read_b128 v[220:223], v16 offset:26112\n"
"v_add_f32 v211, v211, v255\n"
"v_mfma_f32_16x16x32_bf16 v[192:195], a[128:131], v[180:183], v[192:195]\n"
"ds_read_b128 v[188:191], v16 offset:26368\n"
"v_add_f32 v212, v212, v232\n"
"v_mfma_f32_16x16x32_bf16 v[228:231], a[176:179], v[72:75], v[228:231]\n"
"v_add_f32 v213, v213, v233\n"
"v_add_f32 v214, v214, v234\n"
"v_mfma_f32_16x16x32_bf16 v[228:231], a[52:55], v[152:155], v[228:231]\n"
"v_add_f32 v224, v224, v208\n"
"v_add_f32 v225, v225, v209\n"
"v_mfma_f32_16x16x32_bf16 v[228:231], a[180:183], v[52:55], v[228:231]\n"
"v_add_f32 v226, v226, v210\n"
"v_add_f32 v227, v227, v211\n"
"v_mfma_f32_16x16x32_bf16 v[228:231], a[40:43], v[244:247], v[228:231]\n"
"v_mfma_f32_16x16x32_bf16 v[228:231], a[168:171], v[116:119], v[228:231]\n"
"ds_write_addtid_b32 v192 offset:4096\n"
"v_add_f32 v215, v215, v235\n"
"v_mfma_f32_16x16x32_bf16 v[228:231], a[44:47], v[196:199], v[228:231]\n"
"ds_write_addtid_b32 v193 offset:7968\n"
"v_cvt_pk_bf16_f32 v252, v224, v225\n"
"v_mfma_f32_16x16x32_bf16 v[228:231], a[172:175], v[236:239], v[228:231]\n"
"ds_write_addtid_b32 v194 offset:11904\n"
"v_cvt_pk_bf16_f32 v253, v226, v227\n"
"v_mfma_f32_16x16x32_bf16 v[228:231], a[32:35], v[56:59], v[228:231]\n"
"ds_write_addtid_b32 v195 offset:15776\n"
"v_add_f32 v172, v172, v212\n"
"v_mfma_f32_16x16x32_bf16 v[228:231], a[160:163], v[180:183], v[228:231]\n"
"v_add_f32 v173, v173, v213\n"
"v_add_f32 v174, v174, v214\n"
"s_waitcnt lgkmcnt(4)\n"
"v_mfma_f32_16x16x32_bf16 v[220:223], v[72:75], a[208:211], v[220:223]\n"
"v_add_f32 v175, v175, v215\n"
"v_cvt_pk_bf16_f32 v254, v172, v173\n"
"v_mfma_f32_16x16x32_bf16 v[220:223], v[152:155], a[84:87], v[220:223]\n"
"v_mfma_f32_16x16x32_bf16 v[220:223], v[52:55], a[212:215], v[220:223]\n"
"v_mfma_f32_16x16x32_bf16 v[220:223], v[244:247], a[72:75], v[220:223]\n"
"v_mfma_f32_16x16x32_bf16 v[220:223], v[116:119], a[200:203], v[220:223]\n"
"ds_write_addtid_b32 v228 offset:4352\n"
"v_cvt_pk_bf16_f32 v255, v174, v175\n"
"v_mfma_f32_16x16x32_bf16 v[220:223], v[196:199], a[76:79], v[220:223]\n"
"ds_write_addtid_b32 v229 offset:8224\n"
"v_permlane16_swap_b32 v252, v254\n"
"v_mfma_f32_16x16x32_bf16 v[220:223], v[236:239], a[204:207], v[220:223]\n"
"ds_write_addtid_b32 v230 offset:12160\n"
"v_permlane16_swap_b32 v253, v255\n"
"buffer_store_dwordx4 v[252:255], v18, s[44:47], s22 offen offset:0 sc1\n"
"v_mfma_f32_16x16x32_bf16 v[220:223], v[56:59], a[64:67], v[220:223]\n"
"ds_write_addtid_b32 v231 offset:16032\n"
"v_mfma_f32_16x16x32_bf16 v[220:223], v[180:183], a[192:195], v[220:223]\n"
"s_waitcnt vmcnt(2)\n"
"v_max3_u32 v1, v241, v243, v249\n"
"s_waitcnt vmcnt(1)\n"
"v_max3_u32 v1, v251, v205, v1\n"
"v_mfma_f32_16x16x32_bf16 v[188:191], v[72:75], a[240:243], v[188:191]\n"
"v_max_u32 v1, v207, v1\n"
"v_cmp_le_u32 vcc, 0xffc00000, v1\n"
"v_mfma_f32_16x16x32_bf16 v[188:191], v[152:155], a[116:119], v[188:191]\n"
"v_mfma_f32_16x16x32_bf16 v[188:191], v[52:55], a[244:247], v[188:191]\n"
"s_waitcnt lgkmcnt(0)\n"
"s_barrier\n"
"s_cbranch_vccnz .Lgs_slow28\n"
".Lgs_back28:\n"
"v_mfma_f32_16x16x32_bf16 v[188:191], v[244:247], a[104:107], v[188:191]\n"
"ds_read_b128 v[252:255], v15 offset:24576\n"
"v_lshlrev_b32 v232, 16, v240\n"
"v_and_b32 v233, 0xffff0000, v240\n"
"v_mfma_f32_16x16x32_bf16 v[188:191], v[116:119], a[232:235], v[188:191]\n"
"ds_read_b128 v[244:247], v15 offset:24832\n"
"v_lshlrev_b32 v234, 16, v241\n"
"v_and_b32 v235, 0xffff0000, v241\n"
"v_mfma_f32_16x16x32_bf16 v[188:191], v[196:199], a[108:111], v[188:191]\n"
"v_add_f32 v216, v216, v232\n"
"v_add_f32 v217, v217, v233\n"
"v_add_f32 v218, v218, v234\n"
"v_mfma_f32_16x16x32_bf16 v[188:191], v[236:239], a[236:239], v[188:191]\n"
"v_add_f32 v219, v219, v235\n"
"v_lshlrev_b32 v228, 16, v248\n"
"v_and_b32 v229, 0xffff0000, v248\n"
"v_mfma_f32_16x16x32_bf16 v[188:191], v[56:59], a[96:99], v[188:191]\n"
"v_lshlrev_b32 v230, 16, v249\n"
"v_and_b32 v231, 0xffff0000, v249\n"
"v_add_f32 v216, v216, v228\n"
"v_mfma_f32_16x16x32_bf16 v[188:191], v[180:183], a[224:227], v[188:191]\n"
"v_add_f32 v217, v217, v229\n"
"v_add_f32 v218, v218, v230\n"
"v_add_f32 v219, v219, v231\n"
"v_lshlrev_b32 v236, 16, v204\n"
"v_and_b32 v237, 0xffff0000, v204\n"
"v_lshlrev_b32 v238, 16, v205\n"
"v_and_b32 v239, 0xffff0000, v205\n"
"v_add_f32 v216, v216, v236\n"
"v_add_f32 v217, v217, v237\n"
"v_add_f32 v218, v218, v238\n"
"v_add_f32 v219, v219, v239\n"
"s_waitcnt lgkmcnt(1)\n"
"v_add_f32 v216, v216, v252\n"
"v_add_f32 v217, v217, v253\n"
"v_add_f32 v218, v218, v254\n"
"v_add_f32 v219, v219, v255\n"
"v_lshlrev_b32 v236, 16, v242\n"
"v_and_b32 v237, 0xffff0000, v242\n"
"v_lshlrev_b32 v238, 16, v243\n"
"v_and_b32 v239, 0xffff0000, v243\n"
"v_add_f32 v200, v200, v236\n"
"v_add_f32 v201, v201, v237\n"
"v_add_f32 v202, v202, v238\n"
"v_add_f32 v203, v203, v239\n"
"v_lshlrev_b32 v252, 16, v250\n"
"v_and_b32 v253, 0xffff0000, v250\n"
"v_lshlrev_b32 v254, 16, v251\n"
"v_and_b32 v255, 0xffff0000, v251\n"
"v_add_f32 v200, v200, v252\n"
"v_add_f32 v201, v201, v253\n"
"v_add_f32 v202, v202, v254\n"
"v_add_f32 v203, v203, v255\n"
"v_lshlrev_b32 v248, 16, v206\n"
"v_and_b32 v249, 0xffff0000, v206\n"
"v_lshlrev_b32 v250, 16, v207\n"
"v_and_b32 v251, 0xffff0000, v207\n"
"v_add_f32 v200, v200, v248\n"
"v_add_f32 v201, v201, v249\n"
"v_add_f32 v202, v202, v250\n"
"v_add_f32 v203, v203, v251\n"
"s_waitcnt lgkmcnt(0)\n"
"v_add_f32 v200, v200, v244\n"
"v_add_f32 v201, v201, v245\n"
"v_add_f32 v202, v202, v246\n"
"v_add_f32 v203, v203, v247\n"
"v_add_f32 v216, v216, v220\n"
"v_add_f32 v217, v217, v221\n"
"v_add_f32 v218, v218, v222\n"
"v_add_f32 v219, v219, v223\n"
"v_cvt_pk_bf16_f32 v252, v216, v217\n"
"v_cvt_pk_bf16_f32 v253, v218, v219\n"
"v_add_f32 v200, v200, v188\n"
"v_add_f32 v201, v201, v189\n"
"v_add_f32 v202, v202, v190\n"
"v_add_f32 v203, v203, v191\n"
"v_cvt_pk_bf16_f32 v254, v200, v201\n"
"v_cvt_pk_bf16_f32 v255, v202, v203\n"
"s_nop 0\n"
"v_permlane16_swap_b32 v252, v254\n"
"v_permlane16_swap_b32 v253, v255\n"
"buffer_store_dwordx4 v[252:255], v18, s[44:47], s23 offen offset:0 sc1\n"
"s_endpgm\n"
".Lgs_slow28:\n"
"s_mov_b32 s24, 0\n"
".Lgs_loop28:\n"
"buffer_load_dwordx4 v[240:243], v0, s[36:39], s88 offen offset:2048 sc1\n"
"buffer_load_dwordx4 v[248:251], v0, s[36:39], s92 offen offset:2048 sc1\n"
"buffer_load_dwordx4 v[204:207], v0, s[36:39], s93 offen offset:2048 sc1\n"
"s_waitcnt vmcnt(0)\n"
"v_max3_u32 v1, v241, v243, v249\n"
"v_max3_u32 v1, v251, v205, v1\n"
"v_max_u32 v1, v207, v1\n"
"v_cmp_le_u32 vcc, 0xffc00000, v1\n"
"s_cbranch_vccz .Lgs_done28\n"
"s_sleep 1\n"
"s_add_u32 s24, s24, 1\n"
"s_cmp_lt_u32 s24, 0x10000\n"
"s_cbranch_scc1 .Lgs_loop28\n"
".Lgs_done28:\n"
"s_branch .Lgs_back28\n"
".Lgs_slow27:\n"
"s_mov_b32 s24, 0\n"
".Lgs_loop27:\n"
"buffer_load_dwordx4 v[48:51], v0, s[36:39], s88 offen offset:1024 sc1\n"
"buffer_load_dwordx4 v[120:123], v0, s[36:39], s92 offen offset:1024 sc1\n"
"buffer_load_dwordx4 v[36:39], v0, s[36:39], s93 offen offset:1024 sc1\n"
"s_waitcnt vmcnt(0)\n"
"v_max3_u32 v19, v49, v51, v121\n"
"v_max3_u32 v19, v123, v37, v19\n"
"v_max_u32 v19, v39, v19\n"
"v_cmp_le_u32 vcc, 0xffc00000, v19\n"
"s_cbranch_vccz .Lgs_done27\n"
"s_sleep 1\n"
"s_add_u32 s24, s24, 1\n"
"s_cmp_lt_u32 s24, 0x10000\n"
"s_cbranch_scc1 .Lgs_loop27\n"
".Lgs_done27:\n"
"s_branch .Lgs_back27\n"
".Lgs_slow25:\n"
"s_mov_b32 s24, 0\n"
".Lgs_loop25:\n"
"buffer_load_dwordx2 v[218:219], v22, s[36:39], 0 offen offset:32 sc1\n"
"s_waitcnt vmcnt(0)\n"
"v_cmp_ne_u64 vcc, s[78:79], v[218:219]\n"
"s_cbranch_vccz .Lgs_done25\n"
"s_sleep 2\n"
"s_add_u32 s24, s24, 1\n"
"s_cmp_lt_u32 s24, 0x10000\n"
"s_cbranch_scc1 .Lgs_loop25\n"
".Lgs_done25:\n"
"s_branch .Lgs_back25\n"
".Lgs_slow26:\n"
"s_mov_b32 s24, 0\n"
".Lgs_loop26:\n"
"buffer_load_dwordx4 v[56:59], v12, s[36:39], s88 offen offset:0 sc1\n"
"buffer_load_dwordx4 v[72:75], v12, s[36:39], s92 offen offset:0 sc1\n"
"buffer_load_dwordx4 v[28:31], v12, s[36:39], s93 offen offset:0 sc1\n"
"s_waitcnt vmcnt(0)\n"
"v_max3_u32 v13, v57, v59, v73\n"
"v_max3_u32 v13, v75, v29, v13\n"
"v_max_u32 v13, v31, v13\n"
"v_cmp_le_u32 vcc, 0xffc00000, v13\n"
"s_cbranch_vccz .Lgs_done26\n"
"s_sleep 1\n"
"s_add_u32 s24, s24, 1\n"
"s_cmp_lt_u32 s24, 0x10000\n"
"s_cbranch_scc1 .Lgs_loop26\n"
".Lgs_done26:\n"
"s_branch .Lgs_back26\n"
".Lgs_slow24:\n"
"s_mov_b32 s24, 0\n"
".Lgs_loop24:\n"
"buffer_load_dwordx2 v[86:87], v22, s[36:39], 0 offen offset:0 sc1\n"
"s_waitcnt vmcnt(0)\n"
"v_cmp_ne_u64 vcc, s[78:79], v[86:87]\n"
"s_cbranch_vccz .Lgs_done24\n"
"s_sleep 2\n"
"s_add_u32 s24, s24, 1\n"
"s_cmp_lt_u32 s24, 0x10000\n"
"s_cbranch_scc1 .Lgs_loop24\n"
".Lgs_done24:\n"
"s_branch .Lgs_back24\n"
".Lgs_slow23:\n"
"s_mov_b32 s24, 0\n"
".Lgs_loop23:\n"
"buffer_load_dwordx4 v[44:47], v5, s[36:39], s22 offen offset:1024 sc1\n"
"s_waitcnt vmcnt(0)\n"
"v_max_u32 v4, v45, v47\n"
"v_cmp_le_u32 vcc, 0xffc00000, v4\n"
"s_cbranch_vccz .Lgs_done23\n"
"s_sleep 1\n"
"s_add_u32 s24, s24, 1\n"
"s_cmp_lt_u32 s24, 0x10000\n"
"s_cbranch_scc1 .Lgs_loop23\n"
".Lgs_done23:\n"
"s_branch .Lgs_back23\n"
".Lgs_slow22:\n"
"s_mov_b32 s24, 0\n"
".Lgs_loop22:\n"
"buffer_load_dwordx4 v[36:39], v5, s[36:39], s22 offen offset:0 sc1\n"
"s_waitcnt vmcnt(0)\n"
"v_max_u32 v24, v37, v39\n"
"v_cmp_le_u32 vcc, 0xffc00000, v24\n"
"s_cbranch_vccz .Lgs_done22\n"
"s_sleep 1\n"
"s_add_u32 s24, s24, 1\n"
"s_cmp_lt_u32 s24, 0x10000\n"
"s_cbranch_scc1 .Lgs_loop22\n"
".Lgs_done22:\n"
"s_branch .Lgs_back22\n"
".Lgs_slow21:\n"
"s_mov_b32 s24, 0\n"
".Lgs_loop21:\n"
"buffer_load_dwordx4 v[52:55], v5, s[36:39], s81 offen offset:3072 sc1\n"
"s_waitcnt vmcnt(0)\n"
"v_max_u32 v24, v53, v55\n"
"v_cmp_le_u32 vcc, 0xffc00000, v24\n"
"s_cbranch_vccz .Lgs_done21\n"
"s_sleep 1\n"
"s_add_u32 s24, s24, 1\n"
"s_cmp_lt_u32 s24, 0x10000\n"
"s_cbranch_scc1 .Lgs_loop21\n"
".Lgs_done21:\n"
"s_branch .Lgs_back21\n"
".Lgs_slow20:\n"
"s_mov_b32 s24, 0\n"
".Lgs_loop20:\n"
"buffer_load_dwordx4 v[124:127], v5, s[36:39], s81 offen offset:2048 sc1\n"
"s_waitcnt vmcnt(0)\n"
"v_max_u32 v24, v125, v127\n"
"v_cmp_le_u32 vcc, 0xffc00000, v24\n"
"s_cbranch_vccz .Lgs_done20\n"
"s_sleep 1\n"
"s_add_u32 s24, s24, 1\n"
"s_cmp_lt_u32 s24, 0x10000\n"
"s_cbranch_scc1 .Lgs_loop20\n"
".Lgs_done20:\n"
"s_branch .Lgs_back20\n"
".Lgs_slow19:\n"
"s_mov_b32 s24, 0\n"
".Lgs_loop19:\n"
"buffer_load_dwordx4 v[80:83], v5, s[36:39], s81 offen offset:1024 sc1\n"
"s_waitcnt vmcnt(0)\n"
"v_max_u32 v24, v81, v83\n"
"v_cmp_le_u32 vcc, 0xffc00000, v24\n"
"s_cbranch_vccz .Lgs_done19\n"
"s_sleep 1\n"
"s_add_u32 s24, s24, 1\n"
"s_cmp_lt_u32 s24, 0x10000\n"
"s_cbranch_scc1 .Lgs_loop19\n"
".Lgs_done19:\n"
"s_branch .Lgs_back19\n"
".Lgs_slow18:\n"
"s_mov_b32 s24, 0\n"
".Lgs_loop18:\n"
"buffer_load_dwordx4 v[136:139], v5, s[36:39], s81 offen offset:0 sc1\n"
"s_waitcnt vmcnt(0)\n"
"v_max_u32 v14, v137, v139\n"
"v_cmp_le_u32 vcc, 0xffc00000, v14\n"
"s_cbranch_vccz .Lgs_done18\n"
"s_sleep 1\n"
"s_add_u32 s24, s24, 1\n"
"s_cmp_lt_u32 s24, 0x10000\n"
"s_cbranch_scc1 .Lgs_loop18\n"
".Lgs_done18:\n"
"s_branch .Lgs_back18\n"
".Lgs_slow17:\n"
"s_mov_b32 s24, 0\n"
".Lgs_loop17:\n"
"buffer_load_dword v14, v20, s[36:39], 0 offen offset:0 sc1\n"
"s_waitcnt vmcnt(0)\n"
"v_cmp_ne_u32 vcc, 32, v14\n"
"s_cbranch_vccz .Lgs_done17\n"
"s_sleep 2\n"
"s_add_u32 s24, s24, 1\n"
"s_cmp_lt_u32 s24, 0x10000\n"
"s_cbranch_scc1 .Lgs_loop17\n"
".Lgs_done17:\n"
"s_branch .Lgs_back17\n"
".Lgs_slow16:\n"
"s_mov_b32 s24, 0\n"
".Lgs_loop16:\n"
"buffer_load_dwordx4 v[72:75], v17, s[36:39], s56 offen sc1\n"
"buffer_load_dwordx4 v[152:155], v17, s[36:39], s57 offen sc1\n"
"buffer_load_dwordx4 v[52:55], v17, s[36:39], s58 offen sc1\n"
"buffer_load_dwordx4 v[244:247], v17, s[36:39], s59 offen sc1\n"
"buffer_load_dwordx4 v[116:119], v17, s[36:39], s60 offen sc1\n"
"buffer_load_dwordx4 v[196:199], v17, s[36:39], s61 offen sc1\n"
"buffer_load_dwordx4 v[236:239], v17, s[36:39], s62 offen sc1\n"
"buffer_load_dwordx4 v[56:59], v17, s[36:39], s63 offen sc1\n"
"buffer_load_dwordx4 v[180:183], v17, s[36:39], s64 offen sc1\n"
"s_waitcnt vmcnt(0)\n"
"v_max3_u32 v19, v73, v75, v153\n"
"v_max3_u32 v19, v155, v53, v19\n"
"v_max3_u32 v19, v55, v245, v19\n"
"v_max3_u32 v19, v247, v117, v19\n"
"v_max3_u32 v19, v119, v197, v19\n"
"v_max3_u32 v19, v199, v237, v19\n"
"v_max3_u32 v19, v239, v57, v19\n"
"v_max3_u32 v19, v59, v181, v19\n"
"v_max_u32 v19, v183, v19\n"
"v_cmp_le_u32 vcc, 0xffc00000, v19\n"
"s_cbranch_vccz .Lgs_done16\n"
"s_sleep 4\n"
"s_add_u32 s24, s24, 1\n"
"s_cmp_lt_u32 s24, 0x10000\n"
"s_cbranch_scc1 .Lgs_loop16\n"
".Lgs_done16:\n"
"v_mfma_f32_16x16x32_bf16 v[216:219], v[72:75], v[208:211], 0\n"
"v_mfma_f32_16x16x32_bf16 v[216:219], v[152:155], v[184:187], v[216:219]\n"
"v_mfma_f32_16x16x32_bf16 v[216:219], v[52:55], v[232:235], v[216:219]\n"
"v_mfma_f32_16x16x32_bf16 v[216:219], v[244:247], v[248:251], v[216:219]\n"
"v_mfma_f32_16x16x32_bf16 v[216:219], v[116:119], a[24:27], v[216:219]\n"
"v_mfma_f32_16x16x32_bf16 v[216:219], v[196:199], a[152:155], v[216:219]\n"
"v_mfma_f32_16x16x32_bf16 v[216:219], v[236:239], a[28:31], v[216:219]\n"
"v_mfma_f32_16x16x32_bf16 v[216:219], v[56:59], a[156:159], v[216:219]\n"
"v_mfma_f32_16x16x32_bf16 v[216:219], v[180:183], a[16:19], v[216:219]\n"
"v_mfma_f32_16x16x32_bf16 v[128:131], v[72:75], v[204:207], 0\n"
"v_mfma_f32_16x16x32_bf16 v[128:131], v[152:155], v[168:171], v[128:131]\n"
"v_mfma_f32_16x16x32_bf16 v[128:131], v[52:55], v[156:159], v[128:131]\n"
"v_mfma_f32_16x16x32_bf16 v[128:131], v[244:247], v[68:71], v[128:131]\n"
"v_mfma_f32_16x16x32_bf16 v[128:131], v[116:119], a[56:59], v[128:131]\n"
"v_mfma_f32_16x16x32_bf16 v[128:131], v[196:199], a[184:187], v[128:131]\n"
"v_mfma_f32_16x16x32_bf16 v[128:131], v[236:239], a[60:63], v[128:131]\n"
"v_mfma_f32_16x16x32_bf16 v[128:131], v[56:59], a[188:191], v[128:131]\n"
"v_mfma_f32_16x16x32_bf16 v[128:131], v[180:183], a[48:51], v[128:131]\n"
"s_nop 7\n"
"s_waitcnt vmcnt(0)\n"
"s_branch .Lgs_back16\n"
".Lgs_slow15:\n"
"s_mov_b32 s24, 0\n"
".Lgs_loop15:\n"
"buffer_load_dwordx4 v[220:223], v19, s[36:39], s56 offen sc1\n"
"buffer_load_dwordx4 v[100:103], v19, s[36:39], s57 offen sc1\n"
"buffer_load_dwordx4 v[60:63], v19, s[36:39], s58 offen sc1\n"
"buffer_load_dwordx4 v[164:167], v19, s[36:39], s59 offen sc1\n"
"buffer_load_dwordx4 v[252:255], v19, s[36:39], s60 offen sc1\n"
"buffer_load_dwordx4 v[136:139], v19, s[36:39], s61 offen sc1\n"
"buffer_load_dwordx4 v[160:163], v19, s[36:39], s62 offen sc1\n"
"buffer_load_dwordx4 v[80:83], v19, s[36:39], s63 offen sc1\n"
"buffer_load_dwordx4 v[76:79], v19, s[36:39], s64 offen sc1\n"
"buffer_load_dwordx4 v[72:75], v17, s[36:39], s56 offen sc1\n"
"buffer_load_dwordx4 v[152:155], v17, s[36:39], s57 offen sc1\n"
"buffer_load_dwordx4 v[52:55], v17, s[36:39], s58 offen sc1\n"
"buffer_load_dwordx4 v[244:247], v17, s[36:39], s59 offen sc1\n"
"buffer_load_dwordx4 v[116:119], v17, s[36:39], s60 offen sc1\n"
"buffer_load_dwordx4 v[196:199], v17, s[36:39], s61 offen sc1\n"
"buffer_load_dwordx4 v[236:239], v17, s[36:39], s62 offen sc1\n"
"buffer_load_dwordx4 v[56:59], v17, s[36:39], s63 offen sc1\n"
"buffer_load_dwordx4 v[180:183], v17, s[36:39], s64 offen sc1\n"
"s_waitcnt vmcnt(9)\n"
"v_max3_u32 v216, v221, v223, v101\n"
"v_max3_u32 v216, v103, v61, v216\n"
"v_max3_u32 v216, v63, v165, v216\n"
"v_max3_u32 v216, v167, v253, v216\n"
"v_max3_u32 v216, v255, v137, v216\n"
"v_max3_u32 v216, v139, v161, v216\n"
"v_max3_u32 v216, v163, v81, v216\n"
"v_max3_u32 v216, v83, v77, v216\n"
"v_max_u32 v216, v79, v216\n"
"v_cmp_le_u32 vcc, 0xffc00000, v216\n"
"s_cbranch_vccz .Lgs_done15\n"
"s_sleep 4\n"
"s_add_u32 s24, s24, 1\n"
"s_cmp_lt_u32 s24, 0x10000\n"
"s_cbranch_scc1 .Lgs_loop15\n"
".Lgs_done15:\n"
"v_mfma_f32_16x16x32_bf16 v[144:147], v[220:223], v[208:211], 0\n"
"v_mfma_f32_16x16x32_bf16 v[144:147], v[100:103], v[184:187], v[144:147]\n"
"v_mfma_f32_16x16x32_bf16 v[144:147], v[60:63], v[232:235], v[144:147]\n"
"v_mfma_f32_16x16x32_bf16 v[144:147], v[164:167], v[248:251], v[144:147]\n"
"v_mfma_f32_16x16x32_bf16 v[144:147], v[252:255], a[24:27], v[144:147]\n"
"v_mfma_f32_16x16x32_bf16 v[144:147], v[136:139], a[152:155], v[144:147]\n"
"v_mfma_f32_16x16x32_bf16 v[144:147], v[160:163], a[28:31], v[144:147]\n"
"v_mfma_f32_16x16x32_bf16 v[144:147], v[80:83], a[156:159], v[144:147]\n"
"v_mfma_f32_16x16x32_bf16 v[144:147], v[76:79], a[16:19], v[144:147]\n"
"v_mfma_f32_16x16x32_bf16 v[176:179], v[220:223], v[204:207], 0\n"
"v_mfma_f32_16x16x32_bf16 v[176:179], v[100:103], v[168:171], v[176:179]\n"
"v_mfma_f32_16x16x32_bf16 v[176:179], v[60:63], v[156:159], v[176:179]\n"
"v_mfma_f32_16x16x32_bf16 v[176:179], v[164:167], v[68:71], v[176:179]\n"
"v_mfma_f32_16x16x32_bf16 v[176:179], v[252:255], a[56:59], v[176:179]\n"
"v_mfma_f32_16x16x32_bf16 v[176:179], v[136:139], a[184:187], v[176:179]\n"
"v_mfma_f32_16x16x32_bf16 v[176:179], v[160:163], a[60:63], v[176:179]\n"
"v_mfma_f32_16x16x32_bf16 v[176:179], v[80:83], a[188:191], v[176:179]\n"
"v_mfma_f32_16x16x32_bf16 v[176:179], v[76:79], a[48:51], v[176:179]\n"
"s_nop 7\n"
"s_waitcnt vmcnt(0)\n"
"s_branch .Lgs_back15\n"
".Lgs_slow14:\n"
"s_mov_b32 s24, 0\n"
".Lgs_loop14:\n"
"buffer_load_dwordx4 v[188:191], v20, s[36:39], s56 offen sc1\n"
"buffer_load_dwordx4 v[172:175], v20, s[36:39], s57 offen sc1\n"
"buffer_load_dwordx4 v[8:11], v20, s[36:39], s58 offen sc1\n"
"buffer_load_dwordx4 v[128:131], v20, s[36:39], s59 offen sc1\n"
"buffer_load_dwordx4 v[176:179], v20, s[36:39], s60 offen sc1\n"
"buffer_load_dwordx4 v[144:147], v20, s[36:39], s61 offen sc1\n"
"buffer_load_dwordx4 v[124:127], v20, s[36:39], s62 offen sc1\n"
"buffer_load_dwordx4 v[84:87], v20, s[36:39], s63 offen sc1\n"
"buffer_load_dwordx4 v[224:227], v20, s[36:39], s64 offen sc1\n"
"buffer_load_dwordx4 v[220:223], v19, s[36:39], s56 offen sc1\n"
"buffer_load_dwordx4 v[100:103], v19, s[36:39], s57 offen sc1\n"
"buffer_load_dwordx4 v[60:63], v19, s[36:39], s58 offen sc1\n"
"buffer_load_dwordx4 v[164:167], v19, s[36:39], s59 offen sc1\n"
"buffer_load_dwordx4 v[252:255], v19, s[36:39], s60 offen sc1\n"
"buffer_load_dwordx4 v[136:139], v19, s[36:39], s61 offen sc1\n"
"buffer_load_dwordx4 v[160:163], v19, s[36:39], s62 offen sc1\n"
"buffer_load_dwordx4 v[80:83], v19, s[36:39], s63 offen sc1\n"
"s_waitcnt vmcnt(8)\n"
"v_max3_u32 v17, v189, v191, v173\n"
"v_max3_u32 v17, v175, v9, v17\n"
"v_max3_u32 v17, v11, v129, v17\n"
"v_max3_u32 v17, v131, v177, v17\n"
"v_max3_u32 v17, v179, v145, v17\n"
"v_max3_u32 v17, v147, v125, v17\n"
"v_max3_u32 v17, v127, v85, v17\n"
"v_max3_u32 v17, v87, v225, v17\n"
"v_max_u32 v17, v227, v17\n"
"v_cmp_le_u32 vcc, 0xffc00000, v17\n"
"s_cbranch_vccz .Lgs_done14\n"
"s_sleep 4\n"
"s_add_u32 s24, s24, 1\n"
"s_cmp_lt_u32 s24, 0x10000\n"
"s_cbranch_scc1 .Lgs_loop14\n"
".Lgs_done14:\n"
"v_mfma_f32_16x16x32_bf16 v[120:123], v[188:191], v[208:211], 0\n"
"v_mfma_f32_16x16x32_bf16 v[120:123], v[172:175], v[184:187], v[120:123]\n"
"v_mfma_f32_16x16x32_bf16 v[120:123], v[8:11], v[232:235], v[120:123]\n"
"v_mfma_f32_16x16x32_bf16 v[120:123], v[128:131], v[248:251], v[120:123]\n"
"v_mfma_f32_16x16x32_bf16 v[120:123], v[176:179], a[24:27], v[120:123]\n"
"v_mfma_f32_16x16x32_bf16 v[120:123], v[144:147], a[152:155], v[120:123]\n"
"v_mfma_f32_16x16x32_bf16 v[120:123], v[124:127], a[28:31], v[120:123]\n"
"v_mfma_f32_16x16x32_bf16 v[120:123], v[84:87], a[156:159], v[120:123]\n"
"v_mfma_f32_16x16x32_bf16 v[120:123], v[224:227], a[16:19], v[120:123]\n"
"v_mfma_f32_16x16x32_bf16 v[76:79], v[188:191], v[204:207], 0\n"
"v_mfma_f32_16x16x32_bf16 v[76:79], v[172:175], v[168:171], v[76:79]\n"
"v_mfma_f32_16x16x32_bf16 v[76:79], v[8:11], v[156:159], v[76:79]\n"
"v_mfma_f32_16x16x32_bf16 v[76:79], v[128:131], v[68:71], v[76:79]\n"
"v_mfma_f32_16x16x32_bf16 v[76:79], v[176:179], a[56:59], v[76:79]\n"
"v_mfma_f32_16x16x32_bf16 v[76:79], v[144:147], a[184:187], v[76:79]\n"
"v_mfma_f32_16x16x32_bf16 v[76:79], v[124:127], a[60:63], v[76:79]\n"
"v_mfma_f32_16x16x32_bf16 v[76:79], v[84:87], a[188:191], v[76:79]\n"
"v_mfma_f32_16x16x32_bf16 v[76:79], v[224:227], a[48:51], v[76:79]\n"
"s_nop 7\n"
"s_waitcnt vmcnt(0)\n"
"s_branch .Lgs_back14\n"
".Lgs_slow13:\n"
"s_mov_b32 s24, 0\n"
".Lgs_loop13:\n"
"buffer_load_dwordx4 v[216:219], v17, s[36:39], s56 offen sc1\n"
"buffer_load_dwordx4 v[108:111], v17, s[36:39], s57 offen sc1\n"
"buffer_load_dwordx4 v[32:35], v17, s[36:39], s58 offen sc1\n"
"buffer_load_dwordx4 v[244:247], v17, s[36:39], s59 offen sc1\n"
"buffer_load_dwordx4 v[236:239], v17, s[36:39], s60 offen sc1\n"
"buffer_load_dwordx4 v[116:119], v17, s[36:39], s61 offen sc1\n"
"buffer_load_dwordx4 v[92:95], v17, s[36:39], s62 offen sc1\n"
"buffer_load_dwordx4 v[88:91], v17, s[36:39], s63 offen sc1\n"
"buffer_load_dwordx4 v[196:199], v17, s[36:39], s64 offen sc1\n"
"buffer_load_dwordx4 v[188:191], v20, s[36:39], s56 offen sc1\n"
"buffer_load_dwordx4 v[172:175], v20, s[36:39], s57 offen sc1\n"
"buffer_load_dwordx4 v[8:11], v20, s[36:39], s58 offen sc1\n"
"buffer_load_dwordx4 v[128:131], v20, s[36:39], s59 offen sc1\n"
"buffer_load_dwordx4 v[176:179], v20, s[36:39], s60 offen sc1\n"
"buffer_load_dwordx4 v[144:147], v20, s[36:39], s61 offen sc1\n"
"buffer_load_dwordx4 v[124:127], v20, s[36:39], s62 offen sc1\n"
"buffer_load_dwordx4 v[84:87], v20, s[36:39], s63 offen sc1\n"
"buffer_load_dwordx4 v[224:227], v20, s[36:39], s64 offen sc1\n"
"buffer_load_dwordx4 v[220:223], v19, s[36:39], s56 offen sc1\n"
"s_waitcnt vmcnt(10)\n"
"v_max3_u32 v13, v217, v219, v109\n"
"v_max3_u32 v13, v111, v33, v13\n"
"v_max3_u32 v13, v35, v245, v13\n"
"v_max3_u32 v13, v247, v237, v13\n"
"v_max3_u32 v13, v239, v117, v13\n"
"v_max3_u32 v13, v119, v93, v13\n"
"v_max3_u32 v13, v95, v89, v13\n"
"v_max3_u32 v13, v91, v197, v13\n"
"v_max_u32 v13, v199, v13\n"
"v_cmp_le_u32 vcc, 0xffc00000, v13\n"
"s_cbranch_vccz .Lgs_done13\n"
"s_sleep 4\n"
"s_add_u32 s24, s24, 1\n"
"s_cmp_lt_u32 s24, 0x10000\n"
"s_cbranch_scc1 .Lgs_loop13\n"
".Lgs_done13:\n"
"v_mfma_f32_16x16x32_bf16 v[160:163], v[216:219], v[208:211], 0\n"
"v_mfma_f32_16x16x32_bf16 v[160:163], v[108:111], v[184:187], v[160:163]\n"
"v_mfma_f32_16x16x32_bf16 v[160:163], v[32:35], v[232:235], v[160:163]\n"
"v_mfma_f32_16x16x32_bf16 v[160:163], v[244:247], v[248:251], v[160:163]\n"
"v_mfma_f32_16x16x32_bf16 v[160:163], v[236:239], a[24:27], v[160:163]\n"
"v_mfma_f32_16x16x32_bf16 v[160:163], v[116:119], a[152:155], v[160:163]\n"
"v_mfma_f32_16x16x32_bf16 v[160:163], v[92:95], a[28:31], v[160:163]\n"
"v_mfma_f32_16x16x32_bf16 v[160:163], v[88:91], a[156:159], v[160:163]\n"
"v_mfma_f32_16x16x32_bf16 v[160:163], v[196:199], a[16:19], v[160:163]\n"
"v_mfma_f32_16x16x32_bf16 v[100:103], v[216:219], v[204:207], 0\n"
"v_mfma_f32_16x16x32_bf16 v[100:103], v[108:111], v[168:171], v[100:103]\n"
"v_mfma_f32_16x16x32_bf16 v[100:103], v[32:35], v[156:159], v[100:103]\n"
"v_mfma_f32_16x16x32_bf16 v[100:103], v[244:247], v[68:71], v[100:103]\n"
"v_mfma_f32_16x16x32_bf16 v[100:103], v[236:239], a[56:59], v[100:103]\n"
"v_mfma_f32_16x16x32_bf16 v[100:103], v[116:119], a[184:187], v[100:103]\n"
"v_mfma_f32_16x16x32_bf16 v[100:103], v[92:95], a[60:63], v[100:103]\n"
"v_mfma_f32_16x16x32_bf16 v[100:103], v[88:91], a[188:191], v[100:103]\n"
"v_mfma_f32_16x16x32_bf16 v[100:103], v[196:199], a[48:51], v[100:103]\n"
"s_nop 7\n"
"s_waitcnt vmcnt(0)\n"
"s_branch .Lgs_back13\n"
".Lgs_slow12:\n"
"s_mov_b32 s24, 0\n"
".Lgs_loop12:\n"
"buffer_load_dwordx4 v[164:167], v13, s[36:39], s56 offen sc1\n"
"buffer_load_dwordx4 v[76:79], v13, s[36:39], s57 offen sc1\n"
"buffer_load_dwordx4 v[36:39], v13, s[36:39], s58 offen sc1\n"
"buffer_load_dwordx4 v[136:139], v13, s[36:39], s59 offen sc1\n"
"buffer_load_dwordx4 v[252:255], v13, s[36:39], s60 offen sc1\n"
"buffer_load_dwordx4 v[180:183], v13, s[36:39], s61 offen sc1\n"
"buffer_load_dwordx4 v[152:155], v13, s[36:39], s62 offen sc1\n"
"buffer_load_dwordx4 v[112:115], v13, s[36:39], s63 offen sc1\n"
"buffer_load_dwordx4 v[80:83], v13, s[36:39], s64 offen sc1\n"
"buffer_load_dwordx4 v[216:219], v17, s[36:39], s56 offen sc1\n"
"buffer_load_dwordx4 v[108:111], v17, s[36:39], s57 offen sc1\n"
"buffer_load_dwordx4 v[32:35], v17, s[36:39], s58 offen sc1\n"
"buffer_load_dwordx4 v[244:247], v17, s[36:39], s59 offen sc1\n"
"buffer_load_dwordx4 v[236:239], v17, s[36:39], s60 offen sc1\n"
"buffer_load_dwordx4 v[116:119], v17, s[36:39], s61 offen sc1\n"
"buffer_load_dwordx4 v[92:95], v17, s[36:39], s62 offen sc1\n"
"buffer_load_dwordx4 v[88:91], v17, s[36:39], s63 offen sc1\n"
"buffer_load_dwordx4 v[196:199], v17, s[36:39], s64 offen sc1\n"
"buffer_load_dwordx4 v[188:191], v20, s[36:39], s56 offen sc1\n"
"buffer_load_dwordx4 v[172:175], v20, s[36:39], s57 offen sc1\n"
"s_waitcnt vmcnt(11)\n"
"v_max3_u32 v19, v165, v167, v77\n"
"v_max3_u32 v19, v79, v37, v19\n"
"v_max3_u32 v19, v39, v137, v19\n"
"v_max3_u32 v19, v139, v253, v19\n"
"v_max3_u32 v19, v255, v181, v19\n"
"v_max3_u32 v19, v183, v153, v19\n"
"v_max3_u32 v19, v155, v113, v19\n"
"v_max3_u32 v19, v115, v81, v19\n"
"v_max_u32 v19, v83, v19\n"
"v_cmp_le_u32 vcc, 0xffc00000, v19\n"
"s_cbranch_vccz .Lgs_done12\n"
"s_sleep 4\n"
"s_add_u32 s24, s24, 1\n"
"s_cmp_lt_u32 s24, 0x10000\n"
"s_cbranch_scc1 .Lgs_loop12\n"
".Lgs_done12:\n"
"v_mfma_f32_16x16x32_bf16 v[124:127], v[164:167], v[208:211], 0\n"
"v_mfma_f32_16x16x32_bf16 v[124:127], v[76:79], v[184:187], v[124:127]\n"
"v_mfma_f32_16x16x32_bf16 v[124:127], v[36:39], v[232:235], v[124:127]\n"
"v_mfma_f32_16x16x32_bf16 v[124:127], v[136:139], v[248:251], v[124:127]\n"
"v_mfma_f32_16x16x32_bf16 v[124:127], v[252:255], a[24:27], v[124:127]\n"
"v_mfma_f32_16x16x32_bf16 v[124:127], v[180:183], a[152:155], v[124:127]\n"
"v_mfma_f32_16x16x32_bf16 v[124:127], v[152:155], a[28:31], v[124:127]\n"
"v_mfma_f32_16x16x32_bf16 v[124:127], v[112:115], a[156:159], v[124:127]\n"
"v_mfma_f32_16x16x32_bf16 v[124:127], v[80:83], a[16:19], v[124:127]\n"
"v_mfma_f32_16x16x32_bf16 v[72:75], v[164:167], v[204:207], 0\n"
"v_mfma_f32_16x16x32_bf16 v[72:75], v[76:79], v[168:171], v[72:75]\n"
"v_mfma_f32_16x16x32_bf16 v[72:75], v[36:39], v[156:159], v[72:75]\n"
"v_mfma_f32_16x16x32_bf16 v[72:75], v[136:139], v[68:71], v[72:75]\n"
"v_mfma_f32_16x16x32_bf16 v[72:75], v[252:255], a[56:59], v[72:75]\n"
"v_mfma_f32_16x16x32_bf16 v[72:75], v[180:183], a[184:187], v[72:75]\n"
"v_mfma_f32_16x16x32_bf16 v[72:75], v[152:155], a[60:63], v[72:75]\n"
"v_mfma_f32_16x16x32_bf16 v[72:75], v[112:115], a[188:191], v[72:75]\n"
"v_mfma_f32_16x16x32_bf16 v[72:75], v[80:83], a[48:51], v[72:75]\n"
"s_nop 7\n"
"s_waitcnt vmcnt(0)\n"
"s_branch .Lgs_back12\n"
".Lgs_slow11:\n"
"s_mov_b32 s24, 0\n"
".Lgs_loop11:\n"
"buffer_load_dwordx4 v[128:131], v14, s[36:39], s56 offen sc1\n"
"buffer_load_dwordx4 v[52:55], v14, s[36:39], s57 offen sc1\n"
"buffer_load_dwordx4 v[40:43], v14, s[36:39], s58 offen sc1\n"
"buffer_load_dwordx4 v[224:227], v14, s[36:39], s59 offen sc1\n"
"buffer_load_dwordx4 v[176:179], v14, s[36:39], s60 offen sc1\n"
"buffer_load_dwordx4 v[220:223], v14, s[36:39], s61 offen sc1\n"
"buffer_load_dwordx4 v[160:163], v14, s[36:39], s62 offen sc1\n"
"buffer_load_dwordx4 v[144:147], v14, s[36:39], s63 offen sc1\n"
"buffer_load_dwordx4 v[100:103], v14, s[36:39], s64 offen sc1\n"
"buffer_load_dwordx4 v[164:167], v13, s[36:39], s56 offen sc1\n"
"buffer_load_dwordx4 v[76:79], v13, s[36:39], s57 offen sc1\n"
"buffer_load_dwordx4 v[36:39], v13, s[36:39], s58 offen sc1\n"
"buffer_load_dwordx4 v[136:139], v13, s[36:39], s59 offen sc1\n"
"buffer_load_dwordx4 v[252:255], v13, s[36:39], s60 offen sc1\n"
"buffer_load_dwordx4 v[180:183], v13, s[36:39], s61 offen sc1\n"
"buffer_load_dwordx4 v[152:155], v13, s[36:39], s62 offen sc1\n"
"buffer_load_dwordx4 v[112:115], v13, s[36:39], s63 offen sc1\n"
"buffer_load_dwordx4 v[80:83], v13, s[36:39], s64 offen sc1\n"
"buffer_load_dwordx4 v[216:219], v17, s[36:39], s56 offen sc1\n"
"s_waitcnt vmcnt(10)\n"
"v_max3_u32 v12, v129, v131, v53\n"
"v_max3_u32 v12, v55, v41, v12\n"
"v_max3_u32 v12, v43, v225, v12\n"
"v_max3_u32 v12, v227, v177, v12\n"
"v_max3_u32 v12, v179, v221, v12\n"
"v_max3_u32 v12, v223, v161, v12\n"
"v_max3_u32 v12, v163, v145, v12\n"
"v_max3_u32 v12, v147, v101, v12\n"
"v_max_u32 v12, v103, v12\n"
"v_cmp_le_u32 vcc, 0xffc00000, v12\n"
"s_cbranch_vccz .Lgs_done11\n"
"s_sleep 4\n"
"s_add_u32 s24, s24, 1\n"
"s_cmp_lt_u32 s24, 0x10000\n"
"s_cbranch_scc1 .Lgs_loop11\n"
".Lgs_done11:\n"
"v_mfma_f32_16x16x32_bf16 v[188:191], v[128:131], v[208:211], 0\n"
"v_mfma_f32_16x16x32_bf16 v[188:191], v[52:55], v[184:187], v[188:191]\n"
"v_mfma_f32_16x16x32_bf16 v[188:191], v[40:43], v[232:235], v[188:191]\n"
"v_mfma_f32_16x16x32_bf16 v[188:191], v[224:227], v[248:251], v[188:191]\n"
"v_mfma_f32_16x16x32_bf16 v[188:191], v[176:179], a[24:27], v[188:191]\n"
"v_mfma_f32_16x16x32_bf16 v[188:191], v[220:223], a[152:155], v[188:191]\n"
"v_mfma_f32_16x16x32_bf16 v[188:191], v[160:163], a[28:31], v[188:191]\n"
"v_mfma_f32_16x16x32_bf16 v[188:191], v[144:147], a[156:159], v[188:191]\n"
"v_mfma_f32_16x16x32_bf16 v[188:191], v[100:103], a[16:19], v[188:191]\n"
"v_mfma_f32_16x16x32_bf16 v[108:111], v[128:131], v[204:207], 0\n"
"v_mfma_f32_16x16x32_bf16 v[108:111], v[52:55], v[168:171], v[108:111]\n"
"v_mfma_f32_16x16x32_bf16 v[108:111], v[40:43], v[156:159], v[108:111]\n"
"v_mfma_f32_16x16x32_bf16 v[108:111], v[224:227], v[68:71], v[108:111]\n"
"v_mfma_f32_16x16x32_bf16 v[108:111], v[176:179], a[56:59], v[108:111]\n"
"v_mfma_f32_16x16x32_bf16 v[108:111], v[220:223], a[184:187], v[108:111]\n"
"v_mfma_f32_16x16x32_bf16 v[108:111], v[160:163], a[60:63], v[108:111]\n"
"v_mfma_f32_16x16x32_bf16 v[108:111], v[144:147], a[188:191], v[108:111]\n"
"v_mfma_f32_16x16x32_bf16 v[108:111], v[100:103], a[48:51], v[108:111]\n"
"s_nop 7\n"
"s_waitcnt vmcnt(0)\n"
"s_branch .Lgs_back11\n"
".Lgs_slow10:\n"
"s_mov_b32 s24, 0\n"
".Lgs_loop10:\n"
"buffer_load_dwordx4 v[244:247], v17, s[36:39], s56 offen sc1\n"
"buffer_load_dwordx4 v[116:119], v17, s[36:39], s57 offen sc1\n"
"buffer_load_dwordx4 v[92:95], v17, s[36:39], s58 offen sc1\n"
"buffer_load_dwordx4 v[196:199], v17, s[36:39], s59 offen sc1\n"
"buffer_load_dwordx4 v[124:127], v17, s[36:39], s60 offen sc1\n"
"buffer_load_dwordx4 v[88:91], v17, s[36:39], s61 offen sc1\n"
"buffer_load_dwordx4 v[72:75], v17, s[36:39], s62 offen sc1\n"
"buffer_load_dwordx4 v[64:67], v17, s[36:39], s63 offen sc1\n"
"buffer_load_dwordx4 v[60:63], v17, s[36:39], s64 offen sc1\n"
"buffer_load_dwordx4 v[128:131], v14, s[36:39], s56 offen sc1\n"
"buffer_load_dwordx4 v[52:55], v14, s[36:39], s57 offen sc1\n"
"buffer_load_dwordx4 v[40:43], v14, s[36:39], s58 offen sc1\n"
"buffer_load_dwordx4 v[224:227], v14, s[36:39], s59 offen sc1\n"
"buffer_load_dwordx4 v[176:179], v14, s[36:39], s60 offen sc1\n"
"buffer_load_dwordx4 v[220:223], v14, s[36:39], s61 offen sc1\n"
"buffer_load_dwordx4 v[160:163], v14, s[36:39], s62 offen sc1\n"
"buffer_load_dwordx4 v[144:147], v14, s[36:39], s63 offen sc1\n"
"buffer_load_dwordx4 v[100:103], v14, s[36:39], s64 offen sc1\n"
"buffer_load_dwordx4 v[164:167], v13, s[36:39], s56 offen sc1\n"
"s_waitcnt vmcnt(10)\n"
"v_max3_u32 v19, v245, v247, v117\n"
"v_max3_u32 v19, v119, v93, v19\n"
"v_max3_u32 v19, v95, v197, v19\n"
"v_max3_u32 v19, v199, v125, v19\n"
"v_max3_u32 v19, v127, v89, v19\n"
"v_max3_u32 v19, v91, v73, v19\n"
"v_max3_u32 v19, v75, v65, v19\n"
"v_max3_u32 v19, v67, v61, v19\n"
"v_max_u32 v19, v63, v19\n"
"v_cmp_le_u32 vcc, 0xffc00000, v19\n"
"s_cbranch_vccz .Lgs_done10\n"
"s_sleep 4\n"
"s_add_u32 s24, s24, 1\n"
"s_cmp_lt_u32 s24, 0x10000\n"
"s_cbranch_scc1 .Lgs_loop10\n"
".Lgs_done10:\n"
"v_mfma_f32_16x16x32_bf16 v[216:219], v[244:247], v[208:211], 0\n"
"v_mfma_f32_16x16x32_bf16 v[216:219], v[116:119], v[184:187], v[216:219]\n"
"v_mfma_f32_16x16x32_bf16 v[216:219], v[92:95], v[232:235], v[216:219]\n"
"v_mfma_f32_16x16x32_bf16 v[216:219], v[196:199], v[248:251], v[216:219]\n"
"v_mfma_f32_16x16x32_bf16 v[216:219], v[124:127], a[24:27], v[216:219]\n"
"v_mfma_f32_16x16x32_bf16 v[216:219], v[88:91], a[152:155], v[216:219]\n"
"v_mfma_f32_16x16x32_bf16 v[216:219], v[72:75], a[28:31], v[216:219]\n"
"v_mfma_f32_16x16x32_bf16 v[216:219], v[64:67], a[156:159], v[216:219]\n"
"v_mfma_f32_16x16x32_bf16 v[216:219], v[60:63], a[16:19], v[216:219]\n"
"v_mfma_f32_16x16x32_bf16 v[120:123], v[244:247], v[204:207], 0\n"
"v_mfma_f32_16x16x32_bf16 v[120:123], v[116:119], v[168:171], v[120:123]\n"
"v_mfma_f32_16x16x32_bf16 v[120:123], v[92:95], v[156:159], v[120:123]\n"
"v_mfma_f32_16x16x32_bf16 v[120:123], v[196:199], v[68:71], v[120:123]\n"
"v_mfma_f32_16x16x32_bf16 v[120:123], v[124:127], a[56:59], v[120:123]\n"
"v_mfma_f32_16x16x32_bf16 v[120:123], v[88:91], a[184:187], v[120:123]\n"
"v_mfma_f32_16x16x32_bf16 v[120:123], v[72:75], a[60:63], v[120:123]\n"
"v_mfma_f32_16x16x32_bf16 v[120:123], v[64:67], a[188:191], v[120:123]\n"
"v_mfma_f32_16x16x32_bf16 v[120:123], v[60:63], a[48:51], v[120:123]\n"
"s_nop 7\n"
"s_waitcnt vmcnt(0)\n"
"s_branch .Lgs_back10\n"
".Lgs_slow9:\n"
"s_mov_b32 s24, 0\n"
".Lgs_loop9:\n"
"buffer_load_dwordx4 v[136:139], v4, s[36:39], s56 offen sc1\n"
"buffer_load_dwordx4 v[80:83], v4, s[36:39], s57 offen sc1\n"
"buffer_load_dwordx4 v[180:183], v4, s[36:39], s58 offen sc1\n"
"buffer_load_dwordx4 v[152:155], v4, s[36:39], s59 offen sc1\n"
"buffer_load_dwordx4 v[236:239], v4, s[36:39], s60 offen sc1\n"
"buffer_load_dwordx4 v[252:255], v4, s[36:39], s61 offen sc1\n"
"buffer_load_dwordx4 v[188:191], v4, s[36:39], s62 offen sc1\n"
"buffer_load_dwordx4 v[112:115], v4, s[36:39], s63 offen sc1\n"
"buffer_load_dwordx4 v[108:111], v4, s[36:39], s64 offen sc1\n"
"buffer_load_dwordx4 v[244:247], v17, s[36:39], s56 offen sc1\n"
"buffer_load_dwordx4 v[116:119], v17, s[36:39], s57 offen sc1\n"
"buffer_load_dwordx4 v[92:95], v17, s[36:39], s58 offen sc1\n"
"buffer_load_dwordx4 v[196:199], v17, s[36:39], s59 offen sc1\n"
"s_waitcnt vmcnt(4)\n"
"v_max3_u32 v14, v137, v139, v81\n"
"v_max3_u32 v14, v83, v181, v14\n"
"v_max3_u32 v14, v183, v153, v14\n"
"v_max3_u32 v14, v155, v237, v14\n"
"v_max3_u32 v14, v239, v253, v14\n"
"v_max3_u32 v14, v255, v189, v14\n"
"v_max3_u32 v14, v191, v113, v14\n"
"v_max3_u32 v14, v115, v109, v14\n"
"v_max_u32 v14, v111, v14\n"
"v_cmp_le_u32 vcc, 0xffc00000, v14\n"
"s_cbranch_vccz .Lgs_done9\n"
"s_sleep 4\n"
"s_add_u32 s24, s24, 1\n"
"s_cmp_lt_u32 s24, 0x10000\n"
"s_cbranch_scc1 .Lgs_loop9\n"
".Lgs_done9:\n"
"v_mfma_f32_16x16x32_bf16 v[160:163], v[136:139], v[208:211], 0\n"
"v_mfma_f32_16x16x32_bf16 v[160:163], v[80:83], v[184:187], v[160:163]\n"
"v_mfma_f32_16x16x32_bf16 v[160:163], v[180:183], v[232:235], v[160:163]\n"
"v_mfma_f32_16x16x32_bf16 v[160:163], v[152:155], v[248:251], v[160:163]\n"
"v_mfma_f32_16x16x32_bf16 v[160:163], v[236:239], a[24:27], v[160:163]\n"
"v_mfma_f32_16x16x32_bf16 v[160:163], v[252:255], a[152:155], v[160:163]\n"
"v_mfma_f32_16x16x32_bf16 v[160:163], v[188:191], a[28:31], v[160:163]\n"
"v_mfma_f32_16x16x32_bf16 v[160:163], v[112:115], a[156:159], v[160:163]\n"
"v_mfma_f32_16x16x32_bf16 v[160:163], v[108:111], a[16:19], v[160:163]\n"
"v_mfma_f32_16x16x32_bf16 v[224:227], v[136:139], v[204:207], 0\n"
"v_mfma_f32_16x16x32_bf16 v[224:227], v[80:83], v[168:171], v[224:227]\n"
"v_mfma_f32_16x16x32_bf16 v[224:227], v[180:183], v[156:159], v[224:227]\n"
"v_mfma_f32_16x16x32_bf16 v[224:227], v[152:155], v[68:71], v[224:227]\n"
"v_mfma_f32_16x16x32_bf16 v[224:227], v[236:239], a[56:59], v[224:227]\n"
"v_mfma_f32_16x16x32_bf16 v[224:227], v[252:255], a[184:187], v[224:227]\n"
"v_mfma_f32_16x16x32_bf16 v[224:227], v[188:191], a[60:63], v[224:227]\n"
"v_mfma_f32_16x16x32_bf16 v[224:227], v[112:115], a[188:191], v[224:227]\n"
"v_mfma_f32_16x16x32_bf16 v[224:227], v[108:111], a[48:51], v[224:227]\n"
"s_nop 7\n"
"s_waitcnt vmcnt(0)\n"
"s_branch .Lgs_back9\n"
".Lgs_slow8:\n"
"s_mov_b32 s24, 0\n"
".Lgs_loop8:\n"
"buffer_load_dwordx4 v[100:103], v24, s[36:39], s56 offen sc1\n"
"buffer_load_dwordx4 v[76:79], v24, s[36:39], s57 offen sc1\n"
"buffer_load_dwordx4 v[144:147], v24, s[36:39], s58 offen sc1\n"
"buffer_load_dwordx4 v[56:59], v24, s[36:39], s59 offen sc1\n"
"buffer_load_dwordx4 v[176:179], v24, s[36:39], s60 offen sc1\n"
"buffer_load_dwordx4 v[172:175], v24, s[36:39], s61 offen sc1\n"
"buffer_load_dwordx4 v[164:167], v24, s[36:39], s62 offen sc1\n"
"buffer_load_dwordx4 v[216:219], v24, s[36:39], s63 offen sc1\n"
"buffer_load_dwordx4 v[220:223], v24, s[36:39], s64 offen sc1\n"
"buffer_load_dwordx4 v[136:139], v4, s[36:39], s56 offen sc1\n"
"buffer_load_dwordx4 v[80:83], v4, s[36:39], s57 offen sc1\n"
"buffer_load_dwordx4 v[180:183], v4, s[36:39], s58 offen sc1\n"
"buffer_load_dwordx4 v[152:155], v4, s[36:39], s59 offen sc1\n"
"buffer_load_dwordx4 v[236:239], v4, s[36:39], s60 offen sc1\n"
"buffer_load_dwordx4 v[252:255], v4, s[36:39], s61 offen sc1\n"
"buffer_load_dwordx4 v[188:191], v4, s[36:39], s62 offen sc1\n"
"buffer_load_dwordx4 v[112:115], v4, s[36:39], s63 offen sc1\n"
"buffer_load_dwordx4 v[108:111], v4, s[36:39], s64 offen sc1\n"
"buffer_load_dwordx4 v[244:247], v17, s[36:39], s56 offen sc1\n"
"buffer_load_dwordx4 v[116:119], v17, s[36:39], s57 offen sc1\n"
"s_waitcnt vmcnt(11)\n"
"v_max3_u32 v19, v101, v103, v77\n"
"v_max3_u32 v19, v79, v145, v19\n"
"v_max3_u32 v19, v147, v57, v19\n"
"v_max3_u32 v19, v59, v177, v19\n"
"v_max3_u32 v19, v179, v173, v19\n"
"v_max3_u32 v19, v175, v165, v19\n"
"v_max3_u32 v19, v167, v217, v19\n"
"v_max3_u32 v19, v219, v221, v19\n"
"v_max_u32 v19, v223, v19\n"
"v_cmp_le_u32 vcc, 0xffc00000, v19\n"
"s_cbranch_vccz .Lgs_done8\n"
"s_sleep 4\n"
"s_add_u32 s24, s24, 1\n"
"s_cmp_lt_u32 s24, 0x10000\n"
"s_cbranch_scc1 .Lgs_loop8\n"
".Lgs_done8:\n"
"v_mfma_f32_16x16x32_bf16 v[196:199], v[100:103], v[208:211], 0\n"
"v_mfma_f32_16x16x32_bf16 v[196:199], v[76:79], v[184:187], v[196:199]\n"
"v_mfma_f32_16x16x32_bf16 v[196:199], v[144:147], v[232:235], v[196:199]\n"
"v_mfma_f32_16x16x32_bf16 v[196:199], v[56:59], v[248:251], v[196:199]\n"
"v_mfma_f32_16x16x32_bf16 v[196:199], v[176:179], a[24:27], v[196:199]\n"
"v_mfma_f32_16x16x32_bf16 v[196:199], v[172:175], a[152:155], v[196:199]\n"
"v_mfma_f32_16x16x32_bf16 v[196:199], v[164:167], a[28:31], v[196:199]\n"
"v_mfma_f32_16x16x32_bf16 v[196:199], v[216:219], a[156:159], v[196:199]\n"
"v_mfma_f32_16x16x32_bf16 v[196:199], v[220:223], a[16:19], v[196:199]\n"
"v_mfma_f32_16x16x32_bf16 v[120:123], v[100:103], v[204:207], 0\n"
"v_mfma_f32_16x16x32_bf16 v[120:123], v[76:79], v[168:171], v[120:123]\n"
"v_mfma_f32_16x16x32_bf16 v[120:123], v[144:147], v[156:159], v[120:123]\n"
"v_mfma_f32_16x16x32_bf16 v[120:123], v[56:59], v[68:71], v[120:123]\n"
"v_mfma_f32_16x16x32_bf16 v[120:123], v[176:179], a[56:59], v[120:123]\n"
"v_mfma_f32_16x16x32_bf16 v[120:123], v[172:175], a[184:187], v[120:123]\n"
"v_mfma_f32_16x16x32_bf16 v[120:123], v[164:167], a[60:63], v[120:123]\n"
"v_mfma_f32_16x16x32_bf16 v[120:123], v[216:219], a[188:191], v[120:123]\n"
"v_mfma_f32_16x16x32_bf16 v[120:123], v[220:223], a[48:51], v[120:123]\n"
"s_nop 7\n"
"s_waitcnt vmcnt(0)\n"
"s_branch .Lgs_back8\n"
".Lgs_slow7:\n"
"s_mov_b32 s24, 0\n"
".Lgs_loop7:\n"
"buffer_load_dwordx4 v[104:107], v3, s[36:39], s48 offen offset:0 sc1\n"
"buffer_load_dwordx4 v[180:183], v3, s[36:39], s49 offen offset:0 sc1\n"
"buffer_load_dwordx4 v[164:167], v3, s[36:39], s50 offen offset:0 sc1\n"
"buffer_load_dwordx4 v[224:227], v3, s[36:39], s51 offen offset:0 sc1\n"
"buffer_load_dwordx4 v[140:143], v3, s[36:39], s52 offen offset:0 sc1\n"
"buffer_load_dwordx4 v[92:95], v3, s[36:39], s53 offen offset:0 sc1\n"
"buffer_load_dwordx4 v[252:255], v3, s[36:39], s54 offen offset:0 sc1\n"
"buffer_load_dwordx4 v[112:115], v3, s[36:39], s55 offen offset:0 sc1\n"
"buffer_load_dwordx4 v[100:103], v4, s[36:39], s48 offen offset:0 sc1\n"
"buffer_load_dwordx4 v[244:247], v4, s[36:39], s49 offen offset:0 sc1\n"
"buffer_load_dwordx4 v[64:67], v4, s[36:39], s50 offen offset:0 sc1\n"
"buffer_load_dwordx4 v[128:131], v4, s[36:39], s51 offen offset:0 sc1\n"
"buffer_load_dwordx4 v[236:239], v4, s[36:39], s52 offen offset:0 sc1\n"
"buffer_load_dwordx4 v[120:123], v4, s[36:39], s53 offen offset:0 sc1\n"
"buffer_load_dwordx4 v[56:59], v4, s[36:39], s54 offen offset:0 sc1\n"
"buffer_load_dwordx4 v[196:199], v4, s[36:39], s55 offen offset:0 sc1\n"
"s_waitcnt vmcnt(8)\n"
"v_max3_u32 v24, v105, v107, v181\n"
"v_max3_u32 v24, v183, v165, v24\n"
"v_max3_u32 v24, v167, v225, v24\n"
"v_max3_u32 v24, v227, v141, v24\n"
"v_max3_u32 v24, v143, v93, v24\n"
"v_max3_u32 v24, v95, v253, v24\n"
"v_max3_u32 v24, v255, v113, v24\n"
"v_max_u32 v24, v115, v24\n"
"v_cmp_le_u32 vcc, 0xffc00000, v24\n"
"s_cbranch_vccz .Lgs_done7\n"
"s_sleep 4\n"
"s_add_u32 s24, s24, 1\n"
"s_cmp_lt_u32 s24, 0x10000\n"
"s_cbranch_scc1 .Lgs_loop7\n"
".Lgs_done7:\n"
"v_mfma_f32_16x16x32_bf16 v[136:139], a[24:27], v[104:107], v[76:79]\n"
"v_mfma_f32_16x16x32_bf16 v[136:139], a[56:59], v[180:183], v[136:139]\n"
"v_mfma_f32_16x16x32_bf16 v[136:139], a[88:91], v[164:167], v[136:139]\n"
"v_mfma_f32_16x16x32_bf16 v[136:139], a[120:123], v[224:227], v[136:139]\n"
"v_mfma_f32_16x16x32_bf16 v[136:139], a[152:155], v[140:143], v[136:139]\n"
"v_mfma_f32_16x16x32_bf16 v[136:139], a[184:187], v[92:95], v[136:139]\n"
"v_mfma_f32_16x16x32_bf16 v[136:139], a[216:219], v[252:255], v[136:139]\n"
"v_mfma_f32_16x16x32_bf16 v[136:139], a[248:251], v[112:115], v[136:139]\n"
"v_mfma_f32_16x16x32_bf16 v[108:111], a[28:31], v[104:107], v[176:179]\n"
"v_mfma_f32_16x16x32_bf16 v[108:111], a[60:63], v[180:183], v[108:111]\n"
"v_mfma_f32_16x16x32_bf16 v[108:111], a[92:95], v[164:167], v[108:111]\n"
"v_mfma_f32_16x16x32_bf16 v[108:111], a[124:127], v[224:227], v[108:111]\n"
"v_mfma_f32_16x16x32_bf16 v[108:111], a[156:159], v[140:143], v[108:111]\n"
"v_mfma_f32_16x16x32_bf16 v[108:111], a[188:191], v[92:95], v[108:111]\n"
"v_mfma_f32_16x16x32_bf16 v[108:111], a[220:223], v[252:255], v[108:111]\n"
"v_mfma_f32_16x16x32_bf16 v[108:111], a[252:255], v[112:115], v[108:111]\n"
"s_nop 7\n"
"s_waitcnt vmcnt(0)\n"
"s_branch .Lgs_back7\n"
".Lgs_slow6:\n"
"s_mov_b32 s24, 0\n"
".Lgs_loop6:\n"
"buffer_load_dwordx4 v[144:147], v2, s[36:39], s48 offen offset:2048 sc1\n"
"buffer_load_dwordx4 v[116:119], v2, s[36:39], s49 offen offset:2048 sc1\n"
"buffer_load_dwordx4 v[220:223], v2, s[36:39], s50 offen offset:2048 sc1\n"
"buffer_load_dwordx4 v[216:219], v2, s[36:39], s51 offen offset:2048 sc1\n"
"buffer_load_dwordx4 v[172:175], v2, s[36:39], s52 offen offset:2048 sc1\n"
"buffer_load_dwordx4 v[160:163], v2, s[36:39], s53 offen offset:2048 sc1\n"
"buffer_load_dwordx4 v[188:191], v2, s[36:39], s54 offen offset:2048 sc1\n"
"buffer_load_dwordx4 v[152:155], v2, s[36:39], s55 offen offset:2048 sc1\n"
"buffer_load_dwordx4 v[104:107], v3, s[36:39], s48 offen offset:0 sc1\n"
"buffer_load_dwordx4 v[180:183], v3, s[36:39], s49 offen offset:0 sc1\n"
"buffer_load_dwordx4 v[164:167], v3, s[36:39], s50 offen offset:0 sc1\n"
"buffer_load_dwordx4 v[224:227], v3, s[36:39], s51 offen offset:0 sc1\n"
"buffer_load_dwordx4 v[140:143], v3, s[36:39], s52 offen offset:0 sc1\n"
"buffer_load_dwordx4 v[92:95], v3, s[36:39], s53 offen offset:0 sc1\n"
"buffer_load_dwordx4 v[252:255], v3, s[36:39], s54 offen offset:0 sc1\n"
"s_waitcnt vmcnt(7)\n"
"v_max3_u32 v1, v145, v147, v117\n"
"v_max3_u32 v1, v119, v221, v1\n"
"v_max3_u32 v1, v223, v217, v1\n"
"v_max3_u32 v1, v219, v173, v1\n"
"v_max3_u32 v1, v175, v161, v1\n"
"v_max3_u32 v1, v163, v189, v1\n"
"v_max3_u32 v1, v191, v153, v1\n"
"v_max_u32 v1, v155, v1\n"
"v_cmp_le_u32 vcc, 0xffc00000, v1\n"
"s_cbranch_vccz .Lgs_done6\n"
"s_sleep 4\n"
"s_add_u32 s24, s24, 1\n"
"s_cmp_lt_u32 s24, 0x10000\n"
"s_cbranch_scc1 .Lgs_loop6\n"
".Lgs_done6:\n"
"v_mfma_f32_16x16x32_bf16 v[248:251], a[24:27], v[144:147], 0\n"
"v_mfma_f32_16x16x32_bf16 v[248:251], a[56:59], v[116:119], v[248:251]\n"
"v_mfma_f32_16x16x32_bf16 v[248:251], a[88:91], v[220:223], v[248:251]\n"
"v_mfma_f32_16x16x32_bf16 v[248:251], a[120:123], v[216:219], v[248:251]\n"
"v_mfma_f32_16x16x32_bf16 v[248:251], a[152:155], v[172:175], v[248:251]\n"
"v_mfma_f32_16x16x32_bf16 v[248:251], a[184:187], v[160:163], v[248:251]\n"
"v_mfma_f32_16x16x32_bf16 v[248:251], a[216:219], v[188:191], v[248:251]\n"
"v_mfma_f32_16x16x32_bf16 v[248:251], a[248:251], v[152:155], v[248:251]\n"
"v_mfma_f32_16x16x32_bf16 v[100:103], a[28:31], v[144:147], 0\n"
"v_mfma_f32_16x16x32_bf16 v[100:103], a[60:63], v[116:119], v[100:103]\n"
"v_mfma_f32_16x16x32_bf16 v[100:103], a[92:95], v[220:223], v[100:103]\n"
"v_mfma_f32_16x16x32_bf16 v[100:103], a[124:127], v[216:219], v[100:103]\n"
"v_mfma_f32_16x16x32_bf16 v[100:103], a[156:159], v[172:175], v[100:103]\n"
"v_mfma_f32_16x16x32_bf16 v[100:103], a[188:191], v[160:163], v[100:103]\n"
"v_mfma_f32_16x16x32_bf16 v[100:103], a[220:223], v[188:191], v[100:103]\n"
"v_mfma_f32_16x16x32_bf16 v[100:103], a[252:255], v[152:155], v[100:103]\n"
"s_nop 7\n"
"s_waitcnt vmcnt(0)\n"
"s_branch .Lgs_back6\n"
".Lgs_slow5:\n"
"s_mov_b32 s24, 0\n"
".Lgs_loop5:\n"
"buffer_load_dwordx4 v[176:179], v1, s[36:39], s48 offen offset:2048 sc1\n"
"buffer_load_dwordx4 v[228:231], v1, s[36:39], s49 offen offset:2048 sc1\n"
"buffer_load_dwordx4 v[128:131], v1, s[36:39], s50 offen offset:2048 sc1\n"
"buffer_load_dwordx4 v[240:243], v1, s[36:39], s51 offen offset:2048 sc1\n"
"buffer_load_dwordx4 v[236:239], v1, s[36:39], s52 offen offset:2048 sc1\n"
"buffer_load_dwordx4 v[196:199], v1, s[36:39], s53 offen offset:2048 sc1\n"
"buffer_load_dwordx4 v[124:127], v1, s[36:39], s54 offen offset:2048 sc1\n"
"buffer_load_dwordx4 v[120:123], v1, s[36:39], s55 offen offset:2048 sc1\n"
"buffer_load_dwordx4 v[144:147], v2, s[36:39], s48 offen offset:2048 sc1\n"
"buffer_load_dwordx4 v[116:119], v2, s[36:39], s49 offen offset:2048 sc1\n"
"buffer_load_dwordx4 v[220:223], v2, s[36:39], s50 offen offset:2048 sc1\n"
"buffer_load_dwordx4 v[216:219], v2, s[36:39], s51 offen offset:2048 sc1\n"
"buffer_load_dwordx4 v[172:175], v2, s[36:39], s52 offen offset:2048 sc1\n"
"buffer_load_dwordx4 v[160:163], v2, s[36:39], s53 offen offset:2048 sc1\n"
"buffer_load_dwordx4 v[188:191], v2, s[36:39], s54 offen offset:2048 sc1\n"
"s_waitcnt vmcnt(7)\n"
"v_max3_u32 v17, v177, v179, v229\n"
"v_max3_u32 v17, v231, v129, v17\n"
"v_max3_u32 v17, v131, v241, v17\n"
"v_max3_u32 v17, v243, v237, v17\n"
"v_max3_u32 v17, v239, v197, v17\n"
"v_max3_u32 v17, v199, v125, v17\n"
"v_max3_u32 v17, v127, v121, v17\n"
"v_max_u32 v17, v123, v17\n"
"v_cmp_le_u32 vcc, 0xffc00000, v17\n"
"s_cbranch_vccz .Lgs_done5\n"
"s_sleep 4\n"
"s_add_u32 s24, s24, 1\n"
"s_cmp_lt_u32 s24, 0x10000\n"
"s_cbranch_scc1 .Lgs_loop5\n"
".Lgs_done5:\n"
"v_mfma_f32_16x16x32_bf16 v[184:187], a[24:27], v[176:179], 0\n"
"v_mfma_f32_16x16x32_bf16 v[184:187], a[56:59], v[228:231], v[184:187]\n"
"v_mfma_f32_16x16x32_bf16 v[184:187], a[88:91], v[128:131], v[184:187]\n"
"v_mfma_f32_16x16x32_bf16 v[184:187], a[120:123], v[240:243], v[184:187]\n"
"v_mfma_f32_16x16x32_bf16 v[184:187], a[152:155], v[236:239], v[184:187]\n"
"v_mfma_f32_16x16x32_bf16 v[184:187], a[184:187], v[196:199], v[184:187]\n"
"v_mfma_f32_16x16x32_bf16 v[184:187], a[216:219], v[124:127], v[184:187]\n"
"v_mfma_f32_16x16x32_bf16 v[184:187], a[248:251], v[120:123], v[184:187]\n"
"v_mfma_f32_16x16x32_bf16 v[168:171], a[28:31], v[176:179], 0\n"
"v_mfma_f32_16x16x32_bf16 v[168:171], a[60:63], v[228:231], v[168:171]\n"
"v_mfma_f32_16x16x32_bf16 v[168:171], a[92:95], v[128:131], v[168:171]\n"
"v_mfma_f32_16x16x32_bf16 v[168:171], a[124:127], v[240:243], v[168:171]\n"
"v_mfma_f32_16x16x32_bf16 v[168:171], a[156:159], v[236:239], v[168:171]\n"
"v_mfma_f32_16x16x32_bf16 v[168:171], a[188:191], v[196:199], v[168:171]\n"
"v_mfma_f32_16x16x32_bf16 v[168:171], a[220:223], v[124:127], v[168:171]\n"
"v_mfma_f32_16x16x32_bf16 v[168:171], a[252:255], v[120:123], v[168:171]\n"
"s_nop 7\n"
"s_waitcnt vmcnt(0)\n"
"s_branch .Lgs_back5\n"
".Lgs_slow4:\n"
"s_mov_b32 s24, 0\n"
".Lgs_loop4:\n"
"buffer_load_dwordx4 v[164:167], v2, s[36:39], s48 offen offset:1024 sc1\n"
"buffer_load_dwordx4 v[140:143], v2, s[36:39], s49 offen offset:1024 sc1\n"
"buffer_load_dwordx4 v[156:159], v2, s[36:39], s50 offen offset:1024 sc1\n"
"buffer_load_dwordx4 v[252:255], v2, s[36:39], s51 offen offset:1024 sc1\n"
"buffer_load_dwordx4 v[248:251], v2, s[36:39], s52 offen offset:1024 sc1\n"
"buffer_load_dwordx4 v[232:235], v2, s[36:39], s53 offen offset:1024 sc1\n"
"buffer_load_dwordx4 v[224:227], v2, s[36:39], s54 offen offset:1024 sc1\n"
"buffer_load_dwordx4 v[244:247], v2, s[36:39], s55 offen offset:1024 sc1\n"
"buffer_load_dwordx4 v[176:179], v1, s[36:39], s48 offen offset:2048 sc1\n"
"buffer_load_dwordx4 v[228:231], v1, s[36:39], s49 offen offset:2048 sc1\n"
"buffer_load_dwordx4 v[128:131], v1, s[36:39], s50 offen offset:2048 sc1\n"
"buffer_load_dwordx4 v[240:243], v1, s[36:39], s51 offen offset:2048 sc1\n"
"buffer_load_dwordx4 v[236:239], v1, s[36:39], s52 offen offset:2048 sc1\n"
"buffer_load_dwordx4 v[196:199], v1, s[36:39], s53 offen offset:2048 sc1\n"
"buffer_load_dwordx4 v[124:127], v1, s[36:39], s54 offen offset:2048 sc1\n"
"buffer_load_dwordx4 v[120:123], v1, s[36:39], s55 offen offset:2048 sc1\n"
"s_waitcnt vmcnt(8)\n"
"v_max3_u32 v14, v165, v167, v141\n"
"v_max3_u32 v14, v143, v157, v14\n"
"v_max3_u32 v14, v159, v253, v14\n"
"v_max3_u32 v14, v255, v249, v14\n"
"v_max3_u32 v14, v251, v233, v14\n"
"v_max3_u32 v14, v235, v225, v14\n"
"v_max3_u32 v14, v227, v245, v14\n"
"v_max_u32 v14, v247, v14\n"
"v_cmp_le_u32 vcc, 0xffc00000, v14\n"
"s_cbranch_vccz .Lgs_done4\n"
"s_sleep 4\n"
"s_add_u32 s24, s24, 1\n"
"s_cmp_lt_u32 s24, 0x10000\n"
"s_cbranch_scc1 .Lgs_loop4\n"
".Lgs_done4:\n"
"v_mfma_f32_16x16x32_bf16 v[216:219], a[24:27], v[164:167], 0\n"
"v_mfma_f32_16x16x32_bf16 v[216:219], a[56:59], v[140:143], v[216:219]\n"
"v_mfma_f32_16x16x32_bf16 v[216:219], a[88:91], v[156:159], v[216:219]\n"
"v_mfma_f32_16x16x32_bf16 v[216:219], a[120:123], v[252:255], v[216:219]\n"
"v_mfma_f32_16x16x32_bf16 v[216:219], a[152:155], v[248:251], v[216:219]\n"
"v_mfma_f32_16x16x32_bf16 v[216:219], a[184:187], v[232:235], v[216:219]\n"
"v_mfma_f32_16x16x32_bf16 v[216:219], a[216:219], v[224:227], v[216:219]\n"
"v_mfma_f32_16x16x32_bf16 v[216:219], a[248:251], v[244:247], v[216:219]\n"
"v_mfma_f32_16x16x32_bf16 v[136:139], a[28:31], v[164:167], 0\n"
"v_mfma_f32_16x16x32_bf16 v[136:139], a[60:63], v[140:143], v[136:139]\n"
"v_mfma_f32_16x16x32_bf16 v[136:139], a[92:95], v[156:159], v[136:139]\n"
"v_mfma_f32_16x16x32_bf16 v[136:139], a[124:127], v[252:255], v[136:139]\n"
"v_mfma_f32_16x16x32_bf16 v[136:139], a[156:159], v[248:251], v[136:139]\n"
"v_mfma_f32_16x16x32_bf16 v[136:139], a[188:191], v[232:235], v[136:139]\n"
"v_mfma_f32_16x16x32_bf16 v[136:139], a[220:223], v[224:227], v[136:139]\n"
"v_mfma_f32_16x16x32_bf16 v[136:139], a[252:255], v[244:247], v[136:139]\n"
"s_nop 7\n"
"s_waitcnt vmcnt(0)\n"
"s_branch .Lgs_back4\n"
".Lgs_slow3:\n"
"s_mov_b32 s24, 0\n"
".Lgs_loop3:\n"
"buffer_load_dwordx4 v[184:187], v1, s[36:39], s48 offen offset:1024 sc1\n"
"buffer_load_dwordx4 v[180:183], v1, s[36:39], s49 offen offset:1024 sc1\n"
"buffer_load_dwordx4 v[172:175], v1, s[36:39], s50 offen offset:1024 sc1\n"
"buffer_load_dwordx4 v[168:171], v1, s[36:39], s51 offen offset:1024 sc1\n"
"buffer_load_dwordx4 v[160:163], v1, s[36:39], s52 offen offset:1024 sc1\n"
"buffer_load_dwordx4 v[188:191], v1, s[36:39], s53 offen offset:1024 sc1\n"
"buffer_load_dwordx4 v[152:155], v1, s[36:39], s54 offen offset:1024 sc1\n"
"buffer_load_dwordx4 v[148:151], v1, s[36:39], s55 offen offset:1024 sc1\n"
"buffer_load_dwordx4 v[164:167], v2, s[36:39], s48 offen offset:1024 sc1\n"
"buffer_load_dwordx4 v[140:143], v2, s[36:39], s49 offen offset:1024 sc1\n"
"buffer_load_dwordx4 v[156:159], v2, s[36:39], s50 offen offset:1024 sc1\n"
"buffer_load_dwordx4 v[252:255], v2, s[36:39], s51 offen offset:1024 sc1\n"
"buffer_load_dwordx4 v[248:251], v2, s[36:39], s52 offen offset:1024 sc1\n"
"buffer_load_dwordx4 v[232:235], v2, s[36:39], s53 offen offset:1024 sc1\n"
"buffer_load_dwordx4 v[224:227], v2, s[36:39], s54 offen offset:1024 sc1\n"
"buffer_load_dwordx4 v[244:247], v2, s[36:39], s55 offen offset:1024 sc1\n"
"buffer_load_dwordx4 v[176:179], v1, s[36:39], s48 offen offset:2048 sc1\n"
"s_waitcnt vmcnt(9)\n"
"v_max3_u32 v14, v185, v187, v181\n"
"v_max3_u32 v14, v183, v173, v14\n"
"v_max3_u32 v14, v175, v169, v14\n"
"v_max3_u32 v14, v171, v161, v14\n"
"v_max3_u32 v14, v163, v189, v14\n"
"v_max3_u32 v14, v191, v153, v14\n"
"v_max3_u32 v14, v155, v149, v14\n"
"v_max_u32 v14, v151, v14\n"
"v_cmp_le_u32 vcc, 0xffc00000, v14\n"
"s_cbranch_vccz .Lgs_done3\n"
"s_sleep 4\n"
"s_add_u32 s24, s24, 1\n"
"s_cmp_lt_u32 s24, 0x10000\n"
"s_cbranch_scc1 .Lgs_loop3\n"
".Lgs_done3:\n"
"v_mfma_f32_16x16x32_bf16 v[240:243], a[24:27], v[184:187], 0\n"
"v_mfma_f32_16x16x32_bf16 v[240:243], a[56:59], v[180:183], v[240:243]\n"
"v_mfma_f32_16x16x32_bf16 v[240:243], a[88:91], v[172:175], v[240:243]\n"
"v_mfma_f32_16x16x32_bf16 v[240:243], a[120:123], v[168:171], v[240:243]\n"
"v_mfma_f32_16x16x32_bf16 v[240:243], a[152:155], v[160:163], v[240:243]\n"
"v_mfma_f32_16x16x32_bf16 v[240:243], a[184:187], v[188:191], v[240:243]\n"
"v_mfma_f32_16x16x32_bf16 v[240:243], a[216:219], v[152:155], v[240:243]\n"
"v_mfma_f32_16x16x32_bf16 v[240:243], a[248:251], v[148:151], v[240:243]\n"
"v_mfma_f32_16x16x32_bf16 v[228:231], a[28:31], v[184:187], 0\n"
"v_mfma_f32_16x16x32_bf16 v[228:231], a[60:63], v[180:183], v[228:231]\n"
"v_mfma_f32_16x16x32_bf16 v[228:231], a[92:95], v[172:175], v[228:231]\n"
"v_mfma_f32_16x16x32_bf16 v[228:231], a[124:127], v[168:171], v[228:231]\n"
"v_mfma_f32_16x16x32_bf16 v[228:231], a[156:159], v[160:163], v[228:231]\n"
"v_mfma_f32_16x16x32_bf16 v[228:231], a[188:191], v[188:191], v[228:231]\n"
"v_mfma_f32_16x16x32_bf16 v[228:231], a[220:223], v[152:155], v[228:231]\n"
"v_mfma_f32_16x16x32_bf16 v[228:231], a[252:255], v[148:151], v[228:231]\n"
"s_nop 7\n"
"s_waitcnt vmcnt(0)\n"
"s_branch .Lgs_back3\n"
".Lgs_slow2:\n"
"s_mov_b32 s24, 0\n"
".Lgs_loop2:\n"
"buffer_load_dwordx4 v[220:223], v2, s[36:39], s48 offen offset:0 sc1\n"
"buffer_load_dwordx4 v[216:219], v2, s[36:39], s49 offen offset:0 sc1\n"
"buffer_load_dwordx4 v[212:215], v2, s[36:39], s50 offen offset:0 sc1\n"
"buffer_load_dwordx4 v[208:211], v2, s[36:39], s51 offen offset:0 sc1\n"
"buffer_load_dwordx4 v[204:207], v2, s[36:39], s52 offen offset:0 sc1\n"
"buffer_load_dwordx4 v[200:203], v2, s[36:39], s53 offen offset:0 sc1\n"
"buffer_load_dwordx4 v[196:199], v2, s[36:39], s54 offen offset:0 sc1\n"
"buffer_load_dwordx4 v[192:195], v2, s[36:39], s55 offen offset:0 sc1\n"
"buffer_load_dwordx4 v[184:187], v1, s[36:39], s48 offen offset:1024 sc1\n"
"buffer_load_dwordx4 v[180:183], v1, s[36:39], s49 offen offset:1024 sc1\n"
"buffer_load_dwordx4 v[172:175], v1, s[36:39], s50 offen offset:1024 sc1\n"
"buffer_load_dwordx4 v[168:171], v1, s[36:39], s51 offen offset:1024 sc1\n"
"buffer_load_dwordx4 v[160:163], v1, s[36:39], s52 offen offset:1024 sc1\n"
"buffer_load_dwordx4 v[188:191], v1, s[36:39], s53 offen offset:1024 sc1\n"
"buffer_load_dwordx4 v[152:155], v1, s[36:39], s54 offen offset:1024 sc1\n"
"buffer_load_dwordx4 v[148:151], v1, s[36:39], s55 offen offset:1024 sc1\n"
"buffer_load_dwordx4 v[164:167], v2, s[36:39], s48 offen offset:1024 sc1\n"
"buffer_load_dwordx4 v[140:143], v2, s[36:39], s49 offen offset:1024 sc1\n"
"buffer_load_dwordx4 v[156:159], v2, s[36:39], s50 offen offset:1024 sc1\n"
"s_waitcnt vmcnt(11)\n"
"v_max3_u32 v14, v221, v223, v217\n"
"v_max3_u32 v14, v219, v213, v14\n"
"v_max3_u32 v14, v215, v209, v14\n"
"v_max3_u32 v14, v211, v205, v14\n"
"v_max3_u32 v14, v207, v201, v14\n"
"v_max3_u32 v14, v203, v197, v14\n"
"v_max3_u32 v14, v199, v193, v14\n"
"v_max_u32 v14, v195, v14\n"
"v_cmp_le_u32 vcc, 0xffc00000, v14\n"
"s_cbranch_vccz .Lgs_done2\n"
"s_sleep 4\n"
"s_add_u32 s24, s24, 1\n"
"s_cmp_lt_u32 s24, 0x10000\n"
"s_cbranch_scc1 .Lgs_loop2\n"
".Lgs_done2:\n"
"v_mfma_f32_16x16x32_bf16 v[176:179], a[24:27], v[220:223], 0\n"
"v_mfma_f32_16x16x32_bf16 v[176:179], a[56:59], v[216:219], v[176:179]\n"
"v_mfma_f32_16x16x32_bf16 v[176:179], a[88:91], v[212:215], v[176:179]\n"
"v_mfma_f32_16x16x32_bf16 v[176:179], a[120:123], v[208:211], v[176:179]\n"
"v_mfma_f32_16x16x32_bf16 v[176:179], a[152:155], v[204:207], v[176:179]\n"
"v_mfma_f32_16x16x32_bf16 v[176:179], a[184:187], v[200:203], v[176:179]\n"
"v_mfma_f32_16x16x32_bf16 v[176:179], a[216:219], v[196:199], v[176:179]\n"
"v_mfma_f32_16x16x32_bf16 v[176:179], a[248:251], v[192:195], v[176:179]\n"
"v_mfma_f32_16x16x32_bf16 v[144:147], a[28:31], v[220:223], 0\n"
"v_mfma_f32_16x16x32_bf16 v[144:147], a[60:63], v[216:219], v[144:147]\n"
"v_mfma_f32_16x16x32_bf16 v[144:147], a[92:95], v[212:215], v[144:147]\n"
"v_mfma_f32_16x16x32_bf16 v[144:147], a[124:127], v[208:211], v[144:147]\n"
"v_mfma_f32_16x16x32_bf16 v[144:147], a[156:159], v[204:207], v[144:147]\n"
"v_mfma_f32_16x16x32_bf16 v[144:147], a[188:191], v[200:203], v[144:147]\n"
"v_mfma_f32_16x16x32_bf16 v[144:147], a[220:223], v[196:199], v[144:147]\n"
"v_mfma_f32_16x16x32_bf16 v[144:147], a[252:255], v[192:195], v[144:147]\n"
"s_nop 7\n"
"s_waitcnt vmcnt(0)\n"
"s_branch .Lgs_back2\n"
".Lgs_slow1:\n"
"s_mov_b32 s24, 0\n"
".Lgs_loop1:\n"
"buffer_load_dwordx4 v[252:255], v1, s[36:39], s48 offen offset:0 sc1\n"
"buffer_load_dwordx4 v[248:251], v1, s[36:39], s49 offen offset:0 sc1\n"
"buffer_load_dwordx4 v[244:247], v1, s[36:39], s50 offen offset:0 sc1\n"
"buffer_load_dwordx4 v[240:243], v1, s[36:39], s51 offen offset:0 sc1\n"
"buffer_load_dwordx4 v[236:239], v1, s[36:39], s52 offen offset:0 sc1\n"
"buffer_load_dwordx4 v[232:235], v1, s[36:39], s53 offen offset:0 sc1\n"
"buffer_load_dwordx4 v[228:231], v1, s[36:39], s54 offen offset:0 sc1\n"
"buffer_load_dwordx4 v[224:227], v1, s[36:39], s55 offen offset:0 sc1\n"
"buffer_load_dwordx4 v[220:223], v2, s[36:39], s48 offen offset:0 sc1\n"
"buffer_load_dwordx4 v[216:219], v2, s[36:39], s49 offen offset:0 sc1\n"
"buffer_load_dwordx4 v[212:215], v2, s[36:39], s50 offen offset:0 sc1\n"
"buffer_load_dwordx4 v[208:211], v2, s[36:39], s51 offen offset:0 sc1\n"
"buffer_load_dwordx4 v[204:207], v2, s[36:39], s52 offen offset:0 sc1\n"
"buffer_load_dwordx4 v[200:203], v2, s[36:39], s53 offen offset:0 sc1\n"
"buffer_load_dwordx4 v[196:199], v2, s[36:39], s54 offen offset:0 sc1\n"
"buffer_load_dwordx4 v[192:195], v2, s[36:39], s55 offen offset:0 sc1\n"
"buffer_load_dwordx4 v[184:187], v1, s[36:39], s48 offen offset:1024 sc1\n"
"buffer_load_dwordx4 v[180:183], v1, s[36:39], s49 offen offset:1024 sc1\n"
"buffer_load_dwordx4 v[172:175], v1, s[36:39], s50 offen offset:1024 sc1\n"
"buffer_load_dwordx4 v[168:171], v1, s[36:39], s51 offen offset:1024 sc1\n"
"s_waitcnt vmcnt(12)\n"
"v_max3_u32 v14, v253, v255, v249\n"
"v_max3_u32 v14, v251, v245, v14\n"
"v_max3_u32 v14, v247, v241, v14\n"
"v_max3_u32 v14, v243, v237, v14\n"
"v_max3_u32 v14, v239, v233, v14\n"
"v_max3_u32 v14, v235, v229, v14\n"
"v_max3_u32 v14, v231, v225, v14\n"
"v_max_u32 v14, v227, v14\n"
"v_cmp_le_u32 vcc, 0xffc00000, v14\n"
"s_cbranch_vccz .Lgs_done1\n"
"s_sleep 4\n"
"s_add_u32 s24, s24, 1\n"
"s_cmp_lt_u32 s24, 0x10000\n"
"s_cbranch_scc1 .Lgs_loop1\n"
".Lgs_done1:\n"
"v_mfma_f32_16x16x32_bf16 v[188:191], a[24:27], v[252:255], 0\n"
"v_mfma_f32_16x16x32_bf16 v[188:191], a[56:59], v[248:251], v[188:191]\n"
"v_mfma_f32_16x16x32_bf16 v[188:191], a[88:91], v[244:247], v[188:191]\n"
"v_mfma_f32_16x16x32_bf16 v[188:191], a[120:123], v[240:243], v[188:191]\n"
"v_mfma_f32_16x16x32_bf16 v[188:191], a[152:155], v[236:239], v[188:191]\n"
"v_mfma_f32_16x16x32_bf16 v[188:191], a[184:187], v[232:235], v[188:191]\n"
"v_mfma_f32_16x16x32_bf16 v[188:191], a[216:219], v[228:231], v[188:191]\n"
"v_mfma_f32_16x16x32_bf16 v[188:191], a[248:251], v[224:227], v[188:191]\n"
"v_mfma_f32_16x16x32_bf16 v[176:179], a[28:31], v[252:255], 0\n"
"v_mfma_f32_16x16x32_bf16 v[176:179], a[60:63], v[248:251], v[176:179]\n"
"v_mfma_f32_16x16x32_bf16 v[176:179], a[92:95], v[244:247], v[176:179]\n"
"v_mfma_f32_16x16x32_bf16 v[176:179], a[124:127], v[240:243], v[176:179]\n"
"v_mfma_f32_16x16x32_bf16 v[176:179], a[156:159], v[236:239], v[176:179]\n"
"v_mfma_f32_16x16x32_bf16 v[176:179], a[188:191], v[232:235], v[176:179]\n"
"v_mfma_f32_16x16x32_bf16 v[176:179], a[220:223], v[228:231], v[176:179]\n"
"v_mfma_f32_16x16x32_bf16 v[176:179], a[252:255], v[224:227], v[176:179]\n"
"s_nop 7\n"
"s_waitcnt vmcnt(0)\n"
"s_branch .Lgs_back1\n"
".Lgs_spare:\n"
"v_readlane_b32 s84, v37, 0\n"
"v_readlane_b32 s85, v37, 1\n"
"v_readlane_b32 s86, v37, 2\n"
"v_readlane_b32 s87, v37, 3\n"
"v_readlane_b32 s68, v36, 0\n"
"v_readlane_b32 s69, v36, 1\n"
"v_readlane_b32 s70, v36, 2\n"
"v_readlane_b32 s71, v36, 3\n"
"v_readlane_b32 s72, v36, 4\n"
"v_readlane_b32 s73, v36, 5\n"
"v_readlane_b32 s74, v36, 6\n"
"v_readlane_b32 s75, v36, 7\n"
"v_readlane_b32 s76, v36, 8\n"
"s_bfe_u32 s65, s89, 0x40004\n"
"s_mul_i32 s66, s65, 147456\n"
"s_add_u32 s56, s91, s66\n"
"s_add_u32 s66, s91, 1327104\n"
"s_add_u32 s57, s56, 147456\n"
"s_add_u32 s58, s57, 147456\n"
"s_add_u32 s59, s58, 147456\n"
"s_cmp_eq_u32 s59, s66\n"
"s_cselect_b32 s59, s91, s59\n"
"s_add_u32 s60, s59, 147456\n"
"s_add_u32 s61, s60, 147456\n"
"s_cmp_eq_u32 s61, s66\n"
"s_cselect_b32 s61, s91, s61\n"
"s_add_u32 s62, s61, 147456\n"
"s_add_u32 s63, s62, 147456\n"
"s_cmp_eq_u32 s63, s66\n"
"s_cselect_b32 s63, s91, s63\n"
"s_add_u32 s64, s63, 147456\n"
"s_lshl_b32 s65, s65, 19\n"
"s_add_u32 s65, s65, s82\n"
"s_add_u32 s66, s82, 4718592\n"
"s_add_u32 s92, s88, 3072\n"
"s_add_u32 s93, s88, 6144\n"
"s_and_b32 s94, s89, 0x7ff00000\n"
"s_xor_b32 s95, s94, 0x7ff00000\n"
"s_mov_b32 s25, 0\n"
"s_mov_b32 s21, 0\n"
"s_mov_b32 s22, 0\n"
"s_mov_b64 vcc, -1\n"
"s_mov_b64 exec, 0\n"
"s_cmp_lt_u32 s77, 40960\n"
"s_cbranch_scc1 .Lgs_pad0\n"
"s_cmp_lt_u32 s77, 81920\n"
"s_cbranch_scc1 .Lgs_pad1\n"
"s_cmp_lt_u32 s77, 122880\n"
"s_cbranch_scc1 .Lgs_pad2\n"
"s_branch .Lgs_pad3\n"
".Lgs_al2:\n"
"s_mov_b64 s[48:49], s[40:41]\n"
"s_min_u32 s50, s66, s42\n"
"s_mov_b32 s51, s43\n"
"s_add_u32 m0, s77, 0\n"
"s_mov_b32 s20, s65\n"
"buffer_load_dwordx4 v6, s[40:43], s20 offen offset:0 lds\n"
"buffer_load_dwordx4 v7, s[40:43], s20 offen offset:1024 lds\n"
"buffer_load_dwordx4 v8, s[40:43], s20 offen offset:2048 lds\n"
"buffer_load_dwordx4 v9, s[40:43], s20 offen offset:3072 lds\n"
"s_add_u32 m0, s77, 4096\n"
"s_add_u32 s20, s20, 524288\n"
"buffer_load_dwordx4 v6, s[40:43], s20 offen offset:0 lds\n"
"buffer_load_dwordx4 v7, s[40:43], s20 offen offset:1024 lds\n"
"buffer_load_dwordx4 v8, s[40:43], s20 offen offset:2048 lds\n"
"buffer_load_dwordx4 v9, s[40:43], s20 offen offset:3072 lds\n"
"s_add_u32 m0, s77, 8192\n"
"s_add_u32 s20, s20, 524288\n"
"buffer_load_dwordx4 v6, s[40:43], s20 offen offset:0 lds\n"
"buffer_load_dwordx4 v7, s[40:43], s20 offen offset:1024 lds\n"
"buffer_load_dwordx4 v8, s[40:43], s20 offen offset:2048 lds\n"
"buffer_load_dwordx4 v9, s[40:43], s20 offen offset:3072 lds\n"
"s_waitcnt vmcnt(1)\n"
"v_lshrrev_b32 v1, 11, v35\n"
"v_and_b32 v1, 0x80, v1\n"
"v_or_b32 v1, v35, v1\n"
"v_add_u32 v35, 0xa0000, v35\n"
"s_add_u32 m0, s77, 40704\n"
"s_nop 0\n"
"buffer_load_dword v1, s[48:51], 0 offen lds\n"
"s_waitcnt vmcnt(1)\n"
"v_lshrrev_b32 v1, 11, v35\n"
"v_and_b32 v1, 0x80, v1\n"
"v_or_b32 v1, v35, v1\n"
"v_add_u32 v35, 0xa0000, v35\n"
"buffer_load_dword v1, s[48:51], 0 offen lds\n"
"s_waitcnt vmcnt(1)\n"
"v_lshrrev_b32 v1, 11, v35\n"
"v_and_b32 v1, 0x80, v1\n"
"v_or_b32 v1, v35, v1\n"
"v_add_u32 v35, 0xa0000, v35\n"
"buffer_load_dword v1, s[48:51], 0 offen lds\n"
"s_waitcnt vmcnt(1)\n"
"v_lshrrev_b32 v1, 11, v35\n"
"v_and_b32 v1, 0x80, v1\n"
"v_or_b32 v1, v35, v1\n"
"v_add_u32 v35, 0xa0000, v35\n"
"buffer_load_dword v1, s[48:51], 0 offen lds\n"
"s_waitcnt vmcnt(1)\n"
"v_lshrrev_b32 v1, 11, v35\n"
"v_and_b32 v1, 0x80, v1\n"
"v_or_b32 v1, v35, v1\n"
"v_add_u32 v35, 0xa0000, v35\n"
"buffer_load_dword v1, s[48:51], 0 offen lds\n"
"v_mov_b32 v100, -1\n"
"v_mov_b32 v101, -1\n"
"v_mov_b32 v102, -1\n"
"v_mov_b32 v103, -1\n"
"v_or_b32 v24, s94, v0\n"
"buffer_store_dwordx4 v[100:103], v24, s[36:39], s84 offen offset:0 sc1\n"
"buffer_store_dwordx4 v[100:103], v0, s[36:39], s85 offen offset:0 sc1\n"
"buffer_store_dwordx4 v[100:103], v0, s[36:39], s86 offen offset:0 sc1\n"
"buffer_store_dwordx4 v[100:103], v0, s[36:39], s87 offen offset:0 sc1\n"
"buffer_load_dword v14, v20, s[36:39], 0 offen offset:0 sc1\n"
"s_waitcnt vmcnt(6)\n"
"v_lshrrev_b32 v1, 11, v35\n"
"v_and_b32 v1, 0x80, v1\n"
"v_or_b32 v1, v35, v1\n"
"v_add_u32 v35, 0xa0000, v35\n"
"buffer_load_dword v1, s[48:51], 0 offen lds\n"
"s_add_u32 m0, s77, 12288\n"
"s_add_u32 s20, s20, 524288\n"
"s_cmp_eq_u32 s20, s66\n"
"s_cselect_b32 s20, s82, s20\n"
"buffer_load_dwordx4 v6, s[40:43], s20 offen offset:0 lds\n"
"buffer_load_dwordx4 v7, s[40:43], s20 offen offset:1024 lds\n"
"buffer_load_dwordx4 v8, s[40:43], s20 offen offset:2048 lds\n"
"buffer_load_dwordx4 v9, s[40:43], s20 offen offset:3072 lds\n"
"s_add_u32 m0, s77, 16384\n"
"s_add_u32 s20, s20, 524288\n"
"buffer_load_dwordx4 v6, s[40:43], s20 offen offset:0 lds\n"
"buffer_load_dwordx4 v7, s[40:43], s20 offen offset:1024 lds\n"
"buffer_load_dwordx4 v8, s[40:43], s20 offen offset:2048 lds\n"
"buffer_load_dwordx4 v9, s[40:43], s20 offen offset:3072 lds\n"
"s_add_u32 m0, s77, 20480\n"
"s_add_u32 s20, s20, 524288\n"
"s_cmp_eq_u32 s20, s66\n"
"s_cselect_b32 s20, s82, s20\n"
"buffer_load_dwordx4 v6, s[40:43], s20 offen offset:0 lds\n"
"buffer_load_dwordx4 v7, s[40:43], s20 offen offset:1024 lds\n"
"buffer_load_dwordx4 v8, s[40:43], s20 offen offset:2048 lds\n"
"buffer_load_dwordx4 v9, s[40:43], s20 offen offset:3072 lds\n"
"s_waitcnt vmcnt(13)\n"
"v_cmp_ne_u32 vcc, 32, v14\n"
"s_cbranch_vccz .Lgs_done29\n"
"s_mov_b32 s24, 0\n"
".Lgs_loop29:\n"
"buffer_load_dword v14, v20, s[36:39], 0 offen offset:0 sc1\n"
"s_waitcnt vmcnt(0)\n"
"v_cmp_ne_u32 vcc, 32, v14\n"
"s_cbranch_vccz .Lgs_done29\n"
"s_sleep 16\n"
"s_add_u32 s24, s24, 1\n"
"s_cmp_lt_u32 s24, 0x10000\n"
"s_cbranch_scc1 .Lgs_loop29\n"
".Lgs_done29:\n"
"ds_read_b64_tr_b16 v[208:209], v12 offset:0\n"
"ds_read_b64_tr_b16 v[210:211], v12 offset:512\n"
"ds_read_b64_tr_b16 v[204:205], v13 offset:0\n"
"ds_read_b64_tr_b16 v[206:207], v13 offset:512\n"
"ds_read_b64_tr_b16 v[200:201], v10 offset:0\n"
"ds_read_b64_tr_b16 v[202:203], v10 offset:512\n"
"ds_read_b64_tr_b16 v[192:193], v11 offset:0\n"
"ds_read_b64_tr_b16 v[194:195], v11 offset:512\n"
"s_add_u32 m0, s77, 0\n"
"s_add_u32 s20, s20, 524288\n"
"s_waitcnt lgkmcnt(0)\n"
"buffer_load_dwordx4 v6, s[40:43], s20 offen offset:0 lds\n"
"buffer_load_dwordx4 v7, s[40:43], s20 offen offset:1024 lds\n"
"buffer_load_dwordx4 v8, s[40:43], s20 offen offset:2048 lds\n"
"buffer_load_dwordx4 v9, s[40:43], s20 offen offset:3072 lds\n"
"ds_read_b64_tr_b16 v[184:185], v12 offset:4096\n"
"ds_read_b64_tr_b16 v[186:187], v12 offset:4608\n"
"ds_read_b64_tr_b16 v[168:169], v13 offset:4096\n"
"ds_read_b64_tr_b16 v[170:171], v13 offset:4608\n"
"ds_read_b64_tr_b16 v[132:133], v10 offset:4096\n"
"ds_read_b64_tr_b16 v[134:135], v10 offset:4608\n"
"ds_read_b64_tr_b16 v[212:213], v11 offset:4096\n"
"ds_read_b64_tr_b16 v[214:215], v11 offset:4608\n"
"s_add_u32 m0, s77, 4096\n"
"s_add_u32 s20, s20, 524288\n"
"s_cmp_eq_u32 s20, s66\n"
"s_cselect_b32 s20, s82, s20\n"
"s_waitcnt lgkmcnt(0)\n"
"buffer_load_dwordx4 v6, s[40:43], s20 offen offset:0 lds\n"
"buffer_load_dwordx4 v7, s[40:43], s20 offen offset:1024 lds\n"
"buffer_load_dwordx4 v8, s[40:43], s20 offen offset:2048 lds\n"
"buffer_load_dwordx4 v9, s[40:43], s20 offen offset:3072 lds\n"
"ds_read_b64_tr_b16 v[232:233], v12 offset:8192\n"
"ds_read_b64_tr_b16 v[234:235], v12 offset:8704\n"
"ds_read_b64_tr_b16 v[156:157], v13 offset:8192\n"
"ds_read_b64_tr_b16 v[158:159], v13 offset:8704\n"
"ds_read_b64_tr_b16 v[148:149], v10 offset:8192\n"
"ds_read_b64_tr_b16 v[150:151], v10 offset:8704\n"
"ds_read_b64_tr_b16 v[96:97], v11 offset:8192\n"
"ds_read_b64_tr_b16 v[98:99], v11 offset:8704\n"
"s_add_u32 m0, s77, 8192\n"
"s_add_u32 s20, s20, 524288\n"
"s_waitcnt lgkmcnt(0)\n"
"buffer_load_dwordx4 v6, s[40:43], s20 offen offset:0 lds\n"
"buffer_load_dwordx4 v7, s[40:43], s20 offen offset:1024 lds\n"
"buffer_load_dwordx4 v8, s[40:43], s20 offen offset:2048 lds\n"
"buffer_load_dwordx4 v9, s[40:43], s20 offen offset:3072 lds\n"
"s_waitcnt vmcnt(20)\n"
"ds_read_b64_tr_b16 v[248:249], v12 offset:12288\n"
"ds_read_b64_tr_b16 v[250:251], v12 offset:12800\n"
"ds_read_b64_tr_b16 v[68:69], v13 offset:12288\n"
"ds_read_b64_tr_b16 v[70:71], v13 offset:12800\n"
"ds_read_b64_tr_b16 v[228:229], v10 offset:12288\n"
"ds_read_b64_tr_b16 v[230:231], v10 offset:12800\n"
"ds_read_b64_tr_b16 v[240:241], v11 offset:12288\n"
"ds_read_b64_tr_b16 v[242:243], v11 offset:12800\n"
"s_add_u32 m0, s77, 12288\n"
"s_xor_b32 s20, s65, s67\n"
"s_xor_b32 s82, s82, s67\n"
"s_xor_b32 s66, s66, s67\n"
"s_waitcnt lgkmcnt(0)\n"
"buffer_load_dwordx4 v6, s[40:43], s20 offen offset:0 lds\n"
"buffer_load_dwordx4 v7, s[40:43], s20 offen offset:1024 lds\n"
"buffer_load_dwordx4 v8, s[40:43], s20 offen offset:2048 lds\n"
"buffer_load_dwordx4 v9, s[40:43], s20 offen offset:3072 lds\n"
"s_waitcnt vmcnt(20)\n"
"ds_read_b64_tr_b16 a[24:25], v12 offset:16384\n"
"ds_read_b64_tr_b16 a[26:27], v12 offset:16896\n"
"ds_read_b64_tr_b16 a[56:57], v13 offset:16384\n"
"ds_read_b64_tr_b16 a[58:59], v13 offset:16896\n"
"ds_read_b64_tr_b16 a[88:89], v10 offset:16384\n"
"ds_read_b64_tr_b16 a[90:91], v10 offset:16896\n"
"ds_read_b64_tr_b16 a[120:121], v11 offset:16384\n"
"ds_read_b64_tr_b16 a[122:123], v11 offset:16896\n"
"buffer_load_dword v14, v34, s[36:39], 0 offen offset:0 sc1\n"
"s_add_u32 m0, s77, 16384\n"
"s_add_u32 s20, s20, 524288\n"
"s_waitcnt lgkmcnt(0)\n"
"buffer_load_dwordx4 v6, s[40:43], s20 offen offset:0 lds\n"
"buffer_load_dwordx4 v7, s[40:43], s20 offen offset:1024 lds\n"
"buffer_load_dwordx4 v8, s[40:43], s20 offen offset:2048 lds\n"
"buffer_load_dwordx4 v9, s[40:43], s20 offen offset:3072 lds\n"
"s_waitcnt vmcnt(21)\n"
"ds_read_b64_tr_b16 a[152:153], v12 offset:20480\n"
"ds_read_b64_tr_b16 a[154:155], v12 offset:20992\n"
"ds_read_b64_tr_b16 a[184:185], v13 offset:20480\n"
"ds_read_b64_tr_b16 a[186:187], v13 offset:20992\n"
"ds_read_b64_tr_b16 a[216:217], v10 offset:20480\n"
"ds_read_b64_tr_b16 a[218:219], v10 offset:20992\n"
"ds_read_b64_tr_b16 a[248:249], v11 offset:20480\n"
"ds_read_b64_tr_b16 a[250:251], v11 offset:20992\n"
"s_add_u32 m0, s77, 20480\n"
"s_add_u32 s20, s20, 524288\n"
"s_waitcnt lgkmcnt(0)\n"
"buffer_load_dwordx4 v6, s[40:43], s20 offen offset:0 lds\n"
"buffer_load_dwordx4 v7, s[40:43], s20 offen offset:1024 lds\n"
"buffer_load_dwordx4 v8, s[40:43], s20 offen offset:2048 lds\n"
"buffer_load_dwordx4 v9, s[40:43], s20 offen offset:3072 lds\n"
"s_waitcnt vmcnt(21)\n"
"ds_read_b64_tr_b16 a[28:29], v12 offset:0\n"
"ds_read_b64_tr_b16 a[30:31], v12 offset:512\n"
"ds_read_b64_tr_b16 a[60:61], v13 offset:0\n"
"ds_read_b64_tr_b16 a[62:63], v13 offset:512\n"
"ds_read_b64_tr_b16 a[92:93], v10 offset:0\n"
"ds_read_b64_tr_b16 a[94:95], v10 offset:512\n"
"ds_read_b64_tr_b16 a[124:125], v11 offset:0\n"
"ds_read_b64_tr_b16 a[126:127], v11 offset:512\n"
"s_add_u32 m0, s77, 0\n"
"s_add_u32 s20, s20, 524288\n"
"s_cmp_eq_u32 s20, s66\n"
"s_cselect_b32 s20, s82, s20\n"
"s_waitcnt lgkmcnt(0)\n"
"buffer_load_dwordx4 v6, s[40:43], s20 offen offset:0 lds\n"
"buffer_load_dwordx4 v7, s[40:43], s20 offen offset:1024 lds\n"
"buffer_load_dwordx4 v8, s[40:43], s20 offen offset:2048 lds\n"
"buffer_load_dwordx4 v9, s[40:43], s20 offen offset:3072 lds\n"
"s_waitcnt vmcnt(21)\n"
"ds_read_b64_tr_b16 a[156:157], v12 offset:4096\n"
"ds_read_b64_tr_b16 a[158:159], v12 offset:4608\n"
"ds_read_b64_tr_b16 a[188:189], v13 offset:4096\n"
"ds_read_b64_tr_b16 a[190:191], v13 offset:4608\n"
"ds_read_b64_tr_b16 a[220:221], v10 offset:4096\n"
"ds_read_b64_tr_b16 a[222:223], v10 offset:4608\n"
"ds_read_b64_tr_b16 a[252:253], v11 offset:4096\n"
"ds_read_b64_tr_b16 a[254:255], v11 offset:4608\n"
"s_waitcnt vmcnt(12)\n"
"v_cmp_le_u32 vcc, 0xffc00000, v14\n"
"s_cbranch_vccz .Lgs_done30\n"
"s_mov_b32 s24, 0\n"
".Lgs_loop30:\n"
"buffer_load_dword v14, v34, s[36:39], 0 offen offset:0 sc1\n"
"s_waitcnt vmcnt(0)\n"
"v_cmp_le_u32 vcc, 0xffc00000, v14\n"
"s_cbranch_vccz .Lgs_done30\n"
"s_sleep 16\n"
"s_add_u32 s24, s24, 1\n"
"s_cmp_lt_u32 s24, 0x10000\n"
"s_cbranch_scc1 .Lgs_loop30\n"
".Lgs_done30:\n"
"v_add_u32 v24, s68, v0\n"
"buffer_load_dwordx4 v[100:103], v24, s[36:39], s56 offen sc1\n"
"buffer_load_dwordx4 v[76:79], v24, s[36:39], s57 offen sc1\n"
"buffer_load_dwordx4 v[144:147], v24, s[36:39], s58 offen sc1\n"
"buffer_load_dwordx4 v[56:59], v24, s[36:39], s59 offen sc1\n"
"buffer_load_dwordx4 v[176:179], v24, s[36:39], s60 offen sc1\n"
"buffer_load_dwordx4 v[172:175], v24, s[36:39], s61 offen sc1\n"
"buffer_load_dwordx4 v[164:167], v24, s[36:39], s62 offen sc1\n"
"buffer_load_dwordx4 v[216:219], v24, s[36:39], s63 offen sc1\n"
"buffer_load_dwordx4 v[220:223], v24, s[36:39], s64 offen sc1\n"
"v_add_u32 v4, s69, v0\n"
"buffer_load_dwordx4 v[136:139], v4, s[36:39], s56 offen sc1\n"
"buffer_load_dwordx4 v[80:83], v4, s[36:39], s57 offen sc1\n"
"buffer_load_dwordx4 v[180:183], v4, s[36:39], s58 offen sc1\n"
"buffer_load_dwordx4 v[152:155], v4, s[36:39], s59 offen sc1\n"
"buffer_load_dwordx4 v[236:239], v4, s[36:39], s60 offen sc1\n"
"buffer_load_dwordx4 v[252:255], v4, s[36:39], s61 offen sc1\n"
"buffer_load_dwordx4 v[188:191], v4, s[36:39], s62 offen sc1\n"
"buffer_load_dwordx4 v[112:115], v4, s[36:39], s63 offen sc1\n"
"buffer_load_dwordx4 v[108:111], v4, s[36:39], s64 offen sc1\n"
"v_add_u32 v17, s70, v0\n"
"buffer_load_dwordx4 v[244:247], v17, s[36:39], s56 offen sc1\n"
"s_add_u32 m0, s77, 4096\n"
"s_add_u32 s20, s20, 524288\n"
"s_waitcnt lgkmcnt(0)\n"
"buffer_load_dwordx4 v6, s[40:43], s20 offen offset:0 lds\n"
"ds_read_b64_tr_b16 a[16:17], v12 offset:8192\n"
"ds_read_b64_tr_b16 a[18:19], v12 offset:8704\n"
"ds_read_b64_tr_b16 a[48:49], v13 offset:8192\n"
"ds_read_b64_tr_b16 a[50:51], v13 offset:8704\n"
"ds_read_b64_tr_b16 a[80:81], v10 offset:8192\n"
"ds_read_b64_tr_b16 a[82:83], v10 offset:8704\n"
"ds_read_b64_tr_b16 a[112:113], v11 offset:8192\n"
"ds_read_b64_tr_b16 a[114:115], v11 offset:8704\n"
"ds_read_b64_tr_b16 a[144:145], v12 offset:12288\n"
"ds_read_b64_tr_b16 a[146:147], v12 offset:12800\n"
"ds_read_b64_tr_b16 a[176:177], v13 offset:12288\n"
"ds_read_b64_tr_b16 a[178:179], v13 offset:12800\n"
"ds_read_b64_tr_b16 a[208:209], v10 offset:12288\n"
"ds_read_b64_tr_b16 a[210:211], v10 offset:12800\n"
"ds_read_b64_tr_b16 a[240:241], v11 offset:12288\n"
"ds_read_b64_tr_b16 a[242:243], v11 offset:12800\n"
"s_waitcnt vmcnt(28)\n"
"ds_read_b64_tr_b16 a[20:21], v12 offset:16384\n"
"ds_read_b64_tr_b16 a[22:23], v12 offset:16896\n"
"ds_read_b64_tr_b16 a[52:53], v13 offset:16384\n"
"ds_read_b64_tr_b16 a[54:55], v13 offset:16896\n"
"ds_read_b64_tr_b16 a[84:85], v10 offset:16384\n"
"ds_read_b64_tr_b16 a[86:87], v10 offset:16896\n"
"ds_read_b64_tr_b16 a[116:117], v11 offset:16384\n"
"ds_read_b64_tr_b16 a[118:119], v11 offset:16896\n"
"s_waitcnt vmcnt(0)\n"
"s_add_u32 m0, s77, 4096\n"
"s_waitcnt lgkmcnt(0)\n"
"s_branch .Lgs_merge\n"
".Lgs_end:\n"
      : "+{v[0:15]}"(v0), "+{v[16:31]}"(v1), "+{v[32:47]}"(v2), "+{s[80:83]}"(s2)
      : "{s[36:39]}"(sw), "{s[40:43]}"(sb2), "{s[44:47]}"(so), "{s[78:79]}"(tg2), "{s77}"(sc[0]), "{s67}"(128 + 256 * sw2), "{s[88:91]}"(s3) : "memory", "vcc", "scc", C10("s", 2), SCLOB, VCLOB);
  }
  }
  asm volatile("" ::: "a255");
}

extern "C" void kernel_launch(void* const* d_in, const int* in_sizes, int n_in,
                              void* d_out, int out_size, void* d_ws, size_t ws_size,
                              hipStream_t stream) {
  k_all<<<256, 256, 0, stream>>>((const u16*)d_in[0], (const u16*)d_in[1], (const u16*)d_in[2], (const u16*)d_in[3], (const u16*)d_in[4],
                                 (const u16*)d_in[5], (const u16*)d_in[6], (const u16*)d_in[8], (const u16*)d_in[9], (const u16*)d_in[10],
                                 (const u16*)d_in[11], (char*)d_ws, (u16*)d_out);
}
```
